```python
import torch

def _softmax(x, axis=-1):
    return torch.softmax(x, dim=axis)

def _gelu(x):
    return torch.nn.functional.gelu(x, approximate='tanh')

def _silu(x):
    return torch.nn.functional.silu(x)

def _sigmoid(x):
    return torch.sigmoid(x)

def _layernorm(x, axis=-1, eps=1e-05):
    m = x.mean(dim=axis, keepdim=True)
    v = x.var(dim=axis, keepdim=True, unbiased=False)
    return (x - m) / torch.sqrt(v + eps)

def _scatter_add(tgt, idx, src, axis=0):
    return tgt.clone().index_add_(axis, idx, src)

def _segment_sum(src, seg, n):
    out = torch.zeros((n,) + src.shape[1:], dtype=src.dtype)
    return out.index_add_(0, seg, src)

def _pad(x, pw):
    flat = tuple((v for pair in reversed(pw) for v in pair))
    return torch.nn.functional.pad(x, flat)
kernel_name = 'adaLN_cond__compressed_kv_cla__mixer_gmlp__prompt'

def setup_inputs():

    _w = lambda a: (lambda t: t.to(torch.bfloat16) if t.is_floating_point() else t)(torch.as_tensor(a))

    def _A_setup():
        B, H, Sq, Sk, D, M = (1, 32, 2048, 2048, 128, 512)
        Dc = 4 * D
        return {'x': _w((lambda *s: torch.randn(*s, dtype=torch.bfloat16) * 0.05)(B, Sq, M)), 'c_kv': _w((lambda *s: torch.randn(*s, dtype=torch.bfloat16) * 1.6)(B, Sk, Dc)), 'w_q': _w((lambda *s: torch.randn(*s, dtype=torch.bfloat16) * 0.8)(M, H, D) * M ** (-0.5)), 'w_uk': _w((lambda *s: torch.randn(*s, dtype=torch.bfloat16) * 0.8)(Dc, H, D) * Dc ** (-0.5)), 'w_uv': _w((lambda *s: torch.randn(*s, dtype=torch.bfloat16) * 1.6)(Dc, H, D) * Dc ** (-0.5)), 'w_o': _w((lambda *s: torch.randn(*s, dtype=torch.bfloat16) * 1.6)(H, D, M) * (H * D) ** (-0.5))}
    _A_in = _A_setup()

    def _B_setup():
        B, S, D, F = (1, 2048, 512, 1376)
        Fh = F // 2
        return {'x': _w((lambda *s: torch.randn(*s, dtype=torch.bfloat16) * 0.05)(B, S, D)), 'w_in': _w((lambda *s: torch.randn(*s, dtype=torch.bfloat16) * 0.4)(D, 2 * Fh)), 'w_sp': _w(0.01 * (lambda *s: torch.randn(*s, dtype=torch.bfloat16) * 0.4)(S, S)), 'b_sp': _w(1.0 + 0.01 * (lambda *s: torch.randn(*s, dtype=torch.bfloat16) * 0.05)(S)), 'w_out': _w((lambda *s: torch.randn(*s, dtype=torch.bfloat16) * 0.05)(Fh, D))}
    _B_in = _B_setup()
    return {'A_x': _A_in['x'], 'A_c_kv': _A_in['c_kv'], 'A_w_q': _A_in['w_q'], 'A_w_uk': _A_in['w_uk'], 'A_w_uv': _A_in['w_uv'], 'A_w_o': _A_in['w_o'], 'B_x': _B_in['x'], 'B_w_in': _B_in['w_in'], 'B_w_sp': _B_in['w_sp'], 'B_b_sp': _B_in['b_sp'], 'B_w_out': _B_in['w_out'], 'c': _w((lambda *s: torch.randn(*s, dtype=torch.bfloat16) * 0.1)(1, 512) * 8.0), 'w_mod': _w((lambda *s: torch.randn(*s, dtype=torch.bfloat16) * 0.1)(512, 6 * 512) * 512 ** (-0.5) * 8.0)}

def reference(A_x, A_c_kv, A_w_q, A_w_uk, A_w_uv, A_w_o, B_x, B_w_in, B_w_sp, B_b_sp, B_w_out, c, w_mod):

    def _A_ref(x, c_kv, w_q, w_uk, w_uv, w_o):
        B, Sq, M = (x.shape[0], x.shape[1], x.shape[2])
        Sk = c_kv.shape[1]
        H, D = (w_q.shape[1], w_q.shape[2])
        q = torch.einsum('bqm,mhd->bhqd', x, w_q)
        k = torch.einsum('bkc,chd->bhkd', c_kv, w_uk)
        v = torch.einsum('bkc,chd->bhkd', c_kv, w_uv)
        scores = torch.einsum('bhqd,bhkd->bhqk', q, k) * D ** (-0.5)
        off = Sk - Sq
        qi = torch.arange(Sq)[:, None]
        kj = torch.arange(Sk)[None, :]
        cmask = kj <= qi + off
        scores = torch.where(cmask[None, None], scores, -1000000000.0)
        p = _softmax(scores, axis=-1)
        ctx = torch.einsum('bhqk,bhkd->bhqd', p, v)
        return torch.einsum('bhqd,hdm->bqm', ctx, w_o)

    def _B_ref(x, w_in, w_sp, b_sp, w_out):
        B, S, D = x.shape
        F = w_in.shape[-1]
        Fh = F // 2
        z = _gelu(torch.einsum('bsd,df->bsf', x, w_in))
        z1 = z[..., :Fh].clone()
        z2 = _layernorm(z[..., Fh:], axis=-1)
        g = torch.einsum('ts,bsf->btf', w_sp, z2) + b_sp[None, :, None]
        return torch.einsum('bsf,fd->bsd', z1 * g, w_out)
    x = A_x
    mod = (_silu(c) @ w_mod).reshape(c.shape[0], 6, 1, -1)
    sa, ca, ga, sb, cb, gb = (mod[:, 0].clone(), mod[:, 1].clone(), mod[:, 2].clone(), mod[:, 3].clone(), mod[:, 4].clone(), mod[:, 5].clone())
    h = _layernorm(x, axis=-1) * (1 + ca) + sa
    x = x + ga * _A_ref(h, A_c_kv, A_w_q, A_w_uk, A_w_uv, A_w_o)
    h = _layernorm(x, axis=-1) * (1 + cb) + sb
    x = x + gb * _B_ref(h, B_w_in, B_w_sp, B_b_sp, B_w_out)
    return x
```

```cpp
#include <hip/hip_runtime.h>
#include <hip/hip_bf16.h>
#include <stdint.h>

constexpr int S = 2048;
constexpr int DM = 512;
constexpr int NH = 32;
constexpr int HD = 128;
constexpr int HDM = NH * HD;
constexpr int FH = 688;
constexpr int FF = 1376;

typedef __attribute__((ext_vector_type(8))) short bf16x8;
typedef __attribute__((ext_vector_type(4))) short s16x4;
typedef __attribute__((ext_vector_type(4))) float f32x4;
typedef __attribute__((ext_vector_type(16))) float f32x16;
typedef __attribute__((ext_vector_type(4))) unsigned u32x4;
typedef __attribute__((ext_vector_type(2))) unsigned u32x2;
typedef __attribute__((address_space(3))) s16x4* lds_s16x4_ptr;
typedef unsigned short u16;

#define DI __device__ __forceinline__

DI float bf2f(u16 v) { return __uint_as_float(((unsigned)v) << 16); }
DI float bflo(unsigned v) { return __uint_as_float(v << 16); }
DI float bfhi(unsigned v) { return __uint_as_float(v & 0xffff0000u); }
DI unsigned pack2(float lo, float hi) {
  __hip_bfloat162 t = __float22bfloat162_rn(make_float2(lo, hi));
  return *reinterpret_cast<unsigned*>(&t);
}
DI float rbf(float x) { return bflo(pack2(x, 0.f)); }

DI void glds16(const void* g, void* lds_wave_base) {
  __builtin_amdgcn_global_load_lds((const unsigned*)g, (unsigned*)lds_wave_base, 16, 0, 0);
}
DI s16x4 tr_read(const char* p) {
  return __builtin_amdgcn_ds_read_tr16_b64_v4i16((lds_s16x4_ptr)(p));
}

constexpr size_t WS_MOD = 0;
constexpr size_t WS_H1 = 16384;
constexpr size_t WS_QF = WS_H1 + (size_t)S * DM * 2;
constexpr size_t WS_KI = WS_QF + (size_t)S * HDM * 2;
constexpr size_t WS_VI = WS_KI + (size_t)S * HDM * 2;
constexpr size_t WS_CTX = WS_VI + (size_t)S * HDM * 2;
constexpr size_t WS_X1 = WS_CTX + (size_t)S * HDM * 2;
constexpr size_t WS_Z1 = WS_X1 + (size_t)S * DM * 2;
constexpr size_t WS_Z2 = WS_Z1 + (size_t)S * FH * 2;
constexpr size_t WS_ST = WS_Z2 + (size_t)S * FH * 2;
constexpr size_t WS_U = WS_ST + (size_t)8 * S * 2 * 4;
constexpr int LDU = 704;

__global__ __launch_bounds__(256) void k_mod(const u16* __restrict__ c, const u16* __restrict__ w,
                                             float* __restrict__ mod) {
  __shared__ float red[64][33];
  const int t = threadIdx.x, cg = t & 3, ks = t >> 2;
  const int col0 = blockIdx.x * 32 + cg * 8;
  float acc[8];
#pragma unroll
  for (int j = 0; j < 8; ++j) acc[j] = 0.f;
#pragma unroll
  for (int kk = 0; kk < 8; ++kk) {
    const int k = ks * 8 + kk;
    float cv = bf2f(c[k]);
    float sv = rbf(cv / (1.f + __expf(-cv)));
    u32x4 wv = *reinterpret_cast<const u32x4*>(w + (size_t)k * 3072 + col0);
#pragma unroll
    for (int j = 0; j < 4; ++j) {
      acc[2 * j] += sv * bflo(wv[j]);
      acc[2 * j + 1] += sv * bfhi(wv[j]);
    }
  }
#pragma unroll
  for (int j = 0; j < 8; ++j) red[ks][cg * 8 + j] = acc[j];
  __syncthreads();
  if (t < 32) {
    float s = 0.f;
    for (int i = 0; i < 64; ++i) s += red[i][t];
    mod[blockIdx.x * 32 + t] = rbf(s);
  }
}

__global__ __launch_bounds__(256) void k_ln(const u16* __restrict__ x, const float* __restrict__ sh,
                                            const float* __restrict__ cs, u16* __restrict__ h) {
  const int row = blockIdx.x * 4 + (threadIdx.x >> 6), lane = threadIdx.x & 63;
  u32x4 xv = *reinterpret_cast<const u32x4*>(x + (size_t)row * DM + lane * 8);
  float f[8];
#pragma unroll
  for (int j = 0; j < 4; ++j) { f[2 * j] = bflo(xv[j]); f[2 * j + 1] = bfhi(xv[j]); }
  float s = 0.f;
#pragma unroll
  for (int j = 0; j < 8; ++j) s += f[j];
#pragma unroll
  for (int o = 32; o >= 1; o >>= 1) s += __shfl_xor(s, o);
  const float mean = s * (1.f / DM);
  float v = 0.f;
#pragma unroll
  for (int j = 0; j < 8; ++j) { f[j] -= mean; v += f[j] * f[j]; }
#pragma unroll
  for (int o = 32; o >= 1; o >>= 1) v += __shfl_xor(v, o);
  const float rstd = rsqrtf(v * (1.f / DM) + 1e-5f);
  u32x4 o4;
#pragma unroll
  for (int j = 0; j < 4; ++j) {
    const int k = lane * 8 + 2 * j;
    float a = f[2 * j] * rstd * (1.f + cs[k]) + sh[k];
    float b = f[2 * j + 1] * rstd * (1.f + cs[k + 1]) + sh[k + 1];
    o4[j] = pack2(a, b);
  }
  *reinterpret_cast<u32x4*>(h + (size_t)row * DM + lane * 8) = o4;
}

template <int BM, int BN, int KS, int NWM, int NWN>
struct Gemm {
  static constexpr int NW = NWM * NWN;
  static constexpr int WM = BM / NWM, WN = BN / NWN;
  static constexpr int MT = WM / 16, NT = WN / 16;
  static constexpr int BK = 64 * KS;
  static constexpr int A_BYTES = BM * BK * 2;
  static constexpr int B_BYTES = BK * BN * 2;
  static constexpr int STAGE_BYTES = A_BYTES + B_BYTES;
  static constexpr int NB = BN / 16;

  static DI void stageA(const u16* __restrict__ A, int lda, int m0, int k0, char* lds, int wave, int lane) {
    constexpr int NI = A_BYTES / 1024;
    const int rr = lane >> 3, ch = (lane & 7) ^ rr;
#pragma unroll
    for (int i = wave; i < NI; i += NW) {
      const int sub = i / (BM / 8), j = i % (BM / 8);
      const u16* src = A + (size_t)(m0 + j * 8 + rr) * lda + k0 + sub * 64 + ch * 8;
      glds16(src, lds + i * 1024);
    }
  }
  static DI void stageB(const u16* __restrict__ B, int ldb, int k0, int n0, int nmax, char* lds, int wave, int lane) {
    constexpr int NI = B_BYTES / 1024;
    const int pos = lane & 15, rw = pos >> 1, hf = pos & 1;
#pragma unroll
    for (int i = wave; i < NI; i += NW) {
      const int blk = i * 4 + (lane >> 4);
      const int kb = blk / NB, nb = blk % NB;
      const int row = rw ^ ((kb & 1) << 2);
      int n = n0 + nb * 16 + hf * 8;
      if (n > nmax) n = nmax;
      const u16* src = B + (size_t)(k0 + kb * 8 + row) * ldb + n;
      glds16(src, lds + i * 1024);
    }
  }
  template <bool SWAP>
  static DI void compute(const char* ldsA, const char* ldsB, f32x4 (&acc)[MT][NT], int wm, int wn, int lane) {
    const int fr = lane & 15, fg = lane >> 4;
    const int q = fr >> 2, p = lane & 3;
#pragma unroll
    for (int sub = 0; sub < KS; ++sub) {
#pragma unroll
      for (int kk = 0; kk < 2; ++kk) {
        bf16x8 af[MT], bfr[NT];
#pragma unroll
        for (int mt = 0; mt < MT; ++mt) {
          const int row = wm * WM + mt * 16 + fr;
          af[mt] = *reinterpret_cast<const bf16x8*>(ldsA + sub * (BM * 128) + row * 128 + (((kk * 4 + fg) ^ (row & 7)) << 4));
        }
#pragma unroll
        for (int nt = 0; nt < NT; ++nt) {
          const int kb = sub * 8 + kk * 4 + fg, nb = wn * NT + nt;
          const char* base = ldsB + (kb * NB + nb) * 256 + p * 8;
          const int sw = (fg & 1) << 2;
          s16x4 lo = tr_read(base + ((q) ^ sw) * 32);
          s16x4 hi = tr_read(base + ((4 + q) ^ sw) * 32);
          bfr[nt] = __builtin_shufflevector(lo, hi, 0, 1, 2, 3, 4, 5, 6, 7);
        }
#pragma unroll
        for (int mt = 0; mt < MT; ++mt)
#pragma unroll
          for (int nt = 0; nt < NT; ++nt) {
            if constexpr (SWAP)
              acc[mt][nt] = __builtin_amdgcn_mfma_f32_16x16x32_bf16(bfr[nt], af[mt], acc[mt][nt], 0, 0, 0);
            else
              acc[mt][nt] = __builtin_amdgcn_mfma_f32_16x16x32_bf16(af[mt], bfr[nt], acc[mt][nt], 0, 0, 0);
          }
      }
    }
  }
};

using G1 = Gemm<128, 128, 1, 2, 2>;

__global__ __launch_bounds__(256, 2) void k_qkv(const u16* __restrict__ h1, const u16* __restrict__ ckv,
                                                const u16* __restrict__ wq, const u16* __restrict__ wk,
                                                const u16* __restrict__ wv, char* __restrict__ Qf,
                                                char* __restrict__ Ki, char* __restrict__ Vi) {
  __shared__ __attribute__((aligned(16))) char smem[2 * G1::STAGE_BYTES];
  const int b = blockIdx.x, xcd = b & 7, j = b >> 3;
  const int head = xcd * 4 + (j & 3), rest = j >> 2, which = rest >> 4, mtile = rest & 15;
  const int tid = threadIdx.x, wave = __builtin_amdgcn_readfirstlane(tid >> 6), lane = tid & 63;
  const int wm = wave >> 1, wn = wave & 1;
  const u16* A = which == 0 ? h1 : ckv;
  const u16* B = which == 0 ? wq : (which == 1 ? wk : wv);
  const int m0 = mtile * 128, n0 = head * 128;
  f32x4 acc[G1::MT][G1::NT];
#pragma unroll
  for (int i = 0; i < G1::MT; ++i)
#pragma unroll
    for (int jj = 0; jj < G1::NT; ++jj) acc[i][jj] = f32x4{0.f, 0.f, 0.f, 0.f};
  constexpr int NK = DM / G1::BK;
  G1::stageA(A, DM, m0, 0, smem, wave, lane);
  G1::stageB(B, HDM, 0, n0, HDM, smem + G1::A_BYTES, wave, lane);
  for (int kt = 0; kt < NK; ++kt) {
    __syncthreads();
    char* cur = smem + (kt & 1) * G1::STAGE_BYTES;
    char* nxt = smem + ((kt + 1) & 1) * G1::STAGE_BYTES;
    if (kt + 1 < NK) {
      G1::stageA(A, DM, m0, (kt + 1) * G1::BK, nxt, wave, lane);
      G1::stageB(B, HDM, (kt + 1) * G1::BK, n0, HDM, nxt + G1::A_BYTES, wave, lane);
    }
    if (which == 2) G1::compute<false>(cur, cur + G1::A_BYTES, acc, wm, wn, lane);
    else G1::compute<true>(cur, cur + G1::A_BYTES, acc, wm, wn, lane);
  }
  __syncthreads();
  const int fr = lane & 15, fg = lane >> 4;
#pragma unroll
  for (int mt = 0; mt < G1::MT; ++mt)
#pragma unroll
    for (int nt = 0; nt < G1::NT; ++nt) {
      u32x2 v;
      v[0] = pack2(acc[mt][nt][0], acc[mt][nt][1]);
      v[1] = pack2(acc[mt][nt][2], acc[mt][nt][3]);
      int off;
      if (which == 2) {
        const int d = wn * 64 + nt * 16 + fr, m = wm * 64 + mt * 16 + fg * 4;
        const int tile = m >> 6, key = m & 63;
        const int pos = (key & 0x33) | ((key & 4) << 1) | ((key & 8) >> 1);
        off = tile * 16384 + d * 128 + (((pos >> 3) ^ ((d >> 1) & 7)) << 4) + (pos & 7) * 2;
      } else {
        const int m = wm * 64 + mt * 16 + fr, d = wn * 64 + nt * 16 + fg * 4;
        if (which == 1) {
          const int tile = m >> 6, key = m & 63;
          off = tile * 16384 + key * 256 + (((d >> 3) ^ (key & 15)) << 4) + (d & 7) * 2;
        } else {
          const int rg = m >> 5, r = m & 31;
          off = rg * 8192 + (d >> 4) * 1024 + ((((d >> 3) & 1) * 32 + r) << 4) + (d & 7) * 2;
        }
      }
      *reinterpret_cast<u32x2*>(smem + off) = v;
    }
  __syncthreads();
  char* dst = (which == 0 ? Qf : (which == 1 ? Ki : Vi)) + ((size_t)head * 16 + mtile) * 32768;
#pragma unroll
  for (int i = 0; i < 8; ++i) {
    const int o = (i * 256 + tid) * 16;
    *reinterpret_cast<u32x4*>(dst + o) = *reinterpret_cast<const u32x4*>(smem + o);
  }
}

__global__ __launch_bounds__(512, 2) void k_attn(const char* __restrict__ Qf, const char* __restrict__ Ki,
                                                 const char* __restrict__ Vi, u16* __restrict__ ctx) {
  __shared__ __attribute__((aligned(16))) char smem[131072];
  const int b = blockIdx.x, xcd = b & 7, j = b >> 3;
  const int head = xcd * 4 + (j & 3), pair = j >> 2;
  const int tid = threadIdx.x, wave = __builtin_amdgcn_readfirstlane(tid >> 6), lane = tid & 63;
  const int s = wave >> 2, g = wave & 3, r = lane & 31, h = lane >> 5;
  const char* Kh = Ki + (size_t)head * (32 * 16384);
  const char* Vh = Vi + (size_t)head * (32 * 16384);
  constexpr float C = 0.08838834764831845f * 1.4426950408889634f;

  for (int qi = 0; qi < 2; ++qi) {
    const int qb = qi ? pair : 15 - pair;
    bf16x8 q[8];
    {
      const char* qp = Qf + ((size_t)head * 64 + qb * 4 + g) * 8192 + lane * 16;
#pragma unroll
      for (int ks = 0; ks < 8; ++ks) q[ks] = *reinterpret_cast<const bf16x8*>(qp + ks * 1024);
    }
    f32x16 o[4];
#pragma unroll
    for (int dt = 0; dt < 4; ++dt)
#pragma unroll
      for (int i = 0; i < 16; ++i) o[dt][i] = 0.f;
    float m = -1e30f, l = 0.f;
    const int n = qb + 1;
#pragma unroll
    for (int i = 0; i < 4; ++i) {
      glds16(Kh + (wave * 4 + i) * 1024 + lane * 16, smem + (wave * 4 + i) * 1024);
      glds16(Vh + (wave * 4 + i) * 1024 + lane * 16, smem + 32768 + (wave * 4 + i) * 1024);
    }
    for (int it = 0; it < n; ++it) {
      __syncthreads();
      if (it + 1 < n) {
        char* nb = smem + ((it + 1) & 1) * 65536;
        const size_t go = (size_t)(it + 1) * 32768;
#pragma unroll
        for (int i = 0; i < 4; ++i) {
          glds16(Kh + go + (wave * 4 + i) * 1024 + lane * 16, nb + (wave * 4 + i) * 1024);
          glds16(Vh + go + (wave * 4 + i) * 1024 + lane * 16, nb + 32768 + (wave * 4 + i) * 1024);
        }
      }
      const bool last = (it == n - 1);
      if (!(last && s == 1 && g < 2)) {
        const char* Kt = smem + (it & 1) * 65536 + s * 16384;
        const char* Vt = smem + (it & 1) * 65536 + 32768 + s * 16384;
        f32x16 st[2];
#pragma unroll
        for (int kt = 0; kt < 2; ++kt) {
#pragma unroll
          for (int i = 0; i < 16; ++i) st[kt][i] = 0.f;
#pragma unroll
          for (int ks = 0; ks < 8; ++ks) {
            bf16x8 a = *reinterpret_cast<const bf16x8*>(Kt + (32 * kt + r) * 256 + (((2 * ks + h) ^ (r & 15)) << 4));
            st[kt] = __builtin_amdgcn_mfma_f32_32x32x16_bf16(a, q[ks], st[kt], 0, 0, 0);
          }
        }
        if (last) {
          const int lim = 32 * g + r - 64 * s - 4 * h;
#pragma unroll
          for (int kt = 0; kt < 2; ++kt)
#pragma unroll
            for (int i = 0; i < 16; ++i) {
              const int kk = 32 * kt + (i & 3) + 8 * (i >> 2);
              if (kk > lim) st[kt][i] = -__builtin_inff();
            }
        }
        float pm = st[0][0];
#pragma unroll
        for (int i = 1; i < 16; ++i) pm = fmaxf(pm, st[0][i]);
#pragma unroll
        for (int i = 0; i < 16; ++i) pm = fmaxf(pm, st[1][i]);
        pm = fmaxf(pm, __shfl_xor(pm, 32));
        const float mn = fmaxf(m, pm);
        const float alpha = __builtin_amdgcn_exp2f((m - mn) * C);
        m = mn;
        const float mc = -mn * C;
        float ps = 0.f;
#pragma unroll
        for (int kt = 0; kt < 2; ++kt)
#pragma unroll
          for (int i = 0; i < 16; ++i) {
            float e = __builtin_amdgcn_exp2f(fmaf(st[kt][i], C, mc));
            st[kt][i] = e;
            ps += e;
          }
        l = l * alpha + ps;
#pragma unroll
        for (int dt = 0; dt < 4; ++dt)
#pragma unroll
          for (int i = 0; i < 16; ++i) o[dt][i] *= alpha;
        bf16x8 p[4];
#pragma unroll
        for (int ks = 0; ks < 4; ++ks) {
          u32x4 w;
#pragma unroll
          for (int jj = 0; jj < 4; ++jj)
            w[jj] = pack2(st[ks >> 1][8 * (ks & 1) + 2 * jj], st[ks >> 1][8 * (ks & 1) + 2 * jj + 1]);
          p[ks] = __builtin_bit_cast(bf16x8, w);
        }
#pragma unroll
        for (int dt = 0; dt < 4; ++dt)
#pragma unroll
          for (int ks = 0; ks < 4; ++ks) {
            bf16x8 a = *reinterpret_cast<const bf16x8*>(Vt + (32 * dt + r) * 128 + (((2 * ks + h) ^ ((r >> 1) & 7)) << 4));
            o[dt] = __builtin_amdgcn_mfma_f32_32x32x16_bf16(a, p[ks], o[dt], 0, 0, 0);
          }
      }
    }
    __syncthreads();
    l += __shfl_xor(l, 32);
    float* xo = reinterpret_cast<float*>(smem) + g * (64 * 68);
    if (s == 1) {
#pragma unroll
      for (int dt = 0; dt < 4; ++dt)
#pragma unroll
        for (int qd = 0; qd < 4; ++qd) {
          f32x4 v = {o[dt][4 * qd], o[dt][4 * qd + 1], o[dt][4 * qd + 2], o[dt][4 * qd + 3]};
          *reinterpret_cast<f32x4*>(xo + ((dt * 4 + qd) * 64 + lane) * 4) = v;
        }
      f32x4 v = {m, l, 0.f, 0.f};
      *reinterpret_cast<f32x4*>(xo + (16 * 64 + lane) * 4) = v;
    }
    __syncthreads();
    if (s == 0) {
      f32x4 ml = *reinterpret_cast<const f32x4*>(xo + (16 * 64 + lane) * 4);
      const float mt = fmaxf(m, ml[0]);
      const float a0 = __builtin_amdgcn_exp2f((m - mt) * C), a1 = __builtin_amdgcn_exp2f((ml[0] - mt) * C);
      const float inv = 1.f / (l * a0 + ml[1] * a1);
      const float f0 = a0 * inv, f1 = a1 * inv;
      u16* crow = ctx + (size_t)(qb * 128 + g * 32 + r) * HDM + head * 128 + 4 * h;
#pragma unroll
      for (int dt = 0; dt < 4; ++dt)
#pragma unroll
        for (int qd = 0; qd < 4; ++qd) {
          f32x4 v = *reinterpret_cast<const f32x4*>(xo + ((dt * 4 + qd) * 64 + lane) * 4);
          u32x2 w;
          w[0] = pack2(o[dt][4 * qd] * f0 + v[0] * f1, o[dt][4 * qd + 1] * f0 + v[1] * f1);
          w[1] = pack2(o[dt][4 * qd + 2] * f0 + v[2] * f1, o[dt][4 * qd + 3] * f0 + v[3] * f1);
          *reinterpret_cast<u32x2*>(crow + dt * 32 + qd * 8) = w;
        }
    }
    __syncthreads();
  }
}

using G3 = Gemm<64, 64, 4, 2, 2>;
__global__ __launch_bounds__(256, 1) void k_oproj(const u16* __restrict__ ctx, const u16* __restrict__ wo,
                                                  const u16* __restrict__ x, const float* __restrict__ ga,
                                                  u16* __restrict__ x1) {
  __shared__ __attribute__((aligned(16))) char smem[2 * G3::STAGE_BYTES];
  const int b = blockIdx.x, xcd = b & 7, j = b >> 3;
  const int mtile = xcd * 4 + (j >> 3), ntile = j & 7;
  const int tid = threadIdx.x, wave = __builtin_amdgcn_readfirstlane(tid >> 6), lane = tid & 63;
  const int wm = wave >> 1, wn = wave & 1;
  const int m0 = mtile * 64, n0 = ntile * 64;
  f32x4 acc[G3::MT][G3::NT];
#pragma unroll
  for (int i = 0; i < G3::MT; ++i)
#pragma unroll
    for (int jj = 0; jj < G3::NT; ++jj) acc[i][jj] = f32x4{0.f, 0.f, 0.f, 0.f};
  constexpr int NK = HDM / G3::BK;
  G3::stageA(ctx, HDM, m0, 0, smem, wave, lane);
  G3::stageB(wo, DM, 0, n0, DM, smem + G3::A_BYTES, wave, lane);
  for (int kt = 0; kt < NK; ++kt) {
    __syncthreads();
    char* cur = smem + (kt & 1) * G3::STAGE_BYTES;
    char* nxt = smem + ((kt + 1) & 1) * G3::STAGE_BYTES;
    if (kt + 1 < NK) {
      G3::stageA(ctx, HDM, m0, (kt + 1) * G3::BK, nxt, wave, lane);
      G3::stageB(wo, DM, (kt + 1) * G3::BK, n0, DM, nxt + G3::A_BYTES, wave, lane);
    }
    G3::compute<true>(cur, cur + G3::A_BYTES, acc, wm, wn, lane);
  }
  const int fr = lane & 15, fg = lane >> 4;
#pragma unroll
  for (int mt = 0; mt < G3::MT; ++mt)
#pragma unroll
    for (int nt = 0; nt < G3::NT; ++nt) {
      const int m = m0 + wm * G3::WM + mt * 16 + fr, n = n0 + wn * G3::WN + nt * 16 + fg * 4;
      u32x2 xv = *reinterpret_cast<const u32x2*>(x + (size_t)m * DM + n);
      f32x4 gv = *reinterpret_cast<const f32x4*>(ga + n);
      u32x2 w;
      w[0] = pack2(bflo(xv[0]) + gv[0] * acc[mt][nt][0], bfhi(xv[0]) + gv[1] * acc[mt][nt][1]);
      w[1] = pack2(bflo(xv[1]) + gv[2] * acc[mt][nt][2], bfhi(xv[1]) + gv[3] * acc[mt][nt][3]);
      *reinterpret_cast<u32x2*>(x1 + (size_t)m * DM + n) = w;
    }
}

using G4 = Gemm<64, 192, 8, 2, 2>;
using G4B = Gemm<64, 192, 1, 2, 2>;
DI float gelu_tanh(float x) {
  const float u = 0.7978845608028654f * (x + 0.044715f * x * x * x);
  const float e = __expf(2.f * u);
  const float t = 1.f - 2.f / (e + 1.f);
  return 0.5f * x * (1.f + t);
}
__global__ __launch_bounds__(256, 1) void k_win(const u16* __restrict__ x1, const float* __restrict__ sb,
                                                const float* __restrict__ cb, const u16* __restrict__ win,
                                                u16* __restrict__ z1, u16* __restrict__ z2,
                                                float* __restrict__ stats) {
  __shared__ __attribute__((aligned(16))) char smem[G4::A_BYTES + 2 * G4B::B_BYTES];
  char* sB = smem + G4::A_BYTES;
  const int b = blockIdx.x, xcd = b & 7, j = b >> 3;
  const int mtile = xcd * 4 + (j >> 3), ntile = j & 7;
  const int tid = threadIdx.x, wave = __builtin_amdgcn_readfirstlane(tid >> 6), lane = tid & 63;
  const int wm = wave >> 1, wn = wave & 1;
  const int m0 = mtile * 64, n0 = ntile * 192;
  G4B::stageB(win, FF, 0, n0, FF - 8, sB, wave, lane);
  {
    float cbv[8], sbv[8];
#pragma unroll
    for (int jj = 0; jj < 8; ++jj) { cbv[jj] = 1.f + cb[lane * 8 + jj]; sbv[jj] = sb[lane * 8 + jj]; }
#pragma unroll 4
    for (int i = 0; i < 16; ++i) {
      const int row = wave * 16 + i;
      u32x4 xv = *reinterpret_cast<const u32x4*>(x1 + (size_t)(m0 + row) * DM + lane * 8);
      float f[8];
#pragma unroll
      for (int jj = 0; jj < 4; ++jj) { f[2 * jj] = bflo(xv[jj]); f[2 * jj + 1] = bfhi(xv[jj]); }
      float s = 0.f;
#pragma unroll
      for (int jj = 0; jj < 8; ++jj) s += f[jj];
#pragma unroll
      for (int o = 32; o >= 1; o >>= 1) s += __shfl_xor(s, o);
      const float mean = s * (1.f / DM);
      float v = 0.f;
#pragma unroll
      for (int jj = 0; jj < 8; ++jj) { f[jj] -= mean; v += f[jj] * f[jj]; }
#pragma unroll
      for (int o = 32; o >= 1; o >>= 1) v += __shfl_xor(v, o);
      const float rstd = rsqrtf(v * (1.f / DM) + 1e-5f);
      u32x4 o4;
#pragma unroll
      for (int jj = 0; jj < 4; ++jj)
        o4[jj] = pack2(f[2 * jj] * rstd * cbv[2 * jj] + sbv[2 * jj], f[2 * jj + 1] * rstd * cbv[2 * jj + 1] + sbv[2 * jj + 1]);
      *reinterpret_cast<u32x4*>(smem + (lane >> 3) * (64 * 128) + row * 128 + (((lane & 7) ^ (row & 7)) << 4)) = o4;
    }
  }
  f32x4 acc[G4B::MT][G4B::NT];
#pragma unroll
  for (int i = 0; i < G4B::MT; ++i)
#pragma unroll
    for (int jj = 0; jj < G4B::NT; ++jj) acc[i][jj] = f32x4{0.f, 0.f, 0.f, 0.f};
  for (int kt = 0; kt < 8; ++kt) {
    __syncthreads();
    if (kt + 1 < 8) G4B::stageB(win, FF, (kt + 1) * 64, n0, FF - 8, sB + ((kt + 1) & 1) * G4B::B_BYTES, wave, lane);
    G4B::compute<true>(smem + kt * (64 * 128), sB + (kt & 1) * G4B::B_BYTES, acc, wm, wn, lane);
  }
  const int fr = lane & 15, fg = lane >> 4;
  __syncthreads();
  float* red = reinterpret_cast<float*>(smem);
#pragma unroll
  for (int mt = 0; mt < G4B::MT; ++mt) {
    const int rowl = wm * G4B::WM + mt * 16 + fr, m = m0 + rowl;
    float s1 = 0.f, s2 = 0.f;
#pragma unroll
    for (int nt = 0; nt < G4B::NT; ++nt) {
      const int n = n0 + wn * G4B::WN + nt * 16 + fg * 4;
      if (n < FF) {
        float gz[4];
#pragma unroll
        for (int jj = 0; jj < 4; ++jj) gz[jj] = rbf(gelu_tanh(acc[mt][nt][jj]));
        u32x2 w;
        w[0] = pack2(gz[0], gz[1]);
        w[1] = pack2(gz[2], gz[3]);
        if (n < FH) {
          *reinterpret_cast<u32x2*>(z1 + (size_t)m * FH + n) = w;
        } else {
          *reinterpret_cast<u32x2*>(z2 + (size_t)m * FH + (n - FH)) = w;
#pragma unroll
          for (int jj = 0; jj < 4; ++jj) { s1 += gz[jj]; s2 += gz[jj] * gz[jj]; }
        }
      }
    }
    s1 += __shfl_xor(s1, 16); s1 += __shfl_xor(s1, 32);
    s2 += __shfl_xor(s2, 16); s2 += __shfl_xor(s2, 32);
    if (fg == 0) { red[(wn * 64 + rowl) * 2] = s1; red[(wn * 64 + rowl) * 2 + 1] = s2; }
  }
  __syncthreads();
  if (tid < 64) {
    float s1 = red[tid * 2] + red[(64 + tid) * 2], s2 = red[tid * 2 + 1] + red[(64 + tid) * 2 + 1];
    stats[((size_t)ntile * S + m0 + tid) * 2] = s1;
    stats[((size_t)ntile * S + m0 + tid) * 2 + 1] = s2;
  }
}

using G5 = Gemm<64, 96, 2, 2, 2>;
__global__ __launch_bounds__(256, 1) void k_spatial(const u16* __restrict__ wsp, const u16* __restrict__ z2,
                                                    const float* __restrict__ stats, const u16* __restrict__ bsp,
                                                    const u16* __restrict__ z1, u16* __restrict__ u) {
  __shared__ __attribute__((aligned(16))) char smem[2 * G5::STAGE_BYTES + S * 8];
  float* mr = reinterpret_cast<float*>(smem + 2 * G5::STAGE_BYTES);
  const int b = blockIdx.x, xcd = b & 7, j = b >> 3;
  const int mtile = xcd * 4 + (j >> 3), ntile = j & 7;
  const int tid = threadIdx.x, wave = __builtin_amdgcn_readfirstlane(tid >> 6), lane = tid & 63;
  const int wm = wave >> 1, wn = wave & 1;
  const int m0 = mtile * 64, n0 = ntile * 96;
  for (int i = tid; i < S; i += 256) {
    float s1 = 0.f, s2 = 0.f;
#pragma unroll
    for (int t = 3; t < 8; ++t) { s1 += stats[((size_t)t * S + i) * 2]; s2 += stats[((size_t)t * S + i) * 2 + 1]; }
    const float mean = s1 * (1.f / FH);
    const float var = fmaxf(s2 * (1.f / FH) - mean * mean, 0.f);
    const float rstd = rsqrtf(var + 1e-5f);
    mr[2 * i] = rstd;
    mr[2 * i + 1] = -mean * rstd;
  }
  __syncthreads();
  auto loadB = [&](int k0, u32x4 (&reg)[6]) {
#pragma unroll
    for (int i = 0; i < 6; ++i) {
      const int c = i * 256 + tid, k = c / 12, nc = c % 12;
      int n = n0 + nc * 8;
      if (n > FH - 8) n = FH - 8;
      reg[i] = *reinterpret_cast<const u32x4*>(z2 + (size_t)(k0 + k) * FH + n);
    }
  };
  auto writeB = [&](int k0, const u32x4 (&reg)[6], char* dst) {
#pragma unroll
    for (int i = 0; i < 6; ++i) {
      const int c = i * 256 + tid, k = c / 12, nc = c % 12;
      const float rs = mr[2 * (k0 + k)], ms = mr[2 * (k0 + k) + 1];
      u32x4 o4;
#pragma unroll
      for (int jj = 0; jj < 4; ++jj) o4[jj] = pack2(fmaf(bflo(reg[i][jj]), rs, ms), fmaf(bfhi(reg[i][jj]), rs, ms));
      const int kb = k >> 3, row = (k & 7) ^ ((kb & 1) << 2), nb = nc >> 1, hf = nc & 1;
      *reinterpret_cast<u32x4*>(dst + (kb * G5::NB + nb) * 256 + row * 32 + hf * 16) = o4;
    }
  };
  f32x4 acc[G5::MT][G5::NT];
#pragma unroll
  for (int i = 0; i < G5::MT; ++i)
#pragma unroll
    for (int jj = 0; jj < G5::NT; ++jj) acc[i][jj] = f32x4{0.f, 0.f, 0.f, 0.f};
  constexpr int NK = S / G5::BK;
  u32x4 breg[6];
  loadB(0, breg);
  G5::stageA(wsp, S, m0, 0, smem, wave, lane);
  writeB(0, breg, smem + G5::A_BYTES);
  for (int kt = 0; kt < NK; ++kt) {
    __syncthreads();
    char* cur = smem + (kt & 1) * G5::STAGE_BYTES;
    char* nxt = smem + ((kt + 1) & 1) * G5::STAGE_BYTES;
    if (kt + 1 < NK) {
      loadB((kt + 1) * G5::BK, breg);
      G5::stageA(wsp, S, m0, (kt + 1) * G5::BK, nxt, wave, lane);
    }
    G5::compute<true>(cur, cur + G5::A_BYTES, acc, wm, wn, lane);
    if (kt + 1 < NK) writeB((kt + 1) * G5::BK, breg, nxt + G5::A_BYTES);
  }
  const int fr = lane & 15, fg = lane >> 4;
#pragma unroll
  for (int mt = 0; mt < G5::MT; ++mt) {
    const int m = m0 + wm * G5::WM + mt * 16 + fr;
    const float bs = bf2f(bsp[m]);
#pragma unroll
    for (int nt = 0; nt < G5::NT; ++nt) {
      const int n = n0 + wn * G5::WN + nt * 16 + fg * 4;
      if (n < FH) {
        u32x2 zv = *reinterpret_cast<const u32x2*>(z1 + (size_t)m * FH + n);
        u32x2 w;
        w[0] = pack2(bflo(zv[0]) * (acc[mt][nt][0] + bs), bfhi(zv[0]) * (acc[mt][nt][1] + bs));
        w[1] = pack2(bflo(zv[1]) * (acc[mt][nt][2] + bs), bfhi(zv[1]) * (acc[mt][nt][3] + bs));
        *reinterpret_cast<u32x2*>(u + (size_t)m * LDU + n) = w;
      } else if (n < LDU) {
        u32x2 w = {0u, 0u};
        *reinterpret_cast<u32x2*>(u + (size_t)m * LDU + n) = w;
      }
    }
  }
}

using G6 = Gemm<64, 64, 1, 2, 2>;
__global__ __launch_bounds__(256, 1) void k_wout(const u16* __restrict__ u, const u16* __restrict__ wout,
                                                 const u16* __restrict__ x1, const float* __restrict__ gb,
                                                 u16* __restrict__ out) {
  __shared__ __attribute__((aligned(16))) char smem[10 * G6::STAGE_BYTES];
  const int b = blockIdx.x, xcd = b & 7, j = b >> 3;
  const int mtile = xcd * 4 + (j >> 3), ntile = j & 7;
  const int tid = threadIdx.x, wave = __builtin_amdgcn_readfirstlane(tid >> 6), lane = tid & 63;
  const int wm = wave >> 1, wn = wave & 1;
  const int m0 = mtile * 64, n0 = ntile * 64;
#pragma unroll
  for (int kt = 0; kt < 10; ++kt) {
    char* dst = smem + kt * G6::STAGE_BYTES;
    G6::stageA(u, LDU, m0, kt * 64, dst, wave, lane);
    G6::stageB(wout, DM, kt * 64, n0, DM, dst + G6::A_BYTES, wave, lane);
  }
  f32x4 acc[G6::MT][G6::NT];
#pragma unroll
  for (int i = 0; i < G6::MT; ++i)
#pragma unroll
    for (int jj = 0; jj < G6::NT; ++jj) acc[i][jj] = f32x4{0.f, 0.f, 0.f, 0.f};
  __syncthreads();
  G6::compute<true>(smem, smem + G6::A_BYTES, acc, wm, wn, lane);
  __syncthreads();
  {
    G6::stageA(u, LDU, m0, 640, smem, wave, lane);
    const int pos = lane & 15, rw = pos >> 1, hf = pos & 1;
#pragma unroll
    for (int i = wave; i < G6::B_BYTES / 1024; i += 4) {
      const int blk = i * 4 + (lane >> 4);
      const int kb = blk / G6::NB, nb = blk % G6::NB;
      int row = 640 + kb * 8 + (rw ^ ((kb & 1) << 2));
      if (row > FH - 1) row = FH - 1;
      glds16(wout + (size_t)row * DM + n0 + nb * 16 + hf * 8, smem + G6::A_BYTES + i * 1024);
    }
  }
#pragma unroll
  for (int kt = 1; kt < 10; ++kt) {
    char* cur = smem + kt * G6::STAGE_BYTES;
    G6::compute<true>(cur, cur + G6::A_BYTES, acc, wm, wn, lane);
  }
  __syncthreads();
  G6::compute<true>(smem, smem + G6::A_BYTES, acc, wm, wn, lane);
  const int fr = lane & 15, fg = lane >> 4;
#pragma unroll
  for (int mt = 0; mt < G6::MT; ++mt)
#pragma unroll
    for (int nt = 0; nt < G6::NT; ++nt) {
      const int m = m0 + wm * G6::WM + mt * 16 + fr, n = n0 + wn * G6::WN + nt * 16 + fg * 4;
      u32x2 xv = *reinterpret_cast<const u32x2*>(x1 + (size_t)m * DM + n);
      f32x4 gv = *reinterpret_cast<const f32x4*>(gb + n);
      u32x2 w;
      w[0] = pack2(bflo(xv[0]) + gv[0] * acc[mt][nt][0], bfhi(xv[0]) + gv[1] * acc[mt][nt][1]);
      w[1] = pack2(bflo(xv[1]) + gv[2] * acc[mt][nt][2], bfhi(xv[1]) + gv[3] * acc[mt][nt][3]);
      *reinterpret_cast<u32x2*>(out + (size_t)m * DM + n) = w;
    }
}

extern "C" void kernel_launch(void* const* d_in, const int* in_sizes, int n_in, void* d_out, int out_size,
                              void* d_ws, size_t ws_size, hipStream_t stream) {
  const u16* x = (const u16*)d_in[0];
  const u16* ckv = (const u16*)d_in[1];
  const u16* wq = (const u16*)d_in[2];
  const u16* wk = (const u16*)d_in[3];
  const u16* wv = (const u16*)d_in[4];
  const u16* wo = (const u16*)d_in[5];
  const u16* win = (const u16*)d_in[7];
  const u16* wsp = (const u16*)d_in[8];
  const u16* bsp = (const u16*)d_in[9];
  const u16* wout = (const u16*)d_in[10];
  const u16* c = (const u16*)d_in[11];
  const u16* wmod = (const u16*)d_in[12];
  char* ws = (char*)d_ws;
  float* mod = (float*)(ws + WS_MOD);
  u16* h1 = (u16*)(ws + WS_H1);
  char* Qf = ws + WS_QF;
  char* Ki = ws + WS_KI;
  char* Vi = ws + WS_VI;
  u16* ctx = (u16*)(ws + WS_CTX);
  u16* x1 = (u16*)(ws + WS_X1);
  u16* z1 = (u16*)(ws + WS_Z1);
  u16* z2 = (u16*)(ws + WS_Z2);
  float* stats = (float*)(ws + WS_ST);
  u16* u = (u16*)(ws + WS_U);
  u16* out = (u16*)d_out;

  k_mod<<<96, 256, 0, stream>>>(c, wmod, mod);
  k_ln<<<S / 4, 256, 0, stream>>>(x, mod + 0 * DM, mod + 1 * DM, h1);
  k_qkv<<<1536, 256, 0, stream>>>(h1, ckv, wq, wk, wv, Qf, Ki, Vi);
  k_attn<<<256, 512, 0, stream>>>(Qf, Ki, Vi, ctx);
  k_oproj<<<256, 256, 0, stream>>>(ctx, wo, x, mod + 2 * DM, x1);
  k_win<<<256, 256, 0, stream>>>(x1, mod + 3 * DM, mod + 4 * DM, win, z1, z2, stats);
  k_spatial<<<256, 256, 0, stream>>>(wsp, z2, stats, bsp, z1, u);
  k_wout<<<256, 256, 0, stream>>>(u, wout, x1, mod + 5 * DM, out);
}
```

```cpp
#include <hip/hip_runtime.h>
#include <hip/hip_bf16.h>
#include <stdint.h>

constexpr int S = 2048;
constexpr int DM = 512;
constexpr int NH = 32;
constexpr int HD = 128;
constexpr int HDM = NH * HD;
constexpr int FH = 688;
constexpr int FF = 1376;

typedef __attribute__((ext_vector_type(8))) short bf16x8;
typedef __attribute__((ext_vector_type(4))) short s16x4;
typedef __attribute__((ext_vector_type(4))) float f32x4;
typedef __attribute__((ext_vector_type(16))) float f32x16;
typedef __attribute__((ext_vector_type(4))) unsigned u32x4;
typedef __attribute__((ext_vector_type(2))) unsigned u32x2;
typedef __attribute__((address_space(3))) s16x4* lds_s16x4_ptr;
typedef unsigned short u16;

#define DI __device__ __forceinline__

DI float bf2f(u16 v) { return __uint_as_float(((unsigned)v) << 16); }
DI float bflo(unsigned v) { return __uint_as_float(v << 16); }
DI float bfhi(unsigned v) { return __uint_as_float(v & 0xffff0000u); }
typedef __attribute__((ext_vector_type(2))) __bf16 bf16x2_t;
typedef __attribute__((ext_vector_type(2))) float f32x2;
DI unsigned pack2(float lo, float hi) {
  f32x2 v = {lo, hi};
  return __builtin_bit_cast(unsigned, __builtin_convertvector(v, bf16x2_t));
}
template <int N> DI void wait_vm() { asm volatile("s_waitcnt vmcnt(%0)" ::"n"(N) : "memory"); }
DI void barrier() { asm volatile("s_waitcnt lgkmcnt(0)" ::: "memory"); __builtin_amdgcn_s_barrier(); }
DI float rbf(float x) { return bflo(pack2(x, 0.f)); }

DI void glds16(const void* g, const void* lds_wave_base) {
  const unsigned a = __builtin_amdgcn_readfirstlane((unsigned)(size_t)lds_wave_base);
  unsigned keep;
  asm volatile("s_mov_b32 %0, m0\n\ts_mov_b32 m0, %2\n\ts_nop 0\n\tglobal_load_lds_dwordx4 %1, off\n\ts_mov_b32 m0, %0"
               : "=&s"(keep) : "v"(g), "s"(a) : "memory");
}
DI s16x4 tr_read(const char* p) {
  return __builtin_amdgcn_ds_read_tr16_b64_v4i16((lds_s16x4_ptr)(p));
}

constexpr size_t WS_MOD = 0;
constexpr size_t WS_H1 = 16384;
constexpr size_t WS_QF = WS_H1 + (size_t)S * DM * 2;
constexpr size_t WS_KI = WS_QF + (size_t)S * HDM * 2;
constexpr size_t WS_VI = WS_KI + (size_t)S * HDM * 2;
constexpr size_t WS_CTX = WS_VI + (size_t)S * HDM * 2;
constexpr size_t WS_X1 = WS_CTX + (size_t)S * HDM * 2;
constexpr size_t WS_Z1 = WS_X1 + (size_t)S * DM * 2;
constexpr size_t WS_Z2 = WS_Z1 + (size_t)S * FH * 2;
constexpr size_t WS_ST = WS_Z2 + (size_t)S * FH * 2;
constexpr size_t WS_U = WS_ST + (size_t)8 * S * 2 * 4;
constexpr int LDU = 704;

__global__ __launch_bounds__(256) void k_mod(const u16* __restrict__ c, const u16* __restrict__ w,
                                             float* __restrict__ mod) {
  __shared__ float red[64][33];
  const int t = threadIdx.x, cg = t & 3, ks = t >> 2;
  const int col0 = blockIdx.x * 32 + cg * 8;
  float acc[8];
#pragma unroll
  for (int j = 0; j < 8; ++j) acc[j] = 0.f;
#pragma unroll
  for (int kk = 0; kk < 8; ++kk) {
    const int k = ks * 8 + kk;
    float cv = bf2f(c[k]);
    float sv = rbf(cv / (1.f + __expf(-cv)));
    u32x4 wv = *reinterpret_cast<const u32x4*>(w + (size_t)k * 3072 + col0);
#pragma unroll
    for (int j = 0; j < 4; ++j) {
      acc[2 * j] += sv * bflo(wv[j]);
      acc[2 * j + 1] += sv * bfhi(wv[j]);
    }
  }
#pragma unroll
  for (int j = 0; j < 8; ++j) red[ks][cg * 8 + j] = acc[j];
  __syncthreads();
  if (t < 32) {
    float s = 0.f;
    for (int i = 0; i < 64; ++i) s += red[i][t];
    mod[blockIdx.x * 32 + t] = rbf(s);
  }
}

__global__ __launch_bounds__(256) void k_ln(const u16* __restrict__ x, const float* __restrict__ sh,
                                            const float* __restrict__ cs, u16* __restrict__ h) {
  const int row = blockIdx.x * 4 + (threadIdx.x >> 6), lane = threadIdx.x & 63;
  u32x4 xv = *reinterpret_cast<const u32x4*>(x + (size_t)row * DM + lane * 8);
  float f[8];
#pragma unroll
  for (int j = 0; j < 4; ++j) { f[2 * j] = bflo(xv[j]); f[2 * j + 1] = bfhi(xv[j]); }
  float s = 0.f;
#pragma unroll
  for (int j = 0; j < 8; ++j) s += f[j];
#pragma unroll
  for (int o = 32; o >= 1; o >>= 1) s += __shfl_xor(s, o);
  const float mean = s * (1.f / DM);
  float v = 0.f;
#pragma unroll
  for (int j = 0; j < 8; ++j) { f[j] -= mean; v += f[j] * f[j]; }
#pragma unroll
  for (int o = 32; o >= 1; o >>= 1) v += __shfl_xor(v, o);
  const float rstd = rsqrtf(v * (1.f / DM) + 1e-5f);
  u32x4 o4;
#pragma unroll
  for (int j = 0; j < 4; ++j) {
    const int k = lane * 8 + 2 * j;
    float a = f[2 * j] * rstd * (1.f + cs[k]) + sh[k];
    float b = f[2 * j + 1] * rstd * (1.f + cs[k + 1]) + sh[k + 1];
    o4[j] = pack2(a, b);
  }
  *reinterpret_cast<u32x4*>(h + (size_t)row * DM + lane * 8) = o4;
}

template <int BM, int BN, int KS, int NWM, int NWN>
struct Gemm {
  static constexpr int NW = NWM * NWN;
  static constexpr int WM = BM / NWM, WN = BN / NWN;
  static constexpr int MT = WM / 16, NT = WN / 16;
  static constexpr int BK = 64 * KS;
  static constexpr int A_BYTES = BM * BK * 2;
  static constexpr int B_BYTES = BK * BN * 2;
  static constexpr int STAGE_BYTES = A_BYTES + B_BYTES;
  static constexpr int NB = BN / 16;

  static DI void stageA(const u16* __restrict__ A, int lda, int m0, int k0, char* lds, int wave, int lane) {
    constexpr int NI = A_BYTES / 1024;
    const int rr = lane >> 3, ch = (lane & 7) ^ rr;
#pragma unroll
    for (int ii = 0; ii < NI / NW; ++ii) {
      const int i = ii * NW + wave;
      const int sub = i / (BM / 8), j = i % (BM / 8);
      const u16* src = A + (size_t)(m0 + j * 8 + rr) * lda + k0 + sub * 64 + ch * 8;
      glds16(src, lds + i * 1024);
    }
  }
  static DI void stageB(const u16* __restrict__ B, int ldb, int k0, int n0, int nmax, char* lds, int wave, int lane) {
    constexpr int NI = B_BYTES / 1024;
    const int pos = lane & 15, rw = pos >> 1, hf = pos & 1;
#pragma unroll
    for (int ii = 0; ii < NI / NW; ++ii) {
      const int i = ii * NW + wave;
      const int blk = i * 4 + (lane >> 4);
      const int kb = blk / NB, nb = blk % NB;
      const int row = rw ^ ((kb & 1) << 2);
      int n = n0 + nb * 16 + hf * 8;
      if (n > nmax) n = nmax;
      const u16* src = B + (size_t)(k0 + kb * 8 + row) * ldb + n;
      glds16(src, lds + i * 1024);
    }
  }
  template <bool SWAP>
  static DI void compute(const char* ldsA, const char* ldsB, f32x4 (&acc)[MT][NT], int wm, int wn, int lane) {
    const int fr = lane & 15, fg = lane >> 4;
    const int q = fr >> 2, p = lane & 3;
#pragma unroll
    for (int sub = 0; sub < KS; ++sub) {
#pragma unroll
      for (int kk = 0; kk < 2; ++kk) {
        bf16x8 af[MT], bfr[NT];
#pragma unroll
        for (int mt = 0; mt < MT; ++mt) {
          const int row = wm * WM + mt * 16 + fr;
          af[mt] = *reinterpret_cast<const bf16x8*>(ldsA + sub * (BM * 128) + row * 128 + (((kk * 4 + fg) ^ (row & 7)) << 4));
        }
#pragma unroll
        for (int nt = 0; nt < NT; ++nt) {
          const int kb = sub * 8 + kk * 4 + fg, nb = wn * NT + nt;
          const char* base = ldsB + (kb * NB + nb) * 256 + p * 8;
          const int sw = (fg & 1) << 2;
          s16x4 lo = tr_read(base + ((q) ^ sw) * 32);
          s16x4 hi = tr_read(base + ((4 + q) ^ sw) * 32);
          bfr[nt] = __builtin_shufflevector(lo, hi, 0, 1, 2, 3, 4, 5, 6, 7);
        }
#pragma unroll
        for (int mt = 0; mt < MT; ++mt)
#pragma unroll
          for (int nt = 0; nt < NT; ++nt) {
            if constexpr (SWAP)
              acc[mt][nt] = __builtin_amdgcn_mfma_f32_16x16x32_bf16(bfr[nt], af[mt], acc[mt][nt], 0, 0, 0);
            else
              acc[mt][nt] = __builtin_amdgcn_mfma_f32_16x16x32_bf16(af[mt], bfr[nt], acc[mt][nt], 0, 0, 0);
          }
      }
    }
  }
};

using G1 = Gemm<128, 128, 1, 2, 2>;

__global__ __launch_bounds__(256, 2) void k_qkv(const u16* __restrict__ h1, const u16* __restrict__ ckv,
                                                const u16* __restrict__ wq, const u16* __restrict__ wk,
                                                const u16* __restrict__ wv, char* __restrict__ Qf,
                                                char* __restrict__ Ki, char* __restrict__ Vi) {
  __shared__ __attribute__((aligned(16))) char smem[2 * G1::STAGE_BYTES];
  const int b = blockIdx.x, xcd = b & 7, j = b >> 3;
  const int head = xcd * 4 + (j & 3), rest = j >> 2, which = rest >> 4, mtile = rest & 15;
  const int tid = threadIdx.x, wave = __builtin_amdgcn_readfirstlane(tid >> 6), lane = tid & 63;
  const int wm = wave >> 1, wn = wave & 1;
  const u16* A = which == 0 ? h1 : ckv;
  const u16* B = which == 0 ? wq : (which == 1 ? wk : wv);
  const int m0 = mtile * 128, n0 = head * 128;
  f32x4 acc[G1::MT][G1::NT];
#pragma unroll
  for (int i = 0; i < G1::MT; ++i)
#pragma unroll
    for (int jj = 0; jj < G1::NT; ++jj) acc[i][jj] = f32x4{0.f, 0.f, 0.f, 0.f};
  constexpr int NK = DM / G1::BK;
  G1::stageA(A, DM, m0, 0, smem, wave, lane);
  G1::stageB(B, HDM, 0, n0, HDM, smem + G1::A_BYTES, wave, lane);
  for (int kt = 0; kt < NK; ++kt) {
    wait_vm<0>(); __syncthreads();
    char* cur = smem + (kt & 1) * G1::STAGE_BYTES;
    char* nxt = smem + ((kt + 1) & 1) * G1::STAGE_BYTES;
    if (kt + 1 < NK) {
      G1::stageA(A, DM, m0, (kt + 1) * G1::BK, nxt, wave, lane);
      G1::stageB(B, HDM, (kt + 1) * G1::BK, n0, HDM, nxt + G1::A_BYTES, wave, lane);
    }
    if (which == 2) G1::compute<false>(cur, cur + G1::A_BYTES, acc, wm, wn, lane);
    else G1::compute<true>(cur, cur + G1::A_BYTES, acc, wm, wn, lane);
  }
  __syncthreads();
  const int fr = lane & 15, fg = lane >> 4;
#pragma unroll
  for (int mt = 0; mt < G1::MT; ++mt)
#pragma unroll
    for (int nt = 0; nt < G1::NT; ++nt) {
      u32x2 v;
      v[0] = pack2(acc[mt][nt][0], acc[mt][nt][1]);
      v[1] = pack2(acc[mt][nt][2], acc[mt][nt][3]);
      int off;
      if (which == 2) {
        const int d = wn * 64 + nt * 16 + fr, m = wm * 64 + mt * 16 + fg * 4;
        const int tile = m >> 6, key = m & 63;
        const int pos = (key & 0x33) | ((key & 4) << 1) | ((key & 8) >> 1);
        off = tile * 16384 + d * 128 + (((pos >> 3) ^ ((d >> 1) & 7)) << 4) + (pos & 7) * 2;
      } else {
        const int m = wm * 64 + mt * 16 + fr, d = wn * 64 + nt * 16 + fg * 4;
        if (which == 1) {
          const int tile = m >> 6, key = m & 63;
          off = tile * 16384 + key * 256 + (((d >> 3) ^ (key & 15)) << 4) + (d & 7) * 2;
        } else {
          const int rg = m >> 5, r = m & 31;
          off = rg * 8192 + (d >> 4) * 1024 + ((((d >> 3) & 1) * 32 + r) << 4) + (d & 7) * 2;
        }
      }
      *reinterpret_cast<u32x2*>(smem + off) = v;
    }
  __syncthreads();
  char* dst = (which == 0 ? Qf : (which == 1 ? Ki : Vi)) + ((size_t)head * 16 + mtile) * 32768;
#pragma unroll
  for (int i = 0; i < 8; ++i) {
    const int o = (i * 256 + tid) * 16;
    *reinterpret_cast<u32x4*>(dst + o) = *reinterpret_cast<const u32x4*>(smem + o);
  }
}

constexpr int KV_NS = 4, KV_STAGE = 32768;
template <bool isV>
DI void kv_body(char* smem, const u16* __restrict__ ckv, const u16* __restrict__ B, char* __restrict__ dstb,
                const u16* __restrict__ cvec, const u16* __restrict__ wmod, float* __restrict__ mod) {
  const int b = blockIdx.x, xcd = b & 7, j = b >> 3;
  const int ntile = xcd * 4 + (j & 3), mtile = j >> 2;
  const int tid = threadIdx.x, wave = __builtin_amdgcn_readfirstlane(tid >> 6), lane = tid & 63;
  const int wm = wave >> 2, wn = wave & 3, r = lane & 31, h = lane >> 5;
  const int m0 = mtile * 256, n0 = (ntile & 15) * 256;
  float* modred = reinterpret_cast<float*>(smem + KV_NS * KV_STAGE);

  const u16* asrc[2];
  const u16* bsrc[2];
#pragma unroll
  for (int ii = 0; ii < 2; ++ii) {
    const int i = ii * 8 + wave;
    const int row = 16 * i + (lane >> 2), ch = (lane & 3) ^ ((lane >> 4) & 3);
    asrc[ii] = ckv + (size_t)(m0 + row) * DM + ch * 8;
    const int blk = i * 4 + (lane >> 4), kb = blk >> 4, nb = blk & 15;
    const int pos = lane & 15, rw = (pos >> 1) ^ ((nb & 1) << 2), hf = pos & 1;
    bsrc[ii] = B + (size_t)(kb * 8 + rw) * HDM + n0 + nb * 16 + hf * 8;
  }
  auto stage = [&](int kt, int slot) {
    char* dst = smem + slot * KV_STAGE;
#pragma unroll
    for (int ii = 0; ii < 2; ++ii) {
      glds16(asrc[ii] + kt * 32, dst + (ii * 8 + wave) * 1024);
      glds16(bsrc[ii] + (size_t)kt * 32 * HDM, dst + 16384 + (ii * 8 + wave) * 1024);
    }
  };
  constexpr int NK = DM / 32;
#pragma unroll
  for (int st = 0; st < KV_NS - 1; ++st) stage(st, st);

  if (b < 192) {
    const u16* p = wmod + (size_t)tid * 3072 + b * 16;
    const u32x4 w0 = *reinterpret_cast<const u32x4*>(p), w1 = *reinterpret_cast<const u32x4*>(p + 8);
    const float c = bf2f(cvec[tid]);
    const float sv = rbf(c / (1.f + __expf(-c)));
    float v[16];
#pragma unroll
    for (int jj = 0; jj < 4; ++jj) {
      v[2 * jj] = sv * bflo(w0[jj]); v[2 * jj + 1] = sv * bfhi(w0[jj]);
      v[8 + 2 * jj] = sv * bflo(w1[jj]); v[8 + 2 * jj + 1] = sv * bfhi(w1[jj]);
    }
#pragma unroll
    for (int st = 0; st < 4; ++st) {
      const int half = 8 >> st, m = 32 >> st;
      const bool up = lane & m;
#pragma unroll
      for (int e = 0; e < half; ++e) {
        const float send = up ? v[e] : v[half + e];
        const float keep = up ? v[half + e] : v[e];
        v[e] = keep + __shfl_xor(send, m);
      }
    }
    v[0] += __shfl_xor(v[0], 2);
    v[0] += __shfl_xor(v[0], 1);
    if ((lane & 3) == 0) {
      const int col = ((lane >> 5) & 1) * 8 + ((lane >> 4) & 1) * 4 + ((lane >> 3) & 1) * 2 + ((lane >> 2) & 1);
      modred[wave * 16 + col] = v[0];
    }
  }

  f32x16 acc[4][2];
#pragma unroll
  for (int i = 0; i < 4; ++i)
#pragma unroll
    for (int jj = 0; jj < 2; ++jj)
#pragma unroll
      for (int e = 0; e < 16; ++e) acc[i][jj][e] = 0.f;
  const int G = lane >> 4, q4 = (lane & 15) >> 2, p4 = lane & 3;
  int aoff[2];
#pragma unroll
  for (int u = 0; u < 2; ++u) aoff[u] = (wm * 128 + r) * 64 + (((2 * u + h) ^ ((r >> 2) & 3)) << 4);
  const int nh = G & 1;
  const int boff = 16384 + (h * 16 + wn * 4 + nh) * 256 + p4 * 8;
  const int brow0 = (q4 ^ (nh << 2)) * 32, brow1 = ((4 + q4) ^ (nh << 2)) * 32;
  for (int kt = 0; kt < NK; ++kt) {
    if (kt + KV_NS - 2 <= NK - 1) wait_vm<(KV_NS - 2) * 4>(); else wait_vm<0>();
    barrier();
    if (kt + KV_NS - 1 < NK) stage(kt + KV_NS - 1, (kt + KV_NS - 1) & (KV_NS - 1));
    const char* cur = smem + (kt & (KV_NS - 1)) * KV_STAGE;
#pragma unroll
    for (int u = 0; u < 2; ++u) {
      bf16x8 af[4], bfr[2];
#pragma unroll
      for (int mt = 0; mt < 4; ++mt) af[mt] = *reinterpret_cast<const bf16x8*>(cur + aoff[u] + mt * 2048);
#pragma unroll
      for (int nt = 0; nt < 2; ++nt) {
        const char* bb = cur + boff + u * (2 * 16 * 256) + nt * 512;
        s16x4 lo = tr_read(bb + brow0), hi = tr_read(bb + brow1);
        bfr[nt] = __builtin_shufflevector(lo, hi, 0, 1, 2, 3, 4, 5, 6, 7);
      }
#pragma unroll
      for (int mt = 0; mt < 4; ++mt)
#pragma unroll
        for (int nt = 0; nt < 2; ++nt) {
          if constexpr (isV)
            acc[mt][nt] = __builtin_amdgcn_mfma_f32_32x32x16_bf16(af[mt], bfr[nt], acc[mt][nt], 0, 0, 0);
          else
            acc[mt][nt] = __builtin_amdgcn_mfma_f32_32x32x16_bf16(bfr[nt], af[mt], acc[mt][nt], 0, 0, 0);
        }
    }
  }
  barrier();
#pragma unroll
  for (int mt = 0; mt < 4; ++mt)
#pragma unroll
    for (int nt = 0; nt < 2; ++nt)
#pragma unroll
      for (int qd = 0; qd < 4; ++qd) {
        u32x2 v;
        v[0] = pack2(acc[mt][nt][4 * qd], acc[mt][nt][4 * qd + 1]);
        v[1] = pack2(acc[mt][nt][4 * qd + 2], acc[mt][nt][4 * qd + 3]);
        int off;
        if constexpr (isV) {
          const int n = wn * 64 + nt * 32 + r, m = wm * 128 + mt * 32 + 8 * qd + 4 * h;
          const int hd = n >> 7, d = n & 127, tile = m >> 6, key = m & 63;
          const int pos = (key & 0x33) | ((key & 4) << 1) | ((key & 8) >> 1);
          off = (hd * 4 + tile) * 16384 + d * 128 + (((pos >> 3) ^ ((d >> 1) & 7)) << 4) + (pos & 7) * 2;
        } else {
          const int m = wm * 128 + mt * 32 + r, n = wn * 64 + nt * 32 + 8 * qd + 4 * h;
          const int hd = n >> 7, d = n & 127, tile = m >> 6, key = m & 63;
          off = (hd * 4 + tile) * 16384 + key * 256 + (((d >> 3) ^ (key & 15)) << 4) + (d & 7) * 2;
        }
        *reinterpret_cast<u32x2*>(smem + off) = v;
      }
  barrier();
  {
    const int head0 = (ntile & 15) * 2;
#pragma unroll
    for (int i = 0; i < 16; ++i) {
      const int o = (i * 512 + tid) * 16;
      const int hd = o >> 16, rem = o & 65535;
      *reinterpret_cast<u32x4*>(dstb + ((size_t)(head0 + hd) * 32 + mtile * 4) * 16384 + rem) =
          *reinterpret_cast<const u32x4*>(smem + o);
    }
  }
  if (b < 192 && tid < 16) {
    float sum = 0.f;
#pragma unroll
    for (int i = 0; i < 8; ++i) sum += modred[i * 16 + tid];
    mod[b * 16 + tid] = rbf(sum);
  }
}
__global__ __launch_bounds__(512, 2) void k_kv(const u16* __restrict__ ckv, const u16* __restrict__ wk,
                                               const u16* __restrict__ wv, char* __restrict__ Ki,
                                               char* __restrict__ Vi, const u16* __restrict__ cvec,
                                               const u16* __restrict__ wmod, float* __restrict__ mod) {
  __shared__ __attribute__((aligned(16))) char smem[KV_NS * KV_STAGE + 512];
  if ((blockIdx.x & 7) >= 4) kv_body<true>(smem, ckv, wv, Vi, cvec, wmod, mod);
  else kv_body<false>(smem, ckv, wk, Ki, cvec, wmod, mod);
}

__global__ __launch_bounds__(512, 2) void k_attn(const char* __restrict__ Qf, const char* __restrict__ Ki,
                                                 const char* __restrict__ Vi, u16* __restrict__ ctx) {
  __shared__ __attribute__((aligned(16))) char smem[131072];
  const int b = blockIdx.x, xcd = b & 7, j = b >> 3;
  const int head = xcd * 4 + (j & 3), pair = j >> 2;
  const int tid = threadIdx.x, wave = __builtin_amdgcn_readfirstlane(tid >> 6), lane = tid & 63;
  const int s = wave >> 2, g = wave & 3, r = lane & 31, h = lane >> 5;
  const char* Kh = Ki + (size_t)head * (32 * 16384);
  const char* Vh = Vi + (size_t)head * (32 * 16384);
  constexpr float C = 0.08838834764831845f * 1.4426950408889634f;

  for (int qi = 0; qi < 2; ++qi) {
    const int qb = qi ? pair : 15 - pair;
    bf16x8 q[8];
    {
      const char* qp = Qf + ((size_t)head * 64 + qb * 4 + g) * 8192 + lane * 16;
#pragma unroll
      for (int ks = 0; ks < 8; ++ks) q[ks] = *reinterpret_cast<const bf16x8*>(qp + ks * 1024);
    }
    f32x16 o[4];
#pragma unroll
    for (int dt = 0; dt < 4; ++dt)
#pragma unroll
      for (int i = 0; i < 16; ++i) o[dt][i] = 0.f;
    float m = -1e30f, l = 0.f;
    const int n = qb + 1;
#pragma unroll
    for (int i = 0; i < 4; ++i) {
      glds16(Kh + (wave * 4 + i) * 1024 + lane * 16, smem + (wave * 4 + i) * 1024);
      glds16(Vh + (wave * 4 + i) * 1024 + lane * 16, smem + 32768 + (wave * 4 + i) * 1024);
    }
    for (int it = 0; it < n; ++it) {
      wait_vm<0>(); __syncthreads();
      if (it + 1 < n) {
        char* nb = smem + ((it + 1) & 1) * 65536;
        const size_t go = (size_t)(it + 1) * 32768;
#pragma unroll
        for (int i = 0; i < 4; ++i) {
          glds16(Kh + go + (wave * 4 + i) * 1024 + lane * 16, nb + (wave * 4 + i) * 1024);
          glds16(Vh + go + (wave * 4 + i) * 1024 + lane * 16, nb + 32768 + (wave * 4 + i) * 1024);
        }
      }
      const bool last = (it == n - 1);
      if (!(last && s == 1 && g < 2)) {
        const char* Kt = smem + (it & 1) * 65536 + s * 16384;
        const char* Vt = smem + (it & 1) * 65536 + 32768 + s * 16384;
        f32x16 st[2];
#pragma unroll
        for (int kt = 0; kt < 2; ++kt) {
#pragma unroll
          for (int i = 0; i < 16; ++i) st[kt][i] = 0.f;
#pragma unroll
          for (int ks = 0; ks < 8; ++ks) {
            bf16x8 a = *reinterpret_cast<const bf16x8*>(Kt + (32 * kt + r) * 256 + (((2 * ks + h) ^ (r & 15)) << 4));
            st[kt] = __builtin_amdgcn_mfma_f32_32x32x16_bf16(a, q[ks], st[kt], 0, 0, 0);
          }
        }
        if (last) {
          const int lim = 32 * g + r - 64 * s - 4 * h;
#pragma unroll
          for (int kt = 0; kt < 2; ++kt)
#pragma unroll
            for (int i = 0; i < 16; ++i) {
              const int kk = 32 * kt + (i & 3) + 8 * (i >> 2);
              if (kk > lim) st[kt][i] = -__builtin_inff();
            }
        }
        float pm = st[0][0];
#pragma unroll
        for (int i = 1; i < 16; ++i) pm = fmaxf(pm, st[0][i]);
#pragma unroll
        for (int i = 0; i < 16; ++i) pm = fmaxf(pm, st[1][i]);
        pm = fmaxf(pm, __shfl_xor(pm, 32));
        const float mn = fmaxf(m, pm);
        const float alpha = __builtin_amdgcn_exp2f((m - mn) * C);
        m = mn;
        const float mc = -mn * C;
        float ps = 0.f;
#pragma unroll
        for (int kt = 0; kt < 2; ++kt)
#pragma unroll
          for (int i = 0; i < 16; ++i) {
            float e = __builtin_amdgcn_exp2f(fmaf(st[kt][i], C, mc));
            st[kt][i] = e;
            ps += e;
          }
        l = l * alpha + ps;
#pragma unroll
        for (int dt = 0; dt < 4; ++dt)
#pragma unroll
          for (int i = 0; i < 16; ++i) o[dt][i] *= alpha;
        bf16x8 p[4];
#pragma unroll
        for (int ks = 0; ks < 4; ++ks) {
          u32x4 w;
#pragma unroll
          for (int jj = 0; jj < 4; ++jj)
            w[jj] = pack2(st[ks >> 1][8 * (ks & 1) + 2 * jj], st[ks >> 1][8 * (ks & 1) + 2 * jj + 1]);
          p[ks] = __builtin_bit_cast(bf16x8, w);
        }
#pragma unroll
        for (int dt = 0; dt < 4; ++dt)
#pragma unroll
          for (int ks = 0; ks < 4; ++ks) {
            bf16x8 a = *reinterpret_cast<const bf16x8*>(Vt + (32 * dt + r) * 128 + (((2 * ks + h) ^ ((r >> 1) & 7)) << 4));
            o[dt] = __builtin_amdgcn_mfma_f32_32x32x16_bf16(a, p[ks], o[dt], 0, 0, 0);
          }
      }
    }
    __syncthreads();
    l += __shfl_xor(l, 32);
    float* xo = reinterpret_cast<float*>(smem) + g * (64 * 68);
    if (s == 1) {
#pragma unroll
      for (int dt = 0; dt < 4; ++dt)
#pragma unroll
        for (int qd = 0; qd < 4; ++qd) {
          f32x4 v = {o[dt][4 * qd], o[dt][4 * qd + 1], o[dt][4 * qd + 2], o[dt][4 * qd + 3]};
          *reinterpret_cast<f32x4*>(xo + ((dt * 4 + qd) * 64 + lane) * 4) = v;
        }
      f32x4 v = {m, l, 0.f, 0.f};
      *reinterpret_cast<f32x4*>(xo + (16 * 64 + lane) * 4) = v;
    }
    __syncthreads();
    if (s == 0) {
      f32x4 ml = *reinterpret_cast<const f32x4*>(xo + (16 * 64 + lane) * 4);
      const float mt = fmaxf(m, ml[0]);
      const float a0 = __builtin_amdgcn_exp2f((m - mt) * C), a1 = __builtin_amdgcn_exp2f((ml[0] - mt) * C);
      const float inv = 1.f / (l * a0 + ml[1] * a1);
      const float f0 = a0 * inv, f1 = a1 * inv;
      u16* crow = ctx + (size_t)(qb * 128 + g * 32 + r) * HDM + head * 128 + 4 * h;
#pragma unroll
      for (int dt = 0; dt < 4; ++dt)
#pragma unroll
        for (int qd = 0; qd < 4; ++qd) {
          f32x4 v = *reinterpret_cast<const f32x4*>(xo + ((dt * 4 + qd) * 64 + lane) * 4);
          u32x2 w;
          w[0] = pack2(o[dt][4 * qd] * f0 + v[0] * f1, o[dt][4 * qd + 1] * f0 + v[1] * f1);
          w[1] = pack2(o[dt][4 * qd + 2] * f0 + v[2] * f1, o[dt][4 * qd + 3] * f0 + v[3] * f1);
          *reinterpret_cast<u32x2*>(crow + dt * 32 + qd * 8) = w;
        }
    }
    __syncthreads();
  }
}

using G3 = Gemm<64, 64, 2, 2, 2>;
constexpr int G3_NS = 5;
__global__ __launch_bounds__(256, 1) void k_oproj(const u16* __restrict__ ctx, const u16* __restrict__ wo,
                                                  const u16* __restrict__ x, const float* __restrict__ ga,
                                                  u16* __restrict__ x1) {
  __shared__ __attribute__((aligned(16))) char smem[G3_NS * G3::STAGE_BYTES];
  const int b = blockIdx.x, xcd = b & 7, j = b >> 3;
  const int mtile = xcd * 4 + (j >> 3), ntile = j & 7;
  const int tid = threadIdx.x, wave = __builtin_amdgcn_readfirstlane(tid >> 6), lane = tid & 63;
  const int wm = wave >> 1, wn = wave & 1;
  const int m0 = mtile * 64, n0 = ntile * 64;
  f32x4 acc[G3::MT][G3::NT];
#pragma unroll
  for (int i = 0; i < G3::MT; ++i)
#pragma unroll
    for (int jj = 0; jj < G3::NT; ++jj) acc[i][jj] = f32x4{0.f, 0.f, 0.f, 0.f};
  constexpr int NK = HDM / G3::BK;
  constexpr int LPS = G3::STAGE_BYTES / 1024 / G3::NW;
#pragma unroll
  for (int st = 0; st < G3_NS - 1; ++st) {
    G3::stageA(ctx, HDM, m0, st * G3::BK, smem + st * G3::STAGE_BYTES, wave, lane);
    G3::stageB(wo, DM, st * G3::BK, n0, DM, smem + st * G3::STAGE_BYTES + G3::A_BYTES, wave, lane);
  }
  int cs = 0, ns = G3_NS - 1;
  for (int kt = 0; kt < NK; ++kt) {
    if (kt + G3_NS - 2 <= NK - 1) wait_vm<(G3_NS - 2) * LPS>(); else wait_vm<0>();
    barrier();
    if (kt + G3_NS - 1 < NK) {
      char* nxt = smem + ns * G3::STAGE_BYTES;
      G3::stageA(ctx, HDM, m0, (kt + G3_NS - 1) * G3::BK, nxt, wave, lane);
      G3::stageB(wo, DM, (kt + G3_NS - 1) * G3::BK, n0, DM, nxt + G3::A_BYTES, wave, lane);
    }
    char* cur = smem + cs * G3::STAGE_BYTES;
    G3::compute<true>(cur, cur + G3::A_BYTES, acc, wm, wn, lane);
    cs = (cs + 1 == G3_NS) ? 0 : cs + 1;
    ns = (ns + 1 == G3_NS) ? 0 : ns + 1;
  }
  const int fr = lane & 15, fg = lane >> 4;
#pragma unroll
  for (int mt = 0; mt < G3::MT; ++mt)
#pragma unroll
    for (int nt = 0; nt < G3::NT; ++nt) {
      const int m = m0 + wm * G3::WM + mt * 16 + fr, n = n0 + wn * G3::WN + nt * 16 + fg * 4;
      u32x2 xv = *reinterpret_cast<const u32x2*>(x + (size_t)m * DM + n);
      f32x4 gv = *reinterpret_cast<const f32x4*>(ga + n);
      u32x2 w;
      w[0] = pack2(bflo(xv[0]) + gv[0] * acc[mt][nt][0], bfhi(xv[0]) + gv[1] * acc[mt][nt][1]);
      w[1] = pack2(bflo(xv[1]) + gv[2] * acc[mt][nt][2], bfhi(xv[1]) + gv[3] * acc[mt][nt][3]);
      *reinterpret_cast<u32x2*>(x1 + (size_t)m * DM + n) = w;
    }
}

using G4 = Gemm<64, 192, 8, 2, 2>;
using G4B = Gemm<64, 192, 1, 2, 2>;
DI float gelu_tanh(float x) {
  const float u = 0.7978845608028654f * (x + 0.044715f * x * x * x);
  const float e = __expf(2.f * u);
  const float t = 1.f - 2.f / (e + 1.f);
  return 0.5f * x * (1.f + t);
}
__global__ __launch_bounds__(256, 1) void k_win(const u16* __restrict__ x1, const float* __restrict__ sb,
                                                const float* __restrict__ cb, const u16* __restrict__ win,
                                                u16* __restrict__ z1, u16* __restrict__ z2,
                                                float* __restrict__ stats) {
  __shared__ __attribute__((aligned(16))) char smem[G4::A_BYTES + 2 * G4B::B_BYTES];
  char* sB = smem + G4::A_BYTES;
  const int b = blockIdx.x, xcd = b & 7, j = b >> 3;
  const int mtile = xcd * 4 + (j >> 3), ntile = j & 7;
  const int tid = threadIdx.x, wave = __builtin_amdgcn_readfirstlane(tid >> 6), lane = tid & 63;
  const int wm = wave >> 1, wn = wave & 1;
  const int m0 = mtile * 64, n0 = ntile * 192;
  G4B::stageB(win, FF, 0, n0, FF - 8, sB, wave, lane);
  {
    float cbv[8], sbv[8];
#pragma unroll
    for (int jj = 0; jj < 8; ++jj) { cbv[jj] = 1.f + cb[lane * 8 + jj]; sbv[jj] = sb[lane * 8 + jj]; }
#pragma unroll 4
    for (int i = 0; i < 16; ++i) {
      const int row = wave * 16 + i;
      u32x4 xv = *reinterpret_cast<const u32x4*>(x1 + (size_t)(m0 + row) * DM + lane * 8);
      float f[8];
#pragma unroll
      for (int jj = 0; jj < 4; ++jj) { f[2 * jj] = bflo(xv[jj]); f[2 * jj + 1] = bfhi(xv[jj]); }
      float s = 0.f;
#pragma unroll
      for (int jj = 0; jj < 8; ++jj) s += f[jj];
#pragma unroll
      for (int o = 32; o >= 1; o >>= 1) s += __shfl_xor(s, o);
      const float mean = s * (1.f / DM);
      float v = 0.f;
#pragma unroll
      for (int jj = 0; jj < 8; ++jj) { f[jj] -= mean; v += f[jj] * f[jj]; }
#pragma unroll
      for (int o = 32; o >= 1; o >>= 1) v += __shfl_xor(v, o);
      const float rstd = rsqrtf(v * (1.f / DM) + 1e-5f);
      u32x4 o4;
#pragma unroll
      for (int jj = 0; jj < 4; ++jj)
        o4[jj] = pack2(f[2 * jj] * rstd * cbv[2 * jj] + sbv[2 * jj], f[2 * jj + 1] * rstd * cbv[2 * jj + 1] + sbv[2 * jj + 1]);
      *reinterpret_cast<u32x4*>(smem + (lane >> 3) * (64 * 128) + row * 128 + (((lane & 7) ^ (row & 7)) << 4)) = o4;
    }
  }
  f32x4 acc[G4B::MT][G4B::NT];
#pragma unroll
  for (int i = 0; i < G4B::MT; ++i)
#pragma unroll
    for (int jj = 0; jj < G4B::NT; ++jj) acc[i][jj] = f32x4{0.f, 0.f, 0.f, 0.f};
  for (int kt = 0; kt < 8; ++kt) {
    wait_vm<0>(); __syncthreads();
    if (kt + 1 < 8) G4B::stageB(win, FF, (kt + 1) * 64, n0, FF - 8, sB + ((kt + 1) & 1) * G4B::B_BYTES, wave, lane);
    G4B::compute<true>(smem + kt * (64 * 128), sB + (kt & 1) * G4B::B_BYTES, acc, wm, wn, lane);
  }
  const int fr = lane & 15, fg = lane >> 4;
  __syncthreads();
  float* red = reinterpret_cast<float*>(smem);
#pragma unroll
  for (int mt = 0; mt < G4B::MT; ++mt) {
    const int rowl = wm * G4B::WM + mt * 16 + fr, m = m0 + rowl;
    float s1 = 0.f, s2 = 0.f;
#pragma unroll
    for (int nt = 0; nt < G4B::NT; ++nt) {
      const int n = n0 + wn * G4B::WN + nt * 16 + fg * 4;
      if (n < FF) {
        float gz[4];
#pragma unroll
        for (int jj = 0; jj < 4; ++jj) gz[jj] = rbf(gelu_tanh(acc[mt][nt][jj]));
        u32x2 w;
        w[0] = pack2(gz[0], gz[1]);
        w[1] = pack2(gz[2], gz[3]);
        if (n < FH) {
          *reinterpret_cast<u32x2*>(z1 + (size_t)m * FH + n) = w;
        } else {
          *reinterpret_cast<u32x2*>(z2 + (size_t)m * FH + (n - FH)) = w;
#pragma unroll
          for (int jj = 0; jj < 4; ++jj) { s1 += gz[jj]; s2 += gz[jj] * gz[jj]; }
        }
      }
    }
    s1 += __shfl_xor(s1, 16); s1 += __shfl_xor(s1, 32);
    s2 += __shfl_xor(s2, 16); s2 += __shfl_xor(s2, 32);
    if (fg == 0) { red[(wn * 64 + rowl) * 2] = s1; red[(wn * 64 + rowl) * 2 + 1] = s2; }
  }
  __syncthreads();
  if (tid < 64) {
    float s1 = red[tid * 2] + red[(64 + tid) * 2], s2 = red[tid * 2 + 1] + red[(64 + tid) * 2 + 1];
    stats[((size_t)ntile * S + m0 + tid) * 2] = s1;
    stats[((size_t)ntile * S + m0 + tid) * 2 + 1] = s2;
  }
}

using G5 = Gemm<64, 96, 2, 2, 2>;
__global__ __launch_bounds__(256, 1) void k_spatial(const u16* __restrict__ wsp, const u16* __restrict__ z2,
                                                    const float* __restrict__ stats, const u16* __restrict__ bsp,
                                                    const u16* __restrict__ z1, u16* __restrict__ u) {
  __shared__ __attribute__((aligned(16))) char smem[2 * G5::STAGE_BYTES + S * 8];
  float* mr = reinterpret_cast<float*>(smem + 2 * G5::STAGE_BYTES);
  const int b = blockIdx.x, xcd = b & 7, j = b >> 3;
  const int mtile = xcd * 4 + (j >> 3), ntile = j & 7;
  const int tid = threadIdx.x, wave = __builtin_amdgcn_readfirstlane(tid >> 6), lane = tid & 63;
  const int wm = wave >> 1, wn = wave & 1;
  const int m0 = mtile * 64, n0 = ntile * 96;
  for (int i = tid; i < S; i += 256) {
    float s1 = 0.f, s2 = 0.f;
#pragma unroll
    for (int t = 3; t < 8; ++t) { s1 += stats[((size_t)t * S + i) * 2]; s2 += stats[((size_t)t * S + i) * 2 + 1]; }
    const float mean = s1 * (1.f / FH);
    const float var = fmaxf(s2 * (1.f / FH) - mean * mean, 0.f);
    const float rstd = rsqrtf(var + 1e-5f);
    mr[2 * i] = rstd;
    mr[2 * i + 1] = -mean * rstd;
  }
  __syncthreads();
  auto loadB = [&](int k0, u32x4 (&reg)[6]) {
#pragma unroll
    for (int i = 0; i < 6; ++i) {
      const int c = i * 256 + tid, k = c / 12, nc = c % 12;
      int n = n0 + nc * 8;
      if (n > FH - 8) n = FH - 8;
      reg[i] = *reinterpret_cast<const u32x4*>(z2 + (size_t)(k0 + k) * FH + n);
    }
  };
  auto writeB = [&](int k0, const u32x4 (&reg)[6], char* dst) {
#pragma unroll
    for (int i = 0; i < 6; ++i) {
      const int c = i * 256 + tid, k = c / 12, nc = c % 12;
      const float rs = mr[2 * (k0 + k)], ms = mr[2 * (k0 + k) + 1];
      u32x4 o4;
#pragma unroll
      for (int jj = 0; jj < 4; ++jj) o4[jj] = pack2(fmaf(bflo(reg[i][jj]), rs, ms), fmaf(bfhi(reg[i][jj]), rs, ms));
      const int kb = k >> 3, row = (k & 7) ^ ((kb & 1) << 2), nb = nc >> 1, hf = nc & 1;
      *reinterpret_cast<u32x4*>(dst + (kb * G5::NB + nb) * 256 + row * 32 + hf * 16) = o4;
    }
  };
  f32x4 acc[G5::MT][G5::NT];
#pragma unroll
  for (int i = 0; i < G5::MT; ++i)
#pragma unroll
    for (int jj = 0; jj < G5::NT; ++jj) acc[i][jj] = f32x4{0.f, 0.f, 0.f, 0.f};
  constexpr int NK = S / G5::BK;
  u32x4 breg[6];
  loadB(0, breg);
  G5::stageA(wsp, S, m0, 0, smem, wave, lane);
  writeB(0, breg, smem + G5::A_BYTES);
  for (int kt = 0; kt < NK; ++kt) {
    wait_vm<0>(); __syncthreads();
    char* cur = smem + (kt & 1) * G5::STAGE_BYTES;
    char* nxt = smem + ((kt + 1) & 1) * G5::STAGE_BYTES;
    if (kt + 1 < NK) {
      loadB((kt + 1) * G5::BK, breg);
      G5::stageA(wsp, S, m0, (kt + 1) * G5::BK, nxt, wave, lane);
    }
    G5::compute<true>(cur, cur + G5::A_BYTES, acc, wm, wn, lane);
    if (kt + 1 < NK) writeB((kt + 1) * G5::BK, breg, nxt + G5::A_BYTES);
  }
  const int fr = lane & 15, fg = lane >> 4;
#pragma unroll
  for (int mt = 0; mt < G5::MT; ++mt) {
    const int m = m0 + wm * G5::WM + mt * 16 + fr;
    const float bs = bf2f(bsp[m]);
#pragma unroll
    for (int nt = 0; nt < G5::NT; ++nt) {
      const int n = n0 + wn * G5::WN + nt * 16 + fg * 4;
      if (n < FH) {
        u32x2 zv = *reinterpret_cast<const u32x2*>(z1 + (size_t)m * FH + n);
        u32x2 w;
        w[0] = pack2(bflo(zv[0]) * (acc[mt][nt][0] + bs), bfhi(zv[0]) * (acc[mt][nt][1] + bs));
        w[1] = pack2(bflo(zv[1]) * (acc[mt][nt][2] + bs), bfhi(zv[1]) * (acc[mt][nt][3] + bs));
        *reinterpret_cast<u32x2*>(u + (size_t)m * LDU + n) = w;
      } else if (n < LDU) {
        u32x2 w = {0u, 0u};
        *reinterpret_cast<u32x2*>(u + (size_t)m * LDU + n) = w;
      }
    }
  }
}

using G6 = Gemm<64, 64, 1, 2, 2>;
__global__ __launch_bounds__(256, 1) void k_wout(const u16* __restrict__ u, const u16* __restrict__ wout,
                                                 const u16* __restrict__ x1, const float* __restrict__ gb,
                                                 u16* __restrict__ out) {
  __shared__ __attribute__((aligned(16))) char smem[10 * G6::STAGE_BYTES];
  const int b = blockIdx.x, xcd = b & 7, j = b >> 3;
  const int mtile = xcd * 4 + (j >> 3), ntile = j & 7;
  const int tid = threadIdx.x, wave = __builtin_amdgcn_readfirstlane(tid >> 6), lane = tid & 63;
  const int wm = wave >> 1, wn = wave & 1;
  const int m0 = mtile * 64, n0 = ntile * 64;
#pragma unroll
  for (int kt = 0; kt < 10; ++kt) {
    char* dst = smem + kt * G6::STAGE_BYTES;
    G6::stageA(u, LDU, m0, kt * 64, dst, wave, lane);
    G6::stageB(wout, DM, kt * 64, n0, DM, dst + G6::A_BYTES, wave, lane);
  }
  f32x4 acc[G6::MT][G6::NT];
#pragma unroll
  for (int i = 0; i < G6::MT; ++i)
#pragma unroll
    for (int jj = 0; jj < G6::NT; ++jj) acc[i][jj] = f32x4{0.f, 0.f, 0.f, 0.f};
  wait_vm<0>(); __syncthreads();
  G6::compute<true>(smem, smem + G6::A_BYTES, acc, wm, wn, lane);
  __syncthreads();
  {
    G6::stageA(u, LDU, m0, 640, smem, wave, lane);
    const int pos = lane & 15, rw = pos >> 1, hf = pos & 1;
#pragma unroll
    for (int ii = 0; ii < G6::B_BYTES / 1024 / 4; ++ii) {
      const int i = ii * 4 + wave;
      const int blk = i * 4 + (lane >> 4);
      const int kb = blk / G6::NB, nb = blk % G6::NB;
      int row = 640 + kb * 8 + (rw ^ ((kb & 1) << 2));
      if (row > FH - 1) row = FH - 1;
      glds16(wout + (size_t)row * DM + n0 + nb * 16 + hf * 8, smem + G6::A_BYTES + i * 1024);
    }
  }
#pragma unroll
  for (int kt = 1; kt < 10; ++kt) {
    char* cur = smem + kt * G6::STAGE_BYTES;
    G6::compute<true>(cur, cur + G6::A_BYTES, acc, wm, wn, lane);
  }
  wait_vm<0>(); __syncthreads();
  G6::compute<true>(smem, smem + G6::A_BYTES, acc, wm, wn, lane);
  const int fr = lane & 15, fg = lane >> 4;
#pragma unroll
  for (int mt = 0; mt < G6::MT; ++mt)
#pragma unroll
    for (int nt = 0; nt < G6::NT; ++nt) {
      const int m = m0 + wm * G6::WM + mt * 16 + fr, n = n0 + wn * G6::WN + nt * 16 + fg * 4;
      u32x2 xv = *reinterpret_cast<const u32x2*>(x1 + (size_t)m * DM + n);
      f32x4 gv = *reinterpret_cast<const f32x4*>(gb + n);
      u32x2 w;
      w[0] = pack2(bflo(xv[0]) + gv[0] * acc[mt][nt][0], bfhi(xv[0]) + gv[1] * acc[mt][nt][1]);
      w[1] = pack2(bflo(xv[1]) + gv[2] * acc[mt][nt][2], bfhi(xv[1]) + gv[3] * acc[mt][nt][3]);
      *reinterpret_cast<u32x2*>(out + (size_t)m * DM + n) = w;
    }
}

extern "C" void kernel_launch(void* const* d_in, const int* in_sizes, int n_in, void* d_out, int out_size,
                              void* d_ws, size_t ws_size, hipStream_t stream) {
  const u16* x = (const u16*)d_in[0];
  const u16* ckv = (const u16*)d_in[1];
  const u16* wq = (const u16*)d_in[2];
  const u16* wk = (const u16*)d_in[3];
  const u16* wv = (const u16*)d_in[4];
  const u16* wo = (const u16*)d_in[5];
  const u16* win = (const u16*)d_in[7];
  const u16* wsp = (const u16*)d_in[8];
  const u16* bsp = (const u16*)d_in[9];
  const u16* wout = (const u16*)d_in[10];
  const u16* c = (const u16*)d_in[11];
  const u16* wmod = (const u16*)d_in[12];
  char* ws = (char*)d_ws;
  float* mod = (float*)(ws + WS_MOD);
  u16* h1 = (u16*)(ws + WS_H1);
  char* Qf = ws + WS_QF;
  char* Ki = ws + WS_KI;
  char* Vi = ws + WS_VI;
  u16* ctx = (u16*)(ws + WS_CTX);
  u16* x1 = (u16*)(ws + WS_X1);
  u16* z1 = (u16*)(ws + WS_Z1);
  u16* z2 = (u16*)(ws + WS_Z2);
  float* stats = (float*)(ws + WS_ST);
  u16* u = (u16*)(ws + WS_U);
  u16* out = (u16*)d_out;

  k_kv<<<256, 512, 0, stream>>>(ckv, wk, wv, Ki, Vi, c, wmod, mod);
  k_ln<<<S / 4, 256, 0, stream>>>(x, mod + 0 * DM, mod + 1 * DM, h1);
  k_qkv<<<512, 256, 0, stream>>>(h1, ckv, wq, wk, wv, Qf, Ki, Vi);
  k_attn<<<256, 512, 0, stream>>>(Qf, Ki, Vi, ctx);
  k_oproj<<<256, 256, 0, stream>>>(ctx, wo, x, mod + 2 * DM, x1);
  k_win<<<256, 256, 0, stream>>>(x1, mod + 3 * DM, mod + 4 * DM, win, z1, z2, stats);
  k_spatial<<<256, 256, 0, stream>>>(wsp, z2, stats, bsp, z1, u);
  k_wout<<<256, 256, 0, stream>>>(u, wout, x1, mod + 5 * DM, out);
}
```

```cpp
#include <hip/hip_runtime.h>
#include <hip/hip_bf16.h>
#include <stdint.h>

constexpr int S = 2048;
constexpr int DM = 512;
constexpr int NH = 32;
constexpr int HD = 128;
constexpr int HDM = NH * HD;
constexpr int FH = 688;
constexpr int FF = 1376;

typedef __attribute__((ext_vector_type(8))) short bf16x8;
typedef __attribute__((ext_vector_type(4))) short s16x4;
typedef __attribute__((ext_vector_type(4))) float f32x4;
typedef __attribute__((ext_vector_type(16))) float f32x16;
typedef __attribute__((ext_vector_type(4))) unsigned u32x4;
typedef __attribute__((ext_vector_type(2))) unsigned u32x2;
typedef __attribute__((address_space(3))) s16x4* lds_s16x4_ptr;
typedef unsigned short u16;

#define DI __device__ __forceinline__

DI float bf2f(u16 v) { return __uint_as_float(((unsigned)v) << 16); }
DI float bflo(unsigned v) { return __uint_as_float(v << 16); }
DI float bfhi(unsigned v) { return __uint_as_float(v & 0xffff0000u); }
typedef __attribute__((ext_vector_type(2))) __bf16 bf16x2_t;
typedef __attribute__((ext_vector_type(2))) float f32x2;
DI unsigned pack2(float lo, float hi) {
  f32x2 v = {lo, hi};
  return __builtin_bit_cast(unsigned, __builtin_convertvector(v, bf16x2_t));
}
template <int N> DI void wait_vm() { asm volatile("s_waitcnt vmcnt(%0)" ::"n"(N) : "memory"); }
DI void barrier() { asm volatile("s_waitcnt lgkmcnt(0)" ::: "memory"); __builtin_amdgcn_s_barrier(); }
DI float rbf(float x) { return bflo(pack2(x, 0.f)); }

DI void glds16(const void* g, const void* lds_wave_base) {
  const unsigned a = __builtin_amdgcn_readfirstlane((unsigned)(size_t)lds_wave_base);
  unsigned keep;
  asm volatile("s_mov_b32 %0, m0\n\ts_mov_b32 m0, %2\n\ts_nop 0\n\tglobal_load_lds_dwordx4 %1, off\n\ts_mov_b32 m0, %0"
               : "=&s"(keep) : "v"(g), "s"(a) : "memory");
}
#define STAMP(i)
DI s16x4 tr_read(const char* p) {
  return __builtin_amdgcn_ds_read_tr16_b64_v4i16((lds_s16x4_ptr)(p));
}

constexpr size_t WS_MOD = 0;
constexpr size_t WS_H1 = 16384;
constexpr size_t WS_QF = WS_H1 + (size_t)S * DM * 2;
constexpr size_t WS_KI = WS_QF + (size_t)S * HDM * 2;
constexpr size_t WS_VI = WS_KI + (size_t)S * HDM * 2;
constexpr size_t WS_CTX = WS_VI + (size_t)S * HDM * 2;
constexpr size_t WS_X1 = WS_CTX + (size_t)S * HDM * 2;
constexpr size_t WS_Z1 = WS_X1 + (size_t)S * DM * 2;
constexpr size_t WS_Z2 = WS_Z1 + (size_t)S * FH * 2;
constexpr size_t WS_ST = WS_Z2 + (size_t)S * FH * 2;
constexpr size_t WS_U = WS_ST + (size_t)8 * S * 2 * 4;
constexpr int LDU = 704;

__global__ __launch_bounds__(256) void k_mod(const u16* __restrict__ c, const u16* __restrict__ w,
                                             float* __restrict__ mod) {
  __shared__ float red[64][33];
  const int t = threadIdx.x, cg = t & 3, ks = t >> 2;
  const int col0 = blockIdx.x * 32 + cg * 8;
  float acc[8];
#pragma unroll
  for (int j = 0; j < 8; ++j) acc[j] = 0.f;
#pragma unroll
  for (int kk = 0; kk < 8; ++kk) {
    const int k = ks * 8 + kk;
    float cv = bf2f(c[k]);
    float sv = rbf(cv / (1.f + __expf(-cv)));
    u32x4 wv = *reinterpret_cast<const u32x4*>(w + (size_t)k * 3072 + col0);
#pragma unroll
    for (int j = 0; j < 4; ++j) {
      acc[2 * j] += sv * bflo(wv[j]);
      acc[2 * j + 1] += sv * bfhi(wv[j]);
    }
  }
#pragma unroll
  for (int j = 0; j < 8; ++j) red[ks][cg * 8 + j] = acc[j];
  __syncthreads();
  if (t < 32) {
    float s = 0.f;
    for (int i = 0; i < 64; ++i) s += red[i][t];
    mod[blockIdx.x * 32 + t] = rbf(s);
  }
}

__global__ __launch_bounds__(256) void k_ln(const u16* __restrict__ x, const float* __restrict__ sh,
                                            const float* __restrict__ cs, u16* __restrict__ h) {
  const int row = blockIdx.x * 4 + (threadIdx.x >> 6), lane = threadIdx.x & 63;
  u32x4 xv = *reinterpret_cast<const u32x4*>(x + (size_t)row * DM + lane * 8);
  float f[8];
#pragma unroll
  for (int j = 0; j < 4; ++j) { f[2 * j] = bflo(xv[j]); f[2 * j + 1] = bfhi(xv[j]); }
  float s = 0.f;
#pragma unroll
  for (int j = 0; j < 8; ++j) s += f[j];
#pragma unroll
  for (int o = 32; o >= 1; o >>= 1) s += __shfl_xor(s, o);
  const float mean = s * (1.f / DM);
  float v = 0.f;
#pragma unroll
  for (int j = 0; j < 8; ++j) { f[j] -= mean; v += f[j] * f[j]; }
#pragma unroll
  for (int o = 32; o >= 1; o >>= 1) v += __shfl_xor(v, o);
  const float rstd = rsqrtf(v * (1.f / DM) + 1e-5f);
  u32x4 o4;
#pragma unroll
  for (int j = 0; j < 4; ++j) {
    const int k = lane * 8 + 2 * j;
    float a = f[2 * j] * rstd * (1.f + cs[k]) + sh[k];
    float b = f[2 * j + 1] * rstd * (1.f + cs[k + 1]) + sh[k + 1];
    o4[j] = pack2(a, b);
  }
  *reinterpret_cast<u32x4*>(h + (size_t)row * DM + lane * 8) = o4;
}

template <int BM, int BN, int KS, int NWM, int NWN>
struct Gemm {
  static constexpr int NW = NWM * NWN;
  static constexpr int WM = BM / NWM, WN = BN / NWN;
  static constexpr int MT = WM / 16, NT = WN / 16;
  static constexpr int BK = 64 * KS;
  static constexpr int A_BYTES = BM * BK * 2;
  static constexpr int B_BYTES = BK * BN * 2;
  static constexpr int STAGE_BYTES = A_BYTES + B_BYTES;
  static constexpr int NB = BN / 16;

  static DI void stageA(const u16* __restrict__ A, int lda, int m0, int k0, char* lds, int wave, int lane) {
    constexpr int NI = A_BYTES / 1024;
    const int rr = lane >> 3, ch = (lane & 7) ^ rr;
#pragma unroll
    for (int ii = 0; ii < NI / NW; ++ii) {
      const int i = ii * NW + wave;
      const int sub = i / (BM / 8), j = i % (BM / 8);
      const u16* src = A + (size_t)(m0 + j * 8 + rr) * lda + k0 + sub * 64 + ch * 8;
      glds16(src, lds + i * 1024);
    }
  }
  static DI void stageB(const u16* __restrict__ B, int ldb, int k0, int n0, int nmax, char* lds, int wave, int lane) {
    constexpr int NI = B_BYTES / 1024;
    const int pos = lane & 15, rw = pos >> 1, hf = pos & 1;
#pragma unroll
    for (int ii = 0; ii < NI / NW; ++ii) {
      const int i = ii * NW + wave;
      const int blk = i * 4 + (lane >> 4);
      const int kb = blk / NB, nb = blk % NB;
      const int row = rw ^ ((kb & 1) << 2);
      int n = n0 + nb * 16 + hf * 8;
      if (n > nmax) n = nmax;
      const u16* src = B + (size_t)(k0 + kb * 8 + row) * ldb + n;
      glds16(src, lds + i * 1024);
    }
  }
  template <bool SWAP>
  static DI void compute(const char* ldsA, const char* ldsB, f32x4 (&acc)[MT][NT], int wm, int wn, int lane) {
    const int fr = lane & 15, fg = lane >> 4;
    const int q = fr >> 2, p = lane & 3;
#pragma unroll
    for (int sub = 0; sub < KS; ++sub) {
#pragma unroll
      for (int kk = 0; kk < 2; ++kk) {
        bf16x8 af[MT], bfr[NT];
#pragma unroll
        for (int mt = 0; mt < MT; ++mt) {
          const int row = wm * WM + mt * 16 + fr;
          af[mt] = *reinterpret_cast<const bf16x8*>(ldsA + sub * (BM * 128) + row * 128 + (((kk * 4 + fg) ^ (row & 7)) << 4));
        }
#pragma unroll
        for (int nt = 0; nt < NT; ++nt) {
          const int kb = sub * 8 + kk * 4 + fg, nb = wn * NT + nt;
          const char* base = ldsB + (kb * NB + nb) * 256 + p * 8;
          const int sw = (fg & 1) << 2;
          s16x4 lo = tr_read(base + ((q) ^ sw) * 32);
          s16x4 hi = tr_read(base + ((4 + q) ^ sw) * 32);
          bfr[nt] = __builtin_shufflevector(lo, hi, 0, 1, 2, 3, 4, 5, 6, 7);
        }
#pragma unroll
        for (int mt = 0; mt < MT; ++mt)
#pragma unroll
          for (int nt = 0; nt < NT; ++nt) {
            if constexpr (SWAP)
              acc[mt][nt] = __builtin_amdgcn_mfma_f32_16x16x32_bf16(bfr[nt], af[mt], acc[mt][nt], 0, 0, 0);
            else
              acc[mt][nt] = __builtin_amdgcn_mfma_f32_16x16x32_bf16(af[mt], bfr[nt], acc[mt][nt], 0, 0, 0);
          }
      }
    }
  }
};

using G1 = Gemm<128, 128, 1, 2, 2>;

__global__ __launch_bounds__(256, 2) void k_qkv(const u16* __restrict__ h1, const u16* __restrict__ ckv,
                                                const u16* __restrict__ wq, const u16* __restrict__ wk,
                                                const u16* __restrict__ wv, char* __restrict__ Qf,
                                                char* __restrict__ Ki, char* __restrict__ Vi) {
  __shared__ __attribute__((aligned(16))) char smem[2 * G1::STAGE_BYTES];
  const int b = blockIdx.x, xcd = b & 7, j = b >> 3;
  const int head = xcd * 4 + (j & 3), rest = j >> 2, which = rest >> 4, mtile = rest & 15;
  const int tid = threadIdx.x, wave = __builtin_amdgcn_readfirstlane(tid >> 6), lane = tid & 63;
  const int wm = wave >> 1, wn = wave & 1;
  const u16* A = which == 0 ? h1 : ckv;
  const u16* B = which == 0 ? wq : (which == 1 ? wk : wv);
  const int m0 = mtile * 128, n0 = head * 128;
  f32x4 acc[G1::MT][G1::NT];
#pragma unroll
  for (int i = 0; i < G1::MT; ++i)
#pragma unroll
    for (int jj = 0; jj < G1::NT; ++jj) acc[i][jj] = f32x4{0.f, 0.f, 0.f, 0.f};
  constexpr int NK = DM / G1::BK;
  G1::stageA(A, DM, m0, 0, smem, wave, lane);
  G1::stageB(B, HDM, 0, n0, HDM, smem + G1::A_BYTES, wave, lane);
  for (int kt = 0; kt < NK; ++kt) {
    wait_vm<0>(); __syncthreads();
    char* cur = smem + (kt & 1) * G1::STAGE_BYTES;
    char* nxt = smem + ((kt + 1) & 1) * G1::STAGE_BYTES;
    if (kt + 1 < NK) {
      G1::stageA(A, DM, m0, (kt + 1) * G1::BK, nxt, wave, lane);
      G1::stageB(B, HDM, (kt + 1) * G1::BK, n0, HDM, nxt + G1::A_BYTES, wave, lane);
    }
    if (which == 2) G1::compute<false>(cur, cur + G1::A_BYTES, acc, wm, wn, lane);
    else G1::compute<true>(cur, cur + G1::A_BYTES, acc, wm, wn, lane);
  }
  __syncthreads();
  const int fr = lane & 15, fg = lane >> 4;
#pragma unroll
  for (int mt = 0; mt < G1::MT; ++mt)
#pragma unroll
    for (int nt = 0; nt < G1::NT; ++nt) {
      u32x2 v;
      v[0] = pack2(acc[mt][nt][0], acc[mt][nt][1]);
      v[1] = pack2(acc[mt][nt][2], acc[mt][nt][3]);
      int off;
      if (which == 2) {
        const int d = wn * 64 + nt * 16 + fr, m = wm * 64 + mt * 16 + fg * 4;
        const int tile = m >> 6, key = m & 63;
        const int pos = (key & 0x33) | ((key & 4) << 1) | ((key & 8) >> 1);
        off = tile * 16384 + d * 128 + (((pos >> 3) ^ ((d >> 1) & 7)) << 4) + (pos & 7) * 2;
      } else {
        const int m = wm * 64 + mt * 16 + fr, d = wn * 64 + nt * 16 + fg * 4;
        if (which == 1) {
          const int tile = m >> 6, key = m & 63;
          off = tile * 16384 + key * 256 + (((d >> 3) ^ (key & 15)) << 4) + (d & 7) * 2;
        } else {
          const int rg = m >> 5, r = m & 31;
          off = rg * 8192 + (d >> 4) * 1024 + ((((d >> 3) & 1) * 32 + r) << 4) + (d & 7) * 2;
        }
      }
      *reinterpret_cast<u32x2*>(smem + off) = v;
    }
  __syncthreads();
  char* dst = (which == 0 ? Qf : (which == 1 ? Ki : Vi)) + ((size_t)head * 16 + mtile) * 32768;
#pragma unroll
  for (int i = 0; i < 8; ++i) {
    const int o = (i * 256 + tid) * 16;
    *reinterpret_cast<u32x4*>(dst + o) = *reinterpret_cast<const u32x4*>(smem + o);
  }
}

constexpr int KV_NS = 4, KV_STAGE = 32768;
template <bool isV>
DI void kv_body(char* smem, const u16* __restrict__ ckv, const u16* __restrict__ B, char* __restrict__ dstb,
                const u16* __restrict__ cvec, const u16* __restrict__ wmod, float* __restrict__ mod) {
  const int b = blockIdx.x, xcd = b & 7, j = b >> 3;
  const int ntile = xcd * 4 + (j & 3), mtile = j >> 2;
  const int tid = threadIdx.x, wave = __builtin_amdgcn_readfirstlane(tid >> 6), lane = tid & 63;
  const int wm = wave >> 2, wn = wave & 3, r = lane & 31, h = lane >> 5;
  const int m0 = mtile * 256, n0 = (ntile & 15) * 256;
  float* modred = reinterpret_cast<float*>(smem + KV_NS * KV_STAGE);
  STAMP(0);

  const u16* asrc[2];
  const u16* bsrc[2];
#pragma unroll
  for (int ii = 0; ii < 2; ++ii) {
    const int i = ii * 8 + wave;
    const int row = 16 * i + (lane >> 2), ch = (lane & 3) ^ ((lane >> 4) & 3);
    asrc[ii] = ckv + (size_t)(m0 + row) * DM + ch * 8;
    const int blk = i * 4 + (lane >> 4), kb = blk >> 4, nb = blk & 15;
    const int pos = lane & 15, rw = (pos >> 1) ^ ((nb & 1) << 2), hf = pos & 1;
    bsrc[ii] = B + (size_t)(kb * 8 + rw) * HDM + n0 + nb * 16 + hf * 8;
  }
  auto stage = [&](int kt0, int slot) {
    char* dst = smem + slot * KV_STAGE;
    const int kt = (kt0 + 2 * mtile) & 15;
#pragma unroll
    for (int ii = 0; ii < 2; ++ii) {
      glds16(asrc[ii] + kt * 32, dst + (ii * 8 + wave) * 1024);
      glds16(bsrc[ii] + (size_t)kt * 32 * HDM, dst + 16384 + (ii * 8 + wave) * 1024);
    }
  };
  constexpr int NK = DM / 32;
#pragma unroll
  for (int st = 0; st < KV_NS - 1; ++st) stage(st, st);

  if (b < 192) {
    const u16* p = wmod + (size_t)tid * 3072 + b * 16;
    const u32x4 w0 = *reinterpret_cast<const u32x4*>(p), w1 = *reinterpret_cast<const u32x4*>(p + 8);
    const float c = bf2f(cvec[tid]);
    const float sv = rbf(c / (1.f + __expf(-c)));
    float v[16];
#pragma unroll
    for (int jj = 0; jj < 4; ++jj) {
      v[2 * jj] = sv * bflo(w0[jj]); v[2 * jj + 1] = sv * bfhi(w0[jj]);
      v[8 + 2 * jj] = sv * bflo(w1[jj]); v[8 + 2 * jj + 1] = sv * bfhi(w1[jj]);
    }
#pragma unroll
    for (int st = 0; st < 4; ++st) {
      const int half = 8 >> st, m = 32 >> st;
      const bool up = lane & m;
#pragma unroll
      for (int e = 0; e < half; ++e) {
        const float send = up ? v[e] : v[half + e];
        const float keep = up ? v[half + e] : v[e];
        v[e] = keep + __shfl_xor(send, m);
      }
    }
    v[0] += __shfl_xor(v[0], 2);
    v[0] += __shfl_xor(v[0], 1);
    if ((lane & 3) == 0) {
      const int col = ((lane >> 5) & 1) * 8 + ((lane >> 4) & 1) * 4 + ((lane >> 3) & 1) * 2 + ((lane >> 2) & 1);
      modred[wave * 16 + col] = v[0];
    }
  }

  STAMP(1);
  f32x16 acc[4][2];
#pragma unroll
  for (int i = 0; i < 4; ++i)
#pragma unroll
    for (int jj = 0; jj < 2; ++jj)
#pragma unroll
      for (int e = 0; e < 16; ++e) acc[i][jj][e] = 0.f;
  const int G = lane >> 4, q4 = (lane & 15) >> 2, p4 = lane & 3;
  int aoff[2];
#pragma unroll
  for (int u = 0; u < 2; ++u) aoff[u] = (wm * 128 + r) * 64 + (((2 * u + h) ^ ((r >> 2) & 3)) << 4);
  const int nh = G & 1;
  const int boff = 16384 + (h * 16 + wn * 4 + nh) * 256 + p4 * 8;
  const int brow0 = (q4 ^ (nh << 2)) * 32, brow1 = ((4 + q4) ^ (nh << 2)) * 32;
  for (int kt = 0; kt < NK; ++kt) {
    if (kt + KV_NS - 2 <= NK - 1) wait_vm<(KV_NS - 2) * 4>(); else wait_vm<0>();
    barrier();
    const bool pf = kt + KV_NS - 1 < NK;
    const int ktn = (kt + KV_NS - 1 + 2 * mtile) & 15;
    char* dst = smem + ((kt + KV_NS - 1) & (KV_NS - 1)) * KV_STAGE;
    const char* cur = smem + (kt & (KV_NS - 1)) * KV_STAGE;
    bf16x8 af[2][4], bfr[2][2];
#pragma unroll
    for (int u = 0; u < 2; ++u) {
#pragma unroll
      for (int nt = 0; nt < 2; ++nt) {
        const char* bb = cur + boff + u * (2 * 16 * 256) + nt * 512;
        s16x4 lo = tr_read(bb + brow0), hi = tr_read(bb + brow1);
        bfr[u][nt] = __builtin_shufflevector(lo, hi, 0, 1, 2, 3, 4, 5, 6, 7);
      }
#pragma unroll
      for (int mt = 0; mt < 4; ++mt) af[u][mt] = *reinterpret_cast<const bf16x8*>(cur + aoff[u] + mt * 2048);
    }
#pragma unroll
    for (int g4 = 0; g4 < 4; ++g4) {
      const int u = g4 >> 1;
      __builtin_amdgcn_sched_barrier(0);
      if (pf) {
        const int ii = g4 >> 1;
        if (g4 & 1) glds16(bsrc[ii] + (size_t)ktn * 32 * HDM, dst + 16384 + (ii * 8 + wave) * 1024);
        else glds16(asrc[ii] + ktn * 32, dst + (ii * 8 + wave) * 1024);
      }
      __builtin_amdgcn_sched_barrier(0);
#pragma unroll
      for (int m2 = 0; m2 < 2; ++m2)
#pragma unroll
        for (int nt = 0; nt < 2; ++nt) {
          const int mt = (g4 & 1) * 2 + m2;
          if constexpr (isV)
            acc[mt][nt] = __builtin_amdgcn_mfma_f32_32x32x16_bf16(af[u][mt], bfr[u][nt], acc[mt][nt], 0, 0, 0);
          else
            acc[mt][nt] = __builtin_amdgcn_mfma_f32_32x32x16_bf16(bfr[u][nt], af[u][mt], acc[mt][nt], 0, 0, 0);
        }
    }
  }
  barrier();
  STAMP(2);
#pragma unroll
  for (int mt = 0; mt < 4; ++mt)
#pragma unroll
    for (int nt = 0; nt < 2; ++nt)
#pragma unroll
      for (int qd = 0; qd < 4; ++qd) {
        u32x2 v;
        v[0] = pack2(acc[mt][nt][4 * qd], acc[mt][nt][4 * qd + 1]);
        v[1] = pack2(acc[mt][nt][4 * qd + 2], acc[mt][nt][4 * qd + 3]);
        int off;
        if constexpr (isV) {
          const int n = wn * 64 + nt * 32 + r, m = wm * 128 + mt * 32 + 8 * qd + 4 * h;
          const int hd = n >> 7, d = n & 127, tile = m >> 6, key = m & 63;
          const int pos = (key & 0x33) | ((key & 4) << 1) | ((key & 8) >> 1);
          off = (hd * 4 + tile) * 16384 + d * 128 + (((pos >> 3) ^ ((d >> 1) & 7)) << 4) + (pos & 7) * 2;
        } else {
          const int m = wm * 128 + mt * 32 + r, n = wn * 64 + nt * 32 + 8 * qd + 4 * h;
          const int hd = n >> 7, d = n & 127, tile = m >> 6, key = m & 63;
          off = (hd * 4 + tile) * 16384 + key * 256 + (((d >> 3) ^ (key & 15)) << 4) + (d & 7) * 2;
        }
        *reinterpret_cast<u32x2*>(smem + off) = v;
      }
  barrier();
  {
    const int head0 = (ntile & 15) * 2;
#pragma unroll
    for (int i = 0; i < 16; ++i) {
      const int o = (i * 512 + tid) * 16;
      const int hd = o >> 16, rem = o & 65535;
      *reinterpret_cast<u32x4*>(dstb + ((size_t)(head0 + hd) * 32 + mtile * 4) * 16384 + rem) =
          *reinterpret_cast<const u32x4*>(smem + o);
    }
  }
  STAMP(3);
  if (b < 192 && tid < 16) {
    float sum = 0.f;
#pragma unroll
    for (int i = 0; i < 8; ++i) sum += modred[i * 16 + tid];
    mod[b * 16 + tid] = rbf(sum);
  }
}
__global__ __launch_bounds__(512, 2) void k_kv(const u16* __restrict__ ckv, const u16* __restrict__ wk,
                                               const u16* __restrict__ wv, char* __restrict__ Ki,
                                               char* __restrict__ Vi, const u16* __restrict__ cvec,
                                               const u16* __restrict__ wmod, float* __restrict__ mod) {
  __shared__ __attribute__((aligned(16))) char smem[KV_NS * KV_STAGE + 512];
  if ((blockIdx.x & 7) >= 4) kv_body<true>(smem, ckv, wv, Vi, cvec, wmod, mod);
  else kv_body<false>(smem, ckv, wk, Ki, cvec, wmod, mod);
}

__global__ __launch_bounds__(512, 2) void k_attn(const char* __restrict__ Qf, const char* __restrict__ Ki,
                                                 const char* __restrict__ Vi, char* __restrict__ ctx) {
  __shared__ __attribute__((aligned(16))) char smem[131072];
  const int b = blockIdx.x, xcd = b & 7, j = b >> 3;
  const int head = xcd * 4 + (j & 3), pair = j >> 2;
  const int tid = threadIdx.x, wave = __builtin_amdgcn_readfirstlane(tid >> 6), lane = tid & 63;
  const int s = wave >> 2, g = wave & 3, r = lane & 31, h = lane >> 5;
  const char* Kh = Ki + (size_t)head * (32 * 16384);
  const char* Vh = Vi + (size_t)head * (32 * 16384);
  constexpr float C = 0.08838834764831845f * 1.4426950408889634f;

  for (int qi = 0; qi < 2; ++qi) {
    const int qb = qi ? pair : 15 - pair;
    bf16x8 q[8];
    {
      const char* qp = Qf + ((size_t)head * 64 + qb * 4 + g) * 8192 + lane * 16;
#pragma unroll
      for (int ks = 0; ks < 8; ++ks) q[ks] = *reinterpret_cast<const bf16x8*>(qp + ks * 1024);
    }
    f32x16 o[4];
#pragma unroll
    for (int dt = 0; dt < 4; ++dt)
#pragma unroll
      for (int i = 0; i < 16; ++i) o[dt][i] = 0.f;
    float m = -1e30f, l = 0.f;
    const int n = qb + 1;
#pragma unroll
    for (int i = 0; i < 4; ++i) {
      glds16(Kh + (wave * 4 + i) * 1024 + lane * 16, smem + (wave * 4 + i) * 1024);
      glds16(Vh + (wave * 4 + i) * 1024 + lane * 16, smem + 32768 + (wave * 4 + i) * 1024);
    }
    for (int it = 0; it < n; ++it) {
      wait_vm<0>(); __syncthreads();
      if (it + 1 < n) {
        char* nb = smem + ((it + 1) & 1) * 65536;
        const size_t go = (size_t)(it + 1) * 32768;
#pragma unroll
        for (int i = 0; i < 4; ++i) {
          glds16(Kh + go + (wave * 4 + i) * 1024 + lane * 16, nb + (wave * 4 + i) * 1024);
          glds16(Vh + go + (wave * 4 + i) * 1024 + lane * 16, nb + 32768 + (wave * 4 + i) * 1024);
        }
      }
      const bool last = (it == n - 1);
      if (!(last && s == 1 && g < 2)) {
        const char* Kt = smem + (it & 1) * 65536 + s * 16384;
        const char* Vt = smem + (it & 1) * 65536 + 32768 + s * 16384;
        f32x16 st[2];
#pragma unroll
        for (int kt = 0; kt < 2; ++kt) {
#pragma unroll
          for (int i = 0; i < 16; ++i) st[kt][i] = 0.f;
#pragma unroll
          for (int ks = 0; ks < 8; ++ks) {
            bf16x8 a = *reinterpret_cast<const bf16x8*>(Kt + (32 * kt + r) * 256 + (((2 * ks + h) ^ (r & 15)) << 4));
            st[kt] = __builtin_amdgcn_mfma_f32_32x32x16_bf16(a, q[ks], st[kt], 0, 0, 0);
          }
        }
        if (last) {
          const int lim = 32 * g + r - 64 * s - 4 * h;
#pragma unroll
          for (int kt = 0; kt < 2; ++kt)
#pragma unroll
            for (int i = 0; i < 16; ++i) {
              const int kk = 32 * kt + (i & 3) + 8 * (i >> 2);
              if (kk > lim) st[kt][i] = -__builtin_inff();
            }
        }
        float pm = st[0][0];
#pragma unroll
        for (int i = 1; i < 16; ++i) pm = fmaxf(pm, st[0][i]);
#pragma unroll
        for (int i = 0; i < 16; ++i) pm = fmaxf(pm, st[1][i]);
        pm = fmaxf(pm, __shfl_xor(pm, 32));
        const float mn = fmaxf(m, pm);
        const float alpha = __builtin_amdgcn_exp2f((m - mn) * C);
        m = mn;
        const float mc = -mn * C;
        float ps = 0.f;
#pragma unroll
        for (int kt = 0; kt < 2; ++kt)
#pragma unroll
          for (int i = 0; i < 16; ++i) {
            float e = __builtin_amdgcn_exp2f(fmaf(st[kt][i], C, mc));
            st[kt][i] = e;
            ps += e;
          }
        l = l * alpha + ps;
#pragma unroll
        for (int dt = 0; dt < 4; ++dt)
#pragma unroll
          for (int i = 0; i < 16; ++i) o[dt][i] *= alpha;
        bf16x8 p[4];
#pragma unroll
        for (int ks = 0; ks < 4; ++ks) {
          u32x4 w;
#pragma unroll
          for (int jj = 0; jj < 4; ++jj)
            w[jj] = pack2(st[ks >> 1][8 * (ks & 1) + 2 * jj], st[ks >> 1][8 * (ks & 1) + 2 * jj + 1]);
          p[ks] = __builtin_bit_cast(bf16x8, w);
        }
#pragma unroll
        for (int dt = 0; dt < 4; ++dt)
#pragma unroll
          for (int ks = 0; ks < 4; ++ks) {
            bf16x8 a = *reinterpret_cast<const bf16x8*>(Vt + (32 * dt + r) * 128 + (((2 * ks + h) ^ ((r >> 1) & 7)) << 4));
            o[dt] = __builtin_amdgcn_mfma_f32_32x32x16_bf16(a, p[ks], o[dt], 0, 0, 0);
          }
      }
    }
    __syncthreads();
    l += __shfl_xor(l, 32);
    float* xo = reinterpret_cast<float*>(smem) + g * (64 * 68);
    if (s == 1) {
#pragma unroll
      for (int dt = 0; dt < 4; ++dt)
#pragma unroll
        for (int qd = 0; qd < 4; ++qd) {
          f32x4 v = {o[dt][4 * qd], o[dt][4 * qd + 1], o[dt][4 * qd + 2], o[dt][4 * qd + 3]};
          *reinterpret_cast<f32x4*>(xo + ((dt * 4 + qd) * 64 + lane) * 4) = v;
        }
      f32x4 v = {m, l, 0.f, 0.f};
      *reinterpret_cast<f32x4*>(xo + (16 * 64 + lane) * 4) = v;
    }
    __syncthreads();
    if (s == 0) {
      f32x4 ml = *reinterpret_cast<const f32x4*>(xo + (16 * 64 + lane) * 4);
      const float mt = fmaxf(m, ml[0]);
      const float a0 = __builtin_amdgcn_exp2f((m - mt) * C), a1 = __builtin_amdgcn_exp2f((ml[0] - mt) * C);
      const float inv = 1.f / (l * a0 + ml[1] * a1);
      const float f0 = a0 * inv, f1 = a1 * inv;
      char* crow = ctx + ((size_t)(qb * 8 + g * 2 + (r >> 4)) * 128 + head * 4) * 1024 + (r & 15) * 16 + h * 8;
#pragma unroll
      for (int dt = 0; dt < 4; ++dt)
#pragma unroll
        for (int qd = 0; qd < 4; ++qd) {
          f32x4 v = *reinterpret_cast<const f32x4*>(xo + ((dt * 4 + qd) * 64 + lane) * 4);
          u32x2 w;
          w[0] = pack2(o[dt][4 * qd] * f0 + v[0] * f1, o[dt][4 * qd + 1] * f0 + v[1] * f1);
          w[1] = pack2(o[dt][4 * qd + 2] * f0 + v[2] * f1, o[dt][4 * qd + 3] * f0 + v[3] * f1);
          *reinterpret_cast<u32x2*>(crow + dt * 1024 + qd * 256) = w;
        }
    }
    __syncthreads();
  }
}

constexpr int SK_AREA = 16384;
template <int NT> struct SkOwn { static constexpr int N = (2 * NT + 3) / 4; };
template <int NT>
DI void splitk_reduce(const f32x4 (&acc)[4][NT], f32x4 (&own)[SkOwn<NT>::N], char* smem, int wave, int lane) {
  const int hi = wave >> 2, wq = wave & 3;
  char* mine = smem + wave * SK_AREA + lane * 16;
  f32x4 r[2 * NT];
#pragma unroll
  for (int f = 0; f < 2 * NT; ++f) {
    const f32x4 lo = acc[f / NT][f % NT], up = acc[2 + f / NT][f % NT];
    f32x4 snd, kp;
#pragma unroll
    for (int e = 0; e < 4; ++e) { snd[e] = hi ? lo[e] : up[e]; kp[e] = hi ? up[e] : lo[e]; }
    *reinterpret_cast<f32x4*>(mine + f * 1024) = snd;
    r[f] = kp;
  }
  barrier();
  {
    const char* part = smem + (wave ^ 4) * SK_AREA + lane * 16;
#pragma unroll
    for (int f = 0; f < 2 * NT; ++f) {
      const f32x4 o = *reinterpret_cast<const f32x4*>(part + f * 1024);
      r[f] += o;
    }
  }
  barrier();
#pragma unroll
  for (int f = 0; f < 2 * NT; ++f) *reinterpret_cast<f32x4*>(mine + f * 1024) = r[f];
  barrier();
#pragma unroll
  for (int i = 0; i < SkOwn<NT>::N; ++i) {
    const int f = wq + 4 * i;
    const char* p = smem + hi * 4 * SK_AREA + lane * 16 + (f < 2 * NT ? f : 0) * 1024;
    const f32x4 a = *reinterpret_cast<const f32x4*>(p), b = *reinterpret_cast<const f32x4*>(p + SK_AREA);
    const f32x4 c = *reinterpret_cast<const f32x4*>(p + 2 * SK_AREA), d = *reinterpret_cast<const f32x4*>(p + 3 * SK_AREA);
    own[i] = (a + b) + (c + d);
  }
}
DI void sk_bfrags(const char* img, bf16x8 (&bfr)[4], int lane) {
  const int fr = lane & 15, fg = lane >> 4, q = fr >> 2, p = lane & 3, sw = (fg & 1) << 2;
  const char* base = img + fg * 1024 + p * 8;
#pragma unroll
  for (int nt = 0; nt < 4; ++nt) {
    s16x4 lo = tr_read(base + nt * 256 + (q ^ sw) * 32);
    s16x4 hi = tr_read(base + nt * 256 + ((4 + q) ^ sw) * 32);
    bfr[nt] = __builtin_shufflevector(lo, hi, 0, 1, 2, 3, 4, 5, 6, 7);
  }
}

constexpr int OP_NS = 16, OP_DB = 4, OP_DA = 6;
constexpr int op_wait(int t) {
  int pos = 0, bpos = -1;
  for (int i = 0; i < OP_DA; ++i) {
    if (i < OP_DB) { pos += 4; if (i == t) bpos = pos; }
    pos += 4;
  }
  for (int s = 0; s < t; ++s) {
    if (s + OP_DB < OP_NS) { pos += 4; if (s + OP_DB == t) bpos = pos; }
    if (s + OP_DA < OP_NS) pos += 4;
  }
  return pos - bpos;
}
template <int T> struct OpWait { static constexpr int v = op_wait(T); };
template <int T> DI void op_step(f32x4 (&acc)[4][4], u32x4 (&areg)[OP_DA][4], char* ring, const char* asrc,
                                 const u16* bs0, const u16* bs1, int lane) {
  wait_vm<OpWait<T>::v>();
  bf16x8 bfr[4];
  sk_bfrags(ring + (T % OP_DB) * 4096, bfr, lane);
#pragma unroll
  for (int mt = 0; mt < 4; ++mt)
#pragma unroll
    for (int nt = 0; nt < 4; ++nt)
      acc[mt][nt] = __builtin_amdgcn_mfma_f32_16x16x32_bf16(bfr[nt], __builtin_bit_cast(bf16x8, areg[T % OP_DA][mt]), acc[mt][nt], 0, 0, 0);
  if constexpr (T + OP_DB < OP_NS) {
    asm volatile("s_waitcnt lgkmcnt(0)" ::: "memory");
#pragma unroll
    for (int i = 0; i < 4; ++i)
      glds16(((i & 1) ? bs1 : bs0) + (size_t)((T + OP_DB) * 32 + i * 8) * DM, ring + (T % OP_DB) * 4096 + i * 1024);
  }
  if constexpr (T + OP_DA < OP_NS) {
#pragma unroll
    for (int mt = 0; mt < 4; ++mt)
      areg[T % OP_DA][mt] = *reinterpret_cast<const u32x4*>(asrc + (size_t)mt * (128 * 1024) + (T + OP_DA) * 1024);
  }
  if constexpr (T + 1 < OP_NS) op_step<T + 1>(acc, areg, ring, asrc, bs0, bs1, lane);
}
__global__ __launch_bounds__(512, 2) void k_oproj(const char* __restrict__ ctxf, const u16* __restrict__ wo,
                                                  const u16* __restrict__ x, const float* __restrict__ ga,
                                                  u16* __restrict__ x1) {
  __shared__ __attribute__((aligned(16))) char smem[8 * SK_AREA];
  const int b = blockIdx.x, xcd = b & 7, j = b >> 3;
  const int mtile = xcd * 4 + (j >> 3), ntile = j & 7;
  const int tid = threadIdx.x, wave = __builtin_amdgcn_readfirstlane(tid >> 6), lane = tid & 63;
  const int m0 = mtile * 64, n0 = ntile * 64;
  char* ring = smem + wave * SK_AREA;
  const char* asrc = ctxf + ((size_t)(mtile * 4) * 128 + wave * 16) * 1024 + lane * 16;
  const int rw = (lane & 15) >> 1;
  const u16* bs0 = wo + (size_t)(wave * 512 + rw) * DM + n0 + (lane >> 4) * 16 + (lane & 1) * 8;
  const u16* bs1 = wo + (size_t)(wave * 512 + (rw ^ 4)) * DM + n0 + (lane >> 4) * 16 + (lane & 1) * 8;
  f32x4 acc[4][4];
#pragma unroll
  for (int i = 0; i < 4; ++i)
#pragma unroll
    for (int jj = 0; jj < 4; ++jj) acc[i][jj] = f32x4{0.f, 0.f, 0.f, 0.f};
  u32x4 areg[OP_DA][4];
#pragma unroll
  for (int i = 0; i < OP_DA; ++i) {
    if (i < OP_DB) {
#pragma unroll
      for (int ii = 0; ii < 4; ++ii) glds16(((ii & 1) ? bs1 : bs0) + (size_t)(i * 32 + ii * 8) * DM, ring + i * 4096 + ii * 1024);
    }
#pragma unroll
    for (int mt = 0; mt < 4; ++mt)
      areg[i][mt] = *reinterpret_cast<const u32x4*>(asrc + (size_t)mt * (128 * 1024) + i * 1024);
  }
  op_step<0>(acc, areg, ring, asrc, bs0, bs1, lane);
  const int fr = lane & 15, fg = lane >> 4, hi = wave >> 2, wq = wave & 3;
  u32x2 xv[2];
  f32x4 gv;
  {
    gv = *reinterpret_cast<const f32x4*>(ga + n0 + wq * 16 + fg * 4);
#pragma unroll
    for (int i = 0; i < 2; ++i)
      xv[i] = *reinterpret_cast<const u32x2*>(x + (size_t)(m0 + (2 * hi + i) * 16 + fr) * DM + n0 + wq * 16 + fg * 4);
  }
  f32x4 own[2];
  splitk_reduce<4>(acc, own, smem, wave, lane);
#pragma unroll
  for (int i = 0; i < 2; ++i) {
    u32x2 w;
    w[0] = pack2(bflo(xv[i][0]) + gv[0] * own[i][0], bfhi(xv[i][0]) + gv[1] * own[i][1]);
    w[1] = pack2(bflo(xv[i][1]) + gv[2] * own[i][2], bfhi(xv[i][1]) + gv[3] * own[i][3]);
    *reinterpret_cast<u32x2*>(x1 + (size_t)(m0 + (2 * hi + i) * 16 + fr) * DM + n0 + wq * 16 + fg * 4) = w;
  }
}

constexpr int NBLK = 43;
using G4 = Gemm<64, 192, 1, 2, 4>;
DI float gelu_tanh(float x) {
  const float u = 0.7978845608028654f * (x + 0.044715f * x * x * x);
  const float e = __expf(2.f * u);
  const float t = 1.f - 2.f / (e + 1.f);
  return 0.5f * x * (1.f + t);
}
DI u32x4 gload_asm(const void* p) {
  u32x4 r;
  asm volatile("global_load_dwordx4 %0, %1, off" : "=v"(r) : "v"(p) : "memory");
  return r;
}
template <bool Z2>
DI void win_body(char* smem, const u16* __restrict__ x1, const float* __restrict__ sb, const float* __restrict__ cb,
                 const u16* __restrict__ win, char* __restrict__ zf, float* __restrict__ stats, int mtile, int t4) {
  char* sB = smem + 65536;
  const int tid = threadIdx.x, wave = __builtin_amdgcn_readfirstlane(tid >> 6), lane = tid & 63;
  const int wm = wave >> 2, wn = wave & 3;
  const int m0 = mtile * 64, n0 = (Z2 ? FH : 0) + t4 * 176;
  u32x4 c0 = gload_asm(cb + lane * 8), c1 = gload_asm(cb + lane * 8 + 4);
  u32x4 s0 = gload_asm(sb + lane * 8), s1 = gload_asm(sb + lane * 8 + 4);
  {
    const int rr = lane >> 3, ch = (lane & 7) ^ rr;
    const u16* src = x1 + (size_t)(m0 + wave * 8 + rr) * DM + ch * 8;
#pragma unroll
    for (int sub = 0; sub < 8; ++sub) glds16(src + sub * 64, smem + sub * 8192 + wave * 1024);
  }
#pragma unroll
  for (int st = 0; st < 4; ++st) G4::stageB(win, FF, st * 64, n0, FF - 8, sB + st * G4::B_BYTES, wave, lane);
  asm volatile("s_waitcnt vmcnt(12)" : "+v"(c0), "+v"(c1), "+v"(s0), "+v"(s1)::"memory");
  {
    float cbv[8], sbv[8];
#pragma unroll
    for (int jj = 0; jj < 4; ++jj) {
      cbv[jj] = 1.f + __uint_as_float(c0[jj]); cbv[4 + jj] = 1.f + __uint_as_float(c1[jj]);
      sbv[jj] = __uint_as_float(s0[jj]); sbv[4 + jj] = __uint_as_float(s1[jj]);
    }
#pragma unroll
    for (int i = 0; i < 8; ++i) {
      char* p = smem + (lane >> 3) * 8192 + (wave * 8 + i) * 128 + (((lane & 7) ^ i) << 4);
      const u32x4 xv = *reinterpret_cast<const u32x4*>(p);
      float f[8];
#pragma unroll
      for (int jj = 0; jj < 4; ++jj) { f[2 * jj] = bflo(xv[jj]); f[2 * jj + 1] = bfhi(xv[jj]); }
      float s = 0.f;
#pragma unroll
      for (int jj = 0; jj < 8; ++jj) s += f[jj];
#pragma unroll
      for (int o = 32; o >= 1; o >>= 1) s += __shfl_xor(s, o);
      const float mean = s * (1.f / DM);
      float v = 0.f;
#pragma unroll
      for (int jj = 0; jj < 8; ++jj) { f[jj] -= mean; v += f[jj] * f[jj]; }
#pragma unroll
      for (int o = 32; o >= 1; o >>= 1) v += __shfl_xor(v, o);
      const float rstd = rsqrtf(v * (1.f / DM) + 1e-5f);
      u32x4 o4;
#pragma unroll
      for (int jj = 0; jj < 4; ++jj)
        o4[jj] = pack2(f[2 * jj] * rstd * cbv[2 * jj] + sbv[2 * jj], f[2 * jj + 1] * rstd * cbv[2 * jj + 1] + sbv[2 * jj + 1]);
      *reinterpret_cast<u32x4*>(p) = o4;
    }
  }
  f32x4 acc[G4::MT][G4::NT];
#pragma unroll
  for (int i = 0; i < G4::MT; ++i)
#pragma unroll
    for (int jj = 0; jj < G4::NT; ++jj) acc[i][jj] = f32x4{0.f, 0.f, 0.f, 0.f};
#pragma unroll
  for (int kt = 0; kt < 8; ++kt) {
    if (kt == 0) wait_vm<9>(); else if (kt <= 5) wait_vm<6>(); else if (kt == 6) wait_vm<3>(); else wait_vm<0>();
    barrier();
    if (kt >= 1 && kt + 3 < 8) G4::stageB(win, FF, (kt + 3) * 64, n0, FF - 8, sB + ((kt + 3) & 3) * G4::B_BYTES, wave, lane);
    G4::compute<!Z2>(smem + kt * 8192, sB + (kt & 3) * G4::B_BYTES, acc, wm, wn, lane);
  }
  const int fr = lane & 15, fg = lane >> 4;
  if constexpr (!Z2) {
#pragma unroll
    for (int nt = 0; nt < G4::NT; ++nt) {
      const int nbl = wn * 3 + nt, nb = t4 * 11 + nbl;
      if (nbl < 11 && nb < NBLK) {
#pragma unroll
        for (int mt = 0; mt < G4::MT; ++mt) {
          u32x2 w;
          w[0] = pack2(gelu_tanh(acc[mt][nt][0]), gelu_tanh(acc[mt][nt][1]));
          w[1] = pack2(gelu_tanh(acc[mt][nt][2]), gelu_tanh(acc[mt][nt][3]));
          *reinterpret_cast<u32x2*>(zf + ((size_t)(mtile * 4 + wm * 2 + mt) * NBLK + nb) * 512 + lane * 8) = w;
        }
      }
    }
  } else {
    float p1[G4::MT][4], p2[G4::MT][4];
#pragma unroll
    for (int mt = 0; mt < G4::MT; ++mt)
#pragma unroll
      for (int i = 0; i < 4; ++i) { p1[mt][i] = 0.f; p2[mt][i] = 0.f; }
#pragma unroll
    for (int nt = 0; nt < G4::NT; ++nt) {
      const int nbl = wn * 3 + nt, nb = t4 * 11 + nbl;
      if (nbl < 11 && nb < NBLK) {
#pragma unroll
        for (int mt = 0; mt < G4::MT; ++mt) {
          float gz[4];
#pragma unroll
          for (int i = 0; i < 4; ++i) {
            gz[i] = rbf(gelu_tanh(acc[mt][nt][i]));
            p1[mt][i] += gz[i]; p2[mt][i] += gz[i] * gz[i];
          }
          u32x2 w;
          w[0] = pack2(gz[0], gz[1]);
          w[1] = pack2(gz[2], gz[3]);
          *reinterpret_cast<u32x2*>(zf + ((size_t)(mtile * 2 + wm) * NBLK + nb) * 1024 + (fr + 16 * (mt * 2 + (fg >> 1))) * 16 + (fg & 1) * 8) = w;
        }
      }
    }
    barrier();
    float* red = reinterpret_cast<float*>(smem);
#pragma unroll
    for (int mt = 0; mt < G4::MT; ++mt)
#pragma unroll
      for (int i = 0; i < 4; ++i) {
        float a = p1[mt][i], c = p2[mt][i];
#pragma unroll
        for (int o = 1; o <= 8; o <<= 1) { a += __shfl_xor(a, o); c += __shfl_xor(c, o); }
        if (fr == 0) {
          const int rowl = wm * 32 + mt * 16 + fg * 4 + i;
          red[(wn * 64 + rowl) * 2] = a; red[(wn * 64 + rowl) * 2 + 1] = c;
        }
      }
    barrier();
    if (tid < 128) {
      const float v = (red[tid] + red[128 + tid]) + (red[256 + tid] + red[384 + tid]);
      stats[((size_t)t4 * S + m0) * 2 + tid] = v;
    }
  }
}
__global__ __launch_bounds__(512, 2) void k_win(const u16* __restrict__ x1, const float* __restrict__ sb,
                                                const float* __restrict__ cb, const u16* __restrict__ win,
                                                char* __restrict__ z1f, char* __restrict__ z2f,
                                                float* __restrict__ stats) {
  __shared__ __attribute__((aligned(16))) char smem[65536 + 4 * G4::B_BYTES];
  const int b = blockIdx.x, xcd = b & 7, j = b >> 3;
  const int mtile = xcd * 4 + (j >> 3), ntile = j & 7;
  if (ntile >= 4) win_body<true>(smem, x1, sb, cb, win, z2f, stats, mtile, ntile - 4);
  else win_body<false>(smem, x1, sb, cb, win, z1f, stats, mtile, ntile);
}

constexpr int UKS = 22;
template <int NT>
DI void spatial_body(char* smem, const u16* __restrict__ wsp, const char* __restrict__ z2f,
                     const float* __restrict__ stats, const u16* __restrict__ bsp, const char* __restrict__ z1f,
                     char* __restrict__ uf, int mtile, int nb0) {
  const int tid = threadIdx.x, wave = __builtin_amdgcn_readfirstlane(tid >> 6), lane = tid & 63;
  const int fr = lane & 15, fg = lane >> 4;
  char* ring = smem + wave * SK_AREA;
  float* rs = reinterpret_cast<float*>(smem + 8 * SK_AREA + wave * 2048);
  float* ms = rs + 256;
  const int m0 = mtile * 64, kb = wave * 256;
  f32x4 sp[4][2];
#pragma unroll
  for (int t = 0; t < 4; ++t) {
    const float* p = stats + ((size_t)t * S + kb + lane * 4) * 2;
    sp[t][0] = *reinterpret_cast<const f32x4*>(p);
    sp[t][1] = *reinterpret_cast<const f32x4*>(p + 4);
  }
  const int rr = lane >> 3, ch = (lane & 7) ^ rr;
  const u16* asrc = wsp + (size_t)(m0 + rr) * S + kb + ch * 8;
  auto stageA = [&](int s) {
#pragma unroll
    for (int i = 0; i < 8; ++i) glds16(asrc + (size_t)i * 8 * S + s * 64, ring + (s & 1) * 8192 + i * 1024);
  };
  const char* bsrc = z2f + ((size_t)(wave * 8) * NBLK + nb0) * 1024 + lane * 16;
  u32x4 breg[3][NT];
  auto loadB = [&](int t) {
#pragma unroll
    for (int nt = 0; nt < NT; ++nt) breg[t % 3][nt] = *reinterpret_cast<const u32x4*>(bsrc + (t * NBLK + nt) * 1024);
  };
  stageA(0); loadB(0); loadB(1); stageA(1); loadB(2);
  {
    f32x4 rv, mv;
#pragma unroll
    for (int i = 0; i < 4; ++i) {
      float s1 = 0.f, s2 = 0.f;
#pragma unroll
      for (int t = 0; t < 4; ++t) { s1 += sp[t][i >> 1][(i & 1) * 2]; s2 += sp[t][i >> 1][(i & 1) * 2 + 1]; }
      const float mean = s1 * (1.f / FH);
      const float var = fmaxf(s2 * (1.f / FH) - mean * mean, 0.f);
      const float rstd = rsqrtf(var + 1e-5f);
      rv[i] = rstd; mv[i] = -mean * rstd;
    }
    *reinterpret_cast<f32x4*>(rs + lane * 4) = rv;
    *reinterpret_cast<f32x4*>(ms + lane * 4) = mv;
  }
  f32x4 acc[4][NT];
#pragma unroll
  for (int i = 0; i < 4; ++i)
#pragma unroll
    for (int jj = 0; jj < NT; ++jj) acc[i][jj] = f32x4{0.f, 0.f, 0.f, 0.f};
#pragma unroll
  for (int s = 0; s < 4; ++s) {
    if (s < 2) wait_vm<3 * NT + 8>(); else if (s == 2) wait_vm<2 * NT + 8>(); else wait_vm<NT>();
#pragma unroll
    for (int kk = 0; kk < 2; ++kk) {
      const int t = 2 * s + kk;
      bf16x8 af[4];
#pragma unroll
      for (int mt = 0; mt < 4; ++mt)
        af[mt] = *reinterpret_cast<const bf16x8*>(ring + (s & 1) * 8192 + (mt * 16 + fr) * 128 + (((kk * 4 + fg) ^ (fr & 7)) << 4));
      f32x4 rv[2], mv[2];
#pragma unroll
      for (int e = 0; e < 2; ++e) {
        rv[e] = *reinterpret_cast<const f32x4*>(rs + t * 32 + fg * 8 + e * 4);
        mv[e] = *reinterpret_cast<const f32x4*>(ms + t * 32 + fg * 8 + e * 4);
      }
#pragma unroll
      for (int nt = 0; nt < NT; ++nt) {
        const u32x4 raw = breg[t % 3][nt];
        u32x4 o4;
#pragma unroll
        for (int jj = 0; jj < 4; ++jj)
          o4[jj] = pack2(fmaf(bflo(raw[jj]), rv[jj >> 1][(jj & 1) * 2], mv[jj >> 1][(jj & 1) * 2]),
                         fmaf(bfhi(raw[jj]), rv[jj >> 1][(jj & 1) * 2 + 1], mv[jj >> 1][(jj & 1) * 2 + 1]));
        const bf16x8 bv = __builtin_bit_cast(bf16x8, o4);
#pragma unroll
        for (int mt = 0; mt < 4; ++mt)
          acc[mt][nt] = __builtin_amdgcn_mfma_f32_16x16x32_bf16(bv, af[mt], acc[mt][nt], 0, 0, 0);
      }
      if (t + 3 < 8) loadB(t + 3);
    }
    if (s + 2 < 4) {
      asm volatile("s_waitcnt lgkmcnt(0)" ::: "memory");
      stageA(s + 2);
    }
  }
  constexpr int NO = SkOwn<NT>::N;
  const int hi = wave >> 2, wq = wave & 3;
  u32x2 zv[NO];
  float bs[NO];
#pragma unroll
  for (int i = 0; i < NO; ++i) {
    int f = wq + 4 * i;
    if (f >= 2 * NT) f = 0;
    const int mt = 2 * hi + f / NT, nb = nb0 + f % NT;
    zv[i] = *reinterpret_cast<const u32x2*>(z1f + ((size_t)(mtile * 4 + mt) * NBLK + nb) * 512 + lane * 8);
    bs[i] = bf2f(bsp[m0 + mt * 16 + fr]);
  }
  f32x4 own[NO];
  splitk_reduce<NT>(acc, own, smem, wave, lane);
#pragma unroll
  for (int i = 0; i < NO; ++i) {
    const int f = wq + 4 * i;
    if (f < 2 * NT) {
      const int mt = 2 * hi + f / NT, nb = nb0 + f % NT;
      u32x2 w;
      w[0] = pack2(bflo(zv[i][0]) * (own[i][0] + bs[i]), bfhi(zv[i][0]) * (own[i][1] + bs[i]));
      w[1] = pack2(bflo(zv[i][1]) * (own[i][2] + bs[i]), bfhi(zv[i][1]) * (own[i][3] + bs[i]));
      char* dst = uf + ((size_t)(mtile * 4 + mt) * UKS + (nb >> 1)) * 1024 + (fr + 16 * (fg >> 1)) * 16 + (fg & 1) * 8;
      *reinterpret_cast<u32x2*>(dst + (nb & 1) * 512) = w;
      if (nb == NBLK - 1) *reinterpret_cast<u32x2*>(dst + 512) = u32x2{0u, 0u};
    }
  }
}
__global__ __launch_bounds__(512, 2) void k_spatial(const u16* __restrict__ wsp, const char* __restrict__ z2f,
                                                    const float* __restrict__ stats, const u16* __restrict__ bsp,
                                                    const char* __restrict__ z1f, char* __restrict__ uf) {
  __shared__ __attribute__((aligned(16))) char smem[8 * SK_AREA + 8 * 2048];
  const int b = blockIdx.x, xcd = b & 7, j = b >> 3;
  const int mtile = xcd * 4 + (j >> 3), ntile = j & 7;
  if (ntile < 5) spatial_body<5>(smem, wsp, z2f, stats, bsp, z1f, uf, mtile, ntile * 5);
  else spatial_body<6>(smem, wsp, z2f, stats, bsp, z1f, uf, mtile, 25 + (ntile - 5) * 6);
}

__global__ __launch_bounds__(512, 2) void k_wout(const char* __restrict__ uf, const u16* __restrict__ wout,
                                                 const u16* __restrict__ x1, const float* __restrict__ gb,
                                                 u16* __restrict__ out) {
  __shared__ __attribute__((aligned(16))) char smem[8 * SK_AREA];
  const int b = blockIdx.x, xcd = b & 7, j = b >> 3;
  const int mtile = xcd * 4 + (j >> 3), ntile = j & 7;
  const int tid = threadIdx.x, wave = __builtin_amdgcn_readfirstlane(tid >> 6), lane = tid & 63;
  const int m0 = mtile * 64, n0 = ntile * 64;
  char* ring = smem + wave * SK_AREA;
  const int ks0 = wave < 6 ? wave * 3 : 18 + (wave - 6) * 2, nks = wave < 6 ? 3 : 2;
  const int rw = (lane & 15) >> 1;
  u32x4 areg[3][4];
#pragma unroll
  for (int t = 0; t < 3; ++t) {
    const int ks = ks0 + (t < nks ? t : 0);
#pragma unroll
    for (int i = 0; i < 4; ++i) {
      int row = ks * 32 + i * 8 + ((i & 1) ? (rw ^ 4) : rw);
      if (row > FH - 1) row = FH - 1;
      glds16(wout + (size_t)row * DM + n0 + (lane >> 4) * 16 + (lane & 1) * 8, ring + t * 4096 + i * 1024);
    }
#pragma unroll
    for (int mt = 0; mt < 4; ++mt)
      areg[t][mt] = *reinterpret_cast<const u32x4*>(uf + ((size_t)(mtile * 4 + mt) * UKS + ks) * 1024 + lane * 16);
  }
  const int fr = lane & 15, fg = lane >> 4, hi = wave >> 2, wq = wave & 3;
  u32x2 xv[2];
  const f32x4 gv = *reinterpret_cast<const f32x4*>(gb + n0 + wq * 16 + fg * 4);
#pragma unroll
  for (int i = 0; i < 2; ++i)
    xv[i] = *reinterpret_cast<const u32x2*>(x1 + (size_t)(m0 + (2 * hi + i) * 16 + fr) * DM + n0 + wq * 16 + fg * 4);
  f32x4 acc[4][4];
#pragma unroll
  for (int i = 0; i < 4; ++i)
#pragma unroll
    for (int jj = 0; jj < 4; ++jj) acc[i][jj] = f32x4{0.f, 0.f, 0.f, 0.f};
  wait_vm<0>();
#pragma unroll
  for (int t = 0; t < 3; ++t) {
    if (t < nks) {
      bf16x8 bfr[4];
      sk_bfrags(ring + t * 4096, bfr, lane);
#pragma unroll
      for (int mt = 0; mt < 4; ++mt)
#pragma unroll
        for (int nt = 0; nt < 4; ++nt)
          acc[mt][nt] = __builtin_amdgcn_mfma_f32_16x16x32_bf16(bfr[nt], __builtin_bit_cast(bf16x8, areg[t][mt]), acc[mt][nt], 0, 0, 0);
    }
  }
  f32x4 own[2];
  splitk_reduce<4>(acc, own, smem, wave, lane);
#pragma unroll
  for (int i = 0; i < 2; ++i) {
    u32x2 w;
    w[0] = pack2(bflo(xv[i][0]) + gv[0] * own[i][0], bfhi(xv[i][0]) + gv[1] * own[i][1]);
    w[1] = pack2(bflo(xv[i][1]) + gv[2] * own[i][2], bfhi(xv[i][1]) + gv[3] * own[i][3]);
    *reinterpret_cast<u32x2*>(out + (size_t)(m0 + (2 * hi + i) * 16 + fr) * DM + n0 + wq * 16 + fg * 4) = w;
  }
}

extern "C" void kernel_launch(void* const* d_in, const int* in_sizes, int n_in, void* d_out, int out_size,
                              void* d_ws, size_t ws_size, hipStream_t stream) {
  const u16* x = (const u16*)d_in[0];
  const u16* ckv = (const u16*)d_in[1];
  const u16* wq = (const u16*)d_in[2];
  const u16* wk = (const u16*)d_in[3];
  const u16* wv = (const u16*)d_in[4];
  const u16* wo = (const u16*)d_in[5];
  const u16* win = (const u16*)d_in[7];
  const u16* wsp = (const u16*)d_in[8];
  const u16* bsp = (const u16*)d_in[9];
  const u16* wout = (const u16*)d_in[10];
  const u16* c = (const u16*)d_in[11];
  const u16* wmod = (const u16*)d_in[12];
  char* ws = (char*)d_ws;
  float* mod = (float*)(ws + WS_MOD);
  u16* h1 = (u16*)(ws + WS_H1);
  char* Qf = ws + WS_QF;
  char* Ki = ws + WS_KI;
  char* Vi = ws + WS_VI;
  char* ctx = ws + WS_CTX;
  u16* x1 = (u16*)(ws + WS_X1);
  char* z1 = ws + WS_Z1;
  char* z2 = ws + WS_Z2;
  float* stats = (float*)(ws + WS_ST);
  char* u = ws + WS_U;
  u16* out = (u16*)d_out;

  k_kv<<<256, 512, 0, stream>>>(ckv, wk, wv, Ki, Vi, c, wmod, mod);
  k_ln<<<S / 4, 256, 0, stream>>>(x, mod + 0 * DM, mod + 1 * DM, h1);
  k_qkv<<<512, 256, 0, stream>>>(h1, ckv, wq, wk, wv, Qf, Ki, Vi);
  k_attn<<<256, 512, 0, stream>>>(Qf, Ki, Vi, ctx);
  k_oproj<<<256, 512, 0, stream>>>(ctx, wo, x, mod + 2 * DM, x1);
  k_win<<<256, 512, 0, stream>>>(x1, mod + 3 * DM, mod + 4 * DM, win, z1, z2, stats);
  k_spatial<<<256, 512, 0, stream>>>(wsp, z2, stats, bsp, z1, u);
  k_wout<<<256, 512, 0, stream>>>(u, wout, x1, mod + 5 * DM, out);
}
```

```cpp
#include <hip/hip_runtime.h>
#include <hip/hip_bf16.h>
#include <stdint.h>

constexpr int S = 2048;
constexpr int DM = 512;
constexpr int NH = 32;
constexpr int HD = 128;
constexpr int HDM = NH * HD;
constexpr int FH = 688;
constexpr int FF = 1376;

typedef __attribute__((ext_vector_type(8))) short bf16x8;
typedef __attribute__((ext_vector_type(4))) short s16x4;
typedef __attribute__((ext_vector_type(4))) float f32x4;
typedef __attribute__((ext_vector_type(16))) float f32x16;
typedef __attribute__((ext_vector_type(4))) unsigned u32x4;
typedef __attribute__((ext_vector_type(2))) unsigned u32x2;
typedef __attribute__((address_space(3))) s16x4* lds_s16x4_ptr;
typedef unsigned short u16;

#define DI __device__ __forceinline__

DI float bf2f(u16 v) { return __uint_as_float(((unsigned)v) << 16); }
DI float bflo(unsigned v) { return __uint_as_float(v << 16); }
DI float bfhi(unsigned v) { return __uint_as_float(v & 0xffff0000u); }
typedef __attribute__((ext_vector_type(2))) __bf16 bf16x2_t;
typedef __attribute__((ext_vector_type(2))) float f32x2;
DI unsigned pack2(float lo, float hi) {
  f32x2 v = {lo, hi};
  return __builtin_bit_cast(unsigned, __builtin_convertvector(v, bf16x2_t));
}
template <int N> DI void wait_vm() { asm volatile("s_waitcnt vmcnt(%0)" ::"n"(N) : "memory"); }
DI void barrier() { asm volatile("s_waitcnt lgkmcnt(0)" ::: "memory"); __builtin_amdgcn_s_barrier(); }
DI float rbf(float x) { return bflo(pack2(x, 0.f)); }

DI void glds16(const void* g, const void* lds_wave_base) {
  const unsigned a = __builtin_amdgcn_readfirstlane((unsigned)(size_t)lds_wave_base);
  unsigned keep;
  asm volatile("s_mov_b32 %0, m0\n\ts_mov_b32 m0, %2\n\ts_nop 0\n\tglobal_load_lds_dwordx4 %1, off\n\ts_mov_b32 m0, %0"
               : "=&s"(keep) : "v"(g), "s"(a) : "memory");
}
DI void glds16s(const void* sbase, unsigned voff, const void* lds_wave_base) {
  const unsigned a = __builtin_amdgcn_readfirstlane((unsigned)(size_t)lds_wave_base);
  unsigned keep;
  asm volatile("s_mov_b32 %0, m0\n\ts_mov_b32 m0, %3\n\ts_nop 0\n\tglobal_load_lds_dwordx4 %1, %2\n\ts_mov_b32 m0, %0"
               : "=&s"(keep) : "v"(voff), "s"(sbase), "s"(a) : "memory");
}
DI void pf_line(const void* g, const void* lds_dummy) {
  const unsigned a = __builtin_amdgcn_readfirstlane((unsigned)(size_t)lds_dummy);
  unsigned keep;
  asm volatile("s_mov_b32 %0, m0\n\ts_mov_b32 m0, %2\n\ts_nop 0\n\tglobal_load_lds_dword %1, off\n\ts_mov_b32 m0, %0"
               : "=&s"(keep) : "v"(g), "s"(a) : "memory");
}
DI void pf_slice(const void* base, int nbytes, int first, int lane, const void* lds_dummy) {
  int l = first + lane;
  const int last = (nbytes >> 7) - 1;
  if (l > last) l = last;
  pf_line((const char*)base + (size_t)l * 128, lds_dummy);
}
#define STAMP(k, i)
template <int CTRL> DI float dpp_add(float v) {
  return v + __uint_as_float(__builtin_amdgcn_update_dpp(0u, __float_as_uint(v), CTRL, 0xf, 0xf, true));
}
DI float row16_sum(float v) {
  v = dpp_add<0xB1>(v); v = dpp_add<0x4E>(v); v = dpp_add<0x141>(v); v = dpp_add<0x140>(v);
  return v;
}
DI float wave_sum(float v) {
  v = row16_sum(v);
  auto a = __builtin_amdgcn_permlane16_swap(__float_as_uint(v), __float_as_uint(v), false, false);
  v = __uint_as_float(a[0]) + __uint_as_float(a[1]);
  auto b = __builtin_amdgcn_permlane32_swap(__float_as_uint(v), __float_as_uint(v), false, false);
  return __uint_as_float(b[0]) + __uint_as_float(b[1]);
}
DI s16x4 tr_read(const char* p) {
  return __builtin_amdgcn_ds_read_tr16_b64_v4i16((lds_s16x4_ptr)(p));
}

constexpr size_t WS_MOD = 0;
constexpr size_t WS_H1 = 16384;
constexpr size_t WS_QF = WS_H1 + (size_t)S * DM * 2;
constexpr size_t WS_KI = WS_QF + (size_t)S * HDM * 2;
constexpr size_t WS_VI = WS_KI + (size_t)S * HDM * 2;
constexpr size_t WS_CTX = WS_VI + (size_t)S * HDM * 2;
constexpr size_t WS_X1 = WS_CTX + (size_t)S * HDM * 2;
constexpr size_t WS_Z1 = WS_X1 + (size_t)S * DM * 2;
constexpr size_t WS_Z2 = WS_Z1 + (size_t)S * FH * 2;
constexpr size_t WS_ST = WS_Z2 + (size_t)S * FH * 2;
constexpr size_t WS_U = WS_ST + (size_t)8 * S * 2 * 4;
constexpr int LDU = 704;

__global__ __launch_bounds__(256) void k_mod(const u16* __restrict__ c, const u16* __restrict__ w,
                                             float* __restrict__ mod) {
  __shared__ float red[64][33];
  const int t = threadIdx.x, cg = t & 3, ks = t >> 2;
  const int col0 = blockIdx.x * 32 + cg * 8;
  float acc[8];
#pragma unroll
  for (int j = 0; j < 8; ++j) acc[j] = 0.f;
#pragma unroll
  for (int kk = 0; kk < 8; ++kk) {
    const int k = ks * 8 + kk;
    float cv = bf2f(c[k]);
    float sv = rbf(cv / (1.f + __expf(-cv)));
    u32x4 wv = *reinterpret_cast<const u32x4*>(w + (size_t)k * 3072 + col0);
#pragma unroll
    for (int j = 0; j < 4; ++j) {
      acc[2 * j] += sv * bflo(wv[j]);
      acc[2 * j + 1] += sv * bfhi(wv[j]);
    }
  }
#pragma unroll
  for (int j = 0; j < 8; ++j) red[ks][cg * 8 + j] = acc[j];
  __syncthreads();
  if (t < 32) {
    float s = 0.f;
    for (int i = 0; i < 64; ++i) s += red[i][t];
    mod[blockIdx.x * 32 + t] = rbf(s);
  }
}

__global__ __launch_bounds__(256) void k_ln(const u16* __restrict__ x, const float* __restrict__ sh,
                                            const float* __restrict__ cs, u16* __restrict__ h) {
  const int row = blockIdx.x * 4 + (threadIdx.x >> 6), lane = threadIdx.x & 63;
  u32x4 xv = *reinterpret_cast<const u32x4*>(x + (size_t)row * DM + lane * 8);
  float f[8];
#pragma unroll
  for (int j = 0; j < 4; ++j) { f[2 * j] = bflo(xv[j]); f[2 * j + 1] = bfhi(xv[j]); }
  float s = 0.f;
#pragma unroll
  for (int j = 0; j < 8; ++j) s += f[j];
#pragma unroll
  for (int o = 32; o >= 1; o >>= 1) s += __shfl_xor(s, o);
  const float mean = s * (1.f / DM);
  float v = 0.f;
#pragma unroll
  for (int j = 0; j < 8; ++j) { f[j] -= mean; v += f[j] * f[j]; }
#pragma unroll
  for (int o = 32; o >= 1; o >>= 1) v += __shfl_xor(v, o);
  const float rstd = rsqrtf(v * (1.f / DM) + 1e-5f);
  u32x4 o4;
#pragma unroll
  for (int j = 0; j < 4; ++j) {
    const int k = lane * 8 + 2 * j;
    float a = f[2 * j] * rstd * (1.f + cs[k]) + sh[k];
    float b = f[2 * j + 1] * rstd * (1.f + cs[k + 1]) + sh[k + 1];
    o4[j] = pack2(a, b);
  }
  *reinterpret_cast<u32x4*>(h + (size_t)row * DM + lane * 8) = o4;
}

template <int BM, int BN, int KS, int NWM, int NWN>
struct Gemm {
  static constexpr int NW = NWM * NWN;
  static constexpr int WM = BM / NWM, WN = BN / NWN;
  static constexpr int MT = WM / 16, NT = WN / 16;
  static constexpr int BK = 64 * KS;
  static constexpr int A_BYTES = BM * BK * 2;
  static constexpr int B_BYTES = BK * BN * 2;
  static constexpr int STAGE_BYTES = A_BYTES + B_BYTES;
  static constexpr int NB = BN / 16;

  static DI void stageA(const u16* __restrict__ A, int lda, int m0, int k0, char* lds, int wave, int lane) {
    constexpr int NI = A_BYTES / 1024;
    const int rr = lane >> 3, ch = (lane & 7) ^ rr;
#pragma unroll
    for (int ii = 0; ii < NI / NW; ++ii) {
      const int i = ii * NW + wave;
      const int sub = i / (BM / 8), j = i % (BM / 8);
      const u16* src = A + (size_t)(m0 + j * 8 + rr) * lda + k0 + sub * 64 + ch * 8;
      glds16(src, lds + i * 1024);
    }
  }
  static DI void stageB(const u16* __restrict__ B, int ldb, int k0, int n0, int nmax, char* lds, int wave, int lane) {
    constexpr int NI = B_BYTES / 1024;
    const int pos = lane & 15, rw = pos >> 1, hf = pos & 1;
#pragma unroll
    for (int ii = 0; ii < NI / NW; ++ii) {
      const int i = ii * NW + wave;
      const int blk = i * 4 + (lane >> 4);
      const int kb = blk / NB, nb = blk % NB;
      const int row = rw ^ ((kb & 1) << 2);
      int n = n0 + nb * 16 + hf * 8;
      if (n > nmax) n = nmax;
      const u16* src = B + (size_t)(k0 + kb * 8 + row) * ldb + n;
      glds16(src, lds + i * 1024);
    }
  }
  template <bool SWAP>
  static DI void compute(const char* ldsA, const char* ldsB, f32x4 (&acc)[MT][NT], int wm, int wn, int lane) {
    const int fr = lane & 15, fg = lane >> 4;
    const int q = fr >> 2, p = lane & 3;
#pragma unroll
    for (int sub = 0; sub < KS; ++sub) {
#pragma unroll
      for (int kk = 0; kk < 2; ++kk) {
        bf16x8 af[MT], bfr[NT];
#pragma unroll
        for (int mt = 0; mt < MT; ++mt) {
          const int row = wm * WM + mt * 16 + fr;
          af[mt] = *reinterpret_cast<const bf16x8*>(ldsA + sub * (BM * 128) + row * 128 + (((kk * 4 + fg) ^ (row & 7)) << 4));
        }
#pragma unroll
        for (int nt = 0; nt < NT; ++nt) {
          const int kb = sub * 8 + kk * 4 + fg, nb = wn * NT + nt;
          const char* base = ldsB + (kb * NB + nb) * 256 + p * 8;
          const int sw = (fg & 1) << 2;
          s16x4 lo = tr_read(base + ((q) ^ sw) * 32);
          s16x4 hi = tr_read(base + ((4 + q) ^ sw) * 32);
          bfr[nt] = __builtin_shufflevector(lo, hi, 0, 1, 2, 3, 4, 5, 6, 7);
        }
#pragma unroll
        for (int mt = 0; mt < MT; ++mt)
#pragma unroll
          for (int nt = 0; nt < NT; ++nt) {
            if constexpr (SWAP)
              acc[mt][nt] = __builtin_amdgcn_mfma_f32_16x16x32_bf16(bfr[nt], af[mt], acc[mt][nt], 0, 0, 0);
            else
              acc[mt][nt] = __builtin_amdgcn_mfma_f32_16x16x32_bf16(af[mt], bfr[nt], acc[mt][nt], 0, 0, 0);
          }
      }
    }
  }
};

using G1 = Gemm<128, 128, 1, 2, 2>;

__global__ __launch_bounds__(256, 2) void k_qkv(const u16* __restrict__ h1, const u16* __restrict__ ckv,
                                                const u16* __restrict__ wq, const u16* __restrict__ wk,
                                                const u16* __restrict__ wv, char* __restrict__ Qf,
                                                char* __restrict__ Ki, char* __restrict__ Vi) {
  __shared__ __attribute__((aligned(16))) char smem[2 * G1::STAGE_BYTES];
  const int b = blockIdx.x, xcd = b & 7, j = b >> 3;
  const int head = xcd * 4 + (j & 3), rest = j >> 2, which = rest >> 4, mtile = rest & 15;
  const int tid = threadIdx.x, wave = __builtin_amdgcn_readfirstlane(tid >> 6), lane = tid & 63;
  const int wm = wave >> 1, wn = wave & 1;
  const u16* A = which == 0 ? h1 : ckv;
  const u16* B = which == 0 ? wq : (which == 1 ? wk : wv);
  const int m0 = mtile * 128, n0 = head * 128;
  f32x4 acc[G1::MT][G1::NT];
#pragma unroll
  for (int i = 0; i < G1::MT; ++i)
#pragma unroll
    for (int jj = 0; jj < G1::NT; ++jj) acc[i][jj] = f32x4{0.f, 0.f, 0.f, 0.f};
  constexpr int NK = DM / G1::BK;
  G1::stageA(A, DM, m0, 0, smem, wave, lane);
  G1::stageB(B, HDM, 0, n0, HDM, smem + G1::A_BYTES, wave, lane);
  for (int kt = 0; kt < NK; ++kt) {
    wait_vm<0>(); __syncthreads();
    char* cur = smem + (kt & 1) * G1::STAGE_BYTES;
    char* nxt = smem + ((kt + 1) & 1) * G1::STAGE_BYTES;
    if (kt + 1 < NK) {
      G1::stageA(A, DM, m0, (kt + 1) * G1::BK, nxt, wave, lane);
      G1::stageB(B, HDM, (kt + 1) * G1::BK, n0, HDM, nxt + G1::A_BYTES, wave, lane);
    }
    if (which == 2) G1::compute<false>(cur, cur + G1::A_BYTES, acc, wm, wn, lane);
    else G1::compute<true>(cur, cur + G1::A_BYTES, acc, wm, wn, lane);
  }
  __syncthreads();
  const int fr = lane & 15, fg = lane >> 4;
#pragma unroll
  for (int mt = 0; mt < G1::MT; ++mt)
#pragma unroll
    for (int nt = 0; nt < G1::NT; ++nt) {
      u32x2 v;
      v[0] = pack2(acc[mt][nt][0], acc[mt][nt][1]);
      v[1] = pack2(acc[mt][nt][2], acc[mt][nt][3]);
      int off;
      if (which == 2) {
        const int d = wn * 64 + nt * 16 + fr, m = wm * 64 + mt * 16 + fg * 4;
        const int tile = m >> 6, key = m & 63;
        const int pos = (key & 0x33) | ((key & 4) << 1) | ((key & 8) >> 1);
        off = tile * 16384 + d * 128 + (((pos >> 3) ^ ((d >> 1) & 7)) << 4) + (pos & 7) * 2;
      } else {
        const int m = wm * 64 + mt * 16 + fr, d = wn * 64 + nt * 16 + fg * 4;
        if (which == 1) {
          const int tile = m >> 6, key = m & 63;
          off = tile * 16384 + key * 256 + (((d >> 3) ^ (key & 15)) << 4) + (d & 7) * 2;
        } else {
          const int rg = m >> 5, r = m & 31;
          off = rg * 8192 + (d >> 4) * 1024 + ((((d >> 3) & 1) * 32 + r) << 4) + (d & 7) * 2;
        }
      }
      *reinterpret_cast<u32x2*>(smem + off) = v;
    }
  __syncthreads();
  char* dst = (which == 0 ? Qf : (which == 1 ? Ki : Vi)) + ((size_t)head * 16 + mtile) * 32768;
#pragma unroll
  for (int i = 0; i < 8; ++i) {
    const int o = (i * 256 + tid) * 16;
    *reinterpret_cast<u32x4*>(dst + o) = *reinterpret_cast<const u32x4*>(smem + o);
  }
}

#define KV_PD 2
constexpr int KV_NS = 4, KV_STAGE = 32768;
template <bool isV>
DI void kv_body(char* smem, const u16* __restrict__ ckv, const u16* __restrict__ B, char* __restrict__ dstb,
                const u16* __restrict__ cvec, const u16* __restrict__ wmod, float* __restrict__ mod) {
  const int b = blockIdx.x, xcd = b & 7, j = b >> 3;
  const int ntile = xcd * 4 + (j & 3), mtile = j >> 2;
  const int tid = threadIdx.x, wave = __builtin_amdgcn_readfirstlane(tid >> 6), lane = tid & 63;
  const int wm = wave >> 2, wn = wave & 3, r = lane & 31, h = lane >> 5;
  const int m0 = mtile * 256, n0 = (ntile & 15) * 256;
  float* modred = reinterpret_cast<float*>(smem + KV_NS * KV_STAGE);
  STAMP(0, 0);

  const u16* asrc[2];
  const u16* bsrc[2];
#pragma unroll
  for (int ii = 0; ii < 2; ++ii) {
    const int i = ii * 8 + wave;
    const int row = 16 * i + (lane >> 2), ch = (lane & 3) ^ ((lane >> 4) & 3);
    asrc[ii] = ckv + (size_t)(m0 + row) * DM + ch * 8;
    const int blk = i * 4 + (lane >> 4), kb = blk >> 4, nb = blk & 15;
    const int pos = lane & 15, rw = (pos >> 1) ^ ((nb & 1) << 2), hf = pos & 1;
    bsrc[ii] = B + (size_t)(kb * 8 + rw) * HDM + n0 + nb * 16 + hf * 8;
  }
  auto stage = [&](int kt0, int slot) {
    char* dst = smem + slot * KV_STAGE;
    const int kt = (kt0 + 2 * mtile) & 15;
#pragma unroll
    for (int ii = 0; ii < 2; ++ii) {
      glds16(asrc[ii] + kt * 32, dst + (ii * 8 + wave) * 1024);
      glds16(bsrc[ii] + (size_t)kt * 32 * HDM, dst + 16384 + (ii * 8 + wave) * 1024);
    }
  };
  constexpr int NK = DM / 32;
#pragma unroll
  for (int st = 0; st < KV_PD; ++st) stage(st, st);

  if (b < 192) {
    const u16* p = wmod + (size_t)tid * 3072 + b * 16;
    const u32x4 w0 = *reinterpret_cast<const u32x4*>(p), w1 = *reinterpret_cast<const u32x4*>(p + 8);
    const float c = bf2f(cvec[tid]);
    const float sv = rbf(c / (1.f + __expf(-c)));
    float v[16];
#pragma unroll
    for (int jj = 0; jj < 4; ++jj) {
      v[2 * jj] = sv * bflo(w0[jj]); v[2 * jj + 1] = sv * bfhi(w0[jj]);
      v[8 + 2 * jj] = sv * bflo(w1[jj]); v[8 + 2 * jj + 1] = sv * bfhi(w1[jj]);
    }
#pragma unroll
    for (int st = 0; st < 4; ++st) {
      const int half = 8 >> st, m = 32 >> st;
      const bool up = lane & m;
#pragma unroll
      for (int e = 0; e < half; ++e) {
        const float send = up ? v[e] : v[half + e];
        const float keep = up ? v[half + e] : v[e];
        v[e] = keep + __shfl_xor(send, m);
      }
    }
    v[0] += __shfl_xor(v[0], 2);
    v[0] += __shfl_xor(v[0], 1);
    if ((lane & 3) == 0) {
      const int col = ((lane >> 5) & 1) * 8 + ((lane >> 4) & 1) * 4 + ((lane >> 3) & 1) * 2 + ((lane >> 2) & 1);
      modred[wave * 16 + col] = v[0];
    }
  }

  STAMP(0, 1);
  f32x16 acc[4][2];
#pragma unroll
  for (int i = 0; i < 4; ++i)
#pragma unroll
    for (int jj = 0; jj < 2; ++jj)
#pragma unroll
      for (int e = 0; e < 16; ++e) acc[i][jj][e] = 0.f;
  const int G = lane >> 4, q4 = (lane & 15) >> 2, p4 = lane & 3;
  int aoff[2];
#pragma unroll
  for (int u = 0; u < 2; ++u) aoff[u] = (wm * 128 + r) * 64 + (((2 * u + h) ^ ((r >> 2) & 3)) << 4);
  const int nh = G & 1;
  const int boff = 16384 + (h * 16 + wn * 4 + nh) * 256 + p4 * 8;
  const int brow0 = (q4 ^ (nh << 2)) * 32, brow1 = ((4 + q4) ^ (nh << 2)) * 32;
  for (int kt = 0; kt < NK; ++kt) {
    if (kt + KV_PD - 1 <= NK - 1) wait_vm<(KV_PD - 1) * 4>(); else wait_vm<0>();
    barrier();
    const bool pf = kt + KV_PD < NK;
    const int ktn = (kt + KV_PD + 2 * mtile) & 15;
    char* dst = smem + ((kt + KV_PD) & (KV_NS - 1)) * KV_STAGE;
    const char* cur = smem + (kt & (KV_NS - 1)) * KV_STAGE;
    bf16x8 af[2][4], bfr[2][2];
#pragma unroll
    for (int u = 0; u < 2; ++u) {
#pragma unroll
      for (int nt = 0; nt < 2; ++nt) {
        const char* bb = cur + boff + u * (2 * 16 * 256) + nt * 512;
        s16x4 lo = tr_read(bb + brow0), hi = tr_read(bb + brow1);
        bfr[u][nt] = __builtin_shufflevector(lo, hi, 0, 1, 2, 3, 4, 5, 6, 7);
      }
#pragma unroll
      for (int mt = 0; mt < 4; ++mt) af[u][mt] = *reinterpret_cast<const bf16x8*>(cur + aoff[u] + mt * 2048);
    }
#pragma unroll
    for (int g4 = 0; g4 < 4; ++g4) {
      const int u = g4 >> 1;
      __builtin_amdgcn_sched_barrier(0);
      if (pf) {
        const int ii = g4 >> 1;
        if (g4 & 1) glds16(bsrc[ii] + (size_t)ktn * 32 * HDM, dst + 16384 + (ii * 8 + wave) * 1024);
        else glds16(asrc[ii] + ktn * 32, dst + (ii * 8 + wave) * 1024);
      }
      __builtin_amdgcn_sched_barrier(0);
#pragma unroll
      for (int m2 = 0; m2 < 2; ++m2)
#pragma unroll
        for (int nt = 0; nt < 2; ++nt) {
          const int mt = (g4 & 1) * 2 + m2;
          if constexpr (isV)
            acc[mt][nt] = __builtin_amdgcn_mfma_f32_32x32x16_bf16(af[u][mt], bfr[u][nt], acc[mt][nt], 0, 0, 0);
          else
            acc[mt][nt] = __builtin_amdgcn_mfma_f32_32x32x16_bf16(bfr[u][nt], af[u][mt], acc[mt][nt], 0, 0, 0);
        }
    }
  }
  barrier();
  STAMP(0, 2);
#pragma unroll
  for (int mt = 0; mt < 4; ++mt)
#pragma unroll
    for (int nt = 0; nt < 2; ++nt)
#pragma unroll
      for (int qd = 0; qd < 4; ++qd) {
        u32x2 v;
        v[0] = pack2(acc[mt][nt][4 * qd], acc[mt][nt][4 * qd + 1]);
        v[1] = pack2(acc[mt][nt][4 * qd + 2], acc[mt][nt][4 * qd + 3]);
        int off;
        if constexpr (isV) {
          const int n = wn * 64 + nt * 32 + r, m = wm * 128 + mt * 32 + 8 * qd + 4 * h;
          const int hd = n >> 7, d = n & 127, tile = m >> 6, key = m & 63;
          const int pos = (key & 0x33) | ((key & 4) << 1) | ((key & 8) >> 1);
          off = (hd * 4 + tile) * 16384 + d * 128 + (((pos >> 3) ^ ((d >> 1) & 7)) << 4) + (pos & 7) * 2;
        } else {
          const int m = wm * 128 + mt * 32 + r, n = wn * 64 + nt * 32 + 8 * qd + 4 * h;
          const int hd = n >> 7, d = n & 127, tile = m >> 6, key = m & 63;
          off = (hd * 4 + tile) * 16384 + key * 256 + (((d >> 3) ^ (key & 15)) << 4) + (d & 7) * 2;
        }
        *reinterpret_cast<u32x2*>(smem + off) = v;
      }
  barrier();
  {
    const int head0 = (ntile & 15) * 2;
#pragma unroll
    for (int i = 0; i < 16; ++i) {
      const int o = (i * 512 + tid) * 16;
      const int hd = o >> 16, rem = o & 65535;
      *reinterpret_cast<u32x4*>(dstb + ((size_t)(head0 + hd) * 32 + mtile * 4) * 16384 + rem) =
          *reinterpret_cast<const u32x4*>(smem + o);
    }
  }
  STAMP(0, 3);
  if (b < 192 && tid < 16) {
    float sum = 0.f;
#pragma unroll
    for (int i = 0; i < 8; ++i) sum += modred[i * 16 + tid];
    mod[b * 16 + tid] = rbf(sum);
  }
}
__global__ __launch_bounds__(512, 2) void k_kv(const u16* __restrict__ ckv, const u16* __restrict__ wk,
                                               const u16* __restrict__ wv, char* __restrict__ Ki,
                                               char* __restrict__ Vi, const u16* __restrict__ cvec,
                                               const u16* __restrict__ wmod, float* __restrict__ mod) {
  __shared__ __attribute__((aligned(16))) char smem[KV_NS * KV_STAGE + 512];
  if ((blockIdx.x & 7) >= 4) kv_body<true>(smem, ckv, wv, Vi, cvec, wmod, mod);
  else kv_body<false>(smem, ckv, wk, Ki, cvec, wmod, mod);
}

constexpr int AT_V0 = 65536, AT_V1 = 98304, AT_DUMMY = 147456;
constexpr float AT_C = 0.08838834764831845f * 1.4426950408889634f;
struct AtLoad {
  const char* ksrc; const char* vsrc; char* kdst; char* v0dst; char* v1dst; unsigned voff; bool on;
  DI void piece(int i) const {
    if (!on) return;
    if (i < 4) glds16s(ksrc + i * 1024, voff, kdst + i * 1024);
    else if (i < 6) glds16s(vsrc + (i - 4) * 1024, voff, v0dst + (i - 4) * 1024);
    else glds16s(vsrc + 16384 + (i - 6) * 1024, voff, v1dst + (i - 6) * 1024);
  }
};
template <int P0>
DI void at_qk(const char* Kt, const bf16x8 (&q)[8], f32x16 (&st)[2], const AtLoad& ld, int r, int h) {
  const char* kb = Kt + r * 256;
  const int x = r & 15;
  bf16x8 fa[2][4];
#pragma unroll
  for (int i = 0; i < 4; ++i) fa[0][i] = *reinterpret_cast<const bf16x8*>(kb + (((2 * i + h) ^ x) << 4));
#pragma unroll
  for (int kt = 0; kt < 2; ++kt)
#pragma unroll
    for (int i = 0; i < 16; ++i) st[kt][i] = 0.f;
#pragma unroll
  for (int j = 0; j < 4; ++j) {
    if (j < 3) {
      const int jn = j + 1;
#pragma unroll
      for (int i = 0; i < 4; ++i)
        fa[jn & 1][i] = *reinterpret_cast<const bf16x8*>(kb + (jn >> 1) * 8192 + (((2 * ((jn & 1) * 4 + i) + h) ^ x) << 4));
    }
    ld.piece(P0 + j);
    __builtin_amdgcn_sched_barrier(0);
#pragma unroll
    for (int i = 0; i < 4; ++i)
      st[j >> 1] = __builtin_amdgcn_mfma_f32_32x32x16_bf16(fa[j & 1][i], q[(j & 1) * 4 + i], st[j >> 1], 0, 0, 0);
    __builtin_amdgcn_sched_barrier(0);
  }
}
template <int P0>
DI void at_pv(const char* Vt, const bf16x8 (&p)[4], f32x16 (&o)[4], const AtLoad& ld, int r, int h) {
  const char* vb = Vt + r * 128;
  const int x = (r >> 1) & 7;
  bf16x8 fa[2][4];
#pragma unroll
  for (int i = 0; i < 4; ++i) fa[0][i] = *reinterpret_cast<const bf16x8*>(vb + (((2 * i + h) ^ x) << 4));
#pragma unroll
  for (int j = 0; j < 4; ++j) {
    if (j < 3) {
#pragma unroll
      for (int i = 0; i < 4; ++i)
        fa[(j + 1) & 1][i] = *reinterpret_cast<const bf16x8*>(vb + (j + 1) * 4096 + (((2 * i + h) ^ x) << 4));
    }
    ld.piece(P0 + j);
    __builtin_amdgcn_sched_barrier(0);
#pragma unroll
    for (int i = 0; i < 4; ++i) o[j] = __builtin_amdgcn_mfma_f32_32x32x16_bf16(fa[j & 1][i], p[i], o[j], 0, 0, 0);
    __builtin_amdgcn_sched_barrier(0);
  }
}
DI void at_softmax(f32x16 (&st)[2], bf16x8 (&p)[4], f32x16 (&o)[4], float& m, float& l, bool diag, int lim) {
  if (diag) {
#pragma unroll
    for (int kt = 0; kt < 2; ++kt)
#pragma unroll
      for (int i = 0; i < 16; ++i) {
        const int kk = 32 * kt + (i & 3) + 8 * (i >> 2);
        if (kk > lim) st[kt][i] = -__builtin_inff();
      }
  }
  float pm = st[0][0];
#pragma unroll
  for (int i = 1; i < 16; ++i) pm = fmaxf(pm, st[0][i]);
#pragma unroll
  for (int i = 0; i < 16; ++i) pm = fmaxf(pm, st[1][i]);
  {
    auto rr = __builtin_amdgcn_permlane32_swap(__float_as_uint(pm), __float_as_uint(pm), false, false);
    pm = fmaxf(__uint_as_float(rr[0]), __uint_as_float(rr[1]));
  }
  if (__builtin_amdgcn_ballot_w64(pm > m) != 0) {
    const float mn = fmaxf(m, pm);
    const float alpha = __builtin_amdgcn_exp2f((m - mn) * AT_C);
    m = mn;
    l *= alpha;
#pragma unroll
    for (int dt = 0; dt < 4; ++dt)
#pragma unroll
      for (int i = 0; i < 16; ++i) o[dt][i] *= alpha;
  }
  const float mc = -m * AT_C;
  float ps = 0.f;
#pragma unroll
  for (int kt = 0; kt < 2; ++kt)
#pragma unroll
    for (int i = 0; i < 16; ++i) {
      const float e = __builtin_amdgcn_exp2f(fmaf(st[kt][i], AT_C, mc));
      st[kt][i] = e;
      ps += e;
    }
  l += ps;
#pragma unroll
  for (int ks = 0; ks < 4; ++ks) {
    u32x4 w;
#pragma unroll
    for (int jj = 0; jj < 4; ++jj)
      w[jj] = pack2(st[ks >> 1][8 * (ks & 1) + 2 * jj], st[ks >> 1][8 * (ks & 1) + 2 * jj + 1]);
    p[ks] = __builtin_bit_cast(bf16x8, w);
  }
}
template <int SG>
DI void at_loop(char* smem, const char* Kh, const char* Vh, const bf16x8 (&q)[8], f32x16 (&o)[4], bf16x8 (&p)[4],
                float& m, float& l, int n, int wave, int lane, int lim, bool pfw, const u16* wo, const u16* win,
                const u16* wsp, const u16* wout) {
  const int g = wave & 3, r = lane & 31, h = lane >> 5;
  int v1c = 0;
  for (int it = 0; it < n; ++it) {
    wait_vm<0>(); barrier();
    const int v1n = v1c == 2 ? 0 : v1c + 1, v1p = v1c == 0 ? 2 : v1c - 1;
    const size_t go = (size_t)(it + 1) * 32768;
    const AtLoad ld{Kh + go, Vh + go, smem + ((it + 1) & 1) * 32768 + wave * 4096,
                    smem + AT_V0 + ((it + 1) & 1) * 16384 + wave * 2048, smem + AT_V1 + v1n * 16384 + wave * 2048,
                    (unsigned)lane * 16u, it + 1 < n};
    if (pfw && it == 1) {
      const int slot = blockIdx.x * 8 + wave;
      if (slot < 1024) pf_slice(wsp, S * S * 2, slot * 64, lane, smem + AT_DUMMY);
      else if (slot < 1536) pf_slice(wo, HDM * DM * 2, (slot - 1024) * 64, lane, smem + AT_DUMMY);
      else if (slot < 1536 + 172) pf_slice(win, DM * FF * 2, (slot - 1536) * 64, lane, smem + AT_DUMMY);
      else if (slot < 1536 + 172 + 86) pf_slice(wout, FH * DM * 2, (slot - 1708) * 64, lane, smem + AT_DUMMY);
    }
    const bool last = (it == n - 1);
    const char* Kt = smem + (it & 1) * 32768 + SG * 16384;
    f32x16 st[2];
    if constexpr (SG == 0) {
      at_qk<0>(Kt, q, st, ld, r, h);
      at_softmax(st, p, o, m, l, last, lim);
      at_pv<4>(smem + AT_V0 + (it & 1) * 16384, p, o, ld, r, h);
    } else {
      if (it > 0) at_pv<0>(smem + AT_V1 + v1p * 16384, p, o, ld, r, h);
      else {
#pragma unroll
        for (int i = 0; i < 4; ++i) ld.piece(i);
      }
      if (!(last && g < 2)) {
        at_qk<4>(Kt, q, st, ld, r, h);
        at_softmax(st, p, o, m, l, last, lim);
      }
    }
    v1c = v1n;
  }
}
__global__ __launch_bounds__(512, 2) void k_attn(const char* __restrict__ Qf, const char* __restrict__ Ki,
                                                 const char* __restrict__ Vi, char* __restrict__ ctx,
                                                 const u16* __restrict__ wo, const u16* __restrict__ win,
                                                 const u16* __restrict__ wsp, const u16* __restrict__ wout) {
  __shared__ __attribute__((aligned(16))) char smem[AT_DUMMY + 256];
  const int b = blockIdx.x, xcd = b & 7, j = b >> 3;
  const int head = xcd * 4 + (j & 3), pair = j >> 2;
  const int tid = threadIdx.x, wave = __builtin_amdgcn_readfirstlane(tid >> 6), lane = tid & 63;
  const int s = wave >> 2, g = wave & 3, r = lane & 31, h = lane >> 5;
  const char* Kh = Ki + (size_t)head * (32 * 16384) + wave * 4096;
  const char* Vh = Vi + (size_t)head * (32 * 16384) + wave * 2048;
  const int lim = 32 * g + r - 64 * s - 4 * h;

  for (int qi = 0; qi < 2; ++qi) {
    const int qb = qi ? pair : 15 - pair;
    const int n = qb + 1;
    {
      AtLoad ld{Kh, Vh, smem + wave * 4096, smem + AT_V0 + wave * 2048, smem + AT_V1 + wave * 2048, (unsigned)lane * 16u, true};
#pragma unroll
      for (int i = 0; i < 8; ++i) ld.piece(i);
    }
    bf16x8 q[8];
    {
      const char* qp = Qf + ((size_t)head * 64 + qb * 4 + g) * 8192 + lane * 16;
#pragma unroll
      for (int ks = 0; ks < 8; ++ks) q[ks] = *reinterpret_cast<const bf16x8*>(qp + ks * 1024);
    }
    f32x16 o[4];
#pragma unroll
    for (int dt = 0; dt < 4; ++dt)
#pragma unroll
      for (int i = 0; i < 16; ++i) o[dt][i] = 0.f;
    float m = -1e30f, l = 0.f;
    bf16x8 p[4];
#pragma unroll
    for (int ks = 0; ks < 4; ++ks) p[ks] = bf16x8{0, 0, 0, 0, 0, 0, 0, 0};
    if (s == 0) at_loop<0>(smem, Kh, Vh, q, o, p, m, l, n, wave, lane, lim, qi == 0, wo, win, wsp, wout);
    else at_loop<1>(smem, Kh, Vh, q, o, p, m, l, n, wave, lane, lim, qi == 0, wo, win, wsp, wout);
    barrier();
    float* xo = reinterpret_cast<float*>(smem) + g * (64 * 68);
    if (s == 0) {
      l += __shfl_xor(l, 32);
#pragma unroll
      for (int dt = 0; dt < 4; ++dt)
#pragma unroll
        for (int qd = 0; qd < 4; ++qd) {
          f32x4 v = {o[dt][4 * qd], o[dt][4 * qd + 1], o[dt][4 * qd + 2], o[dt][4 * qd + 3]};
          *reinterpret_cast<f32x4*>(xo + ((dt * 4 + qd) * 64 + lane) * 4) = v;
        }
      f32x4 v = {m, l, 0.f, 0.f};
      *reinterpret_cast<f32x4*>(xo + (16 * 64 + lane) * 4) = v;
    } else {
      const AtLoad ld{Kh, Vh, smem, smem, smem, 0u, false};
      if (g >= 2) at_pv<0>(smem + AT_V1 + ((n - 1) % 3) * 16384, p, o, ld, r, h);
      l += __shfl_xor(l, 32);
    }
    barrier();
    if (s == 1) {
      f32x4 ml = *reinterpret_cast<const f32x4*>(xo + (16 * 64 + lane) * 4);
      const float mt = fmaxf(m, ml[0]);
      const float a0 = __builtin_amdgcn_exp2f((m - mt) * AT_C), a1 = __builtin_amdgcn_exp2f((ml[0] - mt) * AT_C);
      const float inv = 1.f / (l * a0 + ml[1] * a1);
      const float f0 = a0 * inv, f1 = a1 * inv;
      char* crow = ctx + ((size_t)(qb * 8 + g * 2 + (r >> 4)) * 128 + head * 4) * 1024 + (r & 15) * 16 + h * 8;
#pragma unroll
      for (int dt = 0; dt < 4; ++dt)
#pragma unroll
        for (int qd = 0; qd < 4; ++qd) {
          f32x4 v = *reinterpret_cast<const f32x4*>(xo + ((dt * 4 + qd) * 64 + lane) * 4);
          u32x2 w;
          w[0] = pack2(o[dt][4 * qd] * f0 + v[0] * f1, o[dt][4 * qd + 1] * f0 + v[1] * f1);
          w[1] = pack2(o[dt][4 * qd + 2] * f0 + v[2] * f1, o[dt][4 * qd + 3] * f0 + v[3] * f1);
          *reinterpret_cast<u32x2*>(crow + dt * 1024 + qd * 256) = w;
        }
    }
    barrier();
  }
}

constexpr int SK_AREA = 16384;
template <int NT> struct SkOwn { static constexpr int N = (2 * NT + 3) / 4; };
template <int NT>
DI void splitk_reduce(const f32x4 (&acc)[4][NT], f32x4 (&own)[SkOwn<NT>::N], char* smem, int wave, int lane) {
  const int hi = wave >> 2, wq = wave & 3;
  char* mine = smem + wave * SK_AREA + lane * 16;
  f32x4 r[2 * NT];
#pragma unroll
  for (int f = 0; f < 2 * NT; ++f) {
    const f32x4 lo = acc[f / NT][f % NT], up = acc[2 + f / NT][f % NT];
    f32x4 snd, kp;
#pragma unroll
    for (int e = 0; e < 4; ++e) { snd[e] = hi ? lo[e] : up[e]; kp[e] = hi ? up[e] : lo[e]; }
    *reinterpret_cast<f32x4*>(mine + f * 1024) = snd;
    r[f] = kp;
  }
  barrier();
  {
    const char* part = smem + (wave ^ 4) * SK_AREA + lane * 16;
#pragma unroll
    for (int f = 0; f < 2 * NT; ++f) {
      const f32x4 o = *reinterpret_cast<const f32x4*>(part + f * 1024);
      r[f] += o;
    }
  }
  barrier();
#pragma unroll
  for (int f = 0; f < 2 * NT; ++f) *reinterpret_cast<f32x4*>(mine + f * 1024) = r[f];
  barrier();
#pragma unroll
  for (int i = 0; i < SkOwn<NT>::N; ++i) {
    const int f = wq + 4 * i;
    const char* p = smem + hi * 4 * SK_AREA + lane * 16 + (f < 2 * NT ? f : 0) * 1024;
    const f32x4 a = *reinterpret_cast<const f32x4*>(p), b = *reinterpret_cast<const f32x4*>(p + SK_AREA);
    const f32x4 c = *reinterpret_cast<const f32x4*>(p + 2 * SK_AREA), d = *reinterpret_cast<const f32x4*>(p + 3 * SK_AREA);
    own[i] = (a + b) + (c + d);
  }
}
DI void sk_bfrags(const char* img, bf16x8 (&bfr)[4], int lane) {
  const int fr = lane & 15, fg = lane >> 4, q = fr >> 2, p = lane & 3, sw = (fg & 1) << 2;
  const char* base = img + fg * 1024 + p * 8;
#pragma unroll
  for (int nt = 0; nt < 4; ++nt) {
    s16x4 lo = tr_read(base + nt * 256 + (q ^ sw) * 32);
    s16x4 hi = tr_read(base + nt * 256 + ((4 + q) ^ sw) * 32);
    bfr[nt] = __builtin_shufflevector(lo, hi, 0, 1, 2, 3, 4, 5, 6, 7);
  }
}

constexpr int OP_NS = 16, OP_DB = 4, OP_DA = 6;
constexpr int op_wait(int t) {
  int pos = 0, bpos = -1;
  for (int i = 0; i < OP_DA; ++i) {
    if (i < OP_DB) { pos += 4; if (i == t) bpos = pos; }
    pos += 4;
  }
  for (int s = 0; s < t; ++s) {
    if (s + OP_DB < OP_NS) { pos += 4; if (s + OP_DB == t) bpos = pos; }
    if (s + OP_DA < OP_NS) pos += 4;
  }
  return pos - bpos;
}
template <int T> struct OpWait { static constexpr int v = op_wait(T); };
template <int T> DI void op_step(f32x4 (&acc)[4][4], u32x4 (&areg)[OP_DA][4], char* ring, const char* asrc,
                                 const u16* bs0, const u16* bs1, int g0, int lane) {
  wait_vm<OpWait<T>::v>();
  if constexpr (T == 0) STAMP(2, 2);
  if constexpr (T == 8) STAMP(2, 3);
  bf16x8 bfr[4];
  sk_bfrags(ring + (T % OP_DB) * 4096, bfr, lane);
#pragma unroll
  for (int mt = 0; mt < 4; ++mt)
#pragma unroll
    for (int nt = 0; nt < 4; ++nt)
      acc[mt][nt] = __builtin_amdgcn_mfma_f32_16x16x32_bf16(bfr[nt], __builtin_bit_cast(bf16x8, areg[T % OP_DA][mt]), acc[mt][nt], 0, 0, 0);
  if constexpr (T + OP_DB < OP_NS) {
    asm volatile("s_waitcnt lgkmcnt(0)" ::: "memory");
    const int g = (g0 + T + OP_DB) & 127;
#pragma unroll
    for (int i = 0; i < 4; ++i)
      glds16(((i & 1) ? bs1 : bs0) + (size_t)(g * 32 + i * 8) * DM, ring + (T % OP_DB) * 4096 + i * 1024);
  }
  if constexpr (T + OP_DA < OP_NS) {
    const int g = (g0 + T + OP_DA) & 127;
#pragma unroll
    for (int mt = 0; mt < 4; ++mt)
      areg[T % OP_DA][mt] = *reinterpret_cast<const u32x4*>(asrc + (size_t)mt * (128 * 1024) + g * 1024);
  }
  if constexpr (T + 1 < OP_NS) op_step<T + 1>(acc, areg, ring, asrc, bs0, bs1, g0, lane);
}
__global__ __launch_bounds__(512, 2) void k_oproj(const char* __restrict__ ctxf, const u16* __restrict__ wo,
                                                  const u16* __restrict__ x, const float* __restrict__ ga,
                                                  u16* __restrict__ x1) {
  __shared__ __attribute__((aligned(16))) char smem[8 * SK_AREA + 256];
  const int b = blockIdx.x, xcd = b & 7, j = b >> 3;
  const int mtile = xcd * 4 + (j >> 3), ntile = j & 7;
  const int tid = threadIdx.x, wave = __builtin_amdgcn_readfirstlane(tid >> 6), lane = tid & 63;
  const int m0 = mtile * 64, n0 = ntile * 64;
  char* ring = smem + wave * SK_AREA;
  const char* asrc = ctxf + (size_t)(mtile * 4) * 128 * 1024 + lane * 16;
  const int g0 = wave * 16;
  const int rw = (lane & 15) >> 1;
  const u16* bs0 = wo + (size_t)rw * DM + n0 + (lane >> 4) * 16 + (lane & 1) * 8;
  const u16* bs1 = wo + (size_t)(rw ^ 4) * DM + n0 + (lane >> 4) * 16 + (lane & 1) * 8;
  f32x4 acc[4][4];
#pragma unroll
  for (int i = 0; i < 4; ++i)
#pragma unroll
    for (int jj = 0; jj < 4; ++jj) acc[i][jj] = f32x4{0.f, 0.f, 0.f, 0.f};
  u32x4 areg[OP_DA][4];
  STAMP(2, 0);
#pragma unroll
  for (int i = 0; i < OP_DA; ++i) {
    const int g = (g0 + i) & 127;
    if (i < OP_DB) {
#pragma unroll
      for (int ii = 0; ii < 4; ++ii) glds16(((ii & 1) ? bs1 : bs0) + (size_t)(g * 32 + ii * 8) * DM, ring + i * 4096 + ii * 1024);
    }
#pragma unroll
    for (int mt = 0; mt < 4; ++mt)
      areg[i][mt] = *reinterpret_cast<const u32x4*>(asrc + (size_t)mt * (128 * 1024) + g * 1024);
  }
  STAMP(2, 1);
  op_step<0>(acc, areg, ring, asrc, bs0, bs1, g0, lane);
  STAMP(2, 4);
  const int fr = lane & 15, fg = lane >> 4, hi = wave >> 2, wq = wave & 3;
  u32x2 xv[2];
  f32x4 gv;
  {
    gv = *reinterpret_cast<const f32x4*>(ga + n0 + wq * 16 + fg * 4);
#pragma unroll
    for (int i = 0; i < 2; ++i)
      xv[i] = *reinterpret_cast<const u32x2*>(x + (size_t)(m0 + (2 * hi + i) * 16 + fr) * DM + n0 + wq * 16 + fg * 4);
  }
  f32x4 own[2];
  splitk_reduce<4>(acc, own, smem, wave, lane);
#pragma unroll
  for (int i = 0; i < 2; ++i) {
    u32x2 w;
    w[0] = pack2(bflo(xv[i][0]) + gv[0] * own[i][0], bfhi(xv[i][0]) + gv[1] * own[i][1]);
    w[1] = pack2(bflo(xv[i][1]) + gv[2] * own[i][2], bfhi(xv[i][1]) + gv[3] * own[i][3]);
    *reinterpret_cast<u32x2*>(x1 + (size_t)(m0 + (2 * hi + i) * 16 + fr) * DM + n0 + wq * 16 + fg * 4) = w;
  }
  STAMP(2, 5);
}

constexpr int NBLK = 43;
using G4 = Gemm<64, 192, 1, 2, 4>;
DI float gelu_tanh(float x) {
  constexpr float K1 = -2.f * 0.7978845608028654f * 1.4426950408889634f, K2 = K1 * 0.044715f;
  const float e = __builtin_amdgcn_exp2f(x * fmaf(x * x, K2, K1));
  return x * __builtin_amdgcn_rcpf(1.f + e);
}
DI u32x4 gload_asm(const void* p) {
  u32x4 r;
  asm volatile("global_load_dwordx4 %0, %1, off" : "=v"(r) : "v"(p) : "memory");
  return r;
}
template <bool Z2>
DI void win_body(char* smem, const u16* __restrict__ x1, const float* __restrict__ sb, const float* __restrict__ cb,
                 const u16* __restrict__ win, char* __restrict__ zf, float* __restrict__ stats, int mtile, int t4) {
  char* sB = smem + 65536;
  const int tid = threadIdx.x, wave = __builtin_amdgcn_readfirstlane(tid >> 6), lane = tid & 63;
  const int wm = wave >> 2, wn = wave & 3;
  const int m0 = mtile * 64, n0 = (Z2 ? FH : 0) + t4 * 176;
  STAMP(1, 0);
  u32x4 c0 = gload_asm(cb + lane * 8), c1 = gload_asm(cb + lane * 8 + 4);
  u32x4 s0 = gload_asm(sb + lane * 8), s1 = gload_asm(sb + lane * 8 + 4);
  {
    const int rr = lane >> 3, ch = (lane & 7) ^ rr;
    const u16* src = x1 + (size_t)(m0 + wave * 8 + rr) * DM + ch * 8;
#pragma unroll
    for (int sub = 0; sub < 8; ++sub) glds16(src + sub * 64, smem + sub * 8192 + wave * 1024);
  }
#pragma unroll
  for (int st = 0; st < 4; ++st) G4::stageB(win, FF, st * 64, n0, FF - 8, sB + st * G4::B_BYTES, wave, lane);
  u32x4 breg[4][3];
  {
    const int pos = lane & 15, rw = pos >> 1, hf = pos & 1;
    const u16* src[3];
#pragma unroll
    for (int ii = 0; ii < 3; ++ii) {
      const int blk = (ii * 8 + wave) * 4 + (lane >> 4), kb = blk / G4::NB, nb = blk % G4::NB;
      int n = n0 + nb * 16 + hf * 8;
      if (n > FF - 8) n = FF - 8;
      src[ii] = win + (size_t)(256 + kb * 8 + (rw ^ ((kb & 1) << 2))) * FF + n;
    }
#pragma unroll
    for (int st = 0; st < 4; ++st) {
#pragma unroll
      for (int ii = 0; ii < 3; ++ii) breg[st][ii] = *reinterpret_cast<const u32x4*>(src[ii] + (size_t)st * 64 * FF);
      __builtin_amdgcn_sched_barrier(0);
    }
  }
  asm volatile("s_waitcnt vmcnt(24)" : "+v"(c0), "+v"(c1), "+v"(s0), "+v"(s1)::"memory");
  STAMP(1, 1);
  {
    float cbv[8], sbv[8];
#pragma unroll
    for (int jj = 0; jj < 4; ++jj) {
      cbv[jj] = 1.f + __uint_as_float(c0[jj]); cbv[4 + jj] = 1.f + __uint_as_float(c1[jj]);
      sbv[jj] = __uint_as_float(s0[jj]); sbv[4 + jj] = __uint_as_float(s1[jj]);
    }
    u32x4 xr[8];
#pragma unroll
    for (int i = 0; i < 8; ++i)
      xr[i] = *reinterpret_cast<const u32x4*>(smem + (lane >> 3) * 8192 + (wave * 8 + i) * 128 + (((lane & 7) ^ i) << 4));
#pragma unroll
    for (int i = 0; i < 8; ++i) {
      float f[8];
#pragma unroll
      for (int jj = 0; jj < 4; ++jj) { f[2 * jj] = bflo(xr[i][jj]); f[2 * jj + 1] = bfhi(xr[i][jj]); }
      float s = 0.f;
#pragma unroll
      for (int jj = 0; jj < 8; ++jj) s += f[jj];
      const float mean = wave_sum(s) * (1.f / DM);
      float v = 0.f;
#pragma unroll
      for (int jj = 0; jj < 8; ++jj) { f[jj] -= mean; v += f[jj] * f[jj]; }
      const float rstd = rsqrtf(wave_sum(v) * (1.f / DM) + 1e-5f);
#pragma unroll
      for (int jj = 0; jj < 4; ++jj)
        xr[i][jj] = pack2(f[2 * jj] * rstd * cbv[2 * jj] + sbv[2 * jj], f[2 * jj + 1] * rstd * cbv[2 * jj + 1] + sbv[2 * jj + 1]);
    }
#pragma unroll
    for (int i = 0; i < 8; ++i)
      *reinterpret_cast<u32x4*>(smem + (lane >> 3) * 8192 + (wave * 8 + i) * 128 + (((lane & 7) ^ i) << 4)) = xr[i];
  }
  STAMP(1, 2);
  f32x4 acc[G4::MT][G4::NT];
#pragma unroll
  for (int i = 0; i < G4::MT; ++i)
#pragma unroll
    for (int jj = 0; jj < G4::NT; ++jj) acc[i][jj] = f32x4{0.f, 0.f, 0.f, 0.f};
#pragma unroll
  for (int kt = 0; kt < 8; ++kt) {
    if (kt == 0) wait_vm<21>(); else if (kt == 1) wait_vm<18>(); else if (kt == 2) wait_vm<15>(); else if (kt == 3) wait_vm<12>();
    barrier();
    if (kt == 0) STAMP(1, 3);
    if (kt == 1) STAMP(1, 4);
    if (kt == 3) STAMP(1, 5);
    if (kt == 5) STAMP(1, 6);
    if (kt >= 1 && kt + 3 < 8) {
#pragma unroll
      for (int ii = 0; ii < 3; ++ii)
        *reinterpret_cast<u32x4*>(sB + ((kt + 3) & 3) * G4::B_BYTES + (ii * 8 + wave) * 1024 + lane * 16) = breg[kt - 1][ii];
    }
    G4::compute<!Z2>(smem + kt * 8192, sB + (kt & 3) * G4::B_BYTES, acc, wm, wn, lane);
  }
  STAMP(1, 7);
  const int fr = lane & 15, fg = lane >> 4;
  if constexpr (!Z2) {
#pragma unroll
    for (int nt = 0; nt < G4::NT; ++nt) {
      const int nbl = wn * 3 + nt, nb = t4 * 11 + nbl;
      if (nbl < 11 && nb < NBLK) {
#pragma unroll
        for (int mt = 0; mt < G4::MT; ++mt) {
          u32x2 w;
          w[0] = pack2(gelu_tanh(acc[mt][nt][0]), gelu_tanh(acc[mt][nt][1]));
          w[1] = pack2(gelu_tanh(acc[mt][nt][2]), gelu_tanh(acc[mt][nt][3]));
          *reinterpret_cast<u32x2*>(zf + ((size_t)(mtile * 4 + wm * 2 + mt) * NBLK + nb) * 512 + lane * 8) = w;
        }
      }
    }
  } else {
    float p1[G4::MT][4], p2[G4::MT][4];
#pragma unroll
    for (int mt = 0; mt < G4::MT; ++mt)
#pragma unroll
      for (int i = 0; i < 4; ++i) { p1[mt][i] = 0.f; p2[mt][i] = 0.f; }
#pragma unroll
    for (int nt = 0; nt < G4::NT; ++nt) {
      const int nbl = wn * 3 + nt, nb = t4 * 11 + nbl;
      if (nbl < 11 && nb < NBLK) {
#pragma unroll
        for (int mt = 0; mt < G4::MT; ++mt) {
          float gz[4];
#pragma unroll
          for (int i = 0; i < 4; ++i) {
            gz[i] = rbf(gelu_tanh(acc[mt][nt][i]));
            p1[mt][i] += gz[i]; p2[mt][i] += gz[i] * gz[i];
          }
          u32x2 w;
          w[0] = pack2(gz[0], gz[1]);
          w[1] = pack2(gz[2], gz[3]);
          *reinterpret_cast<u32x2*>(zf + ((size_t)(mtile * 2 + wm) * NBLK + nb) * 1024 + (fr + 16 * (mt * 2 + (fg >> 1))) * 16 + (fg & 1) * 8) = w;
        }
      }
    }
    barrier();
    float* red = reinterpret_cast<float*>(smem);
#pragma unroll
    for (int mt = 0; mt < G4::MT; ++mt)
#pragma unroll
      for (int i = 0; i < 4; ++i) {
        float a = p1[mt][i], c = p2[mt][i];
        a = row16_sum(a); c = row16_sum(c);
        if (fr == 0) {
          const int rowl = wm * 32 + mt * 16 + fg * 4 + i;
          red[(wn * 64 + rowl) * 2] = a; red[(wn * 64 + rowl) * 2 + 1] = c;
        }
      }
    barrier();
    if (tid < 128) {
      const float v = (red[tid] + red[128 + tid]) + (red[256 + tid] + red[384 + tid]);
      stats[((size_t)t4 * S + m0) * 2 + tid] = v;
    }
  }
}
__global__ __launch_bounds__(512, 2) void k_win(const u16* __restrict__ x1, const float* __restrict__ sb,
                                                const float* __restrict__ cb, const u16* __restrict__ win,
                                                char* __restrict__ z1f, char* __restrict__ z2f,
                                                float* __restrict__ stats) {
  __shared__ __attribute__((aligned(16))) char smem[65536 + 4 * G4::B_BYTES];
  const int b = blockIdx.x, xcd = b & 7, j = b >> 3;
  const int mtile = xcd * 4 + (j >> 3), ntile = j & 7;
  if (ntile >= 4) win_body<true>(smem, x1, sb, cb, win, z2f, stats, mtile, ntile - 4);
  else win_body<false>(smem, x1, sb, cb, win, z1f, stats, mtile, ntile);
}

constexpr int UKS = 22;
template <int NT>
DI void spatial_body(char* smem, const u16* __restrict__ wsp, const char* __restrict__ z2f,
                     const float* __restrict__ stats, const u16* __restrict__ bsp, const char* __restrict__ z1f,
                     char* __restrict__ uf, int mtile, int nb0, int rot) {
  const int tid = threadIdx.x, wave = __builtin_amdgcn_readfirstlane(tid >> 6), lane = tid & 63;
  const int fr = lane & 15, fg = lane >> 4;
  char* ring = smem + wave * SK_AREA;
  float* rs = reinterpret_cast<float*>(smem + 8 * SK_AREA + wave * 2048);
  float* ms = rs + 256;
  const int m0 = mtile * 64, g0 = wave * 4 + rot;
  STAMP(3, 0);
  f32x4 sp[4][2];
#pragma unroll
  for (int t = 0; t < 4; ++t) {
    const float* p = stats + ((size_t)t * S + ((g0 + (lane >> 4)) & 31) * 64 + (lane & 15) * 4) * 2;
    sp[t][0] = *reinterpret_cast<const f32x4*>(p);
    sp[t][1] = *reinterpret_cast<const f32x4*>(p + 4);
  }
  const int rr = lane >> 3, ch = (lane & 7) ^ rr;
  const u16* asrc = wsp + (size_t)(m0 + rr) * S + ch * 8;
  auto stageA = [&](int s) {
#pragma unroll
    for (int i = 0; i < 8; ++i) glds16(asrc + (size_t)i * 8 * S + ((g0 + s) & 31) * 64, ring + (s & 1) * 8192 + i * 1024);
  };
  const char* bsrc = z2f + (size_t)nb0 * 1024 + lane * 16;
  u32x4 breg[3][NT];
  auto loadB = [&](int t) {
#pragma unroll
    for (int nt = 0; nt < NT; ++nt) breg[t % 3][nt] = *reinterpret_cast<const u32x4*>(bsrc + (((((g0 + (t >> 1)) & 31) * 2 + (t & 1)) * NBLK) + nt) * 1024);
  };
  stageA(0); loadB(0); loadB(1); stageA(1); loadB(2);
  STAMP(3, 1);
  {
    f32x4 rv, mv;
#pragma unroll
    for (int i = 0; i < 4; ++i) {
      float s1 = 0.f, s2 = 0.f;
#pragma unroll
      for (int t = 0; t < 4; ++t) { s1 += sp[t][i >> 1][(i & 1) * 2]; s2 += sp[t][i >> 1][(i & 1) * 2 + 1]; }
      const float mean = s1 * (1.f / FH);
      const float var = fmaxf(s2 * (1.f / FH) - mean * mean, 0.f);
      const float rstd = rsqrtf(var + 1e-5f);
      rv[i] = rstd; mv[i] = -mean * rstd;
    }
    *reinterpret_cast<f32x4*>(rs + lane * 4) = rv;
    *reinterpret_cast<f32x4*>(ms + lane * 4) = mv;
  }
  f32x4 acc[4][NT];
#pragma unroll
  for (int i = 0; i < 4; ++i)
#pragma unroll
    for (int jj = 0; jj < NT; ++jj) acc[i][jj] = f32x4{0.f, 0.f, 0.f, 0.f};
#pragma unroll
  for (int s = 0; s < 4; ++s) {
    if (s < 2) wait_vm<3 * NT + 8>(); else if (s == 2) wait_vm<2 * NT + 8>(); else wait_vm<NT>();
    if (s == 0) STAMP(3, 2);
    if (s == 2) STAMP(3, 3);
#pragma unroll
    for (int kk = 0; kk < 2; ++kk) {
      const int t = 2 * s + kk;
      bf16x8 af[4];
#pragma unroll
      for (int mt = 0; mt < 4; ++mt)
        af[mt] = *reinterpret_cast<const bf16x8*>(ring + (s & 1) * 8192 + (mt * 16 + fr) * 128 + (((kk * 4 + fg) ^ (fr & 7)) << 4));
      f32x4 rv[2], mv[2];
#pragma unroll
      for (int e = 0; e < 2; ++e) {
        rv[e] = *reinterpret_cast<const f32x4*>(rs + t * 32 + fg * 8 + e * 4);
        mv[e] = *reinterpret_cast<const f32x4*>(ms + t * 32 + fg * 8 + e * 4);
      }
#pragma unroll
      for (int nt = 0; nt < NT; ++nt) {
        const u32x4 raw = breg[t % 3][nt];
        u32x4 o4;
#pragma unroll
        for (int jj = 0; jj < 4; ++jj)
          o4[jj] = pack2(fmaf(bflo(raw[jj]), rv[jj >> 1][(jj & 1) * 2], mv[jj >> 1][(jj & 1) * 2]),
                         fmaf(bfhi(raw[jj]), rv[jj >> 1][(jj & 1) * 2 + 1], mv[jj >> 1][(jj & 1) * 2 + 1]));
        const bf16x8 bv = __builtin_bit_cast(bf16x8, o4);
#pragma unroll
        for (int mt = 0; mt < 4; ++mt)
          acc[mt][nt] = __builtin_amdgcn_mfma_f32_16x16x32_bf16(bv, af[mt], acc[mt][nt], 0, 0, 0);
      }
      if (t + 3 < 8) loadB(t + 3);
    }
    if (s + 2 < 4) {
      asm volatile("s_waitcnt lgkmcnt(0)" ::: "memory");
      stageA(s + 2);
    }
  }
  STAMP(3, 4);
  constexpr int NO = SkOwn<NT>::N;
  const int hi = wave >> 2, wq = wave & 3;
  u32x2 zv[NO];
  float bs[NO];
#pragma unroll
  for (int i = 0; i < NO; ++i) {
    int f = wq + 4 * i;
    if (f >= 2 * NT) f = 0;
    const int mt = 2 * hi + f / NT, nb = nb0 + f % NT;
    zv[i] = *reinterpret_cast<const u32x2*>(z1f + ((size_t)(mtile * 4 + mt) * NBLK + nb) * 512 + lane * 8);
    bs[i] = bf2f(bsp[m0 + mt * 16 + fr]);
  }
  f32x4 own[NO];
  splitk_reduce<NT>(acc, own, smem, wave, lane);
#pragma unroll
  for (int i = 0; i < NO; ++i) {
    const int f = wq + 4 * i;
    if (f < 2 * NT) {
      const int mt = 2 * hi + f / NT, nb = nb0 + f % NT;
      u32x2 w;
      w[0] = pack2(bflo(zv[i][0]) * (own[i][0] + bs[i]), bfhi(zv[i][0]) * (own[i][1] + bs[i]));
      w[1] = pack2(bflo(zv[i][1]) * (own[i][2] + bs[i]), bfhi(zv[i][1]) * (own[i][3] + bs[i]));
      char* dst = uf + ((size_t)(mtile * 4 + mt) * UKS + (nb >> 1)) * 1024 + (fr + 16 * (fg >> 1)) * 16 + (fg & 1) * 8;
      *reinterpret_cast<u32x2*>(dst + (nb & 1) * 512) = w;
      if (nb == NBLK - 1) *reinterpret_cast<u32x2*>(dst + 512) = u32x2{0u, 0u};
    }
  }
  STAMP(3, 5);
}
__global__ __launch_bounds__(512, 2) void k_spatial(const u16* __restrict__ wsp, const char* __restrict__ z2f,
                                                    const float* __restrict__ stats, const u16* __restrict__ bsp,
                                                    const char* __restrict__ z1f, char* __restrict__ uf) {
  __shared__ __attribute__((aligned(16))) char smem[8 * SK_AREA + 8 * 2048 + 256];
  const int b = blockIdx.x, xcd = b & 7, j = b >> 3;
  const int mtile = xcd * 4 + (j >> 3), ntile = j & 7;
  const int rot = 0;
  if (ntile < 5) spatial_body<5>(smem, wsp, z2f, stats, bsp, z1f, uf, mtile, ntile * 5, rot);
  else spatial_body<6>(smem, wsp, z2f, stats, bsp, z1f, uf, mtile, 25 + (ntile - 5) * 6, rot);
}

__global__ __launch_bounds__(512, 2) void k_wout(const char* __restrict__ uf, const u16* __restrict__ wout,
                                                 const u16* __restrict__ x1, const float* __restrict__ gb,
                                                 u16* __restrict__ out) {
  __shared__ __attribute__((aligned(16))) char smem[8 * SK_AREA];
  const int b = blockIdx.x, xcd = b & 7, j = b >> 3;
  const int mtile = xcd * 4 + (j >> 3), ntile = j & 7;
  const int tid = threadIdx.x, wave = __builtin_amdgcn_readfirstlane(tid >> 6), lane = tid & 63;
  const int m0 = mtile * 64, n0 = ntile * 64;
  char* ring = smem + wave * SK_AREA;
  const int ks0 = wave < 6 ? wave * 3 : 18 + (wave - 6) * 2, nks = wave < 6 ? 3 : 2;
  const int rw = (lane & 15) >> 1;
  u32x4 areg[3][4];
#pragma unroll
  for (int t = 0; t < 3; ++t) {
    const int ks = ks0 + (t < nks ? t : 0);
#pragma unroll
    for (int i = 0; i < 4; ++i) {
      int row = ks * 32 + i * 8 + ((i & 1) ? (rw ^ 4) : rw);
      if (row > FH - 1) row = FH - 1;
      glds16(wout + (size_t)row * DM + n0 + (lane >> 4) * 16 + (lane & 1) * 8, ring + t * 4096 + i * 1024);
    }
#pragma unroll
    for (int mt = 0; mt < 4; ++mt)
      areg[t][mt] = *reinterpret_cast<const u32x4*>(uf + ((size_t)(mtile * 4 + mt) * UKS + ks) * 1024 + lane * 16);
  }
  const int fr = lane & 15, fg = lane >> 4, hi = wave >> 2, wq = wave & 3;
  u32x2 xv[2];
  const f32x4 gv = *reinterpret_cast<const f32x4*>(gb + n0 + wq * 16 + fg * 4);
#pragma unroll
  for (int i = 0; i < 2; ++i)
    xv[i] = *reinterpret_cast<const u32x2*>(x1 + (size_t)(m0 + (2 * hi + i) * 16 + fr) * DM + n0 + wq * 16 + fg * 4);
  f32x4 acc[4][4];
#pragma unroll
  for (int i = 0; i < 4; ++i)
#pragma unroll
    for (int jj = 0; jj < 4; ++jj) acc[i][jj] = f32x4{0.f, 0.f, 0.f, 0.f};
  wait_vm<0>();
#pragma unroll
  for (int t = 0; t < 3; ++t) {
    if (t < nks) {
      bf16x8 bfr[4];
      sk_bfrags(ring + t * 4096, bfr, lane);
#pragma unroll
      for (int mt = 0; mt < 4; ++mt)
#pragma unroll
        for (int nt = 0; nt < 4; ++nt)
          acc[mt][nt] = __builtin_amdgcn_mfma_f32_16x16x32_bf16(bfr[nt], __builtin_bit_cast(bf16x8, areg[t][mt]), acc[mt][nt], 0, 0, 0);
    }
  }
  f32x4 own[2];
  splitk_reduce<4>(acc, own, smem, wave, lane);
#pragma unroll
  for (int i = 0; i < 2; ++i) {
    u32x2 w;
    w[0] = pack2(bflo(xv[i][0]) + gv[0] * own[i][0], bfhi(xv[i][0]) + gv[1] * own[i][1]);
    w[1] = pack2(bflo(xv[i][1]) + gv[2] * own[i][2], bfhi(xv[i][1]) + gv[3] * own[i][3]);
    *reinterpret_cast<u32x2*>(out + (size_t)(m0 + (2 * hi + i) * 16 + fr) * DM + n0 + wq * 16 + fg * 4) = w;
  }
}

extern "C" void kernel_launch(void* const* d_in, const int* in_sizes, int n_in, void* d_out, int out_size,
                              void* d_ws, size_t ws_size, hipStream_t stream) {
  const u16* x = (const u16*)d_in[0];
  const u16* ckv = (const u16*)d_in[1];
  const u16* wq = (const u16*)d_in[2];
  const u16* wk = (const u16*)d_in[3];
  const u16* wv = (const u16*)d_in[4];
  const u16* wo = (const u16*)d_in[5];
  const u16* win = (const u16*)d_in[7];
  const u16* wsp = (const u16*)d_in[8];
  const u16* bsp = (const u16*)d_in[9];
  const u16* wout = (const u16*)d_in[10];
  const u16* c = (const u16*)d_in[11];
  const u16* wmod = (const u16*)d_in[12];
  char* ws = (char*)d_ws;
  float* mod = (float*)(ws + WS_MOD);
  u16* h1 = (u16*)(ws + WS_H1);
  char* Qf = ws + WS_QF;
  char* Ki = ws + WS_KI;
  char* Vi = ws + WS_VI;
  char* ctx = ws + WS_CTX;
  u16* x1 = (u16*)(ws + WS_X1);
  char* z1 = ws + WS_Z1;
  char* z2 = ws + WS_Z2;
  float* stats = (float*)(ws + WS_ST);
  char* u = ws + WS_U;
  u16* out = (u16*)d_out;

  k_kv<<<256, 512, 0, stream>>>(ckv, wk, wv, Ki, Vi, c, wmod, mod);
  k_ln<<<S / 4, 256, 0, stream>>>(x, mod + 0 * DM, mod + 1 * DM, h1);
  k_qkv<<<512, 256, 0, stream>>>(h1, ckv, wq, wk, wv, Qf, Ki, Vi);
  k_attn<<<256, 512, 0, stream>>>(Qf, Ki, Vi, ctx, wo, win, wsp, wout);
  k_oproj<<<256, 512, 0, stream>>>(ctx, wo, x, mod + 2 * DM, x1);
  k_win<<<256, 512, 0, stream>>>(x1, mod + 3 * DM, mod + 4 * DM, win, z1, z2, stats);
  k_spatial<<<256, 512, 0, stream>>>(wsp, z2, stats, bsp, z1, u);
  k_wout<<<256, 512, 0, stream>>>(u, wout, x1, mod + 5 * DM, out);
}
```

```cpp
#include <hip/hip_runtime.h>
#include <hip/hip_bf16.h>
#include <stdint.h>

constexpr int S = 2048;
constexpr int DM = 512;
constexpr int NH = 32;
constexpr int HD = 128;
constexpr int HDM = NH * HD;
constexpr int FH = 688;
constexpr int FF = 1376;

typedef __attribute__((ext_vector_type(8))) short bf16x8;
typedef __attribute__((ext_vector_type(4))) short s16x4;
typedef __attribute__((ext_vector_type(4))) float f32x4;
typedef __attribute__((ext_vector_type(16))) float f32x16;
typedef __attribute__((ext_vector_type(4))) unsigned u32x4;
typedef __attribute__((ext_vector_type(2))) unsigned u32x2;
typedef __attribute__((address_space(3))) s16x4* lds_s16x4_ptr;
typedef unsigned short u16;

#define DI __device__ __forceinline__

DI float bf2f(u16 v) { return __uint_as_float(((unsigned)v) << 16); }
DI float bflo(unsigned v) { return __uint_as_float(v << 16); }
DI float bfhi(unsigned v) { return __uint_as_float(v & 0xffff0000u); }
typedef __attribute__((ext_vector_type(2))) __bf16 bf16x2_t;
typedef __attribute__((ext_vector_type(2))) float f32x2;
DI unsigned pack2(float lo, float hi) {
  f32x2 v = {lo, hi};
  return __builtin_bit_cast(unsigned, __builtin_convertvector(v, bf16x2_t));
}
template <int N> DI void wait_vm() { asm volatile("s_waitcnt vmcnt(%0)" ::"n"(N) : "memory"); }
DI void barrier() { asm volatile("s_waitcnt lgkmcnt(0)" ::: "memory"); __builtin_amdgcn_s_barrier(); }
DI float rbf(float x) { return bflo(pack2(x, 0.f)); }

DI void glds16(const void* g, const void* lds_wave_base) {
  const unsigned a = __builtin_amdgcn_readfirstlane((unsigned)(size_t)lds_wave_base);
  unsigned keep;
  asm volatile("s_mov_b32 %0, m0\n\ts_mov_b32 m0, %2\n\ts_nop 0\n\tglobal_load_lds_dwordx4 %1, off\n\ts_mov_b32 m0, %0"
               : "=&s"(keep) : "v"(g), "s"(a) : "memory");
}
template <int OFF> DI void glds16s(const void* sbase, unsigned voff, unsigned lds_base) {
  asm volatile("s_mov_b32 m0, %2\n\ts_nop 0\n\tglobal_load_lds_dwordx4 %0, %1 offset:%3"
               : : "v"(voff), "s"(sbase), "s"(lds_base), "n"(OFF) : "memory");
}
DI unsigned lds_addr(const void* p) { return __builtin_amdgcn_readfirstlane((unsigned)(size_t)p); }
DI void pf_line(const void* g, const void* lds_dummy) {
  const unsigned a = __builtin_amdgcn_readfirstlane((unsigned)(size_t)lds_dummy);
  unsigned keep;
  asm volatile("s_mov_b32 %0, m0\n\ts_mov_b32 m0, %2\n\ts_nop 0\n\tglobal_load_lds_dword %1, off\n\ts_mov_b32 m0, %0"
               : "=&s"(keep) : "v"(g), "s"(a) : "memory");
}
DI void pf_slice(const void* base, int nbytes, int first, int lane, const void* lds_dummy) {
  int l = first + lane;
  const int last = (nbytes >> 7) - 1;
  if (l > last) l = last;
  pf_line((const char*)base + (size_t)l * 128, lds_dummy);
}
#define STAMP(k, i)
template <int CTRL> DI float dpp_add(float v) {
  return v + __uint_as_float(__builtin_amdgcn_update_dpp(0u, __float_as_uint(v), CTRL, 0xf, 0xf, true));
}
DI float row16_sum(float v) {
  v = dpp_add<0xB1>(v); v = dpp_add<0x4E>(v); v = dpp_add<0x141>(v); v = dpp_add<0x140>(v);
  return v;
}
DI float wave_sum(float v) {
  v = row16_sum(v);
  auto a = __builtin_amdgcn_permlane16_swap(__float_as_uint(v), __float_as_uint(v), false, false);
  v = __uint_as_float(a[0]) + __uint_as_float(a[1]);
  auto b = __builtin_amdgcn_permlane32_swap(__float_as_uint(v), __float_as_uint(v), false, false);
  return __uint_as_float(b[0]) + __uint_as_float(b[1]);
}
DI s16x4 tr_read(const char* p) {
  return __builtin_amdgcn_ds_read_tr16_b64_v4i16((lds_s16x4_ptr)(p));
}

constexpr size_t WS_MOD = 0;
constexpr size_t WS_H1 = 16384;
constexpr size_t WS_QF = WS_H1 + (size_t)S * DM * 2;
constexpr size_t WS_KI = WS_QF + (size_t)S * HDM * 2;
constexpr size_t WS_VI = WS_KI + (size_t)S * HDM * 2;
constexpr size_t WS_CTX = WS_VI + (size_t)S * HDM * 2;
constexpr size_t WS_X1 = WS_CTX + (size_t)S * HDM * 2;
constexpr size_t WS_Z1 = WS_X1 + (size_t)S * DM * 2;
constexpr size_t WS_Z2 = WS_Z1 + (size_t)S * FH * 2;
constexpr size_t WS_ST = WS_Z2 + (size_t)S * FH * 2;
constexpr size_t WS_U = WS_ST + (size_t)8 * S * 2 * 4;
constexpr int LDU = 704;

__global__ __launch_bounds__(256) void k_mod(const u16* __restrict__ c, const u16* __restrict__ w,
                                             float* __restrict__ mod) {
  __shared__ float red[64][33];
  const int t = threadIdx.x, cg = t & 3, ks = t >> 2;
  const int col0 = blockIdx.x * 32 + cg * 8;
  float acc[8];
#pragma unroll
  for (int j = 0; j < 8; ++j) acc[j] = 0.f;
#pragma unroll
  for (int kk = 0; kk < 8; ++kk) {
    const int k = ks * 8 + kk;
    float cv = bf2f(c[k]);
    float sv = rbf(cv / (1.f + __expf(-cv)));
    u32x4 wv = *reinterpret_cast<const u32x4*>(w + (size_t)k * 3072 + col0);
#pragma unroll
    for (int j = 0; j < 4; ++j) {
      acc[2 * j] += sv * bflo(wv[j]);
      acc[2 * j + 1] += sv * bfhi(wv[j]);
    }
  }
#pragma unroll
  for (int j = 0; j < 8; ++j) red[ks][cg * 8 + j] = acc[j];
  __syncthreads();
  if (t < 32) {
    float s = 0.f;
    for (int i = 0; i < 64; ++i) s += red[i][t];
    mod[blockIdx.x * 32 + t] = rbf(s);
  }
}

__global__ __launch_bounds__(256) void k_ln(const u16* __restrict__ x, const float* __restrict__ sh,
                                            const float* __restrict__ cs, u16* __restrict__ h) {
  const int row = blockIdx.x * 4 + (threadIdx.x >> 6), lane = threadIdx.x & 63;
  u32x4 xv = *reinterpret_cast<const u32x4*>(x + (size_t)row * DM + lane * 8);
  float f[8];
#pragma unroll
  for (int j = 0; j < 4; ++j) { f[2 * j] = bflo(xv[j]); f[2 * j + 1] = bfhi(xv[j]); }
  float s = 0.f;
#pragma unroll
  for (int j = 0; j < 8; ++j) s += f[j];
#pragma unroll
  for (int o = 32; o >= 1; o >>= 1) s += __shfl_xor(s, o);
  const float mean = s * (1.f / DM);
  float v = 0.f;
#pragma unroll
  for (int j = 0; j < 8; ++j) { f[j] -= mean; v += f[j] * f[j]; }
#pragma unroll
  for (int o = 32; o >= 1; o >>= 1) v += __shfl_xor(v, o);
  const float rstd = rsqrtf(v * (1.f / DM) + 1e-5f);
  u32x4 o4;
#pragma unroll
  for (int j = 0; j < 4; ++j) {
    const int k = lane * 8 + 2 * j;
    float a = f[2 * j] * rstd * (1.f + cs[k]) + sh[k];
    float b = f[2 * j + 1] * rstd * (1.f + cs[k + 1]) + sh[k + 1];
    o4[j] = pack2(a, b);
  }
  *reinterpret_cast<u32x4*>(h + (size_t)row * DM + lane * 8) = o4;
}

template <int BM, int BN, int KS, int NWM, int NWN>
struct Gemm {
  static constexpr int NW = NWM * NWN;
  static constexpr int WM = BM / NWM, WN = BN / NWN;
  static constexpr int MT = WM / 16, NT = WN / 16;
  static constexpr int BK = 64 * KS;
  static constexpr int A_BYTES = BM * BK * 2;
  static constexpr int B_BYTES = BK * BN * 2;
  static constexpr int STAGE_BYTES = A_BYTES + B_BYTES;
  static constexpr int NB = BN / 16;

  static DI void stageA(const u16* __restrict__ A, int lda, int m0, int k0, char* lds, int wave, int lane) {
    constexpr int NI = A_BYTES / 1024;
    const int rr = lane >> 3, ch = (lane & 7) ^ rr;
#pragma unroll
    for (int ii = 0; ii < NI / NW; ++ii) {
      const int i = ii * NW + wave;
      const int sub = i / (BM / 8), j = i % (BM / 8);
      const u16* src = A + (size_t)(m0 + j * 8 + rr) * lda + k0 + sub * 64 + ch * 8;
      glds16(src, lds + i * 1024);
    }
  }
  static DI void stageB(const u16* __restrict__ B, int ldb, int k0, int n0, int nmax, char* lds, int wave, int lane) {
    constexpr int NI = B_BYTES / 1024;
    const int pos = lane & 15, rw = pos >> 1, hf = pos & 1;
#pragma unroll
    for (int ii = 0; ii < NI / NW; ++ii) {
      const int i = ii * NW + wave;
      const int blk = i * 4 + (lane >> 4);
      const int kb = blk / NB, nb = blk % NB;
      const int row = rw ^ ((kb & 1) << 2);
      int n = n0 + nb * 16 + hf * 8;
      if (n > nmax) n = nmax;
      const u16* src = B + (size_t)(k0 + kb * 8 + row) * ldb + n;
      glds16(src, lds + i * 1024);
    }
  }
  template <bool SWAP>
  static DI void compute(const char* ldsA, const char* ldsB, f32x4 (&acc)[MT][NT], int wm, int wn, int lane) {
    const int fr = lane & 15, fg = lane >> 4;
    const int q = fr >> 2, p = lane & 3;
#pragma unroll
    for (int sub = 0; sub < KS; ++sub) {
#pragma unroll
      for (int kk = 0; kk < 2; ++kk) {
        bf16x8 af[MT], bfr[NT];
#pragma unroll
        for (int mt = 0; mt < MT; ++mt) {
          const int row = wm * WM + mt * 16 + fr;
          af[mt] = *reinterpret_cast<const bf16x8*>(ldsA + sub * (BM * 128) + row * 128 + (((kk * 4 + fg) ^ (row & 7)) << 4));
        }
#pragma unroll
        for (int nt = 0; nt < NT; ++nt) {
          const int kb = sub * 8 + kk * 4 + fg, nb = wn * NT + nt;
          const char* base = ldsB + (kb * NB + nb) * 256 + p * 8;
          const int sw = (fg & 1) << 2;
          s16x4 lo = tr_read(base + ((q) ^ sw) * 32);
          s16x4 hi = tr_read(base + ((4 + q) ^ sw) * 32);
          bfr[nt] = __builtin_shufflevector(lo, hi, 0, 1, 2, 3, 4, 5, 6, 7);
        }
#pragma unroll
        for (int mt = 0; mt < MT; ++mt)
#pragma unroll
          for (int nt = 0; nt < NT; ++nt) {
            if constexpr (SWAP)
              acc[mt][nt] = __builtin_amdgcn_mfma_f32_16x16x32_bf16(bfr[nt], af[mt], acc[mt][nt], 0, 0, 0);
            else
              acc[mt][nt] = __builtin_amdgcn_mfma_f32_16x16x32_bf16(af[mt], bfr[nt], acc[mt][nt], 0, 0, 0);
          }
      }
    }
  }
};

using G1 = Gemm<128, 128, 1, 2, 2>;

__global__ __launch_bounds__(256, 2) void k_qkv(const u16* __restrict__ h1, const u16* __restrict__ ckv,
                                                const u16* __restrict__ wq, const u16* __restrict__ wk,
                                                const u16* __restrict__ wv, char* __restrict__ Qf,
                                                char* __restrict__ Ki, char* __restrict__ Vi) {
  __shared__ __attribute__((aligned(16))) char smem[2 * G1::STAGE_BYTES];
  const int b = blockIdx.x, xcd = b & 7, j = b >> 3;
  const int head = xcd * 4 + (j & 3), rest = j >> 2, which = rest >> 4, mtile = rest & 15;
  const int tid = threadIdx.x, wave = __builtin_amdgcn_readfirstlane(tid >> 6), lane = tid & 63;
  const int wm = wave >> 1, wn = wave & 1;
  const u16* A = which == 0 ? h1 : ckv;
  const u16* B = which == 0 ? wq : (which == 1 ? wk : wv);
  const int m0 = mtile * 128, n0 = head * 128;
  f32x4 acc[G1::MT][G1::NT];
#pragma unroll
  for (int i = 0; i < G1::MT; ++i)
#pragma unroll
    for (int jj = 0; jj < G1::NT; ++jj) acc[i][jj] = f32x4{0.f, 0.f, 0.f, 0.f};
  constexpr int NK = DM / G1::BK;
  G1::stageA(A, DM, m0, 0, smem, wave, lane);
  G1::stageB(B, HDM, 0, n0, HDM, smem + G1::A_BYTES, wave, lane);
  for (int kt = 0; kt < NK; ++kt) {
    wait_vm<0>(); __syncthreads();
    char* cur = smem + (kt & 1) * G1::STAGE_BYTES;
    char* nxt = smem + ((kt + 1) & 1) * G1::STAGE_BYTES;
    if (kt + 1 < NK) {
      G1::stageA(A, DM, m0, (kt + 1) * G1::BK, nxt, wave, lane);
      G1::stageB(B, HDM, (kt + 1) * G1::BK, n0, HDM, nxt + G1::A_BYTES, wave, lane);
    }
    if (which == 2) G1::compute<false>(cur, cur + G1::A_BYTES, acc, wm, wn, lane);
    else G1::compute<true>(cur, cur + G1::A_BYTES, acc, wm, wn, lane);
  }
  __syncthreads();
  const int fr = lane & 15, fg = lane >> 4;
#pragma unroll
  for (int mt = 0; mt < G1::MT; ++mt)
#pragma unroll
    for (int nt = 0; nt < G1::NT; ++nt) {
      u32x2 v;
      v[0] = pack2(acc[mt][nt][0], acc[mt][nt][1]);
      v[1] = pack2(acc[mt][nt][2], acc[mt][nt][3]);
      int off;
      if (which == 2) {
        const int d = wn * 64 + nt * 16 + fr, m = wm * 64 + mt * 16 + fg * 4;
        const int tile = m >> 6, key = m & 63;
        const int pos = (key & 0x33) | ((key & 4) << 1) | ((key & 8) >> 1);
        off = tile * 16384 + d * 128 + (((pos >> 3) ^ ((d >> 1) & 7)) << 4) + (pos & 7) * 2;
      } else {
        const int m = wm * 64 + mt * 16 + fr, d = wn * 64 + nt * 16 + fg * 4;
        if (which == 1) {
          const int tile = m >> 6, key = m & 63;
          off = tile * 16384 + key * 256 + (((d >> 3) ^ (key & 15)) << 4) + (d & 7) * 2;
        } else {
          const int rg = m >> 5, r = m & 31;
          off = rg * 8192 + (d >> 4) * 1024 + ((((d >> 3) & 1) * 32 + r) << 4) + (d & 7) * 2;
        }
      }
      *reinterpret_cast<u32x2*>(smem + off) = v;
    }
  __syncthreads();
  char* dst = (which == 0 ? Qf : (which == 1 ? Ki : Vi)) + ((size_t)head * 16 + mtile) * 32768;
#pragma unroll
  for (int i = 0; i < 8; ++i) {
    const int o = (i * 256 + tid) * 16;
    *reinterpret_cast<u32x4*>(dst + o) = *reinterpret_cast<const u32x4*>(smem + o);
  }
}

#define KV_PD 2
constexpr int KV_NS = 4, KV_STAGE = 32768;
template <bool isV>
DI void kv_body(char* smem, const u16* __restrict__ ckv, const u16* __restrict__ B, char* __restrict__ dstb,
                const u16* __restrict__ cvec, const u16* __restrict__ wmod, float* __restrict__ mod) {
  const int b = blockIdx.x, xcd = b & 7, j = b >> 3;
  const int ntile = xcd * 4 + (j & 3), mtile = j >> 2;
  const int tid = threadIdx.x, wave = __builtin_amdgcn_readfirstlane(tid >> 6), lane = tid & 63;
  const int wm = wave >> 2, wn = wave & 3, r = lane & 31, h = lane >> 5;
  const int m0 = mtile * 256, n0 = (ntile & 15) * 256;
  float* modred = reinterpret_cast<float*>(smem + KV_NS * KV_STAGE);
  STAMP(0, 0);

  const u16* asrc[2];
  const u16* bsrc[2];
#pragma unroll
  for (int ii = 0; ii < 2; ++ii) {
    const int i = ii * 8 + wave;
    const int row = 16 * i + (lane >> 2), ch = (lane & 3) ^ ((lane >> 4) & 3);
    asrc[ii] = ckv + (size_t)(m0 + row) * DM + ch * 8;
    const int blk = i * 4 + (lane >> 4), kb = blk >> 4, nb = blk & 15;
    const int pos = lane & 15, rw = (pos >> 1) ^ ((nb & 1) << 2), hf = pos & 1;
    bsrc[ii] = B + (size_t)(kb * 8 + rw) * HDM + n0 + nb * 16 + hf * 8;
  }
  auto stage = [&](int kt0, int slot) {
    char* dst = smem + slot * KV_STAGE;
    const int kt = (kt0 + 2 * mtile) & 15;
#pragma unroll
    for (int ii = 0; ii < 2; ++ii) {
      glds16(asrc[ii] + kt * 32, dst + (ii * 8 + wave) * 1024);
      glds16(bsrc[ii] + (size_t)kt * 32 * HDM, dst + 16384 + (ii * 8 + wave) * 1024);
    }
  };
  constexpr int NK = DM / 32;
#pragma unroll
  for (int st = 0; st < KV_PD; ++st) stage(st, st);

  if (b < 192) {
    const u16* p = wmod + (size_t)tid * 3072 + b * 16;
    const u32x4 w0 = *reinterpret_cast<const u32x4*>(p), w1 = *reinterpret_cast<const u32x4*>(p + 8);
    const float c = bf2f(cvec[tid]);
    const float sv = rbf(c / (1.f + __expf(-c)));
    float v[16];
#pragma unroll
    for (int jj = 0; jj < 4; ++jj) {
      v[2 * jj] = sv * bflo(w0[jj]); v[2 * jj + 1] = sv * bfhi(w0[jj]);
      v[8 + 2 * jj] = sv * bflo(w1[jj]); v[8 + 2 * jj + 1] = sv * bfhi(w1[jj]);
    }
#pragma unroll
    for (int st = 0; st < 4; ++st) {
      const int half = 8 >> st, m = 32 >> st;
      const bool up = lane & m;
#pragma unroll
      for (int e = 0; e < half; ++e) {
        const float send = up ? v[e] : v[half + e];
        const float keep = up ? v[half + e] : v[e];
        v[e] = keep + __shfl_xor(send, m);
      }
    }
    v[0] += __shfl_xor(v[0], 2);
    v[0] += __shfl_xor(v[0], 1);
    if ((lane & 3) == 0) {
      const int col = ((lane >> 5) & 1) * 8 + ((lane >> 4) & 1) * 4 + ((lane >> 3) & 1) * 2 + ((lane >> 2) & 1);
      modred[wave * 16 + col] = v[0];
    }
  }

  STAMP(0, 1);
  f32x16 acc[4][2];
#pragma unroll
  for (int i = 0; i < 4; ++i)
#pragma unroll
    for (int jj = 0; jj < 2; ++jj)
#pragma unroll
      for (int e = 0; e < 16; ++e) acc[i][jj][e] = 0.f;
  const int G = lane >> 4, q4 = (lane & 15) >> 2, p4 = lane & 3;
  int aoff[2];
#pragma unroll
  for (int u = 0; u < 2; ++u) aoff[u] = (wm * 128 + r) * 64 + (((2 * u + h) ^ ((r >> 2) & 3)) << 4);
  const int nh = G & 1;
  const int boff = 16384 + (h * 16 + wn * 4 + nh) * 256 + p4 * 8;
  const int brow0 = (q4 ^ (nh << 2)) * 32, brow1 = ((4 + q4) ^ (nh << 2)) * 32;
  for (int kt = 0; kt < NK; ++kt) {
    if (kt + KV_PD - 1 <= NK - 1) wait_vm<(KV_PD - 1) * 4>(); else wait_vm<0>();
    barrier();
    const bool pf = kt + KV_PD < NK;
    const int ktn = (kt + KV_PD + 2 * mtile) & 15;
    char* dst = smem + ((kt + KV_PD) & (KV_NS - 1)) * KV_STAGE;
    const char* cur = smem + (kt & (KV_NS - 1)) * KV_STAGE;
    bf16x8 af[2][4], bfr[2][2];
#pragma unroll
    for (int u = 0; u < 2; ++u) {
#pragma unroll
      for (int nt = 0; nt < 2; ++nt) {
        const char* bb = cur + boff + u * (2 * 16 * 256) + nt * 512;
        s16x4 lo = tr_read(bb + brow0), hi = tr_read(bb + brow1);
        bfr[u][nt] = __builtin_shufflevector(lo, hi, 0, 1, 2, 3, 4, 5, 6, 7);
      }
#pragma unroll
      for (int mt = 0; mt < 4; ++mt) af[u][mt] = *reinterpret_cast<const bf16x8*>(cur + aoff[u] + mt * 2048);
    }
#pragma unroll
    for (int g4 = 0; g4 < 4; ++g4) {
      const int u = g4 >> 1;
      __builtin_amdgcn_sched_barrier(0);
      if (pf) {
        const int ii = g4 >> 1;
        if (g4 & 1) glds16(bsrc[ii] + (size_t)ktn * 32 * HDM, dst + 16384 + (ii * 8 + wave) * 1024);
        else glds16(asrc[ii] + ktn * 32, dst + (ii * 8 + wave) * 1024);
      }
      __builtin_amdgcn_sched_barrier(0);
#pragma unroll
      for (int m2 = 0; m2 < 2; ++m2)
#pragma unroll
        for (int nt = 0; nt < 2; ++nt) {
          const int mt = (g4 & 1) * 2 + m2;
          if constexpr (isV)
            acc[mt][nt] = __builtin_amdgcn_mfma_f32_32x32x16_bf16(af[u][mt], bfr[u][nt], acc[mt][nt], 0, 0, 0);
          else
            acc[mt][nt] = __builtin_amdgcn_mfma_f32_32x32x16_bf16(bfr[u][nt], af[u][mt], acc[mt][nt], 0, 0, 0);
        }
    }
  }
  barrier();
  STAMP(0, 2);
#pragma unroll
  for (int mt = 0; mt < 4; ++mt)
#pragma unroll
    for (int nt = 0; nt < 2; ++nt)
#pragma unroll
      for (int qd = 0; qd < 4; ++qd) {
        u32x2 v;
        v[0] = pack2(acc[mt][nt][4 * qd], acc[mt][nt][4 * qd + 1]);
        v[1] = pack2(acc[mt][nt][4 * qd + 2], acc[mt][nt][4 * qd + 3]);
        int off;
        if constexpr (isV) {
          const int n = wn * 64 + nt * 32 + r, m = wm * 128 + mt * 32 + 8 * qd + 4 * h;
          const int hd = n >> 7, d = n & 127, tile = m >> 6, key = m & 63;
          const int pos = (key & 0x33) | ((key & 4) << 1) | ((key & 8) >> 1);
          off = (hd * 4 + tile) * 16384 + d * 128 + (((pos >> 3) ^ ((d >> 1) & 7)) << 4) + (pos & 7) * 2;
        } else {
          const int m = wm * 128 + mt * 32 + r, n = wn * 64 + nt * 32 + 8 * qd + 4 * h;
          const int hd = n >> 7, d = n & 127, tile = m >> 6, key = m & 63;
          off = (hd * 4 + tile) * 16384 + key * 256 + (((d >> 3) ^ (key & 15)) << 4) + (d & 7) * 2;
        }
        *reinterpret_cast<u32x2*>(smem + off) = v;
      }
  barrier();
  {
    const int head0 = (ntile & 15) * 2;
#pragma unroll
    for (int i = 0; i < 16; ++i) {
      const int o = (i * 512 + tid) * 16;
      const int hd = o >> 16, rem = o & 65535;
      *reinterpret_cast<u32x4*>(dstb + ((size_t)(head0 + hd) * 32 + mtile * 4) * 16384 + rem) =
          *reinterpret_cast<const u32x4*>(smem + o);
    }
  }
  STAMP(0, 3);
  if (b < 192 && tid < 16) {
    float sum = 0.f;
#pragma unroll
    for (int i = 0; i < 8; ++i) sum += modred[i * 16 + tid];
    mod[b * 16 + tid] = rbf(sum);
  }
}
__global__ __launch_bounds__(512, 2) void k_kv(const u16* __restrict__ ckv, const u16* __restrict__ wk,
                                               const u16* __restrict__ wv, char* __restrict__ Ki,
                                               char* __restrict__ Vi, const u16* __restrict__ cvec,
                                               const u16* __restrict__ wmod, float* __restrict__ mod) {
  __shared__ __attribute__((aligned(16))) char smem[KV_NS * KV_STAGE + 512];
  if ((blockIdx.x & 7) >= 4) kv_body<true>(smem, ckv, wv, Vi, cvec, wmod, mod);
  else kv_body<false>(smem, ckv, wk, Ki, cvec, wmod, mod);
}

constexpr int AT_DUMMY = 131072;
constexpr float AT_C = 0.08838834764831845f * 1.4426950408889634f;
struct AtLoad {
  const char* ksrc; const char* vsrc; unsigned dst; unsigned voff; bool on;
  template <int I> DI void piece() const {
    if (!on) return;
    if constexpr (I < 4) glds16s<I * 1024>(ksrc, voff, dst);
    else glds16s<(I - 4) * 1024>(vsrc, voff, dst + 32768);
  }
  DI void all() const {
    piece<0>(); piece<1>(); piece<2>(); piece<3>(); piece<4>(); piece<5>(); piece<6>(); piece<7>();
  }
};
#define AT_SB() __builtin_amdgcn_sched_barrier(0)
DI bf16x8 at_kfrag(const char* Kt, int j, int i, int r, int h) {
  return *reinterpret_cast<const bf16x8*>(Kt + r * 256 + (j >> 1) * 8192 + (((2 * ((j & 1) * 4 + i) + h) ^ (r & 15)) << 4));
}
DI bf16x8 at_vfrag(const char* Vt, int j, int i, int r, int h) {
  const int dt = 2 * (j & 1) + (i >> 1), ks = 2 * (j >> 1) + (i & 1);
  return *reinterpret_cast<const bf16x8*>(Vt + r * 128 + dt * 4096 + (((2 * ks + h) ^ ((r >> 1) & 7)) << 4));
}
DI float at_max(f32x16& sv, int kt, bool diag, int lim) {
  if (diag) {
    asm volatile("" : "+v"(lim));
#pragma unroll
    for (int i = 0; i < 16; ++i)
      if (32 * kt + (i & 3) + 8 * (i >> 2) > lim) sv[i] = -__builtin_inff();
  }
  float pm = sv[0];
#pragma unroll
  for (int i = 1; i < 16; ++i) pm = fmaxf(pm, sv[i]);
  auto rr = __builtin_amdgcn_permlane32_swap(__float_as_uint(pm), __float_as_uint(pm), false, false);
  return fmaxf(__uint_as_float(rr[0]), __uint_as_float(rr[1]));
}
DI unsigned at_exp2(const f32x16& sv, int e, float mc, float& ps) {
  const float e0 = __builtin_amdgcn_exp2f(fmaf(sv[e], AT_C, mc));
  const float e1 = __builtin_amdgcn_exp2f(fmaf(sv[e + 1], AT_C, mc));
  ps += e0; ps += e1;
  return pack2(e0, e1);
}
DI void at_scale(f32x16 (&o)[4], float a) {
#pragma unroll
  for (int dt = 0; dt < 4; ++dt)
#pragma unroll
    for (int i = 0; i < 16; ++i) o[dt][i] *= a;
}
DI void at_iter(char* smem, const char* Kh, const char* Vh, const bf16x8 (&q)[8], f32x16 (&o)[4], float& m, float& l,
                int it, int n, int wave, int lane, int lim, bool pfw) {
  const int r = lane & 31, h = lane >> 5, sg = wave >> 2;
  if (pfw && it == 5) STAMP(4, 0);
  if (pfw && it == 6) STAMP(4, 5);
  wait_vm<0>(); barrier();
  if (pfw && it == 5) STAMP(4, 1);
  const size_t go = (size_t)(it + 1) * 32768;
  const AtLoad ld{Kh + go, Vh + go, lds_addr(smem) + ((it + 1) & 1) * 65536 + wave * 4096, (unsigned)lane * 16u, it + 1 < n};
  const bool last = (it == n - 1);
  const char* Kt = smem + (it & 1) * 65536 + sg * 16384;
  const char* Vt = Kt + 32768;
  f32x16 st[2];
#pragma unroll
  for (int kt = 0; kt < 2; ++kt)
#pragma unroll
    for (int i = 0; i < 16; ++i) st[kt][i] = 0.f;
  bf16x8 f0[4], f1[4], f2[4];
  u32x4 pw[4];
#pragma unroll
  for (int i = 0; i < 4; ++i) f0[i] = at_kfrag(Kt, 0, i, r, h);
#pragma unroll
  for (int i = 0; i < 4; ++i) f1[i] = at_kfrag(Kt, 1, i, r, h);
  ld.piece<0>(); AT_SB();
#pragma unroll
  for (int i = 0; i < 4; ++i) {
    f2[i] = at_kfrag(Kt, 2, i, r, h);
    st[0] = __builtin_amdgcn_mfma_f32_32x32x16_bf16(f0[i], q[i], st[0], 0, 0, 0); AT_SB();
  }
  ld.piece<1>(); AT_SB();
#pragma unroll
  for (int i = 0; i < 4; ++i) {
    f0[i] = at_kfrag(Kt, 3, i, r, h);
    st[0] = __builtin_amdgcn_mfma_f32_32x32x16_bf16(f1[i], q[4 + i], st[0], 0, 0, 0); AT_SB();
  }
  ld.piece<2>(); AT_SB();
  if (pfw && it == 5) { asm volatile("" :: "v"(st[0][0])); STAMP(4, 2); }
  {
    const float pm = at_max(st[0], 0, last, lim);
    if (__builtin_amdgcn_ballot_w64(pm > m) != 0) {
      const float mn = fmaxf(m, pm), alpha = __builtin_amdgcn_exp2f((m - mn) * AT_C);
      m = mn; l *= alpha;
      at_scale(o, alpha);
    }
  }
  float mc = -m * AT_C, ps = 0.f;
  AT_SB();
#pragma unroll
  for (int i = 0; i < 4; ++i) {
    f1[i] = at_vfrag(Vt, 0, i, r, h);
    st[1] = __builtin_amdgcn_mfma_f32_32x32x16_bf16(f2[i], q[i], st[1], 0, 0, 0);
    pw[0][i] = at_exp2(st[0], 2 * i, mc, ps); AT_SB();
  }
  ld.piece<3>(); AT_SB();
#pragma unroll
  for (int i = 0; i < 4; ++i) {
    f2[i] = at_vfrag(Vt, 1, i, r, h);
    st[1] = __builtin_amdgcn_mfma_f32_32x32x16_bf16(f0[i], q[4 + i], st[1], 0, 0, 0);
    pw[1][i] = at_exp2(st[0], 8 + 2 * i, mc, ps); AT_SB();
  }
  ld.piece<4>(); AT_SB();
  if (pfw && it == 5) { asm volatile("" :: "v"(st[1][0])); STAMP(4, 3); }
  l += ps;
  float alpha1 = 1.f;
  bool resc;
  {
    const float pm = at_max(st[1], 1, last, lim);
    resc = __builtin_amdgcn_ballot_w64(pm > m) != 0;
    const float mn = fmaxf(m, pm);
    alpha1 = __builtin_amdgcn_exp2f((m - mn) * AT_C);
    m = mn; l *= alpha1;
  }
  mc = -m * AT_C; ps = 0.f;
  AT_SB();
  {
    const bf16x8 p0 = __builtin_bit_cast(bf16x8, pw[0]), p1 = __builtin_bit_cast(bf16x8, pw[1]);
#pragma unroll
    for (int i = 0; i < 4; ++i) {
      f0[i] = at_vfrag(Vt, 2, i, r, h);
      o[i >> 1] = __builtin_amdgcn_mfma_f32_32x32x16_bf16(f1[i], (i & 1) ? p1 : p0, o[i >> 1], 0, 0, 0);
      pw[2][i] = at_exp2(st[1], 2 * i, mc, ps); AT_SB();
    }
    ld.piece<5>(); AT_SB();
#pragma unroll
    for (int i = 0; i < 4; ++i) {
      f1[i] = at_vfrag(Vt, 3, i, r, h);
      o[2 + (i >> 1)] = __builtin_amdgcn_mfma_f32_32x32x16_bf16(f2[i], (i & 1) ? p1 : p0, o[2 + (i >> 1)], 0, 0, 0);
      pw[3][i] = at_exp2(st[1], 8 + 2 * i, mc, ps); AT_SB();
    }
  }
  l += ps;
  ld.piece<6>(); AT_SB();
  if (pfw && it == 5) { asm volatile("" :: "v"(pw[3][3])); STAMP(4, 4); }
  if (resc) at_scale(o, alpha1);
  AT_SB();
  {
    const bf16x8 p2 = __builtin_bit_cast(bf16x8, pw[2]), p3 = __builtin_bit_cast(bf16x8, pw[3]);
#pragma unroll
    for (int i = 0; i < 4; ++i) o[i >> 1] = __builtin_amdgcn_mfma_f32_32x32x16_bf16(f0[i], (i & 1) ? p3 : p2, o[i >> 1], 0, 0, 0);
    AT_SB(); ld.piece<7>(); AT_SB();
#pragma unroll
    for (int i = 0; i < 4; ++i) o[2 + (i >> 1)] = __builtin_amdgcn_mfma_f32_32x32x16_bf16(f1[i], (i & 1) ? p3 : p2, o[2 + (i >> 1)], 0, 0, 0);
  }
}
__global__ __launch_bounds__(512, 2) void k_attn(const char* __restrict__ Qf, const char* __restrict__ Ki,
                                                 const char* __restrict__ Vi, char* __restrict__ ctx,
                                                 const u16* __restrict__ wo, const u16* __restrict__ win,
                                                 const u16* __restrict__ wsp, const u16* __restrict__ wout) {
  __shared__ __attribute__((aligned(16))) char smem[AT_DUMMY + 256];
  const int b = blockIdx.x, xcd = b & 7, j = b >> 3;
  const int head = xcd * 4 + (j & 3), pair = j >> 2;
  const int tid = threadIdx.x, wave = __builtin_amdgcn_readfirstlane(tid >> 6), lane = tid & 63;
  const int s = wave >> 2, g = wave & 3, r = lane & 31, h = lane >> 5;
  const char* Kh = Ki + (size_t)head * (32 * 16384) + wave * 4096;
  const char* Vh = Vi + (size_t)head * (32 * 16384) + wave * 4096;
  const int lim = 32 * g + r - 64 * s - 4 * h;

  for (int qi = 0; qi < 2; ++qi) {
    const int qb = qi ? pair : 15 - pair;
    const int n = qb + 1;
    STAMP(5, qi * 4);
    {
      AtLoad ld{Kh, Vh, lds_addr(smem) + wave * 4096, (unsigned)lane * 16u, true};
      ld.all();
    }
    bf16x8 q[8];
    {
      const char* qp = Qf + ((size_t)head * 64 + qb * 4 + g) * 8192 + lane * 16;
#pragma unroll
      for (int ks = 0; ks < 8; ++ks) q[ks] = *reinterpret_cast<const bf16x8*>(qp + ks * 1024);
#pragma unroll
      for (int ks = 0; ks < 8; ++ks) asm volatile("" : "+v"(q[ks]));
    }
    f32x16 o[4];
#pragma unroll
    for (int dt = 0; dt < 4; ++dt)
#pragma unroll
      for (int i = 0; i < 16; ++i) o[dt][i] = 0.f;
    float m = -1e30f, l = 0.f;
    STAMP(5, qi * 4 + 1);
    for (int it = 0; it < n; ++it) {
      at_iter(smem, Kh, Vh, q, o, m, l, it, n, wave, lane, lim, qi == 0);
      if (qi == 0 && it == 0) {
        const int slot = b * 8 + wave;
        const u16* base = slot < 1024 ? wsp : slot < 1536 ? wo : slot < 1708 ? win : wout;
        const int nb = slot < 1024 ? S * S * 2 : slot < 1536 ? HDM * DM * 2 : slot < 1708 ? DM * FF * 2 : FH * DM * 2;
        const int first = slot < 1024 ? slot : slot < 1536 ? slot - 1024 : slot < 1708 ? slot - 1536 : slot - 1708;
        pf_slice(base, nb, first * 64, lane, smem + AT_DUMMY);
      }
    }
    STAMP(5, qi * 4 + 2);
    barrier();
    l += __shfl_xor(l, 32);
    float* xo = reinterpret_cast<float*>(smem) + g * (64 * 68);
    if (s == 1) {
#pragma unroll
      for (int dt = 0; dt < 4; ++dt)
#pragma unroll
        for (int qd = 0; qd < 4; ++qd) {
          f32x4 v = {o[dt][4 * qd], o[dt][4 * qd + 1], o[dt][4 * qd + 2], o[dt][4 * qd + 3]};
          *reinterpret_cast<f32x4*>(xo + ((dt * 4 + qd) * 64 + lane) * 4) = v;
        }
      f32x4 v = {m, l, 0.f, 0.f};
      *reinterpret_cast<f32x4*>(xo + (16 * 64 + lane) * 4) = v;
    }
    barrier();
    if (s == 0) {
      f32x4 ml = *reinterpret_cast<const f32x4*>(xo + (16 * 64 + lane) * 4);
      const float mt = fmaxf(m, ml[0]);
      const float a0 = __builtin_amdgcn_exp2f((m - mt) * AT_C), a1 = __builtin_amdgcn_exp2f((ml[0] - mt) * AT_C);
      const float inv = 1.f / (l * a0 + ml[1] * a1);
      const float f0 = a0 * inv, f1 = a1 * inv;
      char* crow = ctx + ((size_t)(qb * 8 + g * 2 + (r >> 4)) * 128 + head * 4) * 1024 + (r & 15) * 16 + h * 8;
#pragma unroll
      for (int dt = 0; dt < 4; ++dt)
#pragma unroll
        for (int qd = 0; qd < 4; ++qd) {
          f32x4 v = *reinterpret_cast<const f32x4*>(xo + ((dt * 4 + qd) * 64 + lane) * 4);
          u32x2 w;
          w[0] = pack2(o[dt][4 * qd] * f0 + v[0] * f1, o[dt][4 * qd + 1] * f0 + v[1] * f1);
          w[1] = pack2(o[dt][4 * qd + 2] * f0 + v[2] * f1, o[dt][4 * qd + 3] * f0 + v[3] * f1);
          *reinterpret_cast<u32x2*>(crow + dt * 1024 + qd * 256) = w;
        }
    }
    barrier();
    STAMP(5, qi * 4 + 3);
  }
}

constexpr int SK_AREA = 16384;
template <int NT> struct SkOwn { static constexpr int N = (2 * NT + 3) / 4; };
template <int NT>
DI void splitk_reduce(const f32x4 (&acc)[4][NT], f32x4 (&own)[SkOwn<NT>::N], char* smem, int wave, int lane) {
  const int hi = wave >> 2, wq = wave & 3;
  char* mine = smem + wave * SK_AREA + lane * 16;
  f32x4 r[2 * NT];
#pragma unroll
  for (int f = 0; f < 2 * NT; ++f) {
    const f32x4 lo = acc[f / NT][f % NT], up = acc[2 + f / NT][f % NT];
    f32x4 snd, kp;
#pragma unroll
    for (int e = 0; e < 4; ++e) { snd[e] = hi ? lo[e] : up[e]; kp[e] = hi ? up[e] : lo[e]; }
    *reinterpret_cast<f32x4*>(mine + f * 1024) = snd;
    r[f] = kp;
  }
  barrier();
  {
    const char* part = smem + (wave ^ 4) * SK_AREA + lane * 16;
#pragma unroll
    for (int f = 0; f < 2 * NT; ++f) {
      const f32x4 o = *reinterpret_cast<const f32x4*>(part + f * 1024);
      r[f] += o;
    }
  }
  barrier();
#pragma unroll
  for (int f = 0; f < 2 * NT; ++f) *reinterpret_cast<f32x4*>(mine + f * 1024) = r[f];
  barrier();
#pragma unroll
  for (int i = 0; i < SkOwn<NT>::N; ++i) {
    const int f = wq + 4 * i;
    const char* p = smem + hi * 4 * SK_AREA + lane * 16 + (f < 2 * NT ? f : 0) * 1024;
    const f32x4 a = *reinterpret_cast<const f32x4*>(p), b = *reinterpret_cast<const f32x4*>(p + SK_AREA);
    const f32x4 c = *reinterpret_cast<const f32x4*>(p + 2 * SK_AREA), d = *reinterpret_cast<const f32x4*>(p + 3 * SK_AREA);
    own[i] = (a + b) + (c + d);
  }
}
DI void sk_bfrags(const char* img, bf16x8 (&bfr)[4], int lane) {
  const int fr = lane & 15, fg = lane >> 4, q = fr >> 2, p = lane & 3, sw = (fg & 1) << 2;
  const char* base = img + fg * 1024 + p * 8;
#pragma unroll
  for (int nt = 0; nt < 4; ++nt) {
    s16x4 lo = tr_read(base + nt * 256 + (q ^ sw) * 32);
    s16x4 hi = tr_read(base + nt * 256 + ((4 + q) ^ sw) * 32);
    bfr[nt] = __builtin_shufflevector(lo, hi, 0, 1, 2, 3, 4, 5, 6, 7);
  }
}

constexpr int OP_NS = 16, OP_DB = 4, OP_DA = 6;
constexpr int op_wait(int t) {
  int pos = 0, bpos = -1;
  for (int i = 0; i < OP_DA; ++i) {
    if (i < OP_DB) { pos += 4; if (i == t) bpos = pos; }
    pos += 4;
  }
  for (int s = 0; s < t; ++s) {
    if (s + OP_DB < OP_NS) { pos += 4; if (s + OP_DB == t) bpos = pos; }
    if (s + OP_DA < OP_NS) pos += 4;
  }
  return pos - bpos;
}
template <int T> struct OpWait { static constexpr int v = op_wait(T); };
template <int T> DI void op_step(f32x4 (&acc)[4][4], u32x4 (&areg)[OP_DA][4], char* ring, const char* asrc,
                                 const u16* bs0, const u16* bs1, int g0, int lane) {
  wait_vm<OpWait<T>::v>();
  if constexpr (T == 0) STAMP(2, 2);
  if constexpr (T == 8) STAMP(2, 3);
  bf16x8 bfr[4];
  sk_bfrags(ring + (T % OP_DB) * 4096, bfr, lane);
#pragma unroll
  for (int mt = 0; mt < 4; ++mt)
#pragma unroll
    for (int nt = 0; nt < 4; ++nt)
      acc[mt][nt] = __builtin_amdgcn_mfma_f32_16x16x32_bf16(bfr[nt], __builtin_bit_cast(bf16x8, areg[T % OP_DA][mt]), acc[mt][nt], 0, 0, 0);
  if constexpr (T + OP_DB < OP_NS) {
    asm volatile("s_waitcnt lgkmcnt(0)" ::: "memory");
    const int g = (g0 + T + OP_DB) & 127;
#pragma unroll
    for (int i = 0; i < 4; ++i)
      glds16(((i & 1) ? bs1 : bs0) + (size_t)(g * 32 + i * 8) * DM, ring + (T % OP_DB) * 4096 + i * 1024);
  }
  if constexpr (T + OP_DA < OP_NS) {
    const int g = (g0 + T + OP_DA) & 127;
#pragma unroll
    for (int mt = 0; mt < 4; ++mt)
      areg[T % OP_DA][mt] = *reinterpret_cast<const u32x4*>(asrc + (size_t)mt * (128 * 1024) + g * 1024);
  }
  if constexpr (T + 1 < OP_NS) op_step<T + 1>(acc, areg, ring, asrc, bs0, bs1, g0, lane);
}
__global__ __launch_bounds__(512, 2) void k_oproj(const char* __restrict__ ctxf, const u16* __restrict__ wo,
                                                  const u16* __restrict__ x, const float* __restrict__ ga,
                                                  u16* __restrict__ x1) {
  __shared__ __attribute__((aligned(16))) char smem[8 * SK_AREA + 256];
  const int b = blockIdx.x, xcd = b & 7, j = b >> 3;
  const int mtile = xcd * 4 + (j >> 3), ntile = j & 7;
  const int tid = threadIdx.x, wave = __builtin_amdgcn_readfirstlane(tid >> 6), lane = tid & 63;
  const int m0 = mtile * 64, n0 = ntile * 64;
  char* ring = smem + wave * SK_AREA;
  const char* asrc = ctxf + (size_t)(mtile * 4) * 128 * 1024 + lane * 16;
  const int g0 = wave * 16;
  const int rw = (lane & 15) >> 1;
  const u16* bs0 = wo + (size_t)rw * DM + n0 + (lane >> 4) * 16 + (lane & 1) * 8;
  const u16* bs1 = wo + (size_t)(rw ^ 4) * DM + n0 + (lane >> 4) * 16 + (lane & 1) * 8;
  f32x4 acc[4][4];
#pragma unroll
  for (int i = 0; i < 4; ++i)
#pragma unroll
    for (int jj = 0; jj < 4; ++jj) acc[i][jj] = f32x4{0.f, 0.f, 0.f, 0.f};
  u32x4 areg[OP_DA][4];
  STAMP(2, 0);
#pragma unroll
  for (int i = 0; i < OP_DA; ++i) {
    const int g = (g0 + i) & 127;
    if (i < OP_DB) {
#pragma unroll
      for (int ii = 0; ii < 4; ++ii) glds16(((ii & 1) ? bs1 : bs0) + (size_t)(g * 32 + ii * 8) * DM, ring + i * 4096 + ii * 1024);
    }
#pragma unroll
    for (int mt = 0; mt < 4; ++mt)
      areg[i][mt] = *reinterpret_cast<const u32x4*>(asrc + (size_t)mt * (128 * 1024) + g * 1024);
  }
  STAMP(2, 1);
  op_step<0>(acc, areg, ring, asrc, bs0, bs1, g0, lane);
  STAMP(2, 4);
  const int fr = lane & 15, fg = lane >> 4, hi = wave >> 2, wq = wave & 3;
  u32x2 xv[2];
  f32x4 gv;
  {
    gv = *reinterpret_cast<const f32x4*>(ga + n0 + wq * 16 + fg * 4);
#pragma unroll
    for (int i = 0; i < 2; ++i)
      xv[i] = *reinterpret_cast<const u32x2*>(x + (size_t)(m0 + (2 * hi + i) * 16 + fr) * DM + n0 + wq * 16 + fg * 4);
  }
  f32x4 own[2];
  splitk_reduce<4>(acc, own, smem, wave, lane);
#pragma unroll
  for (int i = 0; i < 2; ++i) {
    u32x2 w;
    w[0] = pack2(bflo(xv[i][0]) + gv[0] * own[i][0], bfhi(xv[i][0]) + gv[1] * own[i][1]);
    w[1] = pack2(bflo(xv[i][1]) + gv[2] * own[i][2], bfhi(xv[i][1]) + gv[3] * own[i][3]);
    *reinterpret_cast<u32x2*>(x1 + (size_t)(m0 + (2 * hi + i) * 16 + fr) * DM + n0 + wq * 16 + fg * 4) = w;
  }
  STAMP(2, 5);
}

constexpr int NBLK = 43;
using G4 = Gemm<64, 192, 1, 2, 4>;
DI float gelu_tanh(float x) {
  constexpr float K1 = -2.f * 0.7978845608028654f * 1.4426950408889634f, K2 = K1 * 0.044715f;
  const float e = __builtin_amdgcn_exp2f(x * fmaf(x * x, K2, K1));
  return x * __builtin_amdgcn_rcpf(1.f + e);
}
DI u32x4 gload_asm(const void* p) {
  u32x4 r;
  asm volatile("global_load_dwordx4 %0, %1, off" : "=v"(r) : "v"(p) : "memory");
  return r;
}
template <bool Z2>
DI void win_body(char* smem, const u16* __restrict__ x1, const float* __restrict__ sb, const float* __restrict__ cb,
                 const u16* __restrict__ win, char* __restrict__ zf, float* __restrict__ stats, int mtile, int t4) {
  char* sB = smem + 65536;
  const int tid = threadIdx.x, wave = __builtin_amdgcn_readfirstlane(tid >> 6), lane = tid & 63;
  const int wm = wave >> 2, wn = wave & 3;
  const int m0 = mtile * 64, n0 = (Z2 ? FH : 0) + t4 * 176;
  STAMP(1, 0);
  u32x4 c0 = gload_asm(cb + lane * 8), c1 = gload_asm(cb + lane * 8 + 4);
  u32x4 s0 = gload_asm(sb + lane * 8), s1 = gload_asm(sb + lane * 8 + 4);
  {
    const int rr = lane >> 3, ch = (lane & 7) ^ rr;
    const u16* src = x1 + (size_t)(m0 + wave * 8 + rr) * DM + ch * 8;
#pragma unroll
    for (int sub = 0; sub < 8; ++sub) glds16(src + sub * 64, smem + sub * 8192 + wave * 1024);
  }
#pragma unroll
  for (int st = 0; st < 4; ++st) G4::stageB(win, FF, st * 64, n0, FF - 8, sB + st * G4::B_BYTES, wave, lane);
  u32x4 breg[4][3];
  {
    const int pos = lane & 15, rw = pos >> 1, hf = pos & 1;
    const u16* src[3];
#pragma unroll
    for (int ii = 0; ii < 3; ++ii) {
      const int blk = (ii * 8 + wave) * 4 + (lane >> 4), kb = blk / G4::NB, nb = blk % G4::NB;
      int n = n0 + nb * 16 + hf * 8;
      if (n > FF - 8) n = FF - 8;
      src[ii] = win + (size_t)(256 + kb * 8 + (rw ^ ((kb & 1) << 2))) * FF + n;
    }
#pragma unroll
    for (int st = 0; st < 4; ++st) {
#pragma unroll
      for (int ii = 0; ii < 3; ++ii) breg[st][ii] = *reinterpret_cast<const u32x4*>(src[ii] + (size_t)st * 64 * FF);
      __builtin_amdgcn_sched_barrier(0);
    }
  }
  asm volatile("s_waitcnt vmcnt(24)" : "+v"(c0), "+v"(c1), "+v"(s0), "+v"(s1)::"memory");
  STAMP(1, 1);
  {
    float cbv[8], sbv[8];
#pragma unroll
    for (int jj = 0; jj < 4; ++jj) {
      cbv[jj] = 1.f + __uint_as_float(c0[jj]); cbv[4 + jj] = 1.f + __uint_as_float(c1[jj]);
      sbv[jj] = __uint_as_float(s0[jj]); sbv[4 + jj] = __uint_as_float(s1[jj]);
    }
    u32x4 xr[8];
#pragma unroll
    for (int i = 0; i < 8; ++i)
      xr[i] = *reinterpret_cast<const u32x4*>(smem + (lane >> 3) * 8192 + (wave * 8 + i) * 128 + (((lane & 7) ^ i) << 4));
#pragma unroll
    for (int i = 0; i < 8; ++i) {
      float f[8];
#pragma unroll
      for (int jj = 0; jj < 4; ++jj) { f[2 * jj] = bflo(xr[i][jj]); f[2 * jj + 1] = bfhi(xr[i][jj]); }
      float s = 0.f;
#pragma unroll
      for (int jj = 0; jj < 8; ++jj) s += f[jj];
      const float mean = wave_sum(s) * (1.f / DM);
      float v = 0.f;
#pragma unroll
      for (int jj = 0; jj < 8; ++jj) { f[jj] -= mean; v += f[jj] * f[jj]; }
      const float rstd = rsqrtf(wave_sum(v) * (1.f / DM) + 1e-5f);
#pragma unroll
      for (int jj = 0; jj < 4; ++jj)
        xr[i][jj] = pack2(f[2 * jj] * rstd * cbv[2 * jj] + sbv[2 * jj], f[2 * jj + 1] * rstd * cbv[2 * jj + 1] + sbv[2 * jj + 1]);
    }
#pragma unroll
    for (int i = 0; i < 8; ++i)
      *reinterpret_cast<u32x4*>(smem + (lane >> 3) * 8192 + (wave * 8 + i) * 128 + (((lane & 7) ^ i) << 4)) = xr[i];
  }
  STAMP(1, 2);
  f32x4 acc[G4::MT][G4::NT];
#pragma unroll
  for (int i = 0; i < G4::MT; ++i)
#pragma unroll
    for (int jj = 0; jj < G4::NT; ++jj) acc[i][jj] = f32x4{0.f, 0.f, 0.f, 0.f};
#pragma unroll
  for (int kt = 0; kt < 8; ++kt) {
    if (kt == 0) wait_vm<21>(); else if (kt == 1) wait_vm<18>(); else if (kt == 2) wait_vm<15>(); else if (kt == 3) wait_vm<12>();
    barrier();
    if (kt == 0) STAMP(1, 3);
    if (kt == 1) STAMP(1, 4);
    if (kt == 3) STAMP(1, 5);
    if (kt == 5) STAMP(1, 6);
    if (kt >= 1 && kt + 3 < 8) {
#pragma unroll
      for (int ii = 0; ii < 3; ++ii)
        *reinterpret_cast<u32x4*>(sB + ((kt + 3) & 3) * G4::B_BYTES + (ii * 8 + wave) * 1024 + lane * 16) = breg[kt - 1][ii];
    }
    G4::compute<!Z2>(smem + kt * 8192, sB + (kt & 3) * G4::B_BYTES, acc, wm, wn, lane);
  }
  STAMP(1, 7);
  const int fr = lane & 15, fg = lane >> 4;
  if constexpr (!Z2) {
#pragma unroll
    for (int nt = 0; nt < G4::NT; ++nt) {
      const int nbl = wn * 3 + nt, nb = t4 * 11 + nbl;
      if (nbl < 11 && nb < NBLK) {
#pragma unroll
        for (int mt = 0; mt < G4::MT; ++mt) {
          u32x2 w;
          w[0] = pack2(gelu_tanh(acc[mt][nt][0]), gelu_tanh(acc[mt][nt][1]));
          w[1] = pack2(gelu_tanh(acc[mt][nt][2]), gelu_tanh(acc[mt][nt][3]));
          *reinterpret_cast<u32x2*>(zf + ((size_t)(mtile * 4 + wm * 2 + mt) * NBLK + nb) * 512 + lane * 8) = w;
        }
      }
    }
  } else {
    float p1[G4::MT][4], p2[G4::MT][4];
#pragma unroll
    for (int mt = 0; mt < G4::MT; ++mt)
#pragma unroll
      for (int i = 0; i < 4; ++i) { p1[mt][i] = 0.f; p2[mt][i] = 0.f; }
#pragma unroll
    for (int nt = 0; nt < G4::NT; ++nt) {
      const int nbl = wn * 3 + nt, nb = t4 * 11 + nbl;
      if (nbl < 11 && nb < NBLK) {
#pragma unroll
        for (int mt = 0; mt < G4::MT; ++mt) {
          float gz[4];
#pragma unroll
          for (int i = 0; i < 4; ++i) {
            gz[i] = rbf(gelu_tanh(acc[mt][nt][i]));
            p1[mt][i] += gz[i]; p2[mt][i] += gz[i] * gz[i];
          }
          u32x2 w;
          w[0] = pack2(gz[0], gz[1]);
          w[1] = pack2(gz[2], gz[3]);
          *reinterpret_cast<u32x2*>(zf + ((size_t)(mtile * 2 + wm) * NBLK + nb) * 1024 + (fr + 16 * (mt * 2 + (fg >> 1))) * 16 + (fg & 1) * 8) = w;
        }
      }
    }
    barrier();
    float* red = reinterpret_cast<float*>(smem);
#pragma unroll
    for (int mt = 0; mt < G4::MT; ++mt)
#pragma unroll
      for (int i = 0; i < 4; ++i) {
        float a = p1[mt][i], c = p2[mt][i];
        a = row16_sum(a); c = row16_sum(c);
        if (fr == 0) {
          const int rowl = wm * 32 + mt * 16 + fg * 4 + i;
          red[(wn * 64 + rowl) * 2] = a; red[(wn * 64 + rowl) * 2 + 1] = c;
        }
      }
    barrier();
    if (tid < 128) {
      const float v = (red[tid] + red[128 + tid]) + (red[256 + tid] + red[384 + tid]);
      stats[((size_t)t4 * S + m0) * 2 + tid] = v;
    }
  }
}
__global__ __launch_bounds__(512, 2) void k_win(const u16* __restrict__ x1, const float* __restrict__ sb,
                                                const float* __restrict__ cb, const u16* __restrict__ win,
                                                char* __restrict__ z1f, char* __restrict__ z2f,
                                                float* __restrict__ stats) {
  __shared__ __attribute__((aligned(16))) char smem[65536 + 4 * G4::B_BYTES];
  const int b = blockIdx.x, xcd = b & 7, j = b >> 3;
  const int mtile = xcd * 4 + (j >> 3), ntile = j & 7;
  if (ntile >= 4) win_body<true>(smem, x1, sb, cb, win, z2f, stats, mtile, ntile - 4);
  else win_body<false>(smem, x1, sb, cb, win, z1f, stats, mtile, ntile);
}

constexpr int UKS = 22;
template <int NT>
DI void spatial_body(char* smem, const u16* __restrict__ wsp, const char* __restrict__ z2f,
                     const float* __restrict__ stats, const u16* __restrict__ bsp, const char* __restrict__ z1f,
                     char* __restrict__ uf, int mtile, int nb0, int rot) {
  const int tid = threadIdx.x, wave = __builtin_amdgcn_readfirstlane(tid >> 6), lane = tid & 63;
  const int fr = lane & 15, fg = lane >> 4;
  char* ring = smem + wave * SK_AREA;
  float* rs = reinterpret_cast<float*>(smem + 8 * SK_AREA + wave * 2048);
  float* ms = rs + 256;
  const int m0 = mtile * 64, g0 = wave * 4 + rot;
  STAMP(3, 0);
  f32x4 sp[4][2];
#pragma unroll
  for (int t = 0; t < 4; ++t) {
    const float* p = stats + ((size_t)t * S + ((g0 + (lane >> 4)) & 31) * 64 + (lane & 15) * 4) * 2;
    sp[t][0] = *reinterpret_cast<const f32x4*>(p);
    sp[t][1] = *reinterpret_cast<const f32x4*>(p + 4);
  }
  const int rr = lane >> 3, ch = (lane & 7) ^ rr;
  const u16* asrc = wsp + (size_t)(m0 + rr) * S + ch * 8;
  auto stageA = [&](int s) {
#pragma unroll
    for (int i = 0; i < 8; ++i) glds16(asrc + (size_t)i * 8 * S + ((g0 + s) & 31) * 64, ring + (s & 1) * 8192 + i * 1024);
  };
  const char* bsrc = z2f + (size_t)nb0 * 1024 + lane * 16;
  u32x4 breg[3][NT];
  auto loadB = [&](int t) {
#pragma unroll
    for (int nt = 0; nt < NT; ++nt) breg[t % 3][nt] = *reinterpret_cast<const u32x4*>(bsrc + (((((g0 + (t >> 1)) & 31) * 2 + (t & 1)) * NBLK) + nt) * 1024);
  };
  stageA(0); loadB(0); loadB(1); stageA(1); loadB(2);
  STAMP(3, 1);
  {
    f32x4 rv, mv;
#pragma unroll
    for (int i = 0; i < 4; ++i) {
      float s1 = 0.f, s2 = 0.f;
#pragma unroll
      for (int t = 0; t < 4; ++t) { s1 += sp[t][i >> 1][(i & 1) * 2]; s2 += sp[t][i >> 1][(i & 1) * 2 + 1]; }
      const float mean = s1 * (1.f / FH);
      const float var = fmaxf(s2 * (1.f / FH) - mean * mean, 0.f);
      const float rstd = rsqrtf(var + 1e-5f);
      rv[i] = rstd; mv[i] = -mean * rstd;
    }
    *reinterpret_cast<f32x4*>(rs + lane * 4) = rv;
    *reinterpret_cast<f32x4*>(ms + lane * 4) = mv;
  }
  f32x4 acc[4][NT];
#pragma unroll
  for (int i = 0; i < 4; ++i)
#pragma unroll
    for (int jj = 0; jj < NT; ++jj) acc[i][jj] = f32x4{0.f, 0.f, 0.f, 0.f};
#pragma unroll
  for (int s = 0; s < 4; ++s) {
    if (s < 2) wait_vm<3 * NT + 8>(); else if (s == 2) wait_vm<2 * NT + 8>(); else wait_vm<NT>();
    if (s == 0) STAMP(3, 2);
    if (s == 2) STAMP(3, 3);
#pragma unroll
    for (int kk = 0; kk < 2; ++kk) {
      const int t = 2 * s + kk;
      bf16x8 af[4];
#pragma unroll
      for (int mt = 0; mt < 4; ++mt)
        af[mt] = *reinterpret_cast<const bf16x8*>(ring + (s & 1) * 8192 + (mt * 16 + fr) * 128 + (((kk * 4 + fg) ^ (fr & 7)) << 4));
      f32x4 rv[2], mv[2];
#pragma unroll
      for (int e = 0; e < 2; ++e) {
        rv[e] = *reinterpret_cast<const f32x4*>(rs + t * 32 + fg * 8 + e * 4);
        mv[e] = *reinterpret_cast<const f32x4*>(ms + t * 32 + fg * 8 + e * 4);
      }
#pragma unroll
      for (int nt = 0; nt < NT; ++nt) {
        const u32x4 raw = breg[t % 3][nt];
        u32x4 o4;
#pragma unroll
        for (int jj = 0; jj < 4; ++jj)
          o4[jj] = pack2(fmaf(bflo(raw[jj]), rv[jj >> 1][(jj & 1) * 2], mv[jj >> 1][(jj & 1) * 2]),
                         fmaf(bfhi(raw[jj]), rv[jj >> 1][(jj & 1) * 2 + 1], mv[jj >> 1][(jj & 1) * 2 + 1]));
        const bf16x8 bv = __builtin_bit_cast(bf16x8, o4);
#pragma unroll
        for (int mt = 0; mt < 4; ++mt)
          acc[mt][nt] = __builtin_amdgcn_mfma_f32_16x16x32_bf16(bv, af[mt], acc[mt][nt], 0, 0, 0);
      }
      if (t + 3 < 8) loadB(t + 3);
    }
    if (s + 2 < 4) {
      asm volatile("s_waitcnt lgkmcnt(0)" ::: "memory");
      stageA(s + 2);
    }
  }
  STAMP(3, 4);
  constexpr int NO = SkOwn<NT>::N;
  const int hi = wave >> 2, wq = wave & 3;
  u32x2 zv[NO];
  float bs[NO];
#pragma unroll
  for (int i = 0; i < NO; ++i) {
    int f = wq + 4 * i;
    if (f >= 2 * NT) f = 0;
    const int mt = 2 * hi + f / NT, nb = nb0 + f % NT;
    zv[i] = *reinterpret_cast<const u32x2*>(z1f + ((size_t)(mtile * 4 + mt) * NBLK + nb) * 512 + lane * 8);
    bs[i] = bf2f(bsp[m0 + mt * 16 + fr]);
  }
  f32x4 own[NO];
  splitk_reduce<NT>(acc, own, smem, wave, lane);
#pragma unroll
  for (int i = 0; i < NO; ++i) {
    const int f = wq + 4 * i;
    if (f < 2 * NT) {
      const int mt = 2 * hi + f / NT, nb = nb0 + f % NT;
      u32x2 w;
      w[0] = pack2(bflo(zv[i][0]) * (own[i][0] + bs[i]), bfhi(zv[i][0]) * (own[i][1] + bs[i]));
      w[1] = pack2(bflo(zv[i][1]) * (own[i][2] + bs[i]), bfhi(zv[i][1]) * (own[i][3] + bs[i]));
      char* dst = uf + ((size_t)(mtile * 4 + mt) * UKS + (nb >> 1)) * 1024 + (fr + 16 * (fg >> 1)) * 16 + (fg & 1) * 8;
      *reinterpret_cast<u32x2*>(dst + (nb & 1) * 512) = w;
      if (nb == NBLK - 1) *reinterpret_cast<u32x2*>(dst + 512) = u32x2{0u, 0u};
    }
  }
  STAMP(3, 5);
}
__global__ __launch_bounds__(512, 2) void k_spatial(const u16* __restrict__ wsp, const char* __restrict__ z2f,
                                                    const float* __restrict__ stats, const u16* __restrict__ bsp,
                                                    const char* __restrict__ z1f, char* __restrict__ uf) {
  __shared__ __attribute__((aligned(16))) char smem[8 * SK_AREA + 8 * 2048 + 256];
  const int b = blockIdx.x, xcd = b & 7, j = b >> 3;
  const int mtile = xcd * 4 + (j >> 3), ntile = j & 7;
  const int rot = 0;
  if (ntile < 5) spatial_body<5>(smem, wsp, z2f, stats, bsp, z1f, uf, mtile, ntile * 5, rot);
  else spatial_body<6>(smem, wsp, z2f, stats, bsp, z1f, uf, mtile, 25 + (ntile - 5) * 6, rot);
}

__global__ __launch_bounds__(512, 2) void k_wout(const char* __restrict__ uf, const u16* __restrict__ wout,
                                                 const u16* __restrict__ x1, const float* __restrict__ gb,
                                                 u16* __restrict__ out) {
  __shared__ __attribute__((aligned(16))) char smem[8 * SK_AREA];
  const int b = blockIdx.x, xcd = b & 7, j = b >> 3;
  const int mtile = xcd * 4 + (j >> 3), ntile = j & 7;
  const int tid = threadIdx.x, wave = __builtin_amdgcn_readfirstlane(tid >> 6), lane = tid & 63;
  const int m0 = mtile * 64, n0 = ntile * 64;
  char* ring = smem + wave * SK_AREA;
  const int ks0 = wave < 6 ? wave * 3 : 18 + (wave - 6) * 2, nks = wave < 6 ? 3 : 2;
  const int rw = (lane & 15) >> 1;
  u32x4 areg[3][4];
#pragma unroll
  for (int t = 0; t < 3; ++t) {
    const int ks = ks0 + (t < nks ? t : 0);
#pragma unroll
    for (int i = 0; i < 4; ++i) {
      int row = ks * 32 + i * 8 + ((i & 1) ? (rw ^ 4) : rw);
      if (row > FH - 1) row = FH - 1;
      glds16(wout + (size_t)row * DM + n0 + (lane >> 4) * 16 + (lane & 1) * 8, ring + t * 4096 + i * 1024);
    }
#pragma unroll
    for (int mt = 0; mt < 4; ++mt)
      areg[t][mt] = *reinterpret_cast<const u32x4*>(uf + ((size_t)(mtile * 4 + mt) * UKS + ks) * 1024 + lane * 16);
  }
  const int fr = lane & 15, fg = lane >> 4, hi = wave >> 2, wq = wave & 3;
  u32x2 xv[2];
  const f32x4 gv = *reinterpret_cast<const f32x4*>(gb + n0 + wq * 16 + fg * 4);
#pragma unroll
  for (int i = 0; i < 2; ++i)
    xv[i] = *reinterpret_cast<const u32x2*>(x1 + (size_t)(m0 + (2 * hi + i) * 16 + fr) * DM + n0 + wq * 16 + fg * 4);
  f32x4 acc[4][4];
#pragma unroll
  for (int i = 0; i < 4; ++i)
#pragma unroll
    for (int jj = 0; jj < 4; ++jj) acc[i][jj] = f32x4{0.f, 0.f, 0.f, 0.f};
  wait_vm<0>();
#pragma unroll
  for (int t = 0; t < 3; ++t) {
    if (t < nks) {
      bf16x8 bfr[4];
      sk_bfrags(ring + t * 4096, bfr, lane);
#pragma unroll
      for (int mt = 0; mt < 4; ++mt)
#pragma unroll
        for (int nt = 0; nt < 4; ++nt)
          acc[mt][nt] = __builtin_amdgcn_mfma_f32_16x16x32_bf16(bfr[nt], __builtin_bit_cast(bf16x8, areg[t][mt]), acc[mt][nt], 0, 0, 0);
    }
  }
  f32x4 own[2];
  splitk_reduce<4>(acc, own, smem, wave, lane);
#pragma unroll
  for (int i = 0; i < 2; ++i) {
    u32x2 w;
    w[0] = pack2(bflo(xv[i][0]) + gv[0] * own[i][0], bfhi(xv[i][0]) + gv[1] * own[i][1]);
    w[1] = pack2(bflo(xv[i][1]) + gv[2] * own[i][2], bfhi(xv[i][1]) + gv[3] * own[i][3]);
    *reinterpret_cast<u32x2*>(out + (size_t)(m0 + (2 * hi + i) * 16 + fr) * DM + n0 + wq * 16 + fg * 4) = w;
  }
}

extern "C" void kernel_launch(void* const* d_in, const int* in_sizes, int n_in, void* d_out, int out_size,
                              void* d_ws, size_t ws_size, hipStream_t stream) {
  const u16* x = (const u16*)d_in[0];
  const u16* ckv = (const u16*)d_in[1];
  const u16* wq = (const u16*)d_in[2];
  const u16* wk = (const u16*)d_in[3];
  const u16* wv = (const u16*)d_in[4];
  const u16* wo = (const u16*)d_in[5];
  const u16* win = (const u16*)d_in[7];
  const u16* wsp = (const u16*)d_in[8];
  const u16* bsp = (const u16*)d_in[9];
  const u16* wout = (const u16*)d_in[10];
  const u16* c = (const u16*)d_in[11];
  const u16* wmod = (const u16*)d_in[12];
  char* ws = (char*)d_ws;
  float* mod = (float*)(ws + WS_MOD);
  u16* h1 = (u16*)(ws + WS_H1);
  char* Qf = ws + WS_QF;
  char* Ki = ws + WS_KI;
  char* Vi = ws + WS_VI;
  char* ctx = ws + WS_CTX;
  u16* x1 = (u16*)(ws + WS_X1);
  char* z1 = ws + WS_Z1;
  char* z2 = ws + WS_Z2;
  float* stats = (float*)(ws + WS_ST);
  char* u = ws + WS_U;
  u16* out = (u16*)d_out;

  k_kv<<<256, 512, 0, stream>>>(ckv, wk, wv, Ki, Vi, c, wmod, mod);
  k_ln<<<S / 4, 256, 0, stream>>>(x, mod + 0 * DM, mod + 1 * DM, h1);
  k_qkv<<<512, 256, 0, stream>>>(h1, ckv, wq, wk, wv, Qf, Ki, Vi);
  k_attn<<<256, 512, 0, stream>>>(Qf, Ki, Vi, ctx, wo, win, wsp, wout);
  k_oproj<<<256, 512, 0, stream>>>(ctx, wo, x, mod + 2 * DM, x1);
  k_win<<<256, 512, 0, stream>>>(x1, mod + 3 * DM, mod + 4 * DM, win, z1, z2, stats);
  k_spatial<<<256, 512, 0, stream>>>(wsp, z2, stats, bsp, z1, u);
  k_wout<<<256, 512, 0, stream>>>(u, wout, x1, mod + 5 * DM, out);
}
```

```cpp
#include <hip/hip_runtime.h>
#include <hip/hip_bf16.h>
#include <stdint.h>

constexpr int S = 2048;
constexpr int DM = 512;
constexpr int NH = 32;
constexpr int HD = 128;
constexpr int HDM = NH * HD;
constexpr int FH = 688;
constexpr int FF = 1376;

typedef __attribute__((ext_vector_type(8))) short bf16x8;
typedef __attribute__((ext_vector_type(4))) short s16x4;
typedef __attribute__((ext_vector_type(4))) float f32x4;
typedef __attribute__((ext_vector_type(16))) float f32x16;
typedef __attribute__((ext_vector_type(4))) unsigned u32x4;
typedef __attribute__((ext_vector_type(2))) unsigned u32x2;
typedef __attribute__((address_space(3))) s16x4* lds_s16x4_ptr;
typedef unsigned short u16;

#define DI __device__ __forceinline__

DI float bf2f(u16 v) { return __uint_as_float(((unsigned)v) << 16); }
DI float bflo(unsigned v) { return __uint_as_float(v << 16); }
DI float bfhi(unsigned v) { return __uint_as_float(v & 0xffff0000u); }
typedef __attribute__((ext_vector_type(2))) __bf16 bf16x2_t;
typedef __attribute__((ext_vector_type(2))) float f32x2;
DI unsigned pack2(float lo, float hi) {
  f32x2 v = {lo, hi};
  return __builtin_bit_cast(unsigned, __builtin_convertvector(v, bf16x2_t));
}
template <int N> DI void wait_vm() { asm volatile("s_waitcnt vmcnt(%0)" ::"n"(N) : "memory"); }
DI void barrier() { asm volatile("s_waitcnt lgkmcnt(0)" ::: "memory"); __builtin_amdgcn_s_barrier(); }
DI float rbf(float x) { return bflo(pack2(x, 0.f)); }

DI void glds16(const void* g, const void* lds_wave_base) {
  const unsigned a = __builtin_amdgcn_readfirstlane((unsigned)(size_t)lds_wave_base);
  unsigned keep;
  asm volatile("s_mov_b32 %0, m0\n\ts_mov_b32 m0, %2\n\ts_nop 0\n\tglobal_load_lds_dwordx4 %1, off\n\ts_mov_b32 m0, %0"
               : "=&s"(keep) : "v"(g), "s"(a) : "memory");
}
template <int OFF> DI void glds16s(const void* sbase, unsigned voff, unsigned lds_base) {
  asm volatile("s_mov_b32 m0, %2\n\ts_nop 0\n\tglobal_load_lds_dwordx4 %0, %1 offset:%3"
               : : "v"(voff), "s"(sbase), "s"(lds_base), "n"(OFF) : "memory");
}
DI unsigned lds_addr(const void* p) { return __builtin_amdgcn_readfirstlane((unsigned)(size_t)p); }
DI void pf_line(const void* g, const void* lds_dummy) {
  const unsigned a = __builtin_amdgcn_readfirstlane((unsigned)(size_t)lds_dummy);
  unsigned keep;
  asm volatile("s_mov_b32 %0, m0\n\ts_mov_b32 m0, %2\n\ts_nop 0\n\tglobal_load_lds_dword %1, off\n\ts_mov_b32 m0, %0"
               : "=&s"(keep) : "v"(g), "s"(a) : "memory");
}
DI void pf_slice(const void* base, int nbytes, int first, int lane, const void* lds_dummy) {
  int l = first + lane;
  const int last = (nbytes >> 7) - 1;
  if (l > last) l = last;
  pf_line((const char*)base + (size_t)l * 128, lds_dummy);
}
#define STAMP(k, i)
template <int CTRL> DI float dpp_add(float v) {
  return v + __uint_as_float(__builtin_amdgcn_update_dpp(0u, __float_as_uint(v), CTRL, 0xf, 0xf, true));
}
DI float row16_sum(float v) {
  v = dpp_add<0xB1>(v); v = dpp_add<0x4E>(v); v = dpp_add<0x141>(v); v = dpp_add<0x140>(v);
  return v;
}
DI float wave_sum(float v) {
  v = row16_sum(v);
  auto a = __builtin_amdgcn_permlane16_swap(__float_as_uint(v), __float_as_uint(v), false, false);
  v = __uint_as_float(a[0]) + __uint_as_float(a[1]);
  auto b = __builtin_amdgcn_permlane32_swap(__float_as_uint(v), __float_as_uint(v), false, false);
  return __uint_as_float(b[0]) + __uint_as_float(b[1]);
}
DI s16x4 tr_read(const char* p) {
  return __builtin_amdgcn_ds_read_tr16_b64_v4i16((lds_s16x4_ptr)(p));
}

constexpr size_t WS_MOD = 0;
constexpr size_t WS_H1 = 16384;
constexpr size_t WS_QF = WS_H1 + (size_t)S * DM * 2;
constexpr size_t WS_KI = WS_QF + (size_t)S * HDM * 2;
constexpr size_t WS_VI = WS_KI + (size_t)S * HDM * 2;
constexpr size_t WS_CTX = WS_VI + (size_t)S * HDM * 2;
constexpr size_t WS_X1 = WS_CTX + (size_t)S * HDM * 2;
constexpr size_t WS_Z1 = WS_X1 + (size_t)S * DM * 2;
constexpr size_t WS_Z2 = WS_Z1 + (size_t)S * FH * 2;
constexpr size_t WS_ST = WS_Z2 + (size_t)S * FH * 2;
constexpr size_t WS_U = WS_ST + (size_t)8 * S * 2 * 4;
constexpr int LDU = 704;

__global__ __launch_bounds__(256) void k_mod(const u16* __restrict__ c, const u16* __restrict__ w,
                                             float* __restrict__ mod) {
  __shared__ float red[64][33];
  const int t = threadIdx.x, cg = t & 3, ks = t >> 2;
  const int col0 = blockIdx.x * 32 + cg * 8;
  float acc[8];
#pragma unroll
  for (int j = 0; j < 8; ++j) acc[j] = 0.f;
#pragma unroll
  for (int kk = 0; kk < 8; ++kk) {
    const int k = ks * 8 + kk;
    float cv = bf2f(c[k]);
    float sv = rbf(cv / (1.f + __expf(-cv)));
    u32x4 wv = *reinterpret_cast<const u32x4*>(w + (size_t)k * 3072 + col0);
#pragma unroll
    for (int j = 0; j < 4; ++j) {
      acc[2 * j] += sv * bflo(wv[j]);
      acc[2 * j + 1] += sv * bfhi(wv[j]);
    }
  }
#pragma unroll
  for (int j = 0; j < 8; ++j) red[ks][cg * 8 + j] = acc[j];
  __syncthreads();
  if (t < 32) {
    float s = 0.f;
    for (int i = 0; i < 64; ++i) s += red[i][t];
    mod[blockIdx.x * 32 + t] = rbf(s);
  }
}

__global__ __launch_bounds__(256) void k_ln(const u16* __restrict__ x, const float* __restrict__ sh,
                                            const float* __restrict__ cs, u16* __restrict__ h) {
  const int row = blockIdx.x * 4 + (threadIdx.x >> 6), lane = threadIdx.x & 63;
  u32x4 xv = *reinterpret_cast<const u32x4*>(x + (size_t)row * DM + lane * 8);
  float f[8];
#pragma unroll
  for (int j = 0; j < 4; ++j) { f[2 * j] = bflo(xv[j]); f[2 * j + 1] = bfhi(xv[j]); }
  float s = 0.f;
#pragma unroll
  for (int j = 0; j < 8; ++j) s += f[j];
#pragma unroll
  for (int o = 32; o >= 1; o >>= 1) s += __shfl_xor(s, o);
  const float mean = s * (1.f / DM);
  float v = 0.f;
#pragma unroll
  for (int j = 0; j < 8; ++j) { f[j] -= mean; v += f[j] * f[j]; }
#pragma unroll
  for (int o = 32; o >= 1; o >>= 1) v += __shfl_xor(v, o);
  const float rstd = rsqrtf(v * (1.f / DM) + 1e-5f);
  u32x4 o4;
#pragma unroll
  for (int j = 0; j < 4; ++j) {
    const int k = lane * 8 + 2 * j;
    float a = f[2 * j] * rstd * (1.f + cs[k]) + sh[k];
    float b = f[2 * j + 1] * rstd * (1.f + cs[k + 1]) + sh[k + 1];
    o4[j] = pack2(a, b);
  }
  *reinterpret_cast<u32x4*>(h + (size_t)row * DM + lane * 8) = o4;
}

template <int BM, int BN, int KS, int NWM, int NWN>
struct Gemm {
  static constexpr int NW = NWM * NWN;
  static constexpr int WM = BM / NWM, WN = BN / NWN;
  static constexpr int MT = WM / 16, NT = WN / 16;
  static constexpr int BK = 64 * KS;
  static constexpr int A_BYTES = BM * BK * 2;
  static constexpr int B_BYTES = BK * BN * 2;
  static constexpr int STAGE_BYTES = A_BYTES + B_BYTES;
  static constexpr int NB = BN / 16;

  static DI void stageA(const u16* __restrict__ A, int lda, int m0, int k0, char* lds, int wave, int lane) {
    constexpr int NI = A_BYTES / 1024;
    const int rr = lane >> 3, ch = (lane & 7) ^ rr;
#pragma unroll
    for (int ii = 0; ii < NI / NW; ++ii) {
      const int i = ii * NW + wave;
      const int sub = i / (BM / 8), j = i % (BM / 8);
      const u16* src = A + (size_t)(m0 + j * 8 + rr) * lda + k0 + sub * 64 + ch * 8;
      glds16(src, lds + i * 1024);
    }
  }
  static DI void stageB(const u16* __restrict__ B, int ldb, int k0, int n0, int nmax, char* lds, int wave, int lane) {
    constexpr int NI = B_BYTES / 1024;
    const int pos = lane & 15, rw = pos >> 1, hf = pos & 1;
#pragma unroll
    for (int ii = 0; ii < NI / NW; ++ii) {
      const int i = ii * NW + wave;
      const int blk = i * 4 + (lane >> 4);
      const int kb = blk / NB, nb = blk % NB;
      const int row = rw ^ ((kb & 1) << 2);
      int n = n0 + nb * 16 + hf * 8;
      if (n > nmax) n = nmax;
      const u16* src = B + (size_t)(k0 + kb * 8 + row) * ldb + n;
      glds16(src, lds + i * 1024);
    }
  }
  template <bool SWAP>
  static DI void compute(const char* ldsA, const char* ldsB, f32x4 (&acc)[MT][NT], int wm, int wn, int lane) {
    const int fr = lane & 15, fg = lane >> 4;
    const int q = fr >> 2, p = lane & 3;
#pragma unroll
    for (int sub = 0; sub < KS; ++sub) {
#pragma unroll
      for (int kk = 0; kk < 2; ++kk) {
        bf16x8 af[MT], bfr[NT];
#pragma unroll
        for (int mt = 0; mt < MT; ++mt) {
          const int row = wm * WM + mt * 16 + fr;
          af[mt] = *reinterpret_cast<const bf16x8*>(ldsA + sub * (BM * 128) + row * 128 + (((kk * 4 + fg) ^ (row & 7)) << 4));
        }
#pragma unroll
        for (int nt = 0; nt < NT; ++nt) {
          const int kb = sub * 8 + kk * 4 + fg, nb = wn * NT + nt;
          const char* base = ldsB + (kb * NB + nb) * 256 + p * 8;
          const int sw = (fg & 1) << 2;
          s16x4 lo = tr_read(base + ((q) ^ sw) * 32);
          s16x4 hi = tr_read(base + ((4 + q) ^ sw) * 32);
          bfr[nt] = __builtin_shufflevector(lo, hi, 0, 1, 2, 3, 4, 5, 6, 7);
        }
#pragma unroll
        for (int mt = 0; mt < MT; ++mt)
#pragma unroll
          for (int nt = 0; nt < NT; ++nt) {
            if constexpr (SWAP)
              acc[mt][nt] = __builtin_amdgcn_mfma_f32_16x16x32_bf16(bfr[nt], af[mt], acc[mt][nt], 0, 0, 0);
            else
              acc[mt][nt] = __builtin_amdgcn_mfma_f32_16x16x32_bf16(af[mt], bfr[nt], acc[mt][nt], 0, 0, 0);
          }
      }
    }
  }
};

using G1 = Gemm<128, 128, 1, 2, 2>;

__global__ __launch_bounds__(256, 2) void k_qkv(const u16* __restrict__ h1, const u16* __restrict__ ckv,
                                                const u16* __restrict__ wq, const u16* __restrict__ wk,
                                                const u16* __restrict__ wv, char* __restrict__ Qf,
                                                char* __restrict__ Ki, char* __restrict__ Vi) {
  __shared__ __attribute__((aligned(16))) char smem[2 * G1::STAGE_BYTES];
  const int b = blockIdx.x, xcd = b & 7, j = b >> 3;
  const int head = xcd * 4 + (j & 3), rest = j >> 2, which = rest >> 4, mtile = rest & 15;
  const int tid = threadIdx.x, wave = __builtin_amdgcn_readfirstlane(tid >> 6), lane = tid & 63;
  const int wm = wave >> 1, wn = wave & 1;
  const u16* A = which == 0 ? h1 : ckv;
  const u16* B = which == 0 ? wq : (which == 1 ? wk : wv);
  const int m0 = mtile * 128, n0 = head * 128;
  f32x4 acc[G1::MT][G1::NT];
#pragma unroll
  for (int i = 0; i < G1::MT; ++i)
#pragma unroll
    for (int jj = 0; jj < G1::NT; ++jj) acc[i][jj] = f32x4{0.f, 0.f, 0.f, 0.f};
  constexpr int NK = DM / G1::BK;
  G1::stageA(A, DM, m0, 0, smem, wave, lane);
  G1::stageB(B, HDM, 0, n0, HDM, smem + G1::A_BYTES, wave, lane);
  for (int kt = 0; kt < NK; ++kt) {
    wait_vm<0>(); __syncthreads();
    char* cur = smem + (kt & 1) * G1::STAGE_BYTES;
    char* nxt = smem + ((kt + 1) & 1) * G1::STAGE_BYTES;
    if (kt + 1 < NK) {
      G1::stageA(A, DM, m0, (kt + 1) * G1::BK, nxt, wave, lane);
      G1::stageB(B, HDM, (kt + 1) * G1::BK, n0, HDM, nxt + G1::A_BYTES, wave, lane);
    }
    if (which == 2) G1::compute<false>(cur, cur + G1::A_BYTES, acc, wm, wn, lane);
    else G1::compute<true>(cur, cur + G1::A_BYTES, acc, wm, wn, lane);
  }
  __syncthreads();
  const int fr = lane & 15, fg = lane >> 4;
#pragma unroll
  for (int mt = 0; mt < G1::MT; ++mt)
#pragma unroll
    for (int nt = 0; nt < G1::NT; ++nt) {
      u32x2 v;
      v[0] = pack2(acc[mt][nt][0], acc[mt][nt][1]);
      v[1] = pack2(acc[mt][nt][2], acc[mt][nt][3]);
      int off;
      if (which == 2) {
        const int d = wn * 64 + nt * 16 + fr, m = wm * 64 + mt * 16 + fg * 4;
        const int tile = m >> 6, key = m & 63;
        const int pos = (key & 0x33) | ((key & 4) << 1) | ((key & 8) >> 1);
        off = tile * 16384 + d * 128 + (((pos >> 3) ^ ((d >> 1) & 7)) << 4) + (pos & 7) * 2;
      } else {
        const int m = wm * 64 + mt * 16 + fr, d = wn * 64 + nt * 16 + fg * 4;
        if (which == 1) {
          const int tile = m >> 6, key = m & 63;
          off = tile * 16384 + key * 256 + (((d >> 3) ^ (key & 15)) << 4) + (d & 7) * 2;
        } else {
          const int rg = m >> 5, r = m & 31;
          off = rg * 8192 + (d >> 4) * 1024 + ((((d >> 3) & 1) * 32 + r) << 4) + (d & 7) * 2;
        }
      }
      *reinterpret_cast<u32x2*>(smem + off) = v;
    }
  __syncthreads();
  char* dst = (which == 0 ? Qf : (which == 1 ? Ki : Vi)) + ((size_t)head * 16 + mtile) * 32768;
#pragma unroll
  for (int i = 0; i < 8; ++i) {
    const int o = (i * 256 + tid) * 16;
    *reinterpret_cast<u32x4*>(dst + o) = *reinterpret_cast<const u32x4*>(smem + o);
  }
}

#define KV_PD 2
constexpr int KV_NS = 4, KV_STAGE = 32768;
template <bool isV>
DI void kv_body(char* smem, const u16* __restrict__ ckv, const u16* __restrict__ B, char* __restrict__ dstb,
                const u16* __restrict__ cvec, const u16* __restrict__ wmod, float* __restrict__ mod) {
  const int b = blockIdx.x, xcd = b & 7, j = b >> 3;
  const int ntile = xcd * 4 + (j & 3), mtile = j >> 2;
  const int tid = threadIdx.x, wave = __builtin_amdgcn_readfirstlane(tid >> 6), lane = tid & 63;
  const int wm = wave >> 2, wn = wave & 3, r = lane & 31, h = lane >> 5;
  const int m0 = mtile * 256, n0 = (ntile & 15) * 256;
  float* modred = reinterpret_cast<float*>(smem + KV_NS * KV_STAGE);
  STAMP(0, 0);

  const u16* asrc[2];
  const u16* bsrc[2];
#pragma unroll
  for (int ii = 0; ii < 2; ++ii) {
    const int i = ii * 8 + wave;
    const int row = 16 * i + (lane >> 2), ch = (lane & 3) ^ ((lane >> 4) & 3);
    asrc[ii] = ckv + (size_t)(m0 + row) * DM + ch * 8;
    const int blk = i * 4 + (lane >> 4), kb = blk >> 4, nb = blk & 15;
    const int pos = lane & 15, rw = (pos >> 1) ^ ((nb & 1) << 2), hf = pos & 1;
    bsrc[ii] = B + (size_t)(kb * 8 + rw) * HDM + n0 + nb * 16 + hf * 8;
  }
  auto stage = [&](int kt0, int slot) {
    char* dst = smem + slot * KV_STAGE;
    const int kt = (kt0 + 2 * mtile) & 15;
#pragma unroll
    for (int ii = 0; ii < 2; ++ii) {
      glds16(asrc[ii] + kt * 32, dst + (ii * 8 + wave) * 1024);
      glds16(bsrc[ii] + (size_t)kt * 32 * HDM, dst + 16384 + (ii * 8 + wave) * 1024);
    }
  };
  constexpr int NK = DM / 32;
#pragma unroll
  for (int st = 0; st < KV_PD; ++st) stage(st, st);

  if (b < 192) {
    const u16* p = wmod + (size_t)tid * 3072 + b * 16;
    const u32x4 w0 = *reinterpret_cast<const u32x4*>(p), w1 = *reinterpret_cast<const u32x4*>(p + 8);
    const float c = bf2f(cvec[tid]);
    const float sv = rbf(c / (1.f + __expf(-c)));
    float v[16];
#pragma unroll
    for (int jj = 0; jj < 4; ++jj) {
      v[2 * jj] = sv * bflo(w0[jj]); v[2 * jj + 1] = sv * bfhi(w0[jj]);
      v[8 + 2 * jj] = sv * bflo(w1[jj]); v[8 + 2 * jj + 1] = sv * bfhi(w1[jj]);
    }
#pragma unroll
    for (int st = 0; st < 4; ++st) {
      const int half = 8 >> st, m = 32 >> st;
      const bool up = lane & m;
#pragma unroll
      for (int e = 0; e < half; ++e) {
        const float send = up ? v[e] : v[half + e];
        const float keep = up ? v[half + e] : v[e];
        v[e] = keep + __shfl_xor(send, m);
      }
    }
    v[0] += __shfl_xor(v[0], 2);
    v[0] += __shfl_xor(v[0], 1);
    if ((lane & 3) == 0) {
      const int col = ((lane >> 5) & 1) * 8 + ((lane >> 4) & 1) * 4 + ((lane >> 3) & 1) * 2 + ((lane >> 2) & 1);
      modred[wave * 16 + col] = v[0];
    }
  }

  STAMP(0, 1);
  f32x16 acc[4][2];
#pragma unroll
  for (int i = 0; i < 4; ++i)
#pragma unroll
    for (int jj = 0; jj < 2; ++jj)
#pragma unroll
      for (int e = 0; e < 16; ++e) acc[i][jj][e] = 0.f;
  const int G = lane >> 4, q4 = (lane & 15) >> 2, p4 = lane & 3;
  int aoff[2];
#pragma unroll
  for (int u = 0; u < 2; ++u) aoff[u] = (wm * 128 + r) * 64 + (((2 * u + h) ^ ((r >> 2) & 3)) << 4);
  const int nh = G & 1;
  const int boff = 16384 + (h * 16 + wn * 4 + nh) * 256 + p4 * 8;
  const int brow0 = (q4 ^ (nh << 2)) * 32, brow1 = ((4 + q4) ^ (nh << 2)) * 32;
  for (int kt = 0; kt < NK; ++kt) {
    if (kt + KV_PD - 1 <= NK - 1) wait_vm<(KV_PD - 1) * 4>(); else wait_vm<0>();
    barrier();
    const bool pf = kt + KV_PD < NK;
    const int ktn = (kt + KV_PD + 2 * mtile) & 15;
    char* dst = smem + ((kt + KV_PD) & (KV_NS - 1)) * KV_STAGE;
    const char* cur = smem + (kt & (KV_NS - 1)) * KV_STAGE;
    bf16x8 af[2][4], bfr[2][2];
#pragma unroll
    for (int u = 0; u < 2; ++u) {
#pragma unroll
      for (int nt = 0; nt < 2; ++nt) {
        const char* bb = cur + boff + u * (2 * 16 * 256) + nt * 512;
        s16x4 lo = tr_read(bb + brow0), hi = tr_read(bb + brow1);
        bfr[u][nt] = __builtin_shufflevector(lo, hi, 0, 1, 2, 3, 4, 5, 6, 7);
      }
#pragma unroll
      for (int mt = 0; mt < 4; ++mt) af[u][mt] = *reinterpret_cast<const bf16x8*>(cur + aoff[u] + mt * 2048);
    }
#pragma unroll
    for (int g4 = 0; g4 < 4; ++g4) {
      const int u = g4 >> 1;
      __builtin_amdgcn_sched_barrier(0);
      if (pf) {
        const int ii = g4 >> 1;
        if (g4 & 1) glds16(bsrc[ii] + (size_t)ktn * 32 * HDM, dst + 16384 + (ii * 8 + wave) * 1024);
        else glds16(asrc[ii] + ktn * 32, dst + (ii * 8 + wave) * 1024);
      }
      __builtin_amdgcn_sched_barrier(0);
#pragma unroll
      for (int m2 = 0; m2 < 2; ++m2)
#pragma unroll
        for (int nt = 0; nt < 2; ++nt) {
          const int mt = (g4 & 1) * 2 + m2;
          if constexpr (isV)
            acc[mt][nt] = __builtin_amdgcn_mfma_f32_32x32x16_bf16(af[u][mt], bfr[u][nt], acc[mt][nt], 0, 0, 0);
          else
            acc[mt][nt] = __builtin_amdgcn_mfma_f32_32x32x16_bf16(bfr[u][nt], af[u][mt], acc[mt][nt], 0, 0, 0);
        }
    }
  }
  barrier();
  STAMP(0, 2);
#pragma unroll
  for (int mt = 0; mt < 4; ++mt)
#pragma unroll
    for (int nt = 0; nt < 2; ++nt)
#pragma unroll
      for (int qd = 0; qd < 4; ++qd) {
        u32x2 v;
        v[0] = pack2(acc[mt][nt][4 * qd], acc[mt][nt][4 * qd + 1]);
        v[1] = pack2(acc[mt][nt][4 * qd + 2], acc[mt][nt][4 * qd + 3]);
        int off;
        if constexpr (isV) {
          const int n = wn * 64 + nt * 32 + r, m = wm * 128 + mt * 32 + 8 * qd + 4 * h;
          const int hd = n >> 7, d = n & 127, tile = m >> 6, key = m & 63;
          const int pos = (key & 0x33) | ((key & 4) << 1) | ((key & 8) >> 1);
          off = (hd * 4 + tile) * 16384 + d * 128 + (((pos >> 3) ^ ((d >> 1) & 7)) << 4) + (pos & 7) * 2;
        } else {
          const int m = wm * 128 + mt * 32 + r, n = wn * 64 + nt * 32 + 8 * qd + 4 * h;
          const int hd = n >> 7, d = n & 127, tile = m >> 6, key = m & 63;
          off = (hd * 4 + tile) * 16384 + key * 256 + (((d >> 3) ^ (key & 15)) << 4) + (d & 7) * 2;
        }
        *reinterpret_cast<u32x2*>(smem + off) = v;
      }
  barrier();
  {
    const int head0 = (ntile & 15) * 2;
#pragma unroll
    for (int i = 0; i < 16; ++i) {
      const int o = (i * 512 + tid) * 16;
      const int hd = o >> 16, rem = o & 65535;
      *reinterpret_cast<u32x4*>(dstb + ((size_t)(head0 + hd) * 32 + mtile * 4) * 16384 + rem) =
          *reinterpret_cast<const u32x4*>(smem + o);
    }
  }
  STAMP(0, 3);
  if (b < 192 && tid < 16) {
    float sum = 0.f;
#pragma unroll
    for (int i = 0; i < 8; ++i) sum += modred[i * 16 + tid];
    mod[b * 16 + tid] = rbf(sum);
  }
}
__global__ __launch_bounds__(512, 2) void k_kv(const u16* __restrict__ ckv, const u16* __restrict__ wk,
                                               const u16* __restrict__ wv, char* __restrict__ Ki,
                                               char* __restrict__ Vi, const u16* __restrict__ cvec,
                                               const u16* __restrict__ wmod, float* __restrict__ mod) {
  __shared__ __attribute__((aligned(16))) char smem[KV_NS * KV_STAGE + 512];
  if ((blockIdx.x & 7) >= 4) kv_body<true>(smem, ckv, wv, Vi, cvec, wmod, mod);
  else kv_body<false>(smem, ckv, wk, Ki, cvec, wmod, mod);
}

constexpr int AT_V0 = 65536, AT_V1 = 98304, AT_DUMMY = 147456;
constexpr float AT_C = 0.08838834764831845f * 1.4426950408889634f;
struct AtLoad {
  const char* ksrc; const char* vsrc; unsigned kdst, v0dst, v1dst; unsigned voff; bool on;
  template <int I> DI void piece() const {
    if (!on) return;
    if constexpr (I < 4) glds16s<I * 1024>(ksrc, voff, kdst);
    else if constexpr (I < 6) glds16s<(I - 4) * 1024>(vsrc, voff, v0dst);
    else glds16s<(I - 6) * 1024>(vsrc + 16384, voff, v1dst);
  }
  DI void all() const {
    piece<0>(); piece<1>(); piece<2>(); piece<3>(); piece<4>(); piece<5>(); piece<6>(); piece<7>();
  }
};
#define AT_SB() __builtin_amdgcn_sched_barrier(0)
DI void at_rdK(const char* Kt, int j, bf16x8 (&f)[4], int r, int h) {
  const char* kb = Kt + r * 256 + (j >> 1) * 8192;
#pragma unroll
  for (int i = 0; i < 4; ++i) f[i] = *reinterpret_cast<const bf16x8*>(kb + (((2 * ((j & 1) * 4 + i) + h) ^ (r & 15)) << 4));
}
DI void at_rdV(const char* Vt, int j, bf16x8 (&f)[4], int r, int h) {
  const char* vb = Vt + r * 128 + j * 4096;
#pragma unroll
  for (int i = 0; i < 4; ++i) f[i] = *reinterpret_cast<const bf16x8*>(vb + (((2 * i + h) ^ ((r >> 1) & 7)) << 4));
}
DI void at_mmK(int j, const bf16x8 (&f)[4], const bf16x8 (&q)[8], f32x16 (&st)[2]) {
  AT_SB();
#pragma unroll
  for (int i = 0; i < 4; ++i)
    st[j >> 1] = __builtin_amdgcn_mfma_f32_32x32x16_bf16(f[i], q[(j & 1) * 4 + i], st[j >> 1], 0, 0, 0);
  AT_SB();
}
DI void at_mmV(int j, const bf16x8 (&f)[4], const bf16x8 (&p)[4], f32x16 (&o)[4]) {
  AT_SB();
#pragma unroll
  for (int i = 0; i < 4; ++i) o[j] = __builtin_amdgcn_mfma_f32_32x32x16_bf16(f[i], p[i], o[j], 0, 0, 0);
  AT_SB();
}
DI void at_softmax(f32x16 (&st)[2], bf16x8 (&p)[4], f32x16 (&o)[4], float& m, float& l, bool diag, int lim,
                   const AtLoad& ld) {
  __builtin_amdgcn_s_setprio(0);
  if (diag) {
    asm volatile("" : "+v"(lim));
#pragma unroll
    for (int kt = 0; kt < 2; ++kt)
#pragma unroll
      for (int i = 0; i < 16; ++i) {
        const int kk = 32 * kt + (i & 3) + 8 * (i >> 2);
        if (kk > lim) st[kt][i] = -__builtin_inff();
      }
  }
  float pm = st[0][0];
#pragma unroll
  for (int i = 1; i < 16; ++i) pm = fmaxf(pm, st[0][i]);
  AT_SB(); ld.piece<0>(); AT_SB();
#pragma unroll
  for (int i = 0; i < 16; ++i) pm = fmaxf(pm, st[1][i]);
  {
    auto rr = __builtin_amdgcn_permlane32_swap(__float_as_uint(pm), __float_as_uint(pm), false, false);
    pm = fmaxf(__uint_as_float(rr[0]), __uint_as_float(rr[1]));
  }
  if (__builtin_amdgcn_ballot_w64(pm > m) != 0) {
    const float mn = fmaxf(m, pm);
    const float alpha = __builtin_amdgcn_exp2f((m - mn) * AT_C);
    m = mn;
    l *= alpha;
#pragma unroll
    for (int dt = 0; dt < 4; ++dt)
#pragma unroll
      for (int i = 0; i < 16; ++i) o[dt][i] *= alpha;
  }
  const float mc = -m * AT_C;
  float ps = 0.f;
#pragma unroll
  for (int ks = 0; ks < 4; ++ks) {
    AT_SB();
    if (ks == 0) ld.piece<1>(); else if (ks == 1) ld.piece<3>(); else if (ks == 2) ld.piece<5>(); else ld.piece<7>();
    AT_SB();
    u32x4 w;
#pragma unroll
    for (int jj = 0; jj < 4; ++jj) {
      if (jj == 2 && ks < 3) {
        AT_SB();
        if (ks == 0) ld.piece<2>(); else if (ks == 1) ld.piece<4>(); else ld.piece<6>();
        AT_SB();
      }
      const float e0 = __builtin_amdgcn_exp2f(fmaf(st[ks >> 1][8 * (ks & 1) + 2 * jj], AT_C, mc));
      const float e1 = __builtin_amdgcn_exp2f(fmaf(st[ks >> 1][8 * (ks & 1) + 2 * jj + 1], AT_C, mc));
      ps += e0; ps += e1;
      w[jj] = pack2(e0, e1);
    }
    p[ks] = __builtin_bit_cast(bf16x8, w);
  }
  l += ps;
}
template <int SG, bool FIRST>
DI void at_iter(char* smem, const char* Kh, const char* Vh, const bf16x8 (&q)[8], f32x16 (&o)[4], bf16x8 (&p)[4],
                bf16x8 (&f0)[4], bf16x8 (&f1)[4], float& m, float& l, int it, int v1c, int n, int wave, int lane,
                int lim, bool pfw) {
  const int r = lane & 31, h = lane >> 5;
  if (pfw && it == 5) STAMP(4, 0);
  if (pfw && it == 6) STAMP(4, 5);
  wait_vm<0>(); barrier();
  if (pfw && it == 5) STAMP(4, 1);
  const int v1n = v1c == 2 ? 0 : v1c + 1, v1p = v1c == 0 ? 2 : v1c - 1;
  const size_t go = (size_t)(it + 1) * 32768;
  const unsigned lb = lds_addr(smem);
  const AtLoad ld{Kh + go, Vh + go, lb + ((it + 1) & 1) * 32768 + wave * 4096,
                  lb + AT_V0 + ((it + 1) & 1) * 16384 + wave * 2048, lb + AT_V1 + v1n * 16384 + wave * 2048,
                  (unsigned)lane * 16u, it + 1 < n};
  const bool last = (it == n - 1);
  const char* Kt = smem + (it & 1) * 32768 + SG * 16384;
  f32x16 st[2];
#pragma unroll
  for (int kt = 0; kt < 2; ++kt)
#pragma unroll
    for (int i = 0; i < 16; ++i) st[kt][i] = 0.f;
  bf16x8 f2[4];
  __builtin_amdgcn_s_setprio(1);
  if constexpr (SG == 0) {
    const char* Vt = smem + AT_V0 + (it & 1) * 16384;
    at_rdK(Kt, 0, f0, r, h); at_rdK(Kt, 1, f1, r, h);
    at_rdK(Kt, 2, f2, r, h); at_mmK(0, f0, q, st);
    at_rdK(Kt, 3, f0, r, h); at_mmK(1, f1, q, st);
    at_rdV(Vt, 0, f1, r, h); at_mmK(2, f2, q, st);
    at_rdV(Vt, 1, f2, r, h); at_mmK(3, f0, q, st);
    if (pfw && it == 5) { asm volatile("" :: "v"(st[0][0]), "v"(st[1][0])); STAMP(4, 2); }
    at_softmax(st, p, o, m, l, last, lim, ld);
    if (pfw && it == 5) { asm volatile("" :: "v"(p[0]), "v"(p[3])); STAMP(4, 3); }
    __builtin_amdgcn_s_setprio(1);
    at_rdV(Vt, 2, f0, r, h); at_mmV(0, f1, p, o);
    at_rdV(Vt, 3, f1, r, h); at_mmV(1, f2, p, o);
    at_mmV(2, f0, p, o);
    at_mmV(3, f1, p, o);
    if (pfw && it == 5) { asm volatile("" :: "v"(o[0][0]), "v"(o[3][0])); STAMP(4, 4); }
  } else {
    if constexpr (!FIRST) {
      const char* Vt = smem + AT_V1 + v1p * 16384;
      at_rdV(Vt, 2, f2, r, h); at_mmV(0, f0, p, o);
      at_rdV(Vt, 3, f0, r, h); at_mmV(1, f1, p, o);
      at_rdK(Kt, 0, f1, r, h); at_mmV(2, f2, p, o);
      at_rdK(Kt, 1, f2, r, h); at_mmV(3, f0, p, o);
    } else {
      at_rdK(Kt, 0, f1, r, h); at_rdK(Kt, 1, f2, r, h);
    }
    if (pfw && it == 5) { asm volatile("" :: "v"(o[0][0]), "v"(o[3][0])); STAMP(4, 2); }
    at_rdK(Kt, 2, f0, r, h); at_mmK(0, f1, q, st);
    at_rdK(Kt, 3, f1, r, h); at_mmK(1, f2, q, st);
    at_mmK(2, f0, q, st);
    at_mmK(3, f1, q, st);
    if (pfw && it == 5) { asm volatile("" :: "v"(st[0][0]), "v"(st[1][0])); STAMP(4, 3); }
    at_softmax(st, p, o, m, l, last, lim, ld);
    if (pfw && it == 5) { asm volatile("" :: "v"(p[0]), "v"(p[3])); STAMP(4, 4); }
    AT_SB();
    at_rdV(smem + AT_V1 + v1c * 16384, 0, f0, r, h); at_rdV(smem + AT_V1 + v1c * 16384, 1, f1, r, h);
  }
}
template <int SG>
DI void at_loop(char* smem, const char* Kh, const char* Vh, const bf16x8 (&q)[8], f32x16 (&o)[4], bf16x8 (&p)[4],
                bf16x8 (&f0)[4], bf16x8 (&f1)[4], float& m, float& l, int n, int wave, int lane, int lim, bool pfw,
                const u16* wo, const u16* win, const u16* wsp, const u16* wout) {
  at_iter<SG, true>(smem, Kh, Vh, q, o, p, f0, f1, m, l, 0, 0, n, wave, lane, lim, pfw);
  if (pfw) {
    const int slot = blockIdx.x * 8 + wave;
    const u16* base = slot < 1024 ? wsp : slot < 1536 ? wo : slot < 1708 ? win : wout;
    const int nb = slot < 1024 ? S * S * 2 : slot < 1536 ? HDM * DM * 2 : slot < 1708 ? DM * FF * 2 : FH * DM * 2;
    const int first = slot < 1024 ? slot : slot < 1536 ? slot - 1024 : slot < 1708 ? slot - 1536 : slot - 1708;
    pf_slice(base, nb, first * 64, lane, smem + AT_DUMMY);
  }
  int v1c = 1;
  for (int it = 1; it < n; ++it) {
    at_iter<SG, false>(smem, Kh, Vh, q, o, p, f0, f1, m, l, it, v1c, n, wave, lane, lim, pfw);
    v1c = v1c == 2 ? 0 : v1c + 1;
  }
}
__global__ __launch_bounds__(512, 2) void k_attn(const char* __restrict__ Qf, const char* __restrict__ Ki,
                                                 const char* __restrict__ Vi, char* __restrict__ ctx,
                                                 const u16* __restrict__ wo, const u16* __restrict__ win,
                                                 const u16* __restrict__ wsp, const u16* __restrict__ wout) {
  __shared__ __attribute__((aligned(16))) char smem[AT_DUMMY + 256];
  const int b = blockIdx.x, xcd = b & 7, j = b >> 3;
  const int head = xcd * 4 + (j & 3), pair = j >> 2;
  const int tid = threadIdx.x, wave = __builtin_amdgcn_readfirstlane(tid >> 6), lane = tid & 63;
  const int s = wave >> 2, g = wave & 3, r = lane & 31, h = lane >> 5;
  const char* Kh = Ki + (size_t)head * (32 * 16384) + wave * 4096;
  const char* Vh = Vi + (size_t)head * (32 * 16384) + wave * 2048;
  const int lim = 32 * g + r - 64 * s - 4 * h;

  for (int qi = 0; qi < 2; ++qi) {
    const int qb = qi ? pair : 15 - pair;
    const int n = qb + 1;
    STAMP(5, qi * 4);
    {
      const unsigned lb = lds_addr(smem);
      AtLoad ld{Kh, Vh, lb + wave * 4096, lb + AT_V0 + wave * 2048, lb + AT_V1 + wave * 2048, (unsigned)lane * 16u, true};
      ld.all();
    }
    bf16x8 q[8];
    {
      const char* qp = Qf + ((size_t)head * 64 + qb * 4 + g) * 8192 + lane * 16;
#pragma unroll
      for (int ks = 0; ks < 8; ++ks) q[ks] = *reinterpret_cast<const bf16x8*>(qp + ks * 1024);
#pragma unroll
      for (int ks = 0; ks < 8; ++ks) asm volatile("" : "+v"(q[ks]));
    }
    f32x16 o[4];
#pragma unroll
    for (int dt = 0; dt < 4; ++dt)
#pragma unroll
      for (int i = 0; i < 16; ++i) o[dt][i] = 0.f;
    float m = -1e30f, l = 0.f;
    bf16x8 p[4];
    bf16x8 f0[4], f1[4];
    STAMP(5, qi * 4 + 1);
    if (s == 0) at_loop<0>(smem, Kh, Vh, q, o, p, f0, f1, m, l, n, wave, lane, lim, qi == 0, wo, win, wsp, wout);
    else at_loop<1>(smem, Kh, Vh, q, o, p, f0, f1, m, l, n, wave, lane, lim, qi == 0, wo, win, wsp, wout);
    STAMP(5, qi * 4 + 2);
    barrier();
    float* xo = reinterpret_cast<float*>(smem) + g * (64 * 68);
    if (s == 0) {
      l += __shfl_xor(l, 32);
#pragma unroll
      for (int dt = 0; dt < 4; ++dt)
#pragma unroll
        for (int qd = 0; qd < 4; ++qd) {
          f32x4 v = {o[dt][4 * qd], o[dt][4 * qd + 1], o[dt][4 * qd + 2], o[dt][4 * qd + 3]};
          *reinterpret_cast<f32x4*>(xo + ((dt * 4 + qd) * 64 + lane) * 4) = v;
        }
      f32x4 v = {m, l, 0.f, 0.f};
      *reinterpret_cast<f32x4*>(xo + (16 * 64 + lane) * 4) = v;
    } else {
      const char* Vt = smem + AT_V1 + ((n - 1) % 3) * 16384;
      bf16x8 f2[4], f3[4];
      at_rdV(Vt, 2, f2, r, h); at_rdV(Vt, 3, f3, r, h);
      at_mmV(0, f0, p, o); at_mmV(1, f1, p, o); at_mmV(2, f2, p, o); at_mmV(3, f3, p, o);
      l += __shfl_xor(l, 32);
    }
    barrier();
    if (s == 1) {
      f32x4 ml = *reinterpret_cast<const f32x4*>(xo + (16 * 64 + lane) * 4);
      const float mt = fmaxf(m, ml[0]);
      const float a0 = __builtin_amdgcn_exp2f((m - mt) * AT_C), a1 = __builtin_amdgcn_exp2f((ml[0] - mt) * AT_C);
      const float inv = 1.f / (l * a0 + ml[1] * a1);
      const float f0 = a0 * inv, f1 = a1 * inv;
      char* crow = ctx + ((size_t)(qb * 8 + g * 2 + (r >> 4)) * 128 + head * 4) * 1024 + (r & 15) * 16 + h * 8;
#pragma unroll
      for (int dt = 0; dt < 4; ++dt)
#pragma unroll
        for (int qd = 0; qd < 4; ++qd) {
          f32x4 v = *reinterpret_cast<const f32x4*>(xo + ((dt * 4 + qd) * 64 + lane) * 4);
          u32x2 w;
          w[0] = pack2(o[dt][4 * qd] * f0 + v[0] * f1, o[dt][4 * qd + 1] * f0 + v[1] * f1);
          w[1] = pack2(o[dt][4 * qd + 2] * f0 + v[2] * f1, o[dt][4 * qd + 3] * f0 + v[3] * f1);
          *reinterpret_cast<u32x2*>(crow + dt * 1024 + qd * 256) = w;
        }
    }
    barrier();
    STAMP(5, qi * 4 + 3);
  }
}

constexpr int SK_AREA = 16384;
template <int NT> struct SkOwn { static constexpr int N = (2 * NT + 3) / 4; };
template <int NT>
DI void splitk_reduce(const f32x4 (&acc)[4][NT], f32x4 (&own)[SkOwn<NT>::N], char* smem, int wave, int lane) {
  const int hi = wave >> 2, wq = wave & 3;
  char* mine = smem + wave * SK_AREA + lane * 16;
  f32x4 r[2 * NT];
#pragma unroll
  for (int f = 0; f < 2 * NT; ++f) {
    const f32x4 lo = acc[f / NT][f % NT], up = acc[2 + f / NT][f % NT];
    f32x4 snd, kp;
#pragma unroll
    for (int e = 0; e < 4; ++e) { snd[e] = hi ? lo[e] : up[e]; kp[e] = hi ? up[e] : lo[e]; }
    *reinterpret_cast<f32x4*>(mine + f * 1024) = snd;
    r[f] = kp;
  }
  barrier();
  {
    const char* part = smem + (wave ^ 4) * SK_AREA + lane * 16;
#pragma unroll
    for (int f = 0; f < 2 * NT; ++f) {
      const f32x4 o = *reinterpret_cast<const f32x4*>(part + f * 1024);
      r[f] += o;
    }
  }
  barrier();
#pragma unroll
  for (int f = 0; f < 2 * NT; ++f) *reinterpret_cast<f32x4*>(mine + f * 1024) = r[f];
  barrier();
#pragma unroll
  for (int i = 0; i < SkOwn<NT>::N; ++i) {
    const int f = wq + 4 * i;
    const char* p = smem + hi * 4 * SK_AREA + lane * 16 + (f < 2 * NT ? f : 0) * 1024;
    const f32x4 a = *reinterpret_cast<const f32x4*>(p), b = *reinterpret_cast<const f32x4*>(p + SK_AREA);
    const f32x4 c = *reinterpret_cast<const f32x4*>(p + 2 * SK_AREA), d = *reinterpret_cast<const f32x4*>(p + 3 * SK_AREA);
    own[i] = (a + b) + (c + d);
  }
}
DI void sk_bfrags(const char* img, bf16x8 (&bfr)[4], int lane) {
  const int fr = lane & 15, fg = lane >> 4, q = fr >> 2, p = lane & 3, sw = (fg & 1) << 2;
  const char* base = img + fg * 1024 + p * 8;
#pragma unroll
  for (int nt = 0; nt < 4; ++nt) {
    s16x4 lo = tr_read(base + nt * 256 + (q ^ sw) * 32);
    s16x4 hi = tr_read(base + nt * 256 + ((4 + q) ^ sw) * 32);
    bfr[nt] = __builtin_shufflevector(lo, hi, 0, 1, 2, 3, 4, 5, 6, 7);
  }
}

constexpr int OP_NS = 16, OP_DB = 4, OP_DA = 6;
constexpr int op_wait(int t) {
  int pos = 0, bpos = -1;
  for (int i = 0; i < OP_DA; ++i) {
    if (i < OP_DB) { pos += 4; if (i == t) bpos = pos; }
    pos += 4;
  }
  for (int s = 0; s < t; ++s) {
    if (s + OP_DB < OP_NS) { pos += 4; if (s + OP_DB == t) bpos = pos; }
    if (s + OP_DA < OP_NS) pos += 4;
  }
  return pos - bpos;
}
template <int T> struct OpWait { static constexpr int v = op_wait(T); };
template <int T> DI void op_step(f32x4 (&acc)[4][4], u32x4 (&areg)[OP_DA][4], char* ring, const char* asrc,
                                 const u16* bs0, const u16* bs1, int g0, int lane) {
  wait_vm<OpWait<T>::v>();
  if constexpr (T == 0) STAMP(2, 2);
  if constexpr (T == 8) STAMP(2, 3);
  bf16x8 bfr[4];
  sk_bfrags(ring + (T % OP_DB) * 4096, bfr, lane);
#pragma unroll
  for (int mt = 0; mt < 4; ++mt)
#pragma unroll
    for (int nt = 0; nt < 4; ++nt)
      acc[mt][nt] = __builtin_amdgcn_mfma_f32_16x16x32_bf16(bfr[nt], __builtin_bit_cast(bf16x8, areg[T % OP_DA][mt]), acc[mt][nt], 0, 0, 0);
  if constexpr (T + OP_DB < OP_NS) {
    asm volatile("s_waitcnt lgkmcnt(0)" ::: "memory");
    const int g = (g0 + T + OP_DB) & 127;
#pragma unroll
    for (int i = 0; i < 4; ++i)
      glds16(((i & 1) ? bs1 : bs0) + (size_t)(g * 32 + i * 8) * DM, ring + (T % OP_DB) * 4096 + i * 1024);
  }
  if constexpr (T + OP_DA < OP_NS) {
    const int g = (g0 + T + OP_DA) & 127;
#pragma unroll
    for (int mt = 0; mt < 4; ++mt)
      areg[T % OP_DA][mt] = *reinterpret_cast<const u32x4*>(asrc + (size_t)mt * (128 * 1024) + g * 1024);
  }
  if constexpr (T + 1 < OP_NS) op_step<T + 1>(acc, areg, ring, asrc, bs0, bs1, g0, lane);
}
__global__ __launch_bounds__(512, 2) void k_oproj(const char* __restrict__ ctxf, const u16* __restrict__ wo,
                                                  const u16* __restrict__ x, const float* __restrict__ ga,
                                                  u16* __restrict__ x1) {
  __shared__ __attribute__((aligned(16))) char smem[8 * SK_AREA + 256];
  const int b = blockIdx.x, xcd = b & 7, j = b >> 3;
  const int mtile = xcd * 4 + (j >> 3), ntile = j & 7;
  const int tid = threadIdx.x, wave = __builtin_amdgcn_readfirstlane(tid >> 6), lane = tid & 63;
  const int m0 = mtile * 64, n0 = ntile * 64;
  char* ring = smem + wave * SK_AREA;
  const char* asrc = ctxf + (size_t)(mtile * 4) * 128 * 1024 + lane * 16;
  const int g0 = wave * 16;
  const int rw = (lane & 15) >> 1;
  const u16* bs0 = wo + (size_t)rw * DM + n0 + (lane >> 4) * 16 + (lane & 1) * 8;
  const u16* bs1 = wo + (size_t)(rw ^ 4) * DM + n0 + (lane >> 4) * 16 + (lane & 1) * 8;
  f32x4 acc[4][4];
#pragma unroll
  for (int i = 0; i < 4; ++i)
#pragma unroll
    for (int jj = 0; jj < 4; ++jj) acc[i][jj] = f32x4{0.f, 0.f, 0.f, 0.f};
  u32x4 areg[OP_DA][4];
  STAMP(2, 0);
#pragma unroll
  for (int i = 0; i < OP_DA; ++i) {
    const int g = (g0 + i) & 127;
    if (i < OP_DB) {
#pragma unroll
      for (int ii = 0; ii < 4; ++ii) glds16(((ii & 1) ? bs1 : bs0) + (size_t)(g * 32 + ii * 8) * DM, ring + i * 4096 + ii * 1024);
    }
#pragma unroll
    for (int mt = 0; mt < 4; ++mt)
      areg[i][mt] = *reinterpret_cast<const u32x4*>(asrc + (size_t)mt * (128 * 1024) + g * 1024);
  }
  STAMP(2, 1);
  op_step<0>(acc, areg, ring, asrc, bs0, bs1, g0, lane);
  STAMP(2, 4);
  const int fr = lane & 15, fg = lane >> 4, hi = wave >> 2, wq = wave & 3;
  u32x2 xv[2];
  f32x4 gv;
  {
    gv = *reinterpret_cast<const f32x4*>(ga + n0 + wq * 16 + fg * 4);
#pragma unroll
    for (int i = 0; i < 2; ++i)
      xv[i] = *reinterpret_cast<const u32x2*>(x + (size_t)(m0 + (2 * hi + i) * 16 + fr) * DM + n0 + wq * 16 + fg * 4);
  }
  f32x4 own[2];
  splitk_reduce<4>(acc, own, smem, wave, lane);
#pragma unroll
  for (int i = 0; i < 2; ++i) {
    u32x2 w;
    w[0] = pack2(bflo(xv[i][0]) + gv[0] * own[i][0], bfhi(xv[i][0]) + gv[1] * own[i][1]);
    w[1] = pack2(bflo(xv[i][1]) + gv[2] * own[i][2], bfhi(xv[i][1]) + gv[3] * own[i][3]);
    *reinterpret_cast<u32x2*>(x1 + (size_t)(m0 + (2 * hi + i) * 16 + fr) * DM + n0 + wq * 16 + fg * 4) = w;
  }
  STAMP(2, 5);
}

constexpr int NBLK = 43;
using G4 = Gemm<64, 192, 1, 2, 4>;
DI float gelu_tanh(float x) {
  constexpr float K1 = -2.f * 0.7978845608028654f * 1.4426950408889634f, K2 = K1 * 0.044715f;
  const float e = __builtin_amdgcn_exp2f(x * fmaf(x * x, K2, K1));
  return x * __builtin_amdgcn_rcpf(1.f + e);
}
DI u32x4 gload_asm(const void* p) {
  u32x4 r;
  asm volatile("global_load_dwordx4 %0, %1, off" : "=v"(r) : "v"(p) : "memory");
  return r;
}
template <bool Z2>
DI void win_body(char* smem, const u16* __restrict__ x1, const float* __restrict__ sb, const float* __restrict__ cb,
                 const u16* __restrict__ win, char* __restrict__ zf, float* __restrict__ stats, int mtile, int t4) {
  char* sB = smem + 65536;
  const int tid = threadIdx.x, wave = __builtin_amdgcn_readfirstlane(tid >> 6), lane = tid & 63;
  const int wm = wave >> 2, wn = wave & 3;
  const int m0 = mtile * 64, n0 = (Z2 ? FH : 0) + t4 * 176;
  STAMP(1, 0);
  u32x4 c0 = gload_asm(cb + lane * 8), c1 = gload_asm(cb + lane * 8 + 4);
  u32x4 s0 = gload_asm(sb + lane * 8), s1 = gload_asm(sb + lane * 8 + 4);
  {
    const int rr = lane >> 3, ch = (lane & 7) ^ rr;
    const u16* src = x1 + (size_t)(m0 + wave * 8 + rr) * DM + ch * 8;
#pragma unroll
    for (int sub = 0; sub < 8; ++sub) glds16(src + sub * 64, smem + sub * 8192 + wave * 1024);
  }
#pragma unroll
  for (int st = 0; st < 4; ++st) G4::stageB(win, FF, st * 64, n0, FF - 8, sB + st * G4::B_BYTES, wave, lane);
  u32x4 breg[4][3];
  {
    const int pos = lane & 15, rw = pos >> 1, hf = pos & 1;
    const u16* src[3];
#pragma unroll
    for (int ii = 0; ii < 3; ++ii) {
      const int blk = (ii * 8 + wave) * 4 + (lane >> 4), kb = blk / G4::NB, nb = blk % G4::NB;
      int n = n0 + nb * 16 + hf * 8;
      if (n > FF - 8) n = FF - 8;
      src[ii] = win + (size_t)(256 + kb * 8 + (rw ^ ((kb & 1) << 2))) * FF + n;
    }
#pragma unroll
    for (int st = 0; st < 4; ++st) {
#pragma unroll
      for (int ii = 0; ii < 3; ++ii) breg[st][ii] = *reinterpret_cast<const u32x4*>(src[ii] + (size_t)st * 64 * FF);
      __builtin_amdgcn_sched_barrier(0);
    }
  }
  asm volatile("s_waitcnt vmcnt(24)" : "+v"(c0), "+v"(c1), "+v"(s0), "+v"(s1)::"memory");
  STAMP(1, 1);
  {
    float cbv[8], sbv[8];
#pragma unroll
    for (int jj = 0; jj < 4; ++jj) {
      cbv[jj] = 1.f + __uint_as_float(c0[jj]); cbv[4 + jj] = 1.f + __uint_as_float(c1[jj]);
      sbv[jj] = __uint_as_float(s0[jj]); sbv[4 + jj] = __uint_as_float(s1[jj]);
    }
    u32x4 xr[8];
#pragma unroll
    for (int i = 0; i < 8; ++i)
      xr[i] = *reinterpret_cast<const u32x4*>(smem + (lane >> 3) * 8192 + (wave * 8 + i) * 128 + (((lane & 7) ^ i) << 4));
#pragma unroll
    for (int i = 0; i < 8; ++i) {
      float f[8];
#pragma unroll
      for (int jj = 0; jj < 4; ++jj) { f[2 * jj] = bflo(xr[i][jj]); f[2 * jj + 1] = bfhi(xr[i][jj]); }
      float s = 0.f;
#pragma unroll
      for (int jj = 0; jj < 8; ++jj) s += f[jj];
      const float mean = wave_sum(s) * (1.f / DM);
      float v = 0.f;
#pragma unroll
      for (int jj = 0; jj < 8; ++jj) { f[jj] -= mean; v += f[jj] * f[jj]; }
      const float rstd = rsqrtf(wave_sum(v) * (1.f / DM) + 1e-5f);
#pragma unroll
      for (int jj = 0; jj < 4; ++jj)
        xr[i][jj] = pack2(f[2 * jj] * rstd * cbv[2 * jj] + sbv[2 * jj], f[2 * jj + 1] * rstd * cbv[2 * jj + 1] + sbv[2 * jj + 1]);
    }
#pragma unroll
    for (int i = 0; i < 8; ++i)
      *reinterpret_cast<u32x4*>(smem + (lane >> 3) * 8192 + (wave * 8 + i) * 128 + (((lane & 7) ^ i) << 4)) = xr[i];
  }
  STAMP(1, 2);
  f32x4 acc[G4::MT][G4::NT];
#pragma unroll
  for (int i = 0; i < G4::MT; ++i)
#pragma unroll
    for (int jj = 0; jj < G4::NT; ++jj) acc[i][jj] = f32x4{0.f, 0.f, 0.f, 0.f};
#pragma unroll
  for (int kt = 0; kt < 8; ++kt) {
    if (kt == 0) wait_vm<21>(); else if (kt == 1) wait_vm<18>(); else if (kt == 2) wait_vm<15>(); else if (kt == 3) wait_vm<12>();
    barrier();
    if (kt == 0) STAMP(1, 3);
    if (kt == 1) STAMP(1, 4);
    if (kt == 3) STAMP(1, 5);
    if (kt == 5) STAMP(1, 6);
    if (kt >= 1 && kt + 3 < 8) {
#pragma unroll
      for (int ii = 0; ii < 3; ++ii)
        *reinterpret_cast<u32x4*>(sB + ((kt + 3) & 3) * G4::B_BYTES + (ii * 8 + wave) * 1024 + lane * 16) = breg[kt - 1][ii];
    }
    G4::compute<!Z2>(smem + kt * 8192, sB + (kt & 3) * G4::B_BYTES, acc, wm, wn, lane);
  }
  STAMP(1, 7);
  const int fr = lane & 15, fg = lane >> 4;
  if constexpr (!Z2) {
#pragma unroll
    for (int nt = 0; nt < G4::NT; ++nt) {
      const int nbl = wn * 3 + nt, nb = t4 * 11 + nbl;
      if (nbl < 11 && nb < NBLK) {
#pragma unroll
        for (int mt = 0; mt < G4::MT; ++mt) {
          u32x2 w;
          w[0] = pack2(gelu_tanh(acc[mt][nt][0]), gelu_tanh(acc[mt][nt][1]));
          w[1] = pack2(gelu_tanh(acc[mt][nt][2]), gelu_tanh(acc[mt][nt][3]));
          *reinterpret_cast<u32x2*>(zf + ((size_t)(mtile * 4 + wm * 2 + mt) * NBLK + nb) * 512 + lane * 8) = w;
        }
      }
    }
  } else {
    float p1[G4::MT][4], p2[G4::MT][4];
#pragma unroll
    for (int mt = 0; mt < G4::MT; ++mt)
#pragma unroll
      for (int i = 0; i < 4; ++i) { p1[mt][i] = 0.f; p2[mt][i] = 0.f; }
#pragma unroll
    for (int nt = 0; nt < G4::NT; ++nt) {
      const int nbl = wn * 3 + nt, nb = t4 * 11 + nbl;
      if (nbl < 11 && nb < NBLK) {
#pragma unroll
        for (int mt = 0; mt < G4::MT; ++mt) {
          float gz[4];
#pragma unroll
          for (int i = 0; i < 4; ++i) {
            gz[i] = rbf(gelu_tanh(acc[mt][nt][i]));
            p1[mt][i] += gz[i]; p2[mt][i] += gz[i] * gz[i];
          }
          u32x2 w;
          w[0] = pack2(gz[0], gz[1]);
          w[1] = pack2(gz[2], gz[3]);
          *reinterpret_cast<u32x2*>(zf + ((size_t)(mtile * 2 + wm) * NBLK + nb) * 1024 + (fr + 16 * (mt * 2 + (fg >> 1))) * 16 + (fg & 1) * 8) = w;
        }
      }
    }
    barrier();
    float* red = reinterpret_cast<float*>(smem);
#pragma unroll
    for (int mt = 0; mt < G4::MT; ++mt)
#pragma unroll
      for (int i = 0; i < 4; ++i) {
        float a = p1[mt][i], c = p2[mt][i];
        a = row16_sum(a); c = row16_sum(c);
        if (fr == 0) {
          const int rowl = wm * 32 + mt * 16 + fg * 4 + i;
          red[(wn * 64 + rowl) * 2] = a; red[(wn * 64 + rowl) * 2 + 1] = c;
        }
      }
    barrier();
    if (tid < 128) {
      const float v = (red[tid] + red[128 + tid]) + (red[256 + tid] + red[384 + tid]);
      stats[((size_t)t4 * S + m0) * 2 + tid] = v;
    }
  }
}
__global__ __launch_bounds__(512, 2) void k_win(const u16* __restrict__ x1, const float* __restrict__ sb,
                                                const float* __restrict__ cb, const u16* __restrict__ win,
                                                char* __restrict__ z1f, char* __restrict__ z2f,
                                                float* __restrict__ stats) {
  __shared__ __attribute__((aligned(16))) char smem[65536 + 4 * G4::B_BYTES];
  const int b = blockIdx.x, xcd = b & 7, j = b >> 3;
  const int mtile = xcd * 4 + (j >> 3), ntile = j & 7;
  if (ntile >= 4) win_body<true>(smem, x1, sb, cb, win, z2f, stats, mtile, ntile - 4);
  else win_body<false>(smem, x1, sb, cb, win, z1f, stats, mtile, ntile);
}

constexpr int UKS = 22;
template <int NT>
DI void spatial_body(char* smem, const u16* __restrict__ wsp, const char* __restrict__ z2f,
                     const float* __restrict__ stats, const u16* __restrict__ bsp, const char* __restrict__ z1f,
                     char* __restrict__ uf, int mtile, int nb0, int rot) {
  const int tid = threadIdx.x, wave = __builtin_amdgcn_readfirstlane(tid >> 6), lane = tid & 63;
  const int fr = lane & 15, fg = lane >> 4;
  char* ring = smem + wave * SK_AREA;
  float* rs = reinterpret_cast<float*>(smem + 8 * SK_AREA + wave * 2048);
  float* ms = rs + 256;
  const int m0 = mtile * 64, g0 = wave * 4 + rot;
  STAMP(3, 0);
  f32x4 sp[4][2];
#pragma unroll
  for (int t = 0; t < 4; ++t) {
    const float* p = stats + ((size_t)t * S + ((g0 + (lane >> 4)) & 31) * 64 + (lane & 15) * 4) * 2;
    sp[t][0] = *reinterpret_cast<const f32x4*>(p);
    sp[t][1] = *reinterpret_cast<const f32x4*>(p + 4);
  }
  const int rr = lane >> 3, ch = (lane & 7) ^ rr;
  const u16* asrc = wsp + (size_t)(m0 + rr) * S + ch * 8;
  auto stageA = [&](int s) {
#pragma unroll
    for (int i = 0; i < 8; ++i) glds16(asrc + (size_t)i * 8 * S + ((g0 + s) & 31) * 64, ring + (s & 1) * 8192 + i * 1024);
  };
  const char* bsrc = z2f + (size_t)nb0 * 1024 + lane * 16;
  u32x4 breg[3][NT];
  auto loadB = [&](int t) {
#pragma unroll
    for (int nt = 0; nt < NT; ++nt) breg[t % 3][nt] = *reinterpret_cast<const u32x4*>(bsrc + (((((g0 + (t >> 1)) & 31) * 2 + (t & 1)) * NBLK) + nt) * 1024);
  };
  stageA(0); loadB(0); loadB(1); stageA(1); loadB(2);
  STAMP(3, 1);
  {
    f32x4 rv, mv;
#pragma unroll
    for (int i = 0; i < 4; ++i) {
      float s1 = 0.f, s2 = 0.f;
#pragma unroll
      for (int t = 0; t < 4; ++t) { s1 += sp[t][i >> 1][(i & 1) * 2]; s2 += sp[t][i >> 1][(i & 1) * 2 + 1]; }
      const float mean = s1 * (1.f / FH);
      const float var = fmaxf(s2 * (1.f / FH) - mean * mean, 0.f);
      const float rstd = rsqrtf(var + 1e-5f);
      rv[i] = rstd; mv[i] = -mean * rstd;
    }
    *reinterpret_cast<f32x4*>(rs + lane * 4) = rv;
    *reinterpret_cast<f32x4*>(ms + lane * 4) = mv;
  }
  f32x4 acc[4][NT];
#pragma unroll
  for (int i = 0; i < 4; ++i)
#pragma unroll
    for (int jj = 0; jj < NT; ++jj) acc[i][jj] = f32x4{0.f, 0.f, 0.f, 0.f};
#pragma unroll
  for (int s = 0; s < 4; ++s) {
    if (s < 2) wait_vm<3 * NT + 8>(); else if (s == 2) wait_vm<2 * NT + 8>(); else wait_vm<NT>();
    if (s == 0) STAMP(3, 2);
    if (s == 2) STAMP(3, 3);
#pragma unroll
    for (int kk = 0; kk < 2; ++kk) {
      const int t = 2 * s + kk;
      bf16x8 af[4];
#pragma unroll
      for (int mt = 0; mt < 4; ++mt)
        af[mt] = *reinterpret_cast<const bf16x8*>(ring + (s & 1) * 8192 + (mt * 16 + fr) * 128 + (((kk * 4 + fg) ^ (fr & 7)) << 4));
      f32x4 rv[2], mv[2];
#pragma unroll
      for (int e = 0; e < 2; ++e) {
        rv[e] = *reinterpret_cast<const f32x4*>(rs + t * 32 + fg * 8 + e * 4);
        mv[e] = *reinterpret_cast<const f32x4*>(ms + t * 32 + fg * 8 + e * 4);
      }
#pragma unroll
      for (int nt = 0; nt < NT; ++nt) {
        const u32x4 raw = breg[t % 3][nt];
        u32x4 o4;
#pragma unroll
        for (int jj = 0; jj < 4; ++jj)
          o4[jj] = pack2(fmaf(bflo(raw[jj]), rv[jj >> 1][(jj & 1) * 2], mv[jj >> 1][(jj & 1) * 2]),
                         fmaf(bfhi(raw[jj]), rv[jj >> 1][(jj & 1) * 2 + 1], mv[jj >> 1][(jj & 1) * 2 + 1]));
        const bf16x8 bv = __builtin_bit_cast(bf16x8, o4);
#pragma unroll
        for (int mt = 0; mt < 4; ++mt)
          acc[mt][nt] = __builtin_amdgcn_mfma_f32_16x16x32_bf16(bv, af[mt], acc[mt][nt], 0, 0, 0);
      }
      if (t + 3 < 8) loadB(t + 3);
    }
    if (s + 2 < 4) {
      asm volatile("s_waitcnt lgkmcnt(0)" ::: "memory");
      stageA(s + 2);
    }
  }
  STAMP(3, 4);
  constexpr int NO = SkOwn<NT>::N;
  const int hi = wave >> 2, wq = wave & 3;
  u32x2 zv[NO];
  float bs[NO];
#pragma unroll
  for (int i = 0; i < NO; ++i) {
    int f = wq + 4 * i;
    if (f >= 2 * NT) f = 0;
    const int mt = 2 * hi + f / NT, nb = nb0 + f % NT;
    zv[i] = *reinterpret_cast<const u32x2*>(z1f + ((size_t)(mtile * 4 + mt) * NBLK + nb) * 512 + lane * 8);
    bs[i] = bf2f(bsp[m0 + mt * 16 + fr]);
  }
  f32x4 own[NO];
  splitk_reduce<NT>(acc, own, smem, wave, lane);
#pragma unroll
  for (int i = 0; i < NO; ++i) {
    const int f = wq + 4 * i;
    if (f < 2 * NT) {
      const int mt = 2 * hi + f / NT, nb = nb0 + f % NT;
      u32x2 w;
      w[0] = pack2(bflo(zv[i][0]) * (own[i][0] + bs[i]), bfhi(zv[i][0]) * (own[i][1] + bs[i]));
      w[1] = pack2(bflo(zv[i][1]) * (own[i][2] + bs[i]), bfhi(zv[i][1]) * (own[i][3] + bs[i]));
      char* dst = uf + ((size_t)(mtile * 4 + mt) * UKS + (nb >> 1)) * 1024 + (fr + 16 * (fg >> 1)) * 16 + (fg & 1) * 8;
      *reinterpret_cast<u32x2*>(dst + (nb & 1) * 512) = w;
      if (nb == NBLK - 1) *reinterpret_cast<u32x2*>(dst + 512) = u32x2{0u, 0u};
    }
  }
  STAMP(3, 5);
}
__global__ __launch_bounds__(512, 2) void k_spatial(const u16* __restrict__ wsp, const char* __restrict__ z2f,
                                                    const float* __restrict__ stats, const u16* __restrict__ bsp,
                                                    const char* __restrict__ z1f, char* __restrict__ uf) {
  __shared__ __attribute__((aligned(16))) char smem[8 * SK_AREA + 8 * 2048 + 256];
  const int b = blockIdx.x, xcd = b & 7, j = b >> 3;
  const int mtile = xcd * 4 + (j >> 3), ntile = j & 7;
  const int rot = 0;
  if (ntile < 5) spatial_body<5>(smem, wsp, z2f, stats, bsp, z1f, uf, mtile, ntile * 5, rot);
  else spatial_body<6>(smem, wsp, z2f, stats, bsp, z1f, uf, mtile, 25 + (ntile - 5) * 6, rot);
}

__global__ __launch_bounds__(512, 2) void k_wout(const char* __restrict__ uf, const u16* __restrict__ wout,
                                                 const u16* __restrict__ x1, const float* __restrict__ gb,
                                                 u16* __restrict__ out) {
  __shared__ __attribute__((aligned(16))) char smem[8 * SK_AREA];
  const int b = blockIdx.x, xcd = b & 7, j = b >> 3;
  const int mtile = xcd * 4 + (j >> 3), ntile = j & 7;
  const int tid = threadIdx.x, wave = __builtin_amdgcn_readfirstlane(tid >> 6), lane = tid & 63;
  const int m0 = mtile * 64, n0 = ntile * 64;
  char* ring = smem + wave * SK_AREA;
  const int ks0 = wave < 6 ? wave * 3 : 18 + (wave - 6) * 2, nks = wave < 6 ? 3 : 2;
  const int rw = (lane & 15) >> 1;
  u32x4 areg[3][4];
#pragma unroll
  for (int t = 0; t < 3; ++t) {
    const int ks = ks0 + (t < nks ? t : 0);
#pragma unroll
    for (int i = 0; i < 4; ++i) {
      int row = ks * 32 + i * 8 + ((i & 1) ? (rw ^ 4) : rw);
      if (row > FH - 1) row = FH - 1;
      glds16(wout + (size_t)row * DM + n0 + (lane >> 4) * 16 + (lane & 1) * 8, ring + t * 4096 + i * 1024);
    }
#pragma unroll
    for (int mt = 0; mt < 4; ++mt)
      areg[t][mt] = *reinterpret_cast<const u32x4*>(uf + ((size_t)(mtile * 4 + mt) * UKS + ks) * 1024 + lane * 16);
  }
  const int fr = lane & 15, fg = lane >> 4, hi = wave >> 2, wq = wave & 3;
  u32x2 xv[2];
  const f32x4 gv = *reinterpret_cast<const f32x4*>(gb + n0 + wq * 16 + fg * 4);
#pragma unroll
  for (int i = 0; i < 2; ++i)
    xv[i] = *reinterpret_cast<const u32x2*>(x1 + (size_t)(m0 + (2 * hi + i) * 16 + fr) * DM + n0 + wq * 16 + fg * 4);
  f32x4 acc[4][4];
#pragma unroll
  for (int i = 0; i < 4; ++i)
#pragma unroll
    for (int jj = 0; jj < 4; ++jj) acc[i][jj] = f32x4{0.f, 0.f, 0.f, 0.f};
  wait_vm<0>();
#pragma unroll
  for (int t = 0; t < 3; ++t) {
    if (t < nks) {
      bf16x8 bfr[4];
      sk_bfrags(ring + t * 4096, bfr, lane);
#pragma unroll
      for (int mt = 0; mt < 4; ++mt)
#pragma unroll
        for (int nt = 0; nt < 4; ++nt)
          acc[mt][nt] = __builtin_amdgcn_mfma_f32_16x16x32_bf16(bfr[nt], __builtin_bit_cast(bf16x8, areg[t][mt]), acc[mt][nt], 0, 0, 0);
    }
  }
  f32x4 own[2];
  splitk_reduce<4>(acc, own, smem, wave, lane);
#pragma unroll
  for (int i = 0; i < 2; ++i) {
    u32x2 w;
    w[0] = pack2(bflo(xv[i][0]) + gv[0] * own[i][0], bfhi(xv[i][0]) + gv[1] * own[i][1]);
    w[1] = pack2(bflo(xv[i][1]) + gv[2] * own[i][2], bfhi(xv[i][1]) + gv[3] * own[i][3]);
    *reinterpret_cast<u32x2*>(out + (size_t)(m0 + (2 * hi + i) * 16 + fr) * DM + n0 + wq * 16 + fg * 4) = w;
  }
}

extern "C" void kernel_launch(void* const* d_in, const int* in_sizes, int n_in, void* d_out, int out_size,
                              void* d_ws, size_t ws_size, hipStream_t stream) {
  const u16* x = (const u16*)d_in[0];
  const u16* ckv = (const u16*)d_in[1];
  const u16* wq = (const u16*)d_in[2];
  const u16* wk = (const u16*)d_in[3];
  const u16* wv = (const u16*)d_in[4];
  const u16* wo = (const u16*)d_in[5];
  const u16* win = (const u16*)d_in[7];
  const u16* wsp = (const u16*)d_in[8];
  const u16* bsp = (const u16*)d_in[9];
  const u16* wout = (const u16*)d_in[10];
  const u16* c = (const u16*)d_in[11];
  const u16* wmod = (const u16*)d_in[12];
  char* ws = (char*)d_ws;
  float* mod = (float*)(ws + WS_MOD);
  u16* h1 = (u16*)(ws + WS_H1);
  char* Qf = ws + WS_QF;
  char* Ki = ws + WS_KI;
  char* Vi = ws + WS_VI;
  char* ctx = ws + WS_CTX;
  u16* x1 = (u16*)(ws + WS_X1);
  char* z1 = ws + WS_Z1;
  char* z2 = ws + WS_Z2;
  float* stats = (float*)(ws + WS_ST);
  char* u = ws + WS_U;
  u16* out = (u16*)d_out;

  k_kv<<<256, 512, 0, stream>>>(ckv, wk, wv, Ki, Vi, c, wmod, mod);
  k_ln<<<S / 4, 256, 0, stream>>>(x, mod + 0 * DM, mod + 1 * DM, h1);
  k_qkv<<<512, 256, 0, stream>>>(h1, ckv, wq, wk, wv, Qf, Ki, Vi);
  k_attn<<<256, 512, 0, stream>>>(Qf, Ki, Vi, ctx, wo, win, wsp, wout);
  k_oproj<<<256, 512, 0, stream>>>(ctx, wo, x, mod + 2 * DM, x1);
  k_win<<<256, 512, 0, stream>>>(x1, mod + 3 * DM, mod + 4 * DM, win, z1, z2, stats);
  k_spatial<<<256, 512, 0, stream>>>(wsp, z2, stats, bsp, z1, u);
  k_wout<<<256, 512, 0, stream>>>(u, wout, x1, mod + 5 * DM, out);
}
```

```cpp
#include <hip/hip_runtime.h>
#include <hip/hip_bf16.h>
#include <stdint.h>
#include <utility>

constexpr int S = 2048;
constexpr int DM = 512;
constexpr int NH = 32;
constexpr int HD = 128;
constexpr int HDM = NH * HD;
constexpr int FH = 688;
constexpr int FF = 1376;

typedef __attribute__((ext_vector_type(8))) short bf16x8;
typedef __attribute__((ext_vector_type(4))) short s16x4;
typedef __attribute__((ext_vector_type(4))) float f32x4;
typedef __attribute__((ext_vector_type(16))) float f32x16;
typedef __attribute__((ext_vector_type(4))) unsigned u32x4;
typedef __attribute__((ext_vector_type(2))) unsigned u32x2;
typedef __attribute__((address_space(3))) s16x4* lds_s16x4_ptr;
typedef unsigned short u16;

#define DI __device__ __forceinline__

DI float bf2f(u16 v) { return __uint_as_float(((unsigned)v) << 16); }
DI float bflo(unsigned v) { return __uint_as_float(v << 16); }
DI float bfhi(unsigned v) { return __uint_as_float(v & 0xffff0000u); }
typedef __attribute__((ext_vector_type(2))) __bf16 bf16x2_t;
typedef __attribute__((ext_vector_type(2))) float f32x2;
DI unsigned pack2(float lo, float hi) {
  f32x2 v = {lo, hi};
  return __builtin_bit_cast(unsigned, __builtin_convertvector(v, bf16x2_t));
}
template <int N> DI void wait_vm() { asm volatile("s_waitcnt vmcnt(%0)" ::"n"(N) : "memory"); }
DI void barrier() { asm volatile("s_waitcnt lgkmcnt(0)" ::: "memory"); __builtin_amdgcn_s_barrier(); }
DI float rbf(float x) { return bflo(pack2(x, 0.f)); }

DI void glds16(const void* g, const void* lds_wave_base) {
  const unsigned a = __builtin_amdgcn_readfirstlane((unsigned)(size_t)lds_wave_base);
  unsigned keep;
  asm volatile("s_mov_b32 %0, m0\n\ts_mov_b32 m0, %2\n\ts_nop 0\n\tglobal_load_lds_dwordx4 %1, off\n\ts_mov_b32 m0, %0"
               : "=&s"(keep) : "v"(g), "s"(a) : "memory");
}
template <int OFF> DI void glds16s(const void* sbase, unsigned voff, unsigned lds_base) {
  asm volatile("s_mov_b32 m0, %2\n\ts_nop 0\n\tglobal_load_lds_dwordx4 %0, %1 offset:%3"
               : : "v"(voff), "s"(sbase), "s"(lds_base), "n"(OFF) : "memory");
}
DI unsigned lds_addr(const void* p) { return __builtin_amdgcn_readfirstlane((unsigned)(size_t)p); }
DI void pf_line(const void* g, const void* lds_dummy) {
  const unsigned a = __builtin_amdgcn_readfirstlane((unsigned)(size_t)lds_dummy);
  unsigned keep;
  asm volatile("s_mov_b32 %0, m0\n\ts_mov_b32 m0, %2\n\ts_nop 0\n\tglobal_load_lds_dword %1, off\n\ts_mov_b32 m0, %0"
               : "=&s"(keep) : "v"(g), "s"(a) : "memory");
}
DI void pf_slice(const void* base, int nbytes, int first, int lane, const void* lds_dummy) {
  int l = first + lane;
  const int last = (nbytes >> 7) - 1;
  if (l > last) l = last;
  pf_line((const char*)base + (size_t)l * 128, lds_dummy);
}
#define STAMP(k, i)
template <int CTRL> DI float dpp_add(float v) {
  return v + __uint_as_float(__builtin_amdgcn_update_dpp(0u, __float_as_uint(v), CTRL, 0xf, 0xf, true));
}
DI float row16_sum(float v) {
  v = dpp_add<0xB1>(v); v = dpp_add<0x4E>(v); v = dpp_add<0x141>(v); v = dpp_add<0x140>(v);
  return v;
}
DI float wave_sum(float v) {
  v = row16_sum(v);
  auto a = __builtin_amdgcn_permlane16_swap(__float_as_uint(v), __float_as_uint(v), false, false);
  v = __uint_as_float(a[0]) + __uint_as_float(a[1]);
  auto b = __builtin_amdgcn_permlane32_swap(__float_as_uint(v), __float_as_uint(v), false, false);
  return __uint_as_float(b[0]) + __uint_as_float(b[1]);
}
DI s16x4 tr_read(const char* p) {
  return __builtin_amdgcn_ds_read_tr16_b64_v4i16((lds_s16x4_ptr)(p));
}

constexpr size_t WS_MOD = 0;
constexpr size_t WS_H1 = 16384;
constexpr size_t WS_QF = WS_H1 + (size_t)S * DM * 2;
constexpr size_t WS_KI = WS_QF + (size_t)S * HDM * 2;
constexpr size_t WS_VI = WS_KI + (size_t)S * HDM * 2;
constexpr size_t WS_CTX = WS_VI + (size_t)S * HDM * 2;
constexpr size_t WS_X1 = WS_CTX + (size_t)S * HDM * 2;
constexpr size_t WS_Z1 = WS_X1 + (size_t)S * DM * 2;
constexpr size_t WS_Z2 = WS_Z1 + (size_t)S * FH * 2;
constexpr size_t WS_ST = WS_Z2 + (size_t)S * FH * 2;
constexpr size_t WS_U = WS_ST + (size_t)8 * S * 2 * 4;
constexpr size_t WS_SLAB = WS_U + (size_t)S * 704 * 2;
constexpr size_t WS_FLAG = WS_SLAB + (size_t)96 * 4 * 33 * 1024;
constexpr int LDU = 704;

__global__ __launch_bounds__(256) void k_mod(const u16* __restrict__ c, const u16* __restrict__ w,
                                             float* __restrict__ mod) {
  __shared__ float red[64][33];
  const int t = threadIdx.x, cg = t & 3, ks = t >> 2;
  const int col0 = blockIdx.x * 32 + cg * 8;
  float acc[8];
#pragma unroll
  for (int j = 0; j < 8; ++j) acc[j] = 0.f;
#pragma unroll
  for (int kk = 0; kk < 8; ++kk) {
    const int k = ks * 8 + kk;
    float cv = bf2f(c[k]);
    float sv = rbf(cv / (1.f + __expf(-cv)));
    u32x4 wv = *reinterpret_cast<const u32x4*>(w + (size_t)k * 3072 + col0);
#pragma unroll
    for (int j = 0; j < 4; ++j) {
      acc[2 * j] += sv * bflo(wv[j]);
      acc[2 * j + 1] += sv * bfhi(wv[j]);
    }
  }
#pragma unroll
  for (int j = 0; j < 8; ++j) red[ks][cg * 8 + j] = acc[j];
  __syncthreads();
  if (t < 32) {
    float s = 0.f;
    for (int i = 0; i < 64; ++i) s += red[i][t];
    mod[blockIdx.x * 32 + t] = rbf(s);
  }
}

__global__ __launch_bounds__(256) void k_ln(const u16* __restrict__ x, const float* __restrict__ sh,
                                            const float* __restrict__ cs, u16* __restrict__ h) {
  const int row = blockIdx.x * 4 + (threadIdx.x >> 6), lane = threadIdx.x & 63;
  u32x4 xv = *reinterpret_cast<const u32x4*>(x + (size_t)row * DM + lane * 8);
  float f[8];
#pragma unroll
  for (int j = 0; j < 4; ++j) { f[2 * j] = bflo(xv[j]); f[2 * j + 1] = bfhi(xv[j]); }
  float s = 0.f;
#pragma unroll
  for (int j = 0; j < 8; ++j) s += f[j];
#pragma unroll
  for (int o = 32; o >= 1; o >>= 1) s += __shfl_xor(s, o);
  const float mean = s * (1.f / DM);
  float v = 0.f;
#pragma unroll
  for (int j = 0; j < 8; ++j) { f[j] -= mean; v += f[j] * f[j]; }
#pragma unroll
  for (int o = 32; o >= 1; o >>= 1) v += __shfl_xor(v, o);
  const float rstd = rsqrtf(v * (1.f / DM) + 1e-5f);
  u32x4 o4;
#pragma unroll
  for (int j = 0; j < 4; ++j) {
    const int k = lane * 8 + 2 * j;
    float a = f[2 * j] * rstd * (1.f + cs[k]) + sh[k];
    float b = f[2 * j + 1] * rstd * (1.f + cs[k + 1]) + sh[k + 1];
    o4[j] = pack2(a, b);
  }
  *reinterpret_cast<u32x4*>(h + (size_t)row * DM + lane * 8) = o4;
}

template <int BM, int BN, int KS, int NWM, int NWN>
struct Gemm {
  static constexpr int NW = NWM * NWN;
  static constexpr int WM = BM / NWM, WN = BN / NWN;
  static constexpr int MT = WM / 16, NT = WN / 16;
  static constexpr int BK = 64 * KS;
  static constexpr int A_BYTES = BM * BK * 2;
  static constexpr int B_BYTES = BK * BN * 2;
  static constexpr int STAGE_BYTES = A_BYTES + B_BYTES;
  static constexpr int NB = BN / 16;

  static DI void stageA(const u16* __restrict__ A, int lda, int m0, int k0, char* lds, int wave, int lane) {
    constexpr int NI = A_BYTES / 1024;
    const int rr = lane >> 3, ch = (lane & 7) ^ rr;
#pragma unroll
    for (int ii = 0; ii < NI / NW; ++ii) {
      const int i = ii * NW + wave;
      const int sub = i / (BM / 8), j = i % (BM / 8);
      const u16* src = A + (size_t)(m0 + j * 8 + rr) * lda + k0 + sub * 64 + ch * 8;
      glds16(src, lds + i * 1024);
    }
  }
  static DI void stageB(const u16* __restrict__ B, int ldb, int k0, int n0, int nmax, char* lds, int wave, int lane) {
    constexpr int NI = B_BYTES / 1024;
    const int pos = lane & 15, rw = pos >> 1, hf = pos & 1;
#pragma unroll
    for (int ii = 0; ii < NI / NW; ++ii) {
      const int i = ii * NW + wave;
      const int blk = i * 4 + (lane >> 4);
      const int kb = blk / NB, nb = blk % NB;
      const int row = rw ^ ((kb & 1) << 2);
      int n = n0 + nb * 16 + hf * 8;
      if (n > nmax) n = nmax;
      const u16* src = B + (size_t)(k0 + kb * 8 + row) * ldb + n;
      glds16(src, lds + i * 1024);
    }
  }
  template <bool SWAP>
  static DI void compute(const char* ldsA, const char* ldsB, f32x4 (&acc)[MT][NT], int wm, int wn, int lane) {
    const int fr = lane & 15, fg = lane >> 4;
    const int q = fr >> 2, p = lane & 3;
#pragma unroll
    for (int sub = 0; sub < KS; ++sub) {
#pragma unroll
      for (int kk = 0; kk < 2; ++kk) {
        bf16x8 af[MT], bfr[NT];
#pragma unroll
        for (int mt = 0; mt < MT; ++mt) {
          const int row = wm * WM + mt * 16 + fr;
          af[mt] = *reinterpret_cast<const bf16x8*>(ldsA + sub * (BM * 128) + row * 128 + (((kk * 4 + fg) ^ (row & 7)) << 4));
        }
#pragma unroll
        for (int nt = 0; nt < NT; ++nt) {
          const int kb = sub * 8 + kk * 4 + fg, nb = wn * NT + nt;
          const char* base = ldsB + (kb * NB + nb) * 256 + p * 8;
          const int sw = (fg & 1) << 2;
          s16x4 lo = tr_read(base + ((q) ^ sw) * 32);
          s16x4 hi = tr_read(base + ((4 + q) ^ sw) * 32);
          bfr[nt] = __builtin_shufflevector(lo, hi, 0, 1, 2, 3, 4, 5, 6, 7);
        }
#pragma unroll
        for (int mt = 0; mt < MT; ++mt)
#pragma unroll
          for (int nt = 0; nt < NT; ++nt) {
            if constexpr (SWAP)
              acc[mt][nt] = __builtin_amdgcn_mfma_f32_16x16x32_bf16(bfr[nt], af[mt], acc[mt][nt], 0, 0, 0);
            else
              acc[mt][nt] = __builtin_amdgcn_mfma_f32_16x16x32_bf16(af[mt], bfr[nt], acc[mt][nt], 0, 0, 0);
          }
      }
    }
  }
};

using G1 = Gemm<128, 128, 1, 2, 2>;

constexpr float QSCALE = 0.08838834764831845f * 1.4426950408889634f;
__global__ __launch_bounds__(256, 2) void k_qkv(const u16* __restrict__ h1, const u16* __restrict__ wq,
                                                char* __restrict__ Qf, unsigned* __restrict__ flags) {
  __shared__ __attribute__((aligned(16))) char smem[2 * G1::STAGE_BYTES];
  const int b = blockIdx.x, xcd = b & 7, j = b >> 3;
  const int head = xcd * 4 + (j & 3), mtile = j >> 2;
  const int tid = threadIdx.x, wave = __builtin_amdgcn_readfirstlane(tid >> 6), lane = tid & 63;
  const int wm = wave >> 1, wn = wave & 1;
  const u16* A = h1;
  const u16* B = wq;
  const int m0 = mtile * 128, n0 = head * 128;
  if (b == 0 && tid < 192) flags[tid * 32] = 0u;
  f32x4 acc[G1::MT][G1::NT];
#pragma unroll
  for (int i = 0; i < G1::MT; ++i)
#pragma unroll
    for (int jj = 0; jj < G1::NT; ++jj) acc[i][jj] = f32x4{0.f, 0.f, 0.f, 0.f};
  constexpr int NK = DM / G1::BK;
  G1::stageA(A, DM, m0, 0, smem, wave, lane);
  G1::stageB(B, HDM, 0, n0, HDM, smem + G1::A_BYTES, wave, lane);
  for (int kt = 0; kt < NK; ++kt) {
    wait_vm<0>(); __syncthreads();
    char* cur = smem + (kt & 1) * G1::STAGE_BYTES;
    char* nxt = smem + ((kt + 1) & 1) * G1::STAGE_BYTES;
    if (kt + 1 < NK) {
      G1::stageA(A, DM, m0, (kt + 1) * G1::BK, nxt, wave, lane);
      G1::stageB(B, HDM, (kt + 1) * G1::BK, n0, HDM, nxt + G1::A_BYTES, wave, lane);
    }
    G1::compute<true>(cur, cur + G1::A_BYTES, acc, wm, wn, lane);
  }
  __syncthreads();
  const int fr = lane & 15, fg = lane >> 4;
#pragma unroll
  for (int mt = 0; mt < G1::MT; ++mt)
#pragma unroll
    for (int nt = 0; nt < G1::NT; ++nt) {
      u32x2 v;
      v[0] = pack2(acc[mt][nt][0] * QSCALE, acc[mt][nt][1] * QSCALE);
      v[1] = pack2(acc[mt][nt][2] * QSCALE, acc[mt][nt][3] * QSCALE);
      const int m = wm * 64 + mt * 16 + fr, rg = m >> 5, r = m & 31;
      const int off = rg * 8192 + (wn * 4 + nt) * 1024 + (((fg & 1) * 32 + r) << 4) + (fg >> 1) * 8;
      *reinterpret_cast<u32x2*>(smem + off) = v;
    }
  __syncthreads();
  char* dst = Qf + ((size_t)head * 16 + mtile) * 32768;
#pragma unroll
  for (int i = 0; i < 8; ++i) {
    const int o = (i * 256 + tid) * 16;
    *reinterpret_cast<u32x4*>(dst + o) = *reinterpret_cast<const u32x4*>(smem + o);
  }
}

#define KV_PD 2
constexpr int KV_NS = 4, KV_STAGE = 32768;
template <bool isV>
DI void kv_body(char* smem, const u16* __restrict__ ckv, const u16* __restrict__ B, char* __restrict__ dstb,
                const u16* __restrict__ cvec, const u16* __restrict__ wmod, float* __restrict__ mod) {
  const int b = blockIdx.x, xcd = b & 7, j = b >> 3;
  const int ntile = xcd * 4 + (j & 3), mtile = j >> 2;
  const int tid = threadIdx.x, wave = __builtin_amdgcn_readfirstlane(tid >> 6), lane = tid & 63;
  const int wm = wave >> 2, wn = wave & 3, r = lane & 31, h = lane >> 5;
  const int m0 = mtile * 256, n0 = (ntile & 15) * 256;
  float* modred = reinterpret_cast<float*>(smem + KV_NS * KV_STAGE);
  STAMP(0, 0);

  const u16* asrc[2];
  const u16* bsrc[2];
#pragma unroll
  for (int ii = 0; ii < 2; ++ii) {
    const int i = ii * 8 + wave;
    const int row = 16 * i + (lane >> 2), ch = (lane & 3) ^ ((lane >> 4) & 3);
    asrc[ii] = ckv + (size_t)(m0 + row) * DM + ch * 8;
    const int blk = i * 4 + (lane >> 4), kb = blk >> 4, nb = blk & 15;
    const int pos = lane & 15, rw = (pos >> 1) ^ ((nb & 1) << 2), hf = pos & 1;
    bsrc[ii] = B + (size_t)(kb * 8 + rw) * HDM + n0 + nb * 16 + hf * 8;
  }
  auto stage = [&](int kt0, int slot) {
    char* dst = smem + slot * KV_STAGE;
    const int kt = (kt0 + 2 * mtile) & 15;
#pragma unroll
    for (int ii = 0; ii < 2; ++ii) {
      glds16(asrc[ii] + kt * 32, dst + (ii * 8 + wave) * 1024);
      glds16(bsrc[ii] + (size_t)kt * 32 * HDM, dst + 16384 + (ii * 8 + wave) * 1024);
    }
  };
  constexpr int NK = DM / 32;
#pragma unroll
  for (int st = 0; st < KV_PD; ++st) stage(st, st);

  if (b < 192) {
    const u16* p = wmod + (size_t)tid * 3072 + b * 16;
    const u32x4 w0 = *reinterpret_cast<const u32x4*>(p), w1 = *reinterpret_cast<const u32x4*>(p + 8);
    const float c = bf2f(cvec[tid]);
    const float sv = rbf(c / (1.f + __expf(-c)));
    float v[16];
#pragma unroll
    for (int jj = 0; jj < 4; ++jj) {
      v[2 * jj] = sv * bflo(w0[jj]); v[2 * jj + 1] = sv * bfhi(w0[jj]);
      v[8 + 2 * jj] = sv * bflo(w1[jj]); v[8 + 2 * jj + 1] = sv * bfhi(w1[jj]);
    }
#pragma unroll
    for (int st = 0; st < 4; ++st) {
      const int half = 8 >> st, m = 32 >> st;
      const bool up = lane & m;
#pragma unroll
      for (int e = 0; e < half; ++e) {
        const float send = up ? v[e] : v[half + e];
        const float keep = up ? v[half + e] : v[e];
        v[e] = keep + __shfl_xor(send, m);
      }
    }
    v[0] += __shfl_xor(v[0], 2);
    v[0] += __shfl_xor(v[0], 1);
    if ((lane & 3) == 0) {
      const int col = ((lane >> 5) & 1) * 8 + ((lane >> 4) & 1) * 4 + ((lane >> 3) & 1) * 2 + ((lane >> 2) & 1);
      modred[wave * 16 + col] = v[0];
    }
  }

  STAMP(0, 1);
  f32x16 acc[4][2];
#pragma unroll
  for (int i = 0; i < 4; ++i)
#pragma unroll
    for (int jj = 0; jj < 2; ++jj)
#pragma unroll
      for (int e = 0; e < 16; ++e) acc[i][jj][e] = 0.f;
  const int G = lane >> 4, q4 = (lane & 15) >> 2, p4 = lane & 3;
  int aoff[2];
#pragma unroll
  for (int u = 0; u < 2; ++u) aoff[u] = (wm * 128 + r) * 64 + (((2 * u + h) ^ ((r >> 2) & 3)) << 4);
  const int nh = G & 1;
  const int boff = 16384 + (h * 16 + wn * 4 + nh) * 256 + p4 * 8;
  const int brow0 = (q4 ^ (nh << 2)) * 32, brow1 = ((4 + q4) ^ (nh << 2)) * 32;
  for (int kt = 0; kt < NK; ++kt) {
    if (kt + KV_PD - 1 <= NK - 1) wait_vm<(KV_PD - 1) * 4>(); else wait_vm<0>();
    barrier();
    const bool pf = kt + KV_PD < NK;
    const int ktn = (kt + KV_PD + 2 * mtile) & 15;
    char* dst = smem + ((kt + KV_PD) & (KV_NS - 1)) * KV_STAGE;
    const char* cur = smem + (kt & (KV_NS - 1)) * KV_STAGE;
    bf16x8 af[2][4], bfr[2][2];
#pragma unroll
    for (int u = 0; u < 2; ++u) {
#pragma unroll
      for (int nt = 0; nt < 2; ++nt) {
        const char* bb = cur + boff + u * (2 * 16 * 256) + nt * 512;
        s16x4 lo = tr_read(bb + brow0), hi = tr_read(bb + brow1);
        bfr[u][nt] = __builtin_shufflevector(lo, hi, 0, 1, 2, 3, 4, 5, 6, 7);
      }
#pragma unroll
      for (int mt = 0; mt < 4; ++mt) af[u][mt] = *reinterpret_cast<const bf16x8*>(cur + aoff[u] + mt * 2048);
    }
#pragma unroll
    for (int g4 = 0; g4 < 4; ++g4) {
      const int u = g4 >> 1;
      __builtin_amdgcn_sched_barrier(0);
      if (pf) {
        const int ii = g4 >> 1;
        if (g4 & 1) glds16(bsrc[ii] + (size_t)ktn * 32 * HDM, dst + 16384 + (ii * 8 + wave) * 1024);
        else glds16(asrc[ii] + ktn * 32, dst + (ii * 8 + wave) * 1024);
      }
      __builtin_amdgcn_sched_barrier(0);
#pragma unroll
      for (int m2 = 0; m2 < 2; ++m2)
#pragma unroll
        for (int nt = 0; nt < 2; ++nt) {
          const int mt = (g4 & 1) * 2 + m2;
          if constexpr (isV)
            acc[mt][nt] = __builtin_amdgcn_mfma_f32_32x32x16_bf16(af[u][mt], bfr[u][nt], acc[mt][nt], 0, 0, 0);
          else
            acc[mt][nt] = __builtin_amdgcn_mfma_f32_32x32x16_bf16(bfr[u][nt], af[u][mt], acc[mt][nt], 0, 0, 0);
        }
    }
  }
  STAMP(0, 2);
  {
    const int hd = (ntile & 15) * 2 + (wn >> 1);
    char* base = dstb + ((size_t)hd * 32 + mtile * 4 + wm * 2) * 16384 + lane * 16;
#pragma unroll
    for (int mt = 0; mt < 4; ++mt)
#pragma unroll
      for (int nt = 0; nt < 2; ++nt)
#pragma unroll
        for (int u = 0; u < 2; ++u) {
          u32x4 v;
#pragma unroll
          for (int e = 0; e < 4; ++e) v[e] = pack2(acc[mt][nt][8 * u + 2 * e], acc[mt][nt][8 * u + 2 * e + 1]);
          const int db = (wn & 1) * 2 + nt;
          int off;
          if constexpr (isV) off = (mt >> 1) * 16384 + ((mt & 1) * 2 + u) * 4096 + db * 1024;
          else off = (mt >> 1) * 16384 + (mt & 1) * 8192 + (db * 2 + u) * 1024;
          *reinterpret_cast<u32x4*>(base + off) = v;
        }
  }
  barrier();
  STAMP(0, 3);
  if (b < 192 && tid < 16) {
    float sum = 0.f;
#pragma unroll
    for (int i = 0; i < 8; ++i) sum += modred[i * 16 + tid];
    mod[b * 16 + tid] = rbf(sum);
  }
}
__global__ __launch_bounds__(512, 2) void k_kv(const u16* __restrict__ ckv, const u16* __restrict__ wk,
                                               const u16* __restrict__ wv, char* __restrict__ Ki,
                                               char* __restrict__ Vi, const u16* __restrict__ cvec,
                                               const u16* __restrict__ wmod, float* __restrict__ mod) {
  __shared__ __attribute__((aligned(16))) char smem[KV_NS * KV_STAGE + 512];
  if ((blockIdx.x & 7) >= 4) kv_body<true>(smem, ckv, wv, Vi, cvec, wmod, mod);
  else kv_body<false>(smem, ckv, wk, Ki, cvec, wmod, mod);
}

#define ACLOB_LO "a0","a1","a2","a3","a4","a5","a6","a7","a8","a9","a10","a11","a12","a13","a14","a15","a16","a17","a18","a19","a20","a21","a22","a23","a24","a25","a26","a27","a28","a29","a30","a31","a32","a33","a34","a35","a36","a37","a38","a39","a40","a41","a42","a43","a44","a45","a46","a47","a48","a49","a50","a51","a52","a53","a54","a55","a56","a57","a58","a59","a60","a61","a62","a63","a64","a65","a66","a67","a68","a69","a70","a71","a72","a73","a74","a75","a76","a77","a78","a79","a80","a81","a82","a83","a84","a85","a86","a87","a88","a89","a90","a91","a92","a93","a94","a95","a96","a97","a98","a99","a100","a101","a102","a103","a104","a105","a106","a107","a108","a109","a110","a111","a112","a113","a114","a115","a116","a117","a118","a119","a120","a121","a122","a123","a124","a125","a126","a127"
#define ACLOB_HI "a128","a129","a130","a131","a132","a133","a134","a135","a136","a137","a138","a139","a140","a141","a142","a143","a144","a145","a146","a147","a148","a149","a150","a151","a152","a153","a154","a155","a156","a157","a158","a159","a160","a161","a162","a163","a164","a165","a166","a167","a168","a169","a170","a171","a172","a173","a174","a175","a176","a177","a178","a179","a180","a181","a182","a183","a184","a185","a186","a187","a188","a189","a190","a191","a192","a193","a194","a195","a196","a197","a198","a199","a200","a201","a202","a203","a204","a205","a206","a207","a208","a209","a210","a211","a212","a213","a214","a215","a216","a217","a218","a219","a220","a221","a222","a223","a224","a225","a226","a227","a228","a229","a230","a231","a232","a233","a234","a235","a236","a237","a238","a239","a240","a241","a242","a243","a244","a245","a246","a247","a248","a249","a250","a251","a252","a253","a254","a255"
template <int... Is> using iseq = std::integer_sequence<int, Is...>;
template <int N> using make_iseq = std::make_integer_sequence<int, N>;
#define GAP() __builtin_amdgcn_sched_barrier(0)
constexpr float AT_C = 0.08838834764831845f * 1.4426950408889634f;
constexpr float AT_THR = 8.f;
constexpr int AT_MISC = 65536;
constexpr int AT_SPLIT = 18;
constexpr int AT_SLAB = 4 * 33 * 1024;
DI float max3f(float a, float b, float c) { float r; asm("v_max3_f32 %0, %1, %2, %3" : "=v"(r) : "v"(a), "v"(b), "v"(c)); return r; }
DI float max2f(float a, float b) { float r; asm("v_max_f32_e32 %0, %1, %2" : "=v"(r) : "v"(a), "v"(b)); return r; }
DI float fadd_s(float a, float b) { float r; asm("v_add_f32_e32 %0, %1, %2" : "=v"(r) : "v"(a), "v"(b)); return r; }
DI float fmul_s(float a, float b) { float r; asm("v_mul_f32_e32 %0, %1, %2" : "=v"(r) : "v"(a), "v"(b)); return r; }
DI float fsub_s(float a, float b) { float r; asm("v_sub_f32_e32 %0, %1, %2" : "=v"(r) : "v"(a), "v"(b)); return r; }
DI unsigned lane_id() { unsigned l; asm volatile("v_mbcnt_lo_u32_b32 %0, -1, 0\n\tv_mbcnt_hi_u32_b32 %0, -1, %0" : "=v"(l)); return l; }
DI void negm_bcast(f32x16& negm, float neg_mhat) {
  const float aone = lane_id() < 32u ? 1.f : 0.f;
  asm volatile("s_nop 1\n\tv_mfma_f32_32x32x2_f32 %0, %1, %2, 0" : "+v"(negm) : "v"(aone), "v"(neg_mhat));
}
struct AC {
  unsigned vb, voff;
  unsigned m0w;
  const char *Kh, *Vh;
  const char *ka, *va;
  int kt, vt, end0, start1;
  int dd;
  f32x16 S[2][2][2];
  f32x16 NEGM[2];
  bf16x8 Vf[4][4];
  bf16x8 Pk[2][4];
  float mhat[2], lsum[2], fct[2]; bool pend;
  float rs0[2], rs1[2], pmx0[2], pmx1[2], rmax[2];
};
constexpr int AO_(int db, int qb) { return 16 * (2 * db + qb); }
constexpr int AQ_(int qb, int d0) { return 128 + 32 * qb + 4 * d0; }
constexpr int AK_(int kb, int d0) { return 192 + 32 * kb + 4 * d0; }
template <int SL, int I> DI void v_read(AC& c) {
  asm volatile("ds_read_b128 %0, %1 offset:%2" : "=v"(c.Vf[I & 3][I >> 2]) : "v"(c.vb), "i"(32768 + SL * 16384 + I * 1024) : "memory");
}
template <int SL, int I> DI void k_read(AC& c) {
  asm volatile("ds_read_b128 a[%c1:%c2], %0 offset:%c3" ::"v"(c.vb), "i"(192 + 4 * I), "i"(195 + 4 * I), "i"(SL * 16384 + I * 1024) : "memory", ACLOB_HI);
}
template <int I> DI void q_load(const char* qp, unsigned voff) {
  asm volatile("global_load_dwordx4 a[%c2:%c3], %0, %1 offset:%c4" ::"v"(voff), "s"(qp + (I >> 2) * 4096), "i"(128 + 4 * I), "i"(131 + 4 * I), "i"((I & 3) * 1024) : "memory", ACLOB_HI);
}
template <int OFF> DI void dma(const char* src, unsigned voff, unsigned lds) {
  asm volatile("s_mov_b32 m0, %2\n\ts_nop 0\n\tglobal_load_lds_dwordx4 %0, %1 offset:%3" ::"v"(voff), "s"(src), "s"(lds), "n"(OFF) : "memory");
}
template <int X, bool FIRST, int M> DI void qk_mfma(AC& c) {
  constexpr int d0 = M >> 2, kb = (M >> 1) & 1, qb = M & 1, ak = AK_(kb, d0), aq = AQ_(qb, d0);
  if constexpr (d0 == 0) {
    if constexpr (FIRST) asm volatile("v_mfma_f32_32x32x16_bf16 %0, a[%c1:%c2], a[%c3:%c4], 0" : "=&v"(c.S[X][kb][qb]) : "i"(ak), "i"(ak + 3), "i"(aq), "i"(aq + 3) : ACLOB_LO, ACLOB_HI);
    else asm volatile("v_mfma_f32_32x32x16_bf16 %0, a[%c2:%c3], a[%c4:%c5], %1" : "=&v"(c.S[X][kb][qb]) : "v"(c.NEGM[qb]), "i"(ak), "i"(ak + 3), "i"(aq), "i"(aq + 3) : ACLOB_LO, ACLOB_HI);
  } else asm volatile("v_mfma_f32_32x32x16_bf16 %0, a[%c1:%c2], a[%c3:%c4], %0" : "+v"(c.S[X][kb][qb]) : "i"(ak), "i"(ak + 3), "i"(aq), "i"(aq + 3) : ACLOB_LO, ACLOB_HI);
}
template <int M, bool ZERO> DI void pv_mfma(AC& c) {
  constexpr int ks = M >> 3, db = (M >> 1) & 3, qb = M & 1, ao = AO_(db, qb);
  if constexpr (ZERO && ks == 0) asm volatile("v_mfma_f32_32x32x16_bf16 a[%c2:%c3], %0, %1, 0" ::"v"(c.Vf[db][ks]), "v"(c.Pk[qb][ks]), "i"(ao), "i"(ao + 15) : ACLOB_LO);
  else asm volatile("v_mfma_f32_32x32x16_bf16 a[%c2:%c3], %0, %1, a[%c2:%c3]" ::"v"(c.Vf[db][ks]), "v"(c.Pk[qb][ks]), "i"(ao), "i"(ao + 15) : ACLOB_LO);
}
template <int X, int QB, int KS, int P> DI void pack1(AC& c) {
  u32x4 w = __builtin_bit_cast(u32x4, c.Pk[QB][KS]);
  unsigned d = pack2(c.S[X][KS >> 1][QB][8 * (KS & 1) + 2 * P], c.S[X][KS >> 1][QB][8 * (KS & 1) + 2 * P + 1]);
  asm volatile("" : "+v"(d));
  w[P] = d;
  c.Pk[QB][KS] = __builtin_bit_cast(bf16x8, w);
}
template <int X, int J> DI void rowsum1(AC& c) {
  constexpr int qb = J >> 4, ks = (J >> 2) & 3, p = J & 3;
  c.rs0[qb] = fadd_s(c.rs0[qb], c.S[X][ks >> 1][qb][8 * (ks & 1) + 2 * p]);
  c.rs1[qb] = fadd_s(c.rs1[qb], c.S[X][ks >> 1][qb][8 * (ks & 1) + 2 * p + 1]);
}
DI void lsum_merge(AC& c) {
#pragma unroll
  for (int qb = 0; qb < 2; ++qb) {
    const float rs = fadd_s(c.rs0[qb], c.rs1[qb]);
    auto pr = __builtin_amdgcn_permlane32_swap(__float_as_uint(rs), __float_as_uint(rs), false, false);
    c.lsum[qb] = fadd_s(c.lsum[qb], fadd_s(__uint_as_float(pr[0]), __uint_as_float(pr[1])));
  }
  asm volatile("" : "+v"(c.lsum[0]), "+v"(c.lsum[1]));
}
#define AT_VF "+v"(c.Vf[0][0]), "+v"(c.Vf[0][1]), "+v"(c.Vf[0][2]), "+v"(c.Vf[0][3]), "+v"(c.Vf[1][0]), "+v"(c.Vf[1][1]), "+v"(c.Vf[1][2]), "+v"(c.Vf[1][3]), \
              "+v"(c.Vf[2][0]), "+v"(c.Vf[2][1]), "+v"(c.Vf[2][2]), "+v"(c.Vf[2][3]), "+v"(c.Vf[3][0]), "+v"(c.Vf[3][1]), "+v"(c.Vf[3][2]), "+v"(c.Vf[3][3])
template <int X, bool FIRST, int M> DI void gapA(AC& c) {
  constexpr int Xp = X ^ 1;
  qk_mfma<X, FIRST, M>(c);
  if constexpr (M == 16) {
    c.ka = c.Kh + (size_t)(unsigned)c.kt * 16384; asm volatile("" : "+s"(c.ka));
    c.kt = c.kt + 1 == c.end0 ? c.start1 : c.kt + 1; asm volatile("" : "+s"(c.kt));
  }
  if constexpr (M == 18) {
    c.va = c.Vh + (size_t)(unsigned)c.vt * 16384; asm volatile("" : "+s"(c.va));
    c.vt = c.vt + 1 == c.end0 ? c.start1 : c.vt + 1; asm volatile("" : "+s"(c.vt));
  }
  if constexpr (!FIRST && M == 8) asm volatile("" ::"v"(c.NEGM[0]), "v"(c.NEGM[1]));
  if constexpr (!FIRST) {
    if constexpr (M >= 10 && M < 26) v_read<Xp, M - 10>(c);
    if constexpr (M < 16) {
      constexpr int e0 = 2 * M, e1 = 2 * M + 1;
      c.S[Xp][1][e0 >> 4][e0 & 15] = __builtin_amdgcn_exp2f(c.S[Xp][1][e0 >> 4][e0 & 15]);
      c.S[Xp][1][e1 >> 4][e1 & 15] = __builtin_amdgcn_exp2f(c.S[Xp][1][e1 >> 4][e1 & 15]);
      pack1<Xp, (M >> 3), ((M >> 2) & 1), (M & 3)>(c);
    } else {
      constexpr int cc = M - 16;
      pack1<Xp, (cc >> 3), 2 + ((cc >> 2) & 1), (cc & 3)>(c);
      rowsum1<Xp, 16 * (cc >> 3) + 8 + 4 * ((cc >> 2) & 1) + (cc & 3)>(c);
    }
  }
  GAP();
}
template <int X, bool FIRST, int VMN, int... Ms> DI void phaseA_(AC& c, iseq<Ms...>) {
  (gapA<X, FIRST, Ms>(c), ...);
  if constexpr (!FIRST) lsum_merge(c);
  if constexpr (FIRST) asm volatile("s_waitcnt vmcnt(%0) lgkmcnt(0)\n\ts_barrier" ::"n"(VMN) : "memory");
  else asm volatile("s_waitcnt vmcnt(%16) lgkmcnt(0)\n\ts_barrier" : AT_VF : "n"(VMN) : "memory");
  GAP();
}
template <int X, bool FIRST, int VMN = 0> DI void phaseA(AC& c) { phaseA_<X, FIRST, VMN>(c, make_iseq<32>{}); }

template <int KK> DI void mask1(float& x, int lim, float ninf) {
  asm volatile("v_cmp_gt_i32_e32 vcc, %c3, %1\n\tv_cndmask_b32_e32 %0, %0, %2, vcc" : "+v"(x) : "v"(lim), "v"(ninf), "i"(KK) : "vcc");
}
template <int X, int... Es> DI void mask_all(AC& c, int lim, float ninf, iseq<Es...>) {
  (mask1<32 * ((Es >> 4) & 1) + (Es & 3) + 8 * ((Es >> 2) & 3) - 32 * (Es >> 5)>(reinterpret_cast<float*>(&c.S[X][(Es >> 4) & 1][Es >> 5])[Es & 15], lim, ninf), ...);
}
template <int X> DI void at_mask(AC& c) {
  if (c.dd >= 0) {
    const unsigned l = lane_id();
    const int lim = (int)(l & 31u) - (int)((l >> 5) << 2) - 64 * c.dd;
    mask_all<X>(c, lim, -__builtin_inff(), make_iseq<64>{});
  }
}
template <int X, bool PEEL, bool ZERO1, int M> DI void gapB(AC& c) {
  if constexpr (!PEEL) pv_mfma<M, ZERO1>(c);
  if constexpr (M < 4) dma<M * 1024>(c.ka, c.voff, c.m0w + X * 16384);
  else if constexpr (M < 8) dma<(M - 4) * 1024>(c.va, c.voff, c.m0w + 32768 + (X ^ 1) * 16384);
  if constexpr (M >= 1 && M <= 16) k_read<X ^ 1, M - 1>(c);
  if constexpr (M == 8) at_mask<X>(c);
  if constexpr (M >= 8 && M < 16) {
    constexpr int i = M - 8, q = i >> 2, r = 4 * (i & 3);
    if constexpr ((i & 3) == 0) {
      c.pmx0[q] = max3f(c.S[X][0][q][0], c.S[X][0][q][1], c.S[X][1][q][0]); c.pmx1[q] = max3f(c.S[X][0][q][2], c.S[X][0][q][3], c.S[X][1][q][1]);
      c.pmx0[q] = max3f(c.pmx0[q], c.S[X][1][q][2], c.S[X][1][q][3]);
    } else {
      c.pmx0[q] = max3f(c.pmx0[q], c.S[X][0][q][r], c.S[X][0][q][r + 1]); c.pmx1[q] = max3f(c.pmx1[q], c.S[X][0][q][r + 2], c.S[X][0][q][r + 3]);
      c.pmx0[q] = max3f(c.pmx0[q], c.S[X][1][q][r], c.S[X][1][q][r + 1]); c.pmx1[q] = max3f(c.pmx1[q], c.S[X][1][q][r + 2], c.S[X][1][q][r + 3]);
    }
  }
  if constexpr (M == 16 || M == 17) {
    constexpr int q = M - 16;
    const float mm = max2f(c.pmx0[q], c.pmx1[q]);
    auto pr = __builtin_amdgcn_permlane32_swap(__float_as_uint(mm), __float_as_uint(mm), false, false);
    c.rmax[q] = max2f(__uint_as_float(pr[0]), __uint_as_float(pr[1]));
  }
  if constexpr (M == 18) {
    if constexpr (PEEL) {
#pragma unroll
      for (int q = 0; q < 2; ++q) {
        c.mhat[q] = c.rmax[q]; c.lsum[q] = 0.f; c.fct[q] = 1.f;
#pragma unroll
        for (int kb = 0; kb < 2; ++kb)
#pragma unroll
          for (int e = 0; e < 16; ++e) c.S[X][kb][q][e] = fsub_s(c.S[X][kb][q][e], c.rmax[q]);
        negm_bcast(c.NEGM[q], -c.mhat[q]);
      }
    } else {
      const bool grew = (c.rmax[0] > AT_THR) | (c.rmax[1] > AT_THR);
      if (__builtin_expect(__any((int)grew), 0)) {
#pragma unroll
        for (int q = 0; q < 2; ++q) {
          const float dl = max2f(c.rmax[q], 0.f);
          c.mhat[q] = fadd_s(c.mhat[q], dl); c.fct[q] = __builtin_amdgcn_exp2f(-dl);
#pragma unroll
          for (int kb = 0; kb < 2; ++kb)
#pragma unroll
            for (int e = 0; e < 16; ++e) c.S[X][kb][q][e] = fsub_s(c.S[X][kb][q][e], dl);
          negm_bcast(c.NEGM[q], -c.mhat[q]);
        }
        c.pend = true;
      }
    }
    c.rs0[0] = c.rs0[1] = c.rs1[0] = c.rs1[1] = 0.f;
  }
  if constexpr (M >= 19) {
    constexpr int g = M - 19, e_lo = (32 * g) / 13, e_hi = (32 * (g + 1)) / 13;
    constexpr int p_lo = g ? (32 * (g - 1)) / 13 : 0, p_hi = g ? (32 * g) / 13 : 0;
#pragma unroll
    for (int e = p_lo; e < p_hi; ++e) {
      if (e & 1) c.rs1[e >> 4] = fadd_s(c.rs1[e >> 4], c.S[X][0][e >> 4][e & 15]);
      else c.rs0[e >> 4] = fadd_s(c.rs0[e >> 4], c.S[X][0][e >> 4][e & 15]);
    }
#pragma unroll
    for (int e = e_lo; e < e_hi; ++e) c.S[X][0][e >> 4][e & 15] = __builtin_amdgcn_exp2f(c.S[X][0][e >> 4][e & 15]);
  }
  if constexpr (M == 31) asm volatile("" ::"v"(c.S[X][0][0]), "v"(c.S[X][0][1]), "v"(c.NEGM[0]), "v"(c.NEGM[1]), "v"(c.rs0[0]), "v"(c.rs0[1]), "v"(c.rs1[0]), "v"(c.rs1[1]));
  GAP();
}
template <int I> DI void o_scale1(float f) {
  float t;
  asm volatile("v_accvgpr_read_b32 %0, a[%c2]\n\tv_mul_f32 %0, %1, %0\n\tv_accvgpr_write_b32 a[%c2], %0" : "=&v"(t) : "v"(f), "i"(I) : ACLOB_LO);
}
template <int... Is> DI void o_scale(const AC& c, iseq<Is...>) { (o_scale1<Is>(c.fct[(Is >> 4) & 1]), ...); }
template <int X, bool PEEL, bool ZERO1, int... Ms> DI void phaseB_(AC& c, iseq<Ms...>) {
  (gapB<X, PEEL, ZERO1, Ms>(c), ...);
#pragma unroll
  for (int e = (32 * 12) / 13; e < 32; ++e) {
    if (e & 1) c.rs1[e >> 4] = fadd_s(c.rs1[e >> 4], c.S[X][0][e >> 4][e & 15]);
    else c.rs0[e >> 4] = fadd_s(c.rs0[e >> 4], c.S[X][0][e >> 4][e & 15]);
  }
  if constexpr (!PEEL) {
    if (__builtin_expect(c.pend, 0)) {
      asm volatile("s_nop 7\n\ts_nop 7" ::: "memory");
      o_scale(c, make_iseq<128>{});
      asm volatile("s_nop 1" ::: "memory");
      c.lsum[0] = fmul_s(c.lsum[0], c.fct[0]); c.lsum[1] = fmul_s(c.lsum[1], c.fct[1]); c.pend = false;
    }
  }
  c.dd += 1;
  asm volatile("s_waitcnt lgkmcnt(0)" ::: "memory");
  GAP();
}
template <int X, bool PEEL, bool ZERO1> DI void phaseB(AC& c) { phaseB_<X, PEEL, ZERO1>(c, make_iseq<32>{}); }
template <int SL, int... Is> DI void k_reads(AC& c, iseq<Is...>) { (k_read<SL, Is>(c), ...); }
template <int SL, int... Is> DI void v_reads(AC& c, iseq<Is...>) { (v_read<SL, Is>(c), ...); }
template <int... Is> DI void q_loads(const char* qp, unsigned voff, iseq<Is...>) { (q_load<Is>(qp, voff), ...); }
template <int X, int... Cs> DI void packs(AC& c, iseq<Cs...>) { (pack1<X, ((Cs >> 3) & 1), 2 * (Cs >> 4) + ((Cs >> 2) & 1), (Cs & 3)>(c), ...); }
template <int X, int... Js> DI void rowsums(AC& c, iseq<Js...>) { (rowsum1<X, Js>(c), ...); }
template <int M> DI void pv_drain1(AC& c, const char* qn) {
  pv_mfma<M, false>(c);
  if constexpr (M & 1) q_load<(M >> 1)>(qn, c.voff);
  GAP();
}
template <int... Ms> DI void pv_drain(AC& c, const char* qn, iseq<Ms...>) { (pv_drain1<Ms>(c, qn), ...); }
template <int I> DI float o_read() { float r; asm volatile("v_accvgpr_read_b32 %0, a[%c1]" : "=v"(r) : "i"(I)); return r; }
template <int I> DI void o_store(char* p, unsigned voff) {
  asm volatile("global_store_dwordx4 %0, a[%c2:%c3], %1 offset:%c4 sc1\n\ts_nop 1" ::"v"(voff), "s"(p + (I >> 2) * 4096), "i"(4 * I), "i"(4 * I + 3), "i"((I & 3) * 1024) : "memory");
}
template <int... Is> DI void o_stores(char* p, unsigned voff, iseq<Is...>) { (o_store<Is>(p, voff), ...); }
template <bool MERGE, int B> DI void o_final(char* crow, const char* slab, unsigned voff, const float (&f0)[2], const float (&f1)[2]) {
  constexpr int db = B >> 1, qb = B & 1;
  f32x4 ot[4];
  if constexpr (MERGE) {
#pragma unroll
    for (int qd = 0; qd < 4; ++qd) ot[qd] = *reinterpret_cast<const f32x4*>(slab + (B * 4 + qd) * 1024 + voff);
  }
  float o[16] = {o_read<16 * B + 0>(), o_read<16 * B + 1>(), o_read<16 * B + 2>(), o_read<16 * B + 3>(),
                 o_read<16 * B + 4>(), o_read<16 * B + 5>(), o_read<16 * B + 6>(), o_read<16 * B + 7>(),
                 o_read<16 * B + 8>(), o_read<16 * B + 9>(), o_read<16 * B + 10>(), o_read<16 * B + 11>(),
                 o_read<16 * B + 12>(), o_read<16 * B + 13>(), o_read<16 * B + 14>(), o_read<16 * B + 15>()};
#pragma unroll
  for (int qd = 0; qd < 4; ++qd) {
    float v[4];
#pragma unroll
    for (int e = 0; e < 4; ++e) {
      v[e] = fmul_s(o[4 * qd + e], f0[qb]);
      if constexpr (MERGE) v[e] = fadd_s(v[e], fmul_s(ot[qd][e], f1[qb]));
    }
    u32x2 w;
    w[0] = pack2(v[0], v[1]); w[1] = pack2(v[2], v[3]);
    *reinterpret_cast<u32x2*>(crow + qb * (2 * 128 * 1024) + db * 1024 + qd * 256) = w;
  }
}
template <bool MERGE, int... Bs> DI void o_finals(char* crow, const char* slab, unsigned voff, const float (&f0)[2], const float (&f1)[2], iseq<Bs...>) {
  (o_final<MERGE, Bs>(crow, slab, voff, f0, f1), ...);
}
typedef __attribute__((address_space(1))) unsigned gu32;

__global__ __launch_bounds__(256, 1) void k_attn(const char* __restrict__ Qf, const char* __restrict__ Ki,
                                                 const char* __restrict__ Vi, char* __restrict__ ctx,
                                                 char* __restrict__ slabs, unsigned* __restrict__ flags,
                                                 const u16* __restrict__ wo, const u16* __restrict__ win,
                                                 const u16* __restrict__ wsp, const u16* __restrict__ wout) {
  __shared__ __attribute__((aligned(16))) char smem[AT_MISC + 512];
  const int b = blockIdx.x, xcd = b & 7, j = b >> 3;
  const int head = xcd * 4 + (j & 3), role = j >> 2;
  const int wave = __builtin_amdgcn_readfirstlane(threadIdx.x >> 6);
  const int blk0 = role < 6 ? 7 - (role >> 1) : 10 - role;
  const bool second = role < 6 && (role & 1);
  const int t0 = second ? AT_SPLIT : 0, e0 = (role < 6 && !second) ? AT_SPLIT : 4 * blk0 + 4;
  const int nitem = second ? 2 : 1;
  AC c;
  const unsigned ldsb = lds_addr(smem);
  c.voff = (threadIdx.x & 63) * 16u;
  c.vb = ldsb + c.voff;
  c.m0w = ldsb + wave * 4096;
  c.Kh = Ki + (size_t)head * (32 * 16384) + wave * 4096;
  c.Vh = Vi + (size_t)head * (32 * 16384) + wave * 4096;
  c.end0 = e0; c.start1 = 0;
  const char* Qh = Qf + (size_t)head * (64 * 8192) + wave * 16384;
  STAMP(5, 0);
  {
    q_loads(Qh + blk0 * 65536, c.voff, make_iseq<16>{});
    const char* k0 = c.Kh + (size_t)t0 * 16384;
    const char* v0 = c.Vh + (size_t)t0 * 16384;
    dma<0>(k0, c.voff, c.m0w); dma<1024>(k0, c.voff, c.m0w); dma<2048>(k0, c.voff, c.m0w); dma<3072>(k0, c.voff, c.m0w);
    dma<0>(v0, c.voff, c.m0w + 32768); dma<1024>(v0, c.voff, c.m0w + 32768); dma<2048>(v0, c.voff, c.m0w + 32768); dma<3072>(v0, c.voff, c.m0w + 32768);
    k0 += 16384;
    dma<0>(k0, c.voff, c.m0w + 16384); dma<1024>(k0, c.voff, c.m0w + 16384); dma<2048>(k0, c.voff, c.m0w + 16384); dma<3072>(k0, c.voff, c.m0w + 16384);
    c.kt = t0 + 2; c.vt = t0 + 1;
    asm volatile("s_waitcnt vmcnt(8)\n\ts_barrier" ::: "memory");
    k_reads<0>(c, make_iseq<16>{});
    asm volatile("s_waitcnt lgkmcnt(0)" ::: "memory");
  }
  for (int it = 0; it < nitem; ++it) {
    const int blk = it ? (role >> 1) : blk0;
    const int ts = it ? 0 : t0, T = it ? 4 * blk + 4 : e0 - t0;
    c.dd = ts - 4 * blk - wave;
    c.pend = false;
    STAMP(5, it * 4 + 1);
    phaseA<0, true>(c);
    phaseB<0, true, false>(c);
    {
      const int slot = (b * 4 + wave) * 2;
      const int lane = lane_id();
#pragma unroll
      for (int u = 0; u < 2; ++u) {
        const int sl = slot + u;
        const u16* base = sl < 1024 ? wsp : sl < 1536 ? wo : sl < 1708 ? win : wout;
        const int nb = sl < 1024 ? S * S * 2 : sl < 1536 ? HDM * DM * 2 : sl < 1708 ? DM * FF * 2 : FH * DM * 2;
        const int first = sl < 1024 ? sl : sl < 1536 ? sl - 1024 : sl < 1708 ? sl - 1536 : sl - 1708;
        pf_slice(base, nb, first * 64, lane, smem + AT_MISC + 256);
      }
      GAP();
    }
    phaseA<1, false, 2>(c);
    phaseB<1, false, true>(c);
    for (int t = 2; t < T; t += 2) {
      if (t == 6) STAMP(4, 0);
      phaseA<0, false>(c);
      if (t == 6) STAMP(4, 1);
      phaseB<0, false, false>(c);
      if (t == 6) STAMP(4, 2);
      phaseA<1, false>(c);
      if (t == 6) STAMP(4, 3);
      phaseB<1, false, false>(c);
      if (t == 6) STAMP(4, 4);
    }
    STAMP(5, it * 4 + 2);
    const bool split = it == 0 && role < 6;
    gu32* fl = (gu32*)flags + (head * 3 + (role >> 1)) * 64;
    unsigned ticket = 0;
    if (split && threadIdx.x == 0) ticket = __hip_atomic_fetch_add(fl, 1u, __ATOMIC_RELAXED, __HIP_MEMORY_SCOPE_AGENT);
    {
      v_reads<1>(c, make_iseq<16>{});
#pragma unroll
      for (int e = 0; e < 32; ++e) c.S[1][1][e >> 4][e & 15] = __builtin_amdgcn_exp2f(c.S[1][1][e >> 4][e & 15]);
      packs<1>(c, make_iseq<32>{});
      rowsums<1>(c, iseq<8, 9, 10, 11, 12, 13, 14, 15, 24, 25, 26, 27, 28, 29, 30, 31>{});
      lsum_merge(c);
      asm volatile("s_waitcnt lgkmcnt(0)\n\ts_nop 1" : AT_VF::"memory");
      GAP();
      pv_drain(c, Qh + (role >> 1) * 65536, make_iseq<32>{});
      asm volatile("s_nop 7\n\ts_nop 7" ::: "memory");
    }
    const unsigned ln = lane_id();
    const unsigned voff = ln * 16u;
    char* crow = ctx + ((size_t)(blk * 16 + wave * 4 + ((ln >> 4) & 1)) * 128 + head * 4) * 1024 + (ln & 15) * 16 + (ln >> 5) * 8;
    char* slab = slabs + (size_t)(head * 3 + (role >> 1)) * AT_SLAB + wave * (33 * 1024);
    float f0[2], f1[2];
    int mode = 0;
    if (split) {
      unsigned* bc = reinterpret_cast<unsigned*>(smem + AT_MISC);
      if (threadIdx.x == 0) *bc = ticket;
      barrier();
      mode = __builtin_amdgcn_readfirstlane(*bc) == 0 ? 1 : 2;
    }
    if (mode == 1) {
      o_stores(slab, voff, make_iseq<32>{});
      f32x4 ml = {c.mhat[0], c.lsum[0], c.mhat[1], c.lsum[1]};
      asm volatile("global_store_dwordx4 %0, %1, %2 sc1\n\ts_nop 1" ::"v"(voff), "v"(ml), "s"(slab + 32 * 1024) : "memory");
      asm volatile("s_waitcnt vmcnt(0)\n\ts_barrier" ::: "memory");
      if (threadIdx.x == 0) __hip_atomic_store(fl + 32, 1u, __ATOMIC_RELAXED, __HIP_MEMORY_SCOPE_AGENT);
    } else if (mode == 2) {
      if (wave == 0) {
        for (unsigned spins = 0; spins < (1u << 24); ++spins) {
          if (__builtin_amdgcn_readfirstlane(__hip_atomic_load(fl + 32, __ATOMIC_RELAXED, __HIP_MEMORY_SCOPE_AGENT)) != 0u) break;
          __builtin_amdgcn_s_sleep(1);
        }
        __builtin_amdgcn_fence(__ATOMIC_ACQUIRE, "agent");
      }
      asm volatile("s_waitcnt vmcnt(0)\n\ts_barrier" ::: "memory");
      const f32x4 ml = *reinterpret_cast<const f32x4*>(slab + 32 * 1024 + voff);
#pragma unroll
      for (int q = 0; q < 2; ++q) {
        const float mt = fmaxf(c.mhat[q], ml[2 * q]);
        const float a0 = __builtin_amdgcn_exp2f(c.mhat[q] - mt), a1 = __builtin_amdgcn_exp2f(ml[2 * q] - mt);
        const float inv = 1.f / fadd_s(fmul_s(c.lsum[q], a0), fmul_s(ml[2 * q + 1], a1));
        f0[q] = a0 * inv; f1[q] = a1 * inv;
      }
      o_finals<true>(crow, slab, voff, f0, f1, make_iseq<8>{});
    } else {
      f0[0] = 1.f / c.lsum[0]; f0[1] = 1.f / c.lsum[1];
      f1[0] = f1[1] = 0.f;
      o_finals<false>(crow, slab, voff, f0, f1, make_iseq<8>{});
    }
    asm volatile("s_waitcnt vmcnt(0)" ::: "memory");
    STAMP(5, it * 4 + 3);
  }
}

constexpr int SK_AREA = 16384;
template <int NT> struct SkOwn { static constexpr int N = (2 * NT + 3) / 4; };
template <int NT>
DI void splitk_reduce(const f32x4 (&acc)[4][NT], f32x4 (&own)[SkOwn<NT>::N], char* smem, int wave, int lane) {
  const int hi = wave >> 2, wq = wave & 3;
  char* mine = smem + wave * SK_AREA + lane * 16;
  f32x4 r[2 * NT];
#pragma unroll
  for (int f = 0; f < 2 * NT; ++f) {
    const f32x4 lo = acc[f / NT][f % NT], up = acc[2 + f / NT][f % NT];
    f32x4 snd, kp;
#pragma unroll
    for (int e = 0; e < 4; ++e) { snd[e] = hi ? lo[e] : up[e]; kp[e] = hi ? up[e] : lo[e]; }
    *reinterpret_cast<f32x4*>(mine + f * 1024) = snd;
    r[f] = kp;
  }
  barrier();
  {
    const char* part = smem + (wave ^ 4) * SK_AREA + lane * 16;
#pragma unroll
    for (int f = 0; f < 2 * NT; ++f) {
      const f32x4 o = *reinterpret_cast<const f32x4*>(part + f * 1024);
      r[f] += o;
    }
  }
  barrier();
#pragma unroll
  for (int f = 0; f < 2 * NT; ++f) *reinterpret_cast<f32x4*>(mine + f * 1024) = r[f];
  barrier();
#pragma unroll
  for (int i = 0; i < SkOwn<NT>::N; ++i) {
    const int f = wq + 4 * i;
    const char* p = smem + hi * 4 * SK_AREA + lane * 16 + (f < 2 * NT ? f : 0) * 1024;
    const f32x4 a = *reinterpret_cast<const f32x4*>(p), b = *reinterpret_cast<const f32x4*>(p + SK_AREA);
    const f32x4 c = *reinterpret_cast<const f32x4*>(p + 2 * SK_AREA), d = *reinterpret_cast<const f32x4*>(p + 3 * SK_AREA);
    own[i] = (a + b) + (c + d);
  }
}
DI void sk_bfrags(const char* img, bf16x8 (&bfr)[4], int lane) {
  const int fr = lane & 15, fg = lane >> 4, q = fr >> 2, p = lane & 3, sw = (fg & 1) << 2;
  const char* base = img + fg * 1024 + p * 8;
#pragma unroll
  for (int nt = 0; nt < 4; ++nt) {
    s16x4 lo = tr_read(base + nt * 256 + (q ^ sw) * 32);
    s16x4 hi = tr_read(base + nt * 256 + ((4 + q) ^ sw) * 32);
    bfr[nt] = __builtin_shufflevector(lo, hi, 0, 1, 2, 3, 4, 5, 6, 7);
  }
}

constexpr int OP_NS = 16, OP_DB = 4, OP_DA = 6;
constexpr int op_wait(int t) {
  int pos = 0, bpos = -1;
  for (int i = 0; i < OP_DA; ++i) {
    if (i < OP_DB) { pos += 4; if (i == t) bpos = pos; }
    pos += 4;
  }
  for (int s = 0; s < t; ++s) {
    if (s + OP_DB < OP_NS) { pos += 4; if (s + OP_DB == t) bpos = pos; }
    if (s + OP_DA < OP_NS) pos += 4;
  }
  return pos - bpos;
}
template <int T> struct OpWait { static constexpr int v = op_wait(T); };
template <int T> DI void op_step(f32x4 (&acc)[4][4], u32x4 (&areg)[OP_DA][4], char* ring, const char* asrc,
                                 const u16* bs0, const u16* bs1, int g0, int lane) {
  wait_vm<OpWait<T>::v>();
  if constexpr (T == 0) STAMP(2, 2);
  if constexpr (T == 8) STAMP(2, 3);
  bf16x8 bfr[4];
  sk_bfrags(ring + (T % OP_DB) * 4096, bfr, lane);
#pragma unroll
  for (int mt = 0; mt < 4; ++mt)
#pragma unroll
    for (int nt = 0; nt < 4; ++nt)
      acc[mt][nt] = __builtin_amdgcn_mfma_f32_16x16x32_bf16(bfr[nt], __builtin_bit_cast(bf16x8, areg[T % OP_DA][mt]), acc[mt][nt], 0, 0, 0);
  if constexpr (T + OP_DB < OP_NS) {
    asm volatile("s_waitcnt lgkmcnt(0)" ::: "memory");
    const int g = (g0 + T + OP_DB) & 127;
#pragma unroll
    for (int i = 0; i < 4; ++i)
      glds16(((i & 1) ? bs1 : bs0) + (size_t)(g * 32 + i * 8) * DM, ring + (T % OP_DB) * 4096 + i * 1024);
  }
  if constexpr (T + OP_DA < OP_NS) {
    const int g = (g0 + T + OP_DA) & 127;
#pragma unroll
    for (int mt = 0; mt < 4; ++mt)
      areg[T % OP_DA][mt] = *reinterpret_cast<const u32x4*>(asrc + (size_t)mt * (128 * 1024) + g * 1024);
  }
  if constexpr (T + 1 < OP_NS) op_step<T + 1>(acc, areg, ring, asrc, bs0, bs1, g0, lane);
}
__global__ __launch_bounds__(512, 2) void k_oproj(const char* __restrict__ ctxf, const u16* __restrict__ wo,
                                                  const u16* __restrict__ x, const float* __restrict__ ga,
                                                  u16* __restrict__ x1) {
  __shared__ __attribute__((aligned(16))) char smem[8 * SK_AREA + 256];
  const int b = blockIdx.x, xcd = b & 7, j = b >> 3;
  const int mtile = xcd * 4 + (j >> 3), ntile = j & 7;
  const int tid = threadIdx.x, wave = __builtin_amdgcn_readfirstlane(tid >> 6), lane = tid & 63;
  const int m0 = mtile * 64, n0 = ntile * 64;
  char* ring = smem + wave * SK_AREA;
  const char* asrc = ctxf + (size_t)(mtile * 4) * 128 * 1024 + lane * 16;
  const int g0 = wave * 16;
  const int rw = (lane & 15) >> 1;
  const u16* bs0 = wo + (size_t)rw * DM + n0 + (lane >> 4) * 16 + (lane & 1) * 8;
  const u16* bs1 = wo + (size_t)(rw ^ 4) * DM + n0 + (lane >> 4) * 16 + (lane & 1) * 8;
  f32x4 acc[4][4];
#pragma unroll
  for (int i = 0; i < 4; ++i)
#pragma unroll
    for (int jj = 0; jj < 4; ++jj) acc[i][jj] = f32x4{0.f, 0.f, 0.f, 0.f};
  u32x4 areg[OP_DA][4];
  STAMP(2, 0);
#pragma unroll
  for (int i = 0; i < OP_DA; ++i) {
    const int g = (g0 + i) & 127;
    if (i < OP_DB) {
#pragma unroll
      for (int ii = 0; ii < 4; ++ii) glds16(((ii & 1) ? bs1 : bs0) + (size_t)(g * 32 + ii * 8) * DM, ring + i * 4096 + ii * 1024);
    }
#pragma unroll
    for (int mt = 0; mt < 4; ++mt)
      areg[i][mt] = *reinterpret_cast<const u32x4*>(asrc + (size_t)mt * (128 * 1024) + g * 1024);
  }
  STAMP(2, 1);
  op_step<0>(acc, areg, ring, asrc, bs0, bs1, g0, lane);
  STAMP(2, 4);
  const int fr = lane & 15, fg = lane >> 4, hi = wave >> 2, wq = wave & 3;
  u32x2 xv[2];
  f32x4 gv;
  {
    gv = *reinterpret_cast<const f32x4*>(ga + n0 + wq * 16 + fg * 4);
#pragma unroll
    for (int i = 0; i < 2; ++i)
      xv[i] = *reinterpret_cast<const u32x2*>(x + (size_t)(m0 + (2 * hi + i) * 16 + fr) * DM + n0 + wq * 16 + fg * 4);
  }
  f32x4 own[2];
  splitk_reduce<4>(acc, own, smem, wave, lane);
#pragma unroll
  for (int i = 0; i < 2; ++i) {
    u32x2 w;
    w[0] = pack2(bflo(xv[i][0]) + gv[0] * own[i][0], bfhi(xv[i][0]) + gv[1] * own[i][1]);
    w[1] = pack2(bflo(xv[i][1]) + gv[2] * own[i][2], bfhi(xv[i][1]) + gv[3] * own[i][3]);
    *reinterpret_cast<u32x2*>(x1 + (size_t)(m0 + (2 * hi + i) * 16 + fr) * DM + n0 + wq * 16 + fg * 4) = w;
  }
  STAMP(2, 5);
}

constexpr int NBLK = 43;
using G4 = Gemm<64, 192, 1, 2, 4>;
DI float gelu_tanh(float x) {
  constexpr float K1 = -2.f * 0.7978845608028654f * 1.4426950408889634f, K2 = K1 * 0.044715f;
  const float e = __builtin_amdgcn_exp2f(x * fmaf(x * x, K2, K1));
  return x * __builtin_amdgcn_rcpf(1.f + e);
}
DI u32x4 gload_asm(const void* p) {
  u32x4 r;
  asm volatile("global_load_dwordx4 %0, %1, off" : "=v"(r) : "v"(p) : "memory");
  return r;
}
template <bool Z2>
DI void win_body(char* smem, const u16* __restrict__ x1, const float* __restrict__ sb, const float* __restrict__ cb,
                 const u16* __restrict__ win, char* __restrict__ zf, float* __restrict__ stats, int mtile, int t4) {
  char* sB = smem + 65536;
  const int tid = threadIdx.x, wave = __builtin_amdgcn_readfirstlane(tid >> 6), lane = tid & 63;
  const int wm = wave >> 2, wn = wave & 3;
  const int m0 = mtile * 64, n0 = (Z2 ? FH : 0) + t4 * 176;
  STAMP(1, 0);
  u32x4 c0 = gload_asm(cb + lane * 8), c1 = gload_asm(cb + lane * 8 + 4);
  u32x4 s0 = gload_asm(sb + lane * 8), s1 = gload_asm(sb + lane * 8 + 4);
  {
    const int rr = lane >> 3, ch = (lane & 7) ^ rr;
    const u16* src = x1 + (size_t)(m0 + wave * 8 + rr) * DM + ch * 8;
#pragma unroll
    for (int sub = 0; sub < 8; ++sub) glds16(src + sub * 64, smem + sub * 8192 + wave * 1024);
  }
#pragma unroll
  for (int st = 0; st < 4; ++st) G4::stageB(win, FF, st * 64, n0, FF - 8, sB + st * G4::B_BYTES, wave, lane);
  u32x4 breg[4][3];
  {
    const int pos = lane & 15, rw = pos >> 1, hf = pos & 1;
    const u16* src[3];
#pragma unroll
    for (int ii = 0; ii < 3; ++ii) {
      const int blk = (ii * 8 + wave) * 4 + (lane >> 4), kb = blk / G4::NB, nb = blk % G4::NB;
      int n = n0 + nb * 16 + hf * 8;
      if (n > FF - 8) n = FF - 8;
      src[ii] = win + (size_t)(256 + kb * 8 + (rw ^ ((kb & 1) << 2))) * FF + n;
    }
#pragma unroll
    for (int st = 0; st < 4; ++st) {
#pragma unroll
      for (int ii = 0; ii < 3; ++ii) breg[st][ii] = *reinterpret_cast<const u32x4*>(src[ii] + (size_t)st * 64 * FF);
      __builtin_amdgcn_sched_barrier(0);
    }
  }
  asm volatile("s_waitcnt vmcnt(24)" : "+v"(c0), "+v"(c1), "+v"(s0), "+v"(s1)::"memory");
  STAMP(1, 1);
  {
    float cbv[8], sbv[8];
#pragma unroll
    for (int jj = 0; jj < 4; ++jj) {
      cbv[jj] = 1.f + __uint_as_float(c0[jj]); cbv[4 + jj] = 1.f + __uint_as_float(c1[jj]);
      sbv[jj] = __uint_as_float(s0[jj]); sbv[4 + jj] = __uint_as_float(s1[jj]);
    }
    u32x4 xr[8];
#pragma unroll
    for (int i = 0; i < 8; ++i)
      xr[i] = *reinterpret_cast<const u32x4*>(smem + (lane >> 3) * 8192 + (wave * 8 + i) * 128 + (((lane & 7) ^ i) << 4));
#pragma unroll
    for (int i = 0; i < 8; ++i) {
      float f[8];
#pragma unroll
      for (int jj = 0; jj < 4; ++jj) { f[2 * jj] = bflo(xr[i][jj]); f[2 * jj + 1] = bfhi(xr[i][jj]); }
      float s = 0.f;
#pragma unroll
      for (int jj = 0; jj < 8; ++jj) s += f[jj];
      const float mean = wave_sum(s) * (1.f / DM);
      float v = 0.f;
#pragma unroll
      for (int jj = 0; jj < 8; ++jj) { f[jj] -= mean; v += f[jj] * f[jj]; }
      const float rstd = rsqrtf(wave_sum(v) * (1.f / DM) + 1e-5f);
#pragma unroll
      for (int jj = 0; jj < 4; ++jj)
        xr[i][jj] = pack2(f[2 * jj] * rstd * cbv[2 * jj] + sbv[2 * jj], f[2 * jj + 1] * rstd * cbv[2 * jj + 1] + sbv[2 * jj + 1]);
    }
#pragma unroll
    for (int i = 0; i < 8; ++i)
      *reinterpret_cast<u32x4*>(smem + (lane >> 3) * 8192 + (wave * 8 + i) * 128 + (((lane & 7) ^ i) << 4)) = xr[i];
  }
  STAMP(1, 2);
  f32x4 acc[G4::MT][G4::NT];
#pragma unroll
  for (int i = 0; i < G4::MT; ++i)
#pragma unroll
    for (int jj = 0; jj < G4::NT; ++jj) acc[i][jj] = f32x4{0.f, 0.f, 0.f, 0.f};
#pragma unroll
  for (int kt = 0; kt < 8; ++kt) {
    if (kt == 0) wait_vm<21>(); else if (kt == 1) wait_vm<18>(); else if (kt == 2) wait_vm<15>(); else if (kt == 3) wait_vm<12>();
    barrier();
    if (kt == 0) STAMP(1, 3);
    if (kt == 1) STAMP(1, 4);
    if (kt == 3) STAMP(1, 5);
    if (kt == 5) STAMP(1, 6);
    if (kt >= 1 && kt + 3 < 8) {
#pragma unroll
      for (int ii = 0; ii < 3; ++ii)
        *reinterpret_cast<u32x4*>(sB + ((kt + 3) & 3) * G4::B_BYTES + (ii * 8 + wave) * 1024 + lane * 16) = breg[kt - 1][ii];
    }
    G4::compute<!Z2>(smem + kt * 8192, sB + (kt & 3) * G4::B_BYTES, acc, wm, wn, lane);
  }
  STAMP(1, 7);
  const int fr = lane & 15, fg = lane >> 4;
  if constexpr (!Z2) {
#pragma unroll
    for (int nt = 0; nt < G4::NT; ++nt) {
      const int nbl = wn * 3 + nt, nb = t4 * 11 + nbl;
      if (nbl < 11 && nb < NBLK) {
#pragma unroll
        for (int mt = 0; mt < G4::MT; ++mt) {
          u32x2 w;
          w[0] = pack2(gelu_tanh(acc[mt][nt][0]), gelu_tanh(acc[mt][nt][1]));
          w[1] = pack2(gelu_tanh(acc[mt][nt][2]), gelu_tanh(acc[mt][nt][3]));
          *reinterpret_cast<u32x2*>(zf + ((size_t)(mtile * 4 + wm * 2 + mt) * NBLK + nb) * 512 + lane * 8) = w;
        }
      }
    }
  } else {
    float p1[G4::MT][4], p2[G4::MT][4];
#pragma unroll
    for (int mt = 0; mt < G4::MT; ++mt)
#pragma unroll
      for (int i = 0; i < 4; ++i) { p1[mt][i] = 0.f; p2[mt][i] = 0.f; }
#pragma unroll
    for (int nt = 0; nt < G4::NT; ++nt) {
      const int nbl = wn * 3 + nt, nb = t4 * 11 + nbl;
      if (nbl < 11 && nb < NBLK) {
#pragma unroll
        for (int mt = 0; mt < G4::MT; ++mt) {
          float gz[4];
#pragma unroll
          for (int i = 0; i < 4; ++i) {
            gz[i] = rbf(gelu_tanh(acc[mt][nt][i]));
            p1[mt][i] += gz[i]; p2[mt][i] += gz[i] * gz[i];
          }
          u32x2 w;
          w[0] = pack2(gz[0], gz[1]);
          w[1] = pack2(gz[2], gz[3]);
          *reinterpret_cast<u32x2*>(zf + ((size_t)(mtile * 2 + wm) * NBLK + nb) * 1024 + (fr + 16 * (mt * 2 + (fg >> 1))) * 16 + (fg & 1) * 8) = w;
        }
      }
    }
    barrier();
    float* red = reinterpret_cast<float*>(smem);
#pragma unroll
    for (int mt = 0; mt < G4::MT; ++mt)
#pragma unroll
      for (int i = 0; i < 4; ++i) {
        float a = p1[mt][i], c = p2[mt][i];
        a = row16_sum(a); c = row16_sum(c);
        if (fr == 0) {
          const int rowl = wm * 32 + mt * 16 + fg * 4 + i;
          red[(wn * 64 + rowl) * 2] = a; red[(wn * 64 + rowl) * 2 + 1] = c;
        }
      }
    barrier();
    if (tid < 128) {
      const float v = (red[tid] + red[128 + tid]) + (red[256 + tid] + red[384 + tid]);
      stats[((size_t)t4 * S + m0) * 2 + tid] = v;
    }
  }
}
__global__ __launch_bounds__(512, 2) void k_win(const u16* __restrict__ x1, const float* __restrict__ sb,
                                                const float* __restrict__ cb, const u16* __restrict__ win,
                                                char* __restrict__ z1f, char* __restrict__ z2f,
                                                float* __restrict__ stats) {
  __shared__ __attribute__((aligned(16))) char smem[65536 + 4 * G4::B_BYTES];
  const int b = blockIdx.x, xcd = b & 7, j = b >> 3;
  const int mtile = xcd * 4 + (j >> 3), ntile = j & 7;
  if (ntile >= 4) win_body<true>(smem, x1, sb, cb, win, z2f, stats, mtile, ntile - 4);
  else win_body<false>(smem, x1, sb, cb, win, z1f, stats, mtile, ntile);
}

constexpr int UKS = 22;
template <int NT>
DI void spatial_body(char* smem, const u16* __restrict__ wsp, const char* __restrict__ z2f,
                     const float* __restrict__ stats, const u16* __restrict__ bsp, const char* __restrict__ z1f,
                     char* __restrict__ uf, int mtile, int nb0, int rot) {
  const int tid = threadIdx.x, wave = __builtin_amdgcn_readfirstlane(tid >> 6), lane = tid & 63;
  const int fr = lane & 15, fg = lane >> 4;
  char* ring = smem + wave * SK_AREA;
  float* rs = reinterpret_cast<float*>(smem + 8 * SK_AREA + wave * 2048);
  float* ms = rs + 256;
  const int m0 = mtile * 64, g0 = wave * 4 + rot;
  STAMP(3, 0);
  f32x4 sp[4][2];
#pragma unroll
  for (int t = 0; t < 4; ++t) {
    const float* p = stats + ((size_t)t * S + ((g0 + (lane >> 4)) & 31) * 64 + (lane & 15) * 4) * 2;
    sp[t][0] = *reinterpret_cast<const f32x4*>(p);
    sp[t][1] = *reinterpret_cast<const f32x4*>(p + 4);
  }
  const int rr = lane >> 3, ch = (lane & 7) ^ rr;
  const u16* asrc = wsp + (size_t)(m0 + rr) * S + ch * 8;
  auto stageA = [&](int s) {
#pragma unroll
    for (int i = 0; i < 8; ++i) glds16(asrc + (size_t)i * 8 * S + ((g0 + s) & 31) * 64, ring + (s & 1) * 8192 + i * 1024);
  };
  const char* bsrc = z2f + (size_t)nb0 * 1024 + lane * 16;
  u32x4 breg[3][NT];
  auto loadB = [&](int t) {
#pragma unroll
    for (int nt = 0; nt < NT; ++nt) breg[t % 3][nt] = *reinterpret_cast<const u32x4*>(bsrc + (((((g0 + (t >> 1)) & 31) * 2 + (t & 1)) * NBLK) + nt) * 1024);
  };
  stageA(0); loadB(0); loadB(1); stageA(1); loadB(2);
  STAMP(3, 1);
  {
    f32x4 rv, mv;
#pragma unroll
    for (int i = 0; i < 4; ++i) {
      float s1 = 0.f, s2 = 0.f;
#pragma unroll
      for (int t = 0; t < 4; ++t) { s1 += sp[t][i >> 1][(i & 1) * 2]; s2 += sp[t][i >> 1][(i & 1) * 2 + 1]; }
      const float mean = s1 * (1.f / FH);
      const float var = fmaxf(s2 * (1.f / FH) - mean * mean, 0.f);
      const float rstd = rsqrtf(var + 1e-5f);
      rv[i] = rstd; mv[i] = -mean * rstd;
    }
    *reinterpret_cast<f32x4*>(rs + lane * 4) = rv;
    *reinterpret_cast<f32x4*>(ms + lane * 4) = mv;
  }
  f32x4 acc[4][NT];
#pragma unroll
  for (int i = 0; i < 4; ++i)
#pragma unroll
    for (int jj = 0; jj < NT; ++jj) acc[i][jj] = f32x4{0.f, 0.f, 0.f, 0.f};
#pragma unroll
  for (int s = 0; s < 4; ++s) {
    if (s < 2) wait_vm<3 * NT + 8>(); else if (s == 2) wait_vm<2 * NT + 8>(); else wait_vm<NT>();
    if (s == 0) STAMP(3, 2);
    if (s == 2) STAMP(3, 3);
#pragma unroll
    for (int kk = 0; kk < 2; ++kk) {
      const int t = 2 * s + kk;
      bf16x8 af[4];
#pragma unroll
      for (int mt = 0; mt < 4; ++mt)
        af[mt] = *reinterpret_cast<const bf16x8*>(ring + (s & 1) * 8192 + (mt * 16 + fr) * 128 + (((kk * 4 + fg) ^ (fr & 7)) << 4));
      f32x4 rv[2], mv[2];
#pragma unroll
      for (int e = 0; e < 2; ++e) {
        rv[e] = *reinterpret_cast<const f32x4*>(rs + t * 32 + fg * 8 + e * 4);
        mv[e] = *reinterpret_cast<const f32x4*>(ms + t * 32 + fg * 8 + e * 4);
      }
#pragma unroll
      for (int nt = 0; nt < NT; ++nt) {
        const u32x4 raw = breg[t % 3][nt];
        u32x4 o4;
#pragma unroll
        for (int jj = 0; jj < 4; ++jj)
          o4[jj] = pack2(fmaf(bflo(raw[jj]), rv[jj >> 1][(jj & 1) * 2], mv[jj >> 1][(jj & 1) * 2]),
                         fmaf(bfhi(raw[jj]), rv[jj >> 1][(jj & 1) * 2 + 1], mv[jj >> 1][(jj & 1) * 2 + 1]));
        const bf16x8 bv = __builtin_bit_cast(bf16x8, o4);
#pragma unroll
        for (int mt = 0; mt < 4; ++mt)
          acc[mt][nt] = __builtin_amdgcn_mfma_f32_16x16x32_bf16(bv, af[mt], acc[mt][nt], 0, 0, 0);
      }
      if (t + 3 < 8) loadB(t + 3);
    }
    if (s + 2 < 4) {
      asm volatile("s_waitcnt lgkmcnt(0)" ::: "memory");
      stageA(s + 2);
    }
  }
  STAMP(3, 4);
  constexpr int NO = SkOwn<NT>::N;
  const int hi = wave >> 2, wq = wave & 3;
  u32x2 zv[NO];
  float bs[NO];
#pragma unroll
  for (int i = 0; i < NO; ++i) {
    int f = wq + 4 * i;
    if (f >= 2 * NT) f = 0;
    const int mt = 2 * hi + f / NT, nb = nb0 + f % NT;
    zv[i] = *reinterpret_cast<const u32x2*>(z1f + ((size_t)(mtile * 4 + mt) * NBLK + nb) * 512 + lane * 8);
    bs[i] = bf2f(bsp[m0 + mt * 16 + fr]);
  }
  f32x4 own[NO];
  splitk_reduce<NT>(acc, own, smem, wave, lane);
#pragma unroll
  for (int i = 0; i < NO; ++i) {
    const int f = wq + 4 * i;
    if (f < 2 * NT) {
      const int mt = 2 * hi + f / NT, nb = nb0 + f % NT;
      u32x2 w;
      w[0] = pack2(bflo(zv[i][0]) * (own[i][0] + bs[i]), bfhi(zv[i][0]) * (own[i][1] + bs[i]));
      w[1] = pack2(bflo(zv[i][1]) * (own[i][2] + bs[i]), bfhi(zv[i][1]) * (own[i][3] + bs[i]));
      char* dst = uf + ((size_t)(mtile * 4 + mt) * UKS + (nb >> 1)) * 1024 + (fr + 16 * (fg >> 1)) * 16 + (fg & 1) * 8;
      *reinterpret_cast<u32x2*>(dst + (nb & 1) * 512) = w;
      if (nb == NBLK - 1) *reinterpret_cast<u32x2*>(dst + 512) = u32x2{0u, 0u};
    }
  }
  STAMP(3, 5);
}
__global__ __launch_bounds__(512, 2) void k_spatial(const u16* __restrict__ wsp, const char* __restrict__ z2f,
                                                    const float* __restrict__ stats, const u16* __restrict__ bsp,
                                                    const char* __restrict__ z1f, char* __restrict__ uf) {
  __shared__ __attribute__((aligned(16))) char smem[8 * SK_AREA + 8 * 2048 + 256];
  const int b = blockIdx.x, xcd = b & 7, j = b >> 3;
  const int mtile = xcd * 4 + (j >> 3), ntile = j & 7;
  const int rot = 0;
  if (ntile < 5) spatial_body<5>(smem, wsp, z2f, stats, bsp, z1f, uf, mtile, ntile * 5, rot);
  else spatial_body<6>(smem, wsp, z2f, stats, bsp, z1f, uf, mtile, 25 + (ntile - 5) * 6, rot);
}

__global__ __launch_bounds__(512, 2) void k_wout(const char* __restrict__ uf, const u16* __restrict__ wout,
                                                 const u16* __restrict__ x1, const float* __restrict__ gb,
                                                 u16* __restrict__ out) {
  __shared__ __attribute__((aligned(16))) char smem[8 * SK_AREA];
  const int b = blockIdx.x, xcd = b & 7, j = b >> 3;
  const int mtile = xcd * 4 + (j >> 3), ntile = j & 7;
  const int tid = threadIdx.x, wave = __builtin_amdgcn_readfirstlane(tid >> 6), lane = tid & 63;
  const int m0 = mtile * 64, n0 = ntile * 64;
  char* ring = smem + wave * SK_AREA;
  const int ks0 = wave < 6 ? wave * 3 : 18 + (wave - 6) * 2, nks = wave < 6 ? 3 : 2;
  const int rw = (lane & 15) >> 1;
  u32x4 areg[3][4];
#pragma unroll
  for (int t = 0; t < 3; ++t) {
    const int ks = ks0 + (t < nks ? t : 0);
#pragma unroll
    for (int i = 0; i < 4; ++i) {
      int row = ks * 32 + i * 8 + ((i & 1) ? (rw ^ 4) : rw);
      if (row > FH - 1) row = FH - 1;
      glds16(wout + (size_t)row * DM + n0 + (lane >> 4) * 16 + (lane & 1) * 8, ring + t * 4096 + i * 1024);
    }
#pragma unroll
    for (int mt = 0; mt < 4; ++mt)
      areg[t][mt] = *reinterpret_cast<const u32x4*>(uf + ((size_t)(mtile * 4 + mt) * UKS + ks) * 1024 + lane * 16);
  }
  const int fr = lane & 15, fg = lane >> 4, hi = wave >> 2, wq = wave & 3;
  u32x2 xv[2];
  const f32x4 gv = *reinterpret_cast<const f32x4*>(gb + n0 + wq * 16 + fg * 4);
#pragma unroll
  for (int i = 0; i < 2; ++i)
    xv[i] = *reinterpret_cast<const u32x2*>(x1 + (size_t)(m0 + (2 * hi + i) * 16 + fr) * DM + n0 + wq * 16 + fg * 4);
  f32x4 acc[4][4];
#pragma unroll
  for (int i = 0; i < 4; ++i)
#pragma unroll
    for (int jj = 0; jj < 4; ++jj) acc[i][jj] = f32x4{0.f, 0.f, 0.f, 0.f};
  wait_vm<0>();
#pragma unroll
  for (int t = 0; t < 3; ++t) {
    if (t < nks) {
      bf16x8 bfr[4];
      sk_bfrags(ring + t * 4096, bfr, lane);
#pragma unroll
      for (int mt = 0; mt < 4; ++mt)
#pragma unroll
        for (int nt = 0; nt < 4; ++nt)
          acc[mt][nt] = __builtin_amdgcn_mfma_f32_16x16x32_bf16(bfr[nt], __builtin_bit_cast(bf16x8, areg[t][mt]), acc[mt][nt], 0, 0, 0);
    }
  }
  f32x4 own[2];
  splitk_reduce<4>(acc, own, smem, wave, lane);
#pragma unroll
  for (int i = 0; i < 2; ++i) {
    u32x2 w;
    w[0] = pack2(bflo(xv[i][0]) + gv[0] * own[i][0], bfhi(xv[i][0]) + gv[1] * own[i][1]);
    w[1] = pack2(bflo(xv[i][1]) + gv[2] * own[i][2], bfhi(xv[i][1]) + gv[3] * own[i][3]);
    *reinterpret_cast<u32x2*>(out + (size_t)(m0 + (2 * hi + i) * 16 + fr) * DM + n0 + wq * 16 + fg * 4) = w;
  }
}

extern "C" void kernel_launch(void* const* d_in, const int* in_sizes, int n_in, void* d_out, int out_size,
                              void* d_ws, size_t ws_size, hipStream_t stream) {
  const u16* x = (const u16*)d_in[0];
  const u16* ckv = (const u16*)d_in[1];
  const u16* wq = (const u16*)d_in[2];
  const u16* wk = (const u16*)d_in[3];
  const u16* wv = (const u16*)d_in[4];
  const u16* wo = (const u16*)d_in[5];
  const u16* win = (const u16*)d_in[7];
  const u16* wsp = (const u16*)d_in[8];
  const u16* bsp = (const u16*)d_in[9];
  const u16* wout = (const u16*)d_in[10];
  const u16* c = (const u16*)d_in[11];
  const u16* wmod = (const u16*)d_in[12];
  char* ws = (char*)d_ws;
  float* mod = (float*)(ws + WS_MOD);
  u16* h1 = (u16*)(ws + WS_H1);
  char* Qf = ws + WS_QF;
  char* Ki = ws + WS_KI;
  char* Vi = ws + WS_VI;
  char* ctx = ws + WS_CTX;
  u16* x1 = (u16*)(ws + WS_X1);
  char* z1 = ws + WS_Z1;
  char* z2 = ws + WS_Z2;
  float* stats = (float*)(ws + WS_ST);
  char* u = ws + WS_U;
  u16* out = (u16*)d_out;
  unsigned* flags = (unsigned*)(ws + WS_FLAG);

  k_kv<<<256, 512, 0, stream>>>(ckv, wk, wv, Ki, Vi, c, wmod, mod);
  k_ln<<<S / 4, 256, 0, stream>>>(x, mod + 0 * DM, mod + 1 * DM, h1);
  k_qkv<<<512, 256, 0, stream>>>(h1, wq, Qf, flags);
  k_attn<<<256, 256, 0, stream>>>(Qf, Ki, Vi, ctx, ws + WS_SLAB, flags, wo, win, wsp, wout);
  k_oproj<<<256, 512, 0, stream>>>(ctx, wo, x, mod + 2 * DM, x1);
  k_win<<<256, 512, 0, stream>>>(x1, mod + 3 * DM, mod + 4 * DM, win, z1, z2, stats);
  k_spatial<<<256, 512, 0, stream>>>(wsp, z2, stats, bsp, z1, u);
  k_wout<<<256, 512, 0, stream>>>(u, wout, x1, mod + 5 * DM, out);
}
```

```cpp
#include <hip/hip_runtime.h>
#include <hip/hip_bf16.h>
#include <stdint.h>
#include <utility>

constexpr int S = 2048;
constexpr int DM = 512;
constexpr int NH = 32;
constexpr int HD = 128;
constexpr int HDM = NH * HD;
constexpr int FH = 688;
constexpr int FF = 1376;

typedef __attribute__((ext_vector_type(8))) short bf16x8;
typedef __attribute__((ext_vector_type(4))) short s16x4;
typedef __attribute__((ext_vector_type(4))) float f32x4;
typedef __attribute__((ext_vector_type(16))) float f32x16;
typedef __attribute__((ext_vector_type(4))) unsigned u32x4;
typedef __attribute__((ext_vector_type(2))) unsigned u32x2;
typedef __attribute__((address_space(3))) s16x4* lds_s16x4_ptr;
typedef unsigned short u16;

#define DI __device__ __forceinline__

DI float bf2f(u16 v) { return __uint_as_float(((unsigned)v) << 16); }
DI float bflo(unsigned v) { return __uint_as_float(v << 16); }
DI float bfhi(unsigned v) { return __uint_as_float(v & 0xffff0000u); }
typedef __attribute__((ext_vector_type(2))) __bf16 bf16x2_t;
typedef __attribute__((ext_vector_type(2))) float f32x2;
DI unsigned pack2(float lo, float hi) {
  f32x2 v = {lo, hi};
  return __builtin_bit_cast(unsigned, __builtin_convertvector(v, bf16x2_t));
}
template <int N> DI void wait_vm() { asm volatile("s_waitcnt vmcnt(%0)" ::"n"(N) : "memory"); }
DI void barrier() { asm volatile("s_waitcnt lgkmcnt(0)" ::: "memory"); __builtin_amdgcn_s_barrier(); }
DI float rbf(float x) { return bflo(pack2(x, 0.f)); }

DI void glds16(const void* g, const void* lds_wave_base) {
  const unsigned a = __builtin_amdgcn_readfirstlane((unsigned)(size_t)lds_wave_base);
  unsigned keep;
  asm volatile("s_mov_b32 %0, m0\n\ts_mov_b32 m0, %2\n\ts_nop 0\n\tglobal_load_lds_dwordx4 %1, off\n\ts_mov_b32 m0, %0"
               : "=&s"(keep) : "v"(g), "s"(a) : "memory");
}
template <int OFF> DI void glds16s(const void* sbase, unsigned voff, unsigned lds_base) {
  asm volatile("s_mov_b32 m0, %2\n\ts_nop 0\n\tglobal_load_lds_dwordx4 %0, %1 offset:%3"
               : : "v"(voff), "s"(sbase), "s"(lds_base), "n"(OFF) : "memory");
}
DI unsigned lds_addr(const void* p) { return __builtin_amdgcn_readfirstlane((unsigned)(size_t)p); }
DI void pf_line(const void* g, const void* lds_dummy) {
  const unsigned a = __builtin_amdgcn_readfirstlane((unsigned)(size_t)lds_dummy);
  unsigned keep;
  asm volatile("s_mov_b32 %0, m0\n\ts_mov_b32 m0, %2\n\ts_nop 0\n\tglobal_load_lds_dword %1, off\n\ts_mov_b32 m0, %0"
               : "=&s"(keep) : "v"(g), "s"(a) : "memory");
}
DI void pf_slice(const void* base, int nbytes, int first, int lane, const void* lds_dummy) {
  int l = first + lane;
  const int last = (nbytes >> 7) - 1;
  if (l > last) l = last;
  pf_line((const char*)base + (size_t)l * 128, lds_dummy);
}
#define STAMP(k, i)
template <int CTRL> DI float dpp_add(float v) {
  return v + __uint_as_float(__builtin_amdgcn_update_dpp(0u, __float_as_uint(v), CTRL, 0xf, 0xf, true));
}
DI float row16_sum(float v) {
  v = dpp_add<0xB1>(v); v = dpp_add<0x4E>(v); v = dpp_add<0x141>(v); v = dpp_add<0x140>(v);
  return v;
}
DI float wave_sum(float v) {
  v = row16_sum(v);
  auto a = __builtin_amdgcn_permlane16_swap(__float_as_uint(v), __float_as_uint(v), false, false);
  v = __uint_as_float(a[0]) + __uint_as_float(a[1]);
  auto b = __builtin_amdgcn_permlane32_swap(__float_as_uint(v), __float_as_uint(v), false, false);
  return __uint_as_float(b[0]) + __uint_as_float(b[1]);
}
DI s16x4 tr_read(const char* p) {
  return __builtin_amdgcn_ds_read_tr16_b64_v4i16((lds_s16x4_ptr)(p));
}

constexpr size_t WS_MOD = 0;
constexpr size_t WS_H1 = 16384;
constexpr size_t WS_QF = WS_H1 + (size_t)S * DM * 2;
constexpr size_t WS_KI = WS_QF + (size_t)S * HDM * 2;
constexpr size_t WS_VI = WS_KI + (size_t)S * HDM * 2;
constexpr size_t WS_CTX = WS_VI + (size_t)S * HDM * 2;
constexpr size_t WS_X1 = WS_CTX + (size_t)S * HDM * 2;
constexpr size_t WS_Z1 = WS_X1 + (size_t)S * DM * 2;
constexpr size_t WS_Z2 = WS_Z1 + (size_t)S * FH * 2;
constexpr size_t WS_ST = WS_Z2 + (size_t)S * FH * 2;
constexpr size_t WS_U = WS_ST + (size_t)8 * S * 2 * 4;
constexpr size_t WS_SLAB = WS_U + (size_t)S * 704 * 2;
constexpr size_t WS_FLAG = WS_SLAB + (size_t)96 * 4 * 33 * 1024;
constexpr int LDU = 704;

__global__ __launch_bounds__(256) void k_mod(const u16* __restrict__ c, const u16* __restrict__ w,
                                             float* __restrict__ mod) {
  __shared__ float red[64][33];
  const int t = threadIdx.x, cg = t & 3, ks = t >> 2;
  const int col0 = blockIdx.x * 32 + cg * 8;
  float acc[8];
#pragma unroll
  for (int j = 0; j < 8; ++j) acc[j] = 0.f;
#pragma unroll
  for (int kk = 0; kk < 8; ++kk) {
    const int k = ks * 8 + kk;
    float cv = bf2f(c[k]);
    float sv = rbf(cv / (1.f + __expf(-cv)));
    u32x4 wv = *reinterpret_cast<const u32x4*>(w + (size_t)k * 3072 + col0);
#pragma unroll
    for (int j = 0; j < 4; ++j) {
      acc[2 * j] += sv * bflo(wv[j]);
      acc[2 * j + 1] += sv * bfhi(wv[j]);
    }
  }
#pragma unroll
  for (int j = 0; j < 8; ++j) red[ks][cg * 8 + j] = acc[j];
  __syncthreads();
  if (t < 32) {
    float s = 0.f;
    for (int i = 0; i < 64; ++i) s += red[i][t];
    mod[blockIdx.x * 32 + t] = rbf(s);
  }
}

__global__ __launch_bounds__(256) void k_ln(const u16* __restrict__ x, const float* __restrict__ sh,
                                            const float* __restrict__ cs, u16* __restrict__ h) {
  const int row = blockIdx.x * 4 + (threadIdx.x >> 6), lane = threadIdx.x & 63;
  u32x4 xv = *reinterpret_cast<const u32x4*>(x + (size_t)row * DM + lane * 8);
  float f[8];
#pragma unroll
  for (int j = 0; j < 4; ++j) { f[2 * j] = bflo(xv[j]); f[2 * j + 1] = bfhi(xv[j]); }
  float s = 0.f;
#pragma unroll
  for (int j = 0; j < 8; ++j) s += f[j];
#pragma unroll
  for (int o = 32; o >= 1; o >>= 1) s += __shfl_xor(s, o);
  const float mean = s * (1.f / DM);
  float v = 0.f;
#pragma unroll
  for (int j = 0; j < 8; ++j) { f[j] -= mean; v += f[j] * f[j]; }
#pragma unroll
  for (int o = 32; o >= 1; o >>= 1) v += __shfl_xor(v, o);
  const float rstd = rsqrtf(v * (1.f / DM) + 1e-5f);
  u32x4 o4;
#pragma unroll
  for (int j = 0; j < 4; ++j) {
    const int k = lane * 8 + 2 * j;
    float a = f[2 * j] * rstd * (1.f + cs[k]) + sh[k];
    float b = f[2 * j + 1] * rstd * (1.f + cs[k + 1]) + sh[k + 1];
    o4[j] = pack2(a, b);
  }
  *reinterpret_cast<u32x4*>(h + (size_t)row * DM + lane * 8) = o4;
}

template <int BM, int BN, int KS, int NWM, int NWN>
struct Gemm {
  static constexpr int NW = NWM * NWN;
  static constexpr int WM = BM / NWM, WN = BN / NWN;
  static constexpr int MT = WM / 16, NT = WN / 16;
  static constexpr int BK = 64 * KS;
  static constexpr int A_BYTES = BM * BK * 2;
  static constexpr int B_BYTES = BK * BN * 2;
  static constexpr int STAGE_BYTES = A_BYTES + B_BYTES;
  static constexpr int NB = BN / 16;

  static DI void stageA(const u16* __restrict__ A, int lda, int m0, int k0, char* lds, int wave, int lane) {
    constexpr int NI = A_BYTES / 1024;
    const int rr = lane >> 3, ch = (lane & 7) ^ rr;
#pragma unroll
    for (int ii = 0; ii < NI / NW; ++ii) {
      const int i = ii * NW + wave;
      const int sub = i / (BM / 8), j = i % (BM / 8);
      const u16* src = A + (size_t)(m0 + j * 8 + rr) * lda + k0 + sub * 64 + ch * 8;
      glds16(src, lds + i * 1024);
    }
  }
  static DI void stageB(const u16* __restrict__ B, int ldb, int k0, int n0, int nmax, char* lds, int wave, int lane) {
    constexpr int NI = B_BYTES / 1024;
    const int pos = lane & 15, rw = pos >> 1, hf = pos & 1;
#pragma unroll
    for (int ii = 0; ii < NI / NW; ++ii) {
      const int i = ii * NW + wave;
      const int blk = i * 4 + (lane >> 4);
      const int kb = blk / NB, nb = blk % NB;
      const int row = rw ^ ((kb & 1) << 2);
      int n = n0 + nb * 16 + hf * 8;
      if (n > nmax) n = nmax;
      const u16* src = B + (size_t)(k0 + kb * 8 + row) * ldb + n;
      glds16(src, lds + i * 1024);
    }
  }
  template <bool SWAP>
  static DI void compute(const char* ldsA, const char* ldsB, f32x4 (&acc)[MT][NT], int wm, int wn, int lane) {
    const int fr = lane & 15, fg = lane >> 4;
    const int q = fr >> 2, p = lane & 3;
#pragma unroll
    for (int sub = 0; sub < KS; ++sub) {
#pragma unroll
      for (int kk = 0; kk < 2; ++kk) {
        bf16x8 af[MT], bfr[NT];
#pragma unroll
        for (int mt = 0; mt < MT; ++mt) {
          const int row = wm * WM + mt * 16 + fr;
          af[mt] = *reinterpret_cast<const bf16x8*>(ldsA + sub * (BM * 128) + row * 128 + (((kk * 4 + fg) ^ (row & 7)) << 4));
        }
#pragma unroll
        for (int nt = 0; nt < NT; ++nt) {
          const int kb = sub * 8 + kk * 4 + fg, nb = wn * NT + nt;
          const char* base = ldsB + (kb * NB + nb) * 256 + p * 8;
          const int sw = (fg & 1) << 2;
          s16x4 lo = tr_read(base + ((q) ^ sw) * 32);
          s16x4 hi = tr_read(base + ((4 + q) ^ sw) * 32);
          bfr[nt] = __builtin_shufflevector(lo, hi, 0, 1, 2, 3, 4, 5, 6, 7);
        }
#pragma unroll
        for (int mt = 0; mt < MT; ++mt)
#pragma unroll
          for (int nt = 0; nt < NT; ++nt) {
            if constexpr (SWAP)
              acc[mt][nt] = __builtin_amdgcn_mfma_f32_16x16x32_bf16(bfr[nt], af[mt], acc[mt][nt], 0, 0, 0);
            else
              acc[mt][nt] = __builtin_amdgcn_mfma_f32_16x16x32_bf16(af[mt], bfr[nt], acc[mt][nt], 0, 0, 0);
          }
      }
    }
  }
};

using G1 = Gemm<128, 128, 1, 2, 2>;

constexpr float QSCALE = 0.08838834764831845f * 1.4426950408889634f;
__global__ __launch_bounds__(256, 2) void k_qkv(const u16* __restrict__ h1, const u16* __restrict__ wq,
                                                char* __restrict__ Qf, unsigned* __restrict__ flags) {
  __shared__ __attribute__((aligned(16))) char smem[2 * G1::STAGE_BYTES];
  const int b = blockIdx.x, xcd = b & 7, j = b >> 3;
  const int head = xcd * 4 + (j & 3), mtile = j >> 2;
  const int tid = threadIdx.x, wave = __builtin_amdgcn_readfirstlane(tid >> 6), lane = tid & 63;
  const int wm = wave >> 1, wn = wave & 1;
  const u16* A = h1;
  const u16* B = wq;
  const int m0 = mtile * 128, n0 = head * 128;
  if (b < 3) flags[(b * 256 + tid) * 8] = 0u;
  f32x4 acc[G1::MT][G1::NT];
#pragma unroll
  for (int i = 0; i < G1::MT; ++i)
#pragma unroll
    for (int jj = 0; jj < G1::NT; ++jj) acc[i][jj] = f32x4{0.f, 0.f, 0.f, 0.f};
  constexpr int NK = DM / G1::BK;
  G1::stageA(A, DM, m0, 0, smem, wave, lane);
  G1::stageB(B, HDM, 0, n0, HDM, smem + G1::A_BYTES, wave, lane);
  for (int kt = 0; kt < NK; ++kt) {
    wait_vm<0>(); __syncthreads();
    char* cur = smem + (kt & 1) * G1::STAGE_BYTES;
    char* nxt = smem + ((kt + 1) & 1) * G1::STAGE_BYTES;
    if (kt + 1 < NK) {
      G1::stageA(A, DM, m0, (kt + 1) * G1::BK, nxt, wave, lane);
      G1::stageB(B, HDM, (kt + 1) * G1::BK, n0, HDM, nxt + G1::A_BYTES, wave, lane);
    }
    G1::compute<true>(cur, cur + G1::A_BYTES, acc, wm, wn, lane);
  }
  __syncthreads();
  const int fr = lane & 15, fg = lane >> 4;
#pragma unroll
  for (int mt = 0; mt < G1::MT; ++mt)
#pragma unroll
    for (int nt = 0; nt < G1::NT; ++nt) {
      u32x2 v;
      v[0] = pack2(acc[mt][nt][0] * QSCALE, acc[mt][nt][1] * QSCALE);
      v[1] = pack2(acc[mt][nt][2] * QSCALE, acc[mt][nt][3] * QSCALE);
      const int m = wm * 64 + mt * 16 + fr, rg = m >> 5, r = m & 31;
      const int off = rg * 8192 + (wn * 4 + nt) * 1024 + (((fg & 1) * 32 + r) << 4) + (fg >> 1) * 8;
      *reinterpret_cast<u32x2*>(smem + off) = v;
    }
  __syncthreads();
  char* dst = Qf + ((size_t)head * 16 + mtile) * 32768;
#pragma unroll
  for (int i = 0; i < 8; ++i) {
    const int o = (i * 256 + tid) * 16;
    *reinterpret_cast<u32x4*>(dst + o) = *reinterpret_cast<const u32x4*>(smem + o);
  }
}

#define KV_PD 2
constexpr int KV_NS = 4, KV_STAGE = 32768;
template <bool isV>
DI void kv_body(char* smem, const u16* __restrict__ ckv, const u16* __restrict__ B, char* __restrict__ dstb,
                const u16* __restrict__ cvec, const u16* __restrict__ wmod, float* __restrict__ mod) {
  const int b = blockIdx.x, xcd = b & 7, j = b >> 3;
  const int ntile = xcd * 4 + (j & 3), mtile = j >> 2;
  const int tid = threadIdx.x, wave = __builtin_amdgcn_readfirstlane(tid >> 6), lane = tid & 63;
  const int wm = wave >> 2, wn = wave & 3, r = lane & 31, h = lane >> 5;
  const int m0 = mtile * 256, n0 = (ntile & 15) * 256;
  float* modred = reinterpret_cast<float*>(smem + KV_NS * KV_STAGE);
  STAMP(0, 0);

  const u16* asrc[2];
  const u16* bsrc[2];
#pragma unroll
  for (int ii = 0; ii < 2; ++ii) {
    const int i = ii * 8 + wave;
    const int row = 16 * i + (lane >> 2), ch = (lane & 3) ^ ((lane >> 4) & 3);
    asrc[ii] = ckv + (size_t)(m0 + row) * DM + ch * 8;
    const int blk = i * 4 + (lane >> 4), kb = blk >> 4, nb = blk & 15;
    const int pos = lane & 15, rw = (pos >> 1) ^ ((nb & 1) << 2), hf = pos & 1;
    bsrc[ii] = B + (size_t)(kb * 8 + rw) * HDM + n0 + nb * 16 + hf * 8;
  }
  auto stage = [&](int kt0, int slot) {
    char* dst = smem + slot * KV_STAGE;
    const int kt = (kt0 + 2 * mtile) & 15;
#pragma unroll
    for (int ii = 0; ii < 2; ++ii) {
      glds16(asrc[ii] + kt * 32, dst + (ii * 8 + wave) * 1024);
      glds16(bsrc[ii] + (size_t)kt * 32 * HDM, dst + 16384 + (ii * 8 + wave) * 1024);
    }
  };
  constexpr int NK = DM / 32;
#pragma unroll
  for (int st = 0; st < KV_PD; ++st) stage(st, st);

  if (b < 192) {
    const u16* p = wmod + (size_t)tid * 3072 + b * 16;
    const u32x4 w0 = *reinterpret_cast<const u32x4*>(p), w1 = *reinterpret_cast<const u32x4*>(p + 8);
    const float c = bf2f(cvec[tid]);
    const float sv = rbf(c / (1.f + __expf(-c)));
    float v[16];
#pragma unroll
    for (int jj = 0; jj < 4; ++jj) {
      v[2 * jj] = sv * bflo(w0[jj]); v[2 * jj + 1] = sv * bfhi(w0[jj]);
      v[8 + 2 * jj] = sv * bflo(w1[jj]); v[8 + 2 * jj + 1] = sv * bfhi(w1[jj]);
    }
#pragma unroll
    for (int st = 0; st < 4; ++st) {
      const int half = 8 >> st, m = 32 >> st;
      const bool up = lane & m;
#pragma unroll
      for (int e = 0; e < half; ++e) {
        const float send = up ? v[e] : v[half + e];
        const float keep = up ? v[half + e] : v[e];
        v[e] = keep + __shfl_xor(send, m);
      }
    }
    v[0] += __shfl_xor(v[0], 2);
    v[0] += __shfl_xor(v[0], 1);
    if ((lane & 3) == 0) {
      const int col = ((lane >> 5) & 1) * 8 + ((lane >> 4) & 1) * 4 + ((lane >> 3) & 1) * 2 + ((lane >> 2) & 1);
      modred[wave * 16 + col] = v[0];
    }
  }

  STAMP(0, 1);
  f32x16 acc[4][2];
#pragma unroll
  for (int i = 0; i < 4; ++i)
#pragma unroll
    for (int jj = 0; jj < 2; ++jj)
#pragma unroll
      for (int e = 0; e < 16; ++e) acc[i][jj][e] = 0.f;
  const int G = lane >> 4, q4 = (lane & 15) >> 2, p4 = lane & 3;
  int aoff[2];
#pragma unroll
  for (int u = 0; u < 2; ++u) aoff[u] = (wm * 128 + r) * 64 + (((2 * u + h) ^ ((r >> 2) & 3)) << 4);
  const int nh = G & 1;
  const int boff = 16384 + (h * 16 + wn * 4 + nh) * 256 + p4 * 8;
  const int brow0 = (q4 ^ (nh << 2)) * 32, brow1 = ((4 + q4) ^ (nh << 2)) * 32;
  for (int kt = 0; kt < NK; ++kt) {
    if (kt + KV_PD - 1 <= NK - 1) wait_vm<(KV_PD - 1) * 4>(); else wait_vm<0>();
    barrier();
    const bool pf = kt + KV_PD < NK;
    const int ktn = (kt + KV_PD + 2 * mtile) & 15;
    char* dst = smem + ((kt + KV_PD) & (KV_NS - 1)) * KV_STAGE;
    const char* cur = smem + (kt & (KV_NS - 1)) * KV_STAGE;
    bf16x8 af[2][4], bfr[2][2];
#pragma unroll
    for (int u = 0; u < 2; ++u) {
#pragma unroll
      for (int nt = 0; nt < 2; ++nt) {
        const char* bb = cur + boff + u * (2 * 16 * 256) + nt * 512;
        s16x4 lo = tr_read(bb + brow0), hi = tr_read(bb + brow1);
        bfr[u][nt] = __builtin_shufflevector(lo, hi, 0, 1, 2, 3, 4, 5, 6, 7);
      }
#pragma unroll
      for (int mt = 0; mt < 4; ++mt) af[u][mt] = *reinterpret_cast<const bf16x8*>(cur + aoff[u] + mt * 2048);
    }
#pragma unroll
    for (int g4 = 0; g4 < 4; ++g4) {
      const int u = g4 >> 1;
      __builtin_amdgcn_sched_barrier(0);
      if (pf) {
        const int ii = g4 >> 1;
        if (g4 & 1) glds16(bsrc[ii] + (size_t)ktn * 32 * HDM, dst + 16384 + (ii * 8 + wave) * 1024);
        else glds16(asrc[ii] + ktn * 32, dst + (ii * 8 + wave) * 1024);
      }
      __builtin_amdgcn_sched_barrier(0);
#pragma unroll
      for (int m2 = 0; m2 < 2; ++m2)
#pragma unroll
        for (int nt = 0; nt < 2; ++nt) {
          const int mt = (g4 & 1) * 2 + m2;
          if constexpr (isV)
            acc[mt][nt] = __builtin_amdgcn_mfma_f32_32x32x16_bf16(af[u][mt], bfr[u][nt], acc[mt][nt], 0, 0, 0);
          else
            acc[mt][nt] = __builtin_amdgcn_mfma_f32_32x32x16_bf16(bfr[u][nt], af[u][mt], acc[mt][nt], 0, 0, 0);
        }
    }
  }
  STAMP(0, 2);
  {
    const int hd = (ntile & 15) * 2 + (wn >> 1);
    char* base = dstb + ((size_t)hd * 32 + mtile * 4 + wm * 2) * 16384 + lane * 16;
#pragma unroll
    for (int mt = 0; mt < 4; ++mt)
#pragma unroll
      for (int nt = 0; nt < 2; ++nt)
#pragma unroll
        for (int u = 0; u < 2; ++u) {
          u32x4 v;
#pragma unroll
          for (int e = 0; e < 4; ++e) v[e] = pack2(acc[mt][nt][8 * u + 2 * e], acc[mt][nt][8 * u + 2 * e + 1]);
          const int db = (wn & 1) * 2 + nt;
          int off;
          if constexpr (isV) off = (mt >> 1) * 16384 + ((mt & 1) * 2 + u) * 4096 + db * 1024;
          else off = (mt >> 1) * 16384 + (mt & 1) * 8192 + (db * 2 + u) * 1024;
          *reinterpret_cast<u32x4*>(base + off) = v;
        }
  }
  barrier();
  STAMP(0, 3);
  if (b < 192 && tid < 16) {
    float sum = 0.f;
#pragma unroll
    for (int i = 0; i < 8; ++i) sum += modred[i * 16 + tid];
    mod[b * 16 + tid] = rbf(sum);
  }
}
__global__ __launch_bounds__(512, 2) void k_kv(const u16* __restrict__ ckv, const u16* __restrict__ wk,
                                               const u16* __restrict__ wv, char* __restrict__ Ki,
                                               char* __restrict__ Vi, const u16* __restrict__ cvec,
                                               const u16* __restrict__ wmod, float* __restrict__ mod) {
  __shared__ __attribute__((aligned(16))) char smem[KV_NS * KV_STAGE + 512];
  if ((blockIdx.x & 7) >= 4) kv_body<true>(smem, ckv, wv, Vi, cvec, wmod, mod);
  else kv_body<false>(smem, ckv, wk, Ki, cvec, wmod, mod);
}

#define ACLOB_LO "a0","a1","a2","a3","a4","a5","a6","a7","a8","a9","a10","a11","a12","a13","a14","a15","a16","a17","a18","a19","a20","a21","a22","a23","a24","a25","a26","a27","a28","a29","a30","a31","a32","a33","a34","a35","a36","a37","a38","a39","a40","a41","a42","a43","a44","a45","a46","a47","a48","a49","a50","a51","a52","a53","a54","a55","a56","a57","a58","a59","a60","a61","a62","a63","a64","a65","a66","a67","a68","a69","a70","a71","a72","a73","a74","a75","a76","a77","a78","a79","a80","a81","a82","a83","a84","a85","a86","a87","a88","a89","a90","a91","a92","a93","a94","a95","a96","a97","a98","a99","a100","a101","a102","a103","a104","a105","a106","a107","a108","a109","a110","a111","a112","a113","a114","a115","a116","a117","a118","a119","a120","a121","a122","a123","a124","a125","a126","a127"
#define ACLOB_HI "a128","a129","a130","a131","a132","a133","a134","a135","a136","a137","a138","a139","a140","a141","a142","a143","a144","a145","a146","a147","a148","a149","a150","a151","a152","a153","a154","a155","a156","a157","a158","a159","a160","a161","a162","a163","a164","a165","a166","a167","a168","a169","a170","a171","a172","a173","a174","a175","a176","a177","a178","a179","a180","a181","a182","a183","a184","a185","a186","a187","a188","a189","a190","a191","a192","a193","a194","a195","a196","a197","a198","a199","a200","a201","a202","a203","a204","a205","a206","a207","a208","a209","a210","a211","a212","a213","a214","a215","a216","a217","a218","a219","a220","a221","a222","a223","a224","a225","a226","a227","a228","a229","a230","a231","a232","a233","a234","a235","a236","a237","a238","a239","a240","a241","a242","a243","a244","a245","a246","a247","a248","a249","a250","a251","a252","a253","a254","a255"
template <int... Is> using iseq = std::integer_sequence<int, Is...>;
template <int N> using make_iseq = std::make_integer_sequence<int, N>;
#define GAP() __builtin_amdgcn_sched_barrier(0)
constexpr float AT_THR = 8.f;
constexpr int AT_MISC = 131072;
DI float max3f(float a, float b, float c) { float r; asm("v_max3_f32 %0, %1, %2, %3" : "=v"(r) : "v"(a), "v"(b), "v"(c)); return r; }
DI float max2f(float a, float b) { float r; asm("v_max_f32_e32 %0, %1, %2" : "=v"(r) : "v"(a), "v"(b)); return r; }
DI float fadd_s(float a, float b) { float r; asm("v_add_f32_e32 %0, %1, %2" : "=v"(r) : "v"(a), "v"(b)); return r; }
DI float fmul_s(float a, float b) { float r; asm("v_mul_f32_e32 %0, %1, %2" : "=v"(r) : "v"(a), "v"(b)); return r; }
DI float fsub_s(float a, float b) { float r; asm("v_sub_f32_e32 %0, %1, %2" : "=v"(r) : "v"(a), "v"(b)); return r; }
DI unsigned lane_id() { unsigned l; asm volatile("v_mbcnt_lo_u32_b32 %0, -1, 0\n\tv_mbcnt_hi_u32_b32 %0, -1, %0" : "=v"(l)); return l; }
DI void negm_bcast(f32x16& negm, float neg_mhat) {
  const float aone = lane_id() < 32u ? 1.f : 0.f;
  asm volatile("s_nop 1\n\tv_mfma_f32_32x32x2_f32 %0, %1, %2, 0" : "+v"(negm) : "v"(aone), "v"(neg_mhat));
}
struct AC {
  unsigned rb, voff;
  unsigned mk, mv;
  const char *Kh, *Vh;
  const char *ka, *va;
  int kt, vt, end0;
  int dd;
  f32x16 S[2][2][2];
  f32x16 NEGM[2];
  bf16x8 Vf[4][4];
  bf16x8 Pk[2][4];
  float mhat[2], lsum[2], fct[2]; bool pend;
  float rs0[2], rs1[2], pmx[2][4], rmax[2];
};
constexpr int AO_(int db, int qb) { return 16 * (2 * db + qb); }
constexpr int AQ_(int qb, int d0) { return 128 + 32 * qb + 4 * d0; }
constexpr int AK_(int kb, int d0) { return 192 + 32 * kb + 4 * d0; }
template <int I> DI void v_read(AC& c) {
  asm volatile("ds_read_b128 %0, %1 offset:%2" : "=v"(c.Vf[I & 3][I >> 2]) : "v"(c.rb), "i"(32768 + I * 1024) : "memory");
}
template <int I> DI void k_read(unsigned rb) {
  asm volatile("ds_read_b128 a[%c1:%c2], %0 offset:%c3" ::"v"(rb), "i"(192 + 4 * I), "i"(195 + 4 * I), "i"(I * 1024) : "memory", ACLOB_HI);
}
template <int I> DI void q_load(const char* qp, unsigned voff) {
  asm volatile("global_load_dwordx4 a[%c2:%c3], %0, %1 offset:%c4" ::"v"(voff), "s"(qp + (I >> 2) * 4096), "i"(128 + 4 * I), "i"(131 + 4 * I), "i"((I & 3) * 1024) : "memory", ACLOB_HI);
}
template <int I> DI void dma(const char* src, unsigned voff, unsigned lds) {
  asm volatile("s_mov_b32 m0, %2\n\ts_nop 0\n\tglobal_load_lds_dwordx4 %0, %1 offset:%3" ::"v"(voff), "s"(src + (I >> 2) * 4096), "s"(lds + (I >> 2) * 4096), "n"((I & 3) * 1024) : "memory");
}
template <int... Is> DI void dmas(const char* src, unsigned voff, unsigned lds, iseq<Is...>) { (dma<Is>(src, voff, lds), ...); }
template <int X, bool FIRST, int M> DI void qk_mfma(AC& c) {
  constexpr int d0 = M >> 2, kb = (M >> 1) & 1, qb = M & 1, ak = AK_(kb, d0), aq = AQ_(qb, d0);
  if constexpr (d0 == 0) {
    if constexpr (FIRST) asm volatile("v_mfma_f32_32x32x16_bf16 %0, a[%c1:%c2], a[%c3:%c4], 0" : "=&v"(c.S[X][kb][qb]) : "i"(ak), "i"(ak + 3), "i"(aq), "i"(aq + 3) : ACLOB_LO, ACLOB_HI);
    else asm volatile("v_mfma_f32_32x32x16_bf16 %0, a[%c2:%c3], a[%c4:%c5], %1" : "=&v"(c.S[X][kb][qb]) : "v"(c.NEGM[qb]), "i"(ak), "i"(ak + 3), "i"(aq), "i"(aq + 3) : ACLOB_LO, ACLOB_HI);
  } else asm volatile("v_mfma_f32_32x32x16_bf16 %0, a[%c1:%c2], a[%c3:%c4], %0" : "+v"(c.S[X][kb][qb]) : "i"(ak), "i"(ak + 3), "i"(aq), "i"(aq + 3) : ACLOB_LO, ACLOB_HI);
}
template <int M> DI void pv_mfma(AC& c) {
  constexpr int ks = M >> 3, db = (M >> 1) & 3, qb = M & 1, ao = AO_(db, qb);
  asm volatile("v_mfma_f32_32x32x16_bf16 a[%c2:%c3], %0, %1, a[%c2:%c3]" ::"v"(c.Vf[db][ks]), "v"(c.Pk[qb][ks]), "i"(ao), "i"(ao + 15) : ACLOB_LO);
}
template <int M> DI void pv_mfma_ld(AC& c) {
  constexpr int ks = M >> 3, db = (M >> 1) & 3, qb = M & 1, ao = AO_(db, qb), I = M & 7;
  const char* src = (M < 8 ? c.ka : c.va) + (I >> 2) * 4096;
  const unsigned dst = (M < 8 ? c.mk : c.mv) + (I >> 2) * 4096;
  asm volatile("s_mov_b32 m0, %4\n\tv_mfma_f32_32x32x16_bf16 a[%c6:%c7], %0, %1, a[%c6:%c7]\n\tglobal_load_lds_dwordx4 %2, %3 offset:%c8\n\tds_read_b128 a[%c9:%c10], %5 offset:%c11"
               ::"v"(c.Vf[db][ks]), "v"(c.Pk[qb][ks]), "v"(c.voff), "s"(src), "s"(dst), "v"(c.rb), "i"(ao), "i"(ao + 15), "i"((I & 3) * 1024),
                 "i"(192 + 4 * M), "i"(195 + 4 * M), "i"(M * 1024) : "memory", ACLOB_LO, ACLOB_HI);
}
template <int I> DI void o_zero4() {
  asm volatile("v_accvgpr_write_b32 a[%c0], 0\n\tv_accvgpr_write_b32 a[%c1], 0\n\tv_accvgpr_write_b32 a[%c2], 0\n\tv_accvgpr_write_b32 a[%c3], 0"
               ::"i"(4 * I), "i"(4 * I + 1), "i"(4 * I + 2), "i"(4 * I + 3) : ACLOB_LO);
}
template <int X, int QB, int KS, int P> DI void pack1(AC& c) {
  u32x4 w = __builtin_bit_cast(u32x4, c.Pk[QB][KS]);
  unsigned d = pack2(c.S[X][KS >> 1][QB][8 * (KS & 1) + 2 * P], c.S[X][KS >> 1][QB][8 * (KS & 1) + 2 * P + 1]);
  asm volatile("" : "+v"(d));
  w[P] = d;
  c.Pk[QB][KS] = __builtin_bit_cast(bf16x8, w);
}
template <int X, int J> DI void rowsum1(AC& c) {
  constexpr int qb = J >> 4, ks = (J >> 2) & 3, p = J & 3;
  c.rs0[qb] = fadd_s(c.rs0[qb], c.S[X][ks >> 1][qb][8 * (ks & 1) + 2 * p]);
  c.rs1[qb] = fadd_s(c.rs1[qb], c.S[X][ks >> 1][qb][8 * (ks & 1) + 2 * p + 1]);
}
DI void lsum_merge(AC& c) {
#pragma unroll
  for (int qb = 0; qb < 2; ++qb) {
    const float rs = fadd_s(c.rs0[qb], c.rs1[qb]);
    auto pr = __builtin_amdgcn_permlane32_swap(__float_as_uint(rs), __float_as_uint(rs), false, false);
    c.lsum[qb] = fadd_s(c.lsum[qb], fadd_s(__uint_as_float(pr[0]), __uint_as_float(pr[1])));
  }
  asm volatile("" : "+v"(c.lsum[0]), "+v"(c.lsum[1]));
}
#define AT_VF "+v"(c.Vf[0][0]), "+v"(c.Vf[0][1]), "+v"(c.Vf[0][2]), "+v"(c.Vf[0][3]), "+v"(c.Vf[1][0]), "+v"(c.Vf[1][1]), "+v"(c.Vf[1][2]), "+v"(c.Vf[1][3]), \
              "+v"(c.Vf[2][0]), "+v"(c.Vf[2][1]), "+v"(c.Vf[2][2]), "+v"(c.Vf[2][3]), "+v"(c.Vf[3][0]), "+v"(c.Vf[3][1]), "+v"(c.Vf[3][2]), "+v"(c.Vf[3][3])
template <int X, bool FIRST, int M> DI void gapA(AC& c) {
  constexpr int Xp = X ^ 1;
  qk_mfma<X, FIRST, M>(c);
  GAP();
  if constexpr (M == 26) {
    c.ka = c.Kh + (size_t)(unsigned)c.kt * 32768; asm volatile("" : "+s"(c.ka));
    c.kt = c.kt + 1 == c.end0 ? 0 : c.kt + 1; asm volatile("" : "+s"(c.kt));
  }
  if constexpr (M == 28) {
    c.va = c.Vh + (size_t)(unsigned)c.vt * 32768; asm volatile("" : "+s"(c.va));
    c.vt = c.vt + 1 == c.end0 ? 0 : c.vt + 1; asm volatile("" : "+s"(c.vt));
  }
  if constexpr (FIRST) o_zero4<M>();
  if constexpr (!FIRST && M == 8) asm volatile("" ::"v"(c.NEGM[0]), "v"(c.NEGM[1]));
  if constexpr (!FIRST) {
    if constexpr (M >= 10 && M < 26) v_read<M - 10>(c);
    if constexpr (M < 16) {
      constexpr int e0 = 2 * M, e1 = 2 * M + 1;
      c.S[Xp][1][e0 >> 4][e0 & 15] = __builtin_amdgcn_exp2f(c.S[Xp][1][e0 >> 4][e0 & 15]);
      c.S[Xp][1][e1 >> 4][e1 & 15] = __builtin_amdgcn_exp2f(c.S[Xp][1][e1 >> 4][e1 & 15]);
      pack1<Xp, (M >> 3), ((M >> 2) & 1), (M & 3)>(c);
    } else {
      constexpr int cc = M - 16;
      pack1<Xp, (cc >> 3), 2 + ((cc >> 2) & 1), (cc & 3)>(c);
      rowsum1<Xp, 16 * (cc >> 3) + 8 + 4 * ((cc >> 2) & 1) + (cc & 3)>(c);
    }
  }
  GAP();
}
template <int X, bool FIRST, int... Ms> DI void phaseA_(AC& c, bool pf, iseq<Ms...>) {
  (gapA<X, FIRST, Ms>(c), ...);
  if constexpr (FIRST) {
    if (pf) asm volatile("s_waitcnt vmcnt(2)" ::: "memory");
    else asm volatile("s_waitcnt vmcnt(0)" ::: "memory");
    asm volatile("s_waitcnt lgkmcnt(0)\n\ts_barrier" ::: "memory");
  } else {
    lsum_merge(c);
    asm volatile("s_waitcnt vmcnt(0) lgkmcnt(0)\n\ts_barrier" : AT_VF::"memory");
  }
  GAP();
}
template <int X, bool FIRST> DI void phaseA(AC& c, bool pf = false) { phaseA_<X, FIRST>(c, pf, make_iseq<32>{}); }

template <int KK> DI void mask1(float& x, int lim, float ninf) {
  asm volatile("v_cmp_gt_i32_e32 vcc, %c3, %1\n\tv_cndmask_b32_e32 %0, %0, %2, vcc" : "+v"(x) : "v"(lim), "v"(ninf), "i"(KK) : "vcc");
}
template <int X, int... Es> DI void mask_all(AC& c, int lim, float ninf, iseq<Es...>) {
  (mask1<32 * ((Es >> 4) & 1) + (Es & 3) + 8 * ((Es >> 2) & 3) - 32 * (Es >> 5)>(reinterpret_cast<float*>(&c.S[X][(Es >> 4) & 1][Es >> 5])[Es & 15], lim, ninf), ...);
}
template <int X> DI void at_mask(AC& c) {
  if (c.dd >= 0) {
    const unsigned l = lane_id();
    const int lim = (int)(l & 31u) - (int)((l >> 5) << 2) - 64 * c.dd;
    mask_all<X>(c, lim, -__builtin_inff(), make_iseq<64>{});
  }
}
template <int X, bool PEEL, int M> DI void gapB(AC& c) {
  if constexpr (PEEL) {
    if constexpr (M < 8) dma<M>(c.ka, c.voff, c.mk);
    else if constexpr (M < 16) dma<M - 8>(c.va, c.voff, c.mv);
    if constexpr (M < 16) k_read<M>(c.rb);
  } else {
    if constexpr (M < 16) pv_mfma_ld<M>(c);
    else pv_mfma<M>(c);
  }
  GAP();
  if constexpr (M == 4) at_mask<X>(c);
  if constexpr (M >= 4 && M < 15) {
    constexpr int s0 = 3 * (M - 4);
#pragma unroll
    for (int s = s0; s < s0 + 3; ++s) {
      if (s < 32) {
        const int q = s >> 4, i = s & 15, ch = i & 3, kb = ch >> 1, e = (ch & 1) * 8 + (i >> 2) * 2;
        if (i < 4) c.pmx[q][ch] = max2f(c.S[X][kb][q][e], c.S[X][kb][q][e + 1]);
        else c.pmx[q][ch] = max3f(c.pmx[q][ch], c.S[X][kb][q][e], c.S[X][kb][q][e + 1]);
      }
    }
  }
  if constexpr (M == 15 || M == 16) {
    constexpr int q = M - 15;
    const float mm = max2f(max3f(c.pmx[q][0], c.pmx[q][1], c.pmx[q][2]), c.pmx[q][3]);
    auto pr = __builtin_amdgcn_permlane32_swap(__float_as_uint(mm), __float_as_uint(mm), false, false);
    c.rmax[q] = max2f(__uint_as_float(pr[0]), __uint_as_float(pr[1]));
  }
  if constexpr (M == 17) {
    if constexpr (PEEL) {
#pragma unroll
      for (int q = 0; q < 2; ++q) {
        c.mhat[q] = max2f(c.rmax[q], -1e30f); c.lsum[q] = 0.f; c.fct[q] = 1.f;
#pragma unroll
        for (int kb = 0; kb < 2; ++kb)
#pragma unroll
          for (int e = 0; e < 16; ++e) c.S[X][kb][q][e] = fsub_s(c.S[X][kb][q][e], c.mhat[q]);
        negm_bcast(c.NEGM[q], -c.mhat[q]);
      }
    } else {
      const bool grew = max2f(c.rmax[0], c.rmax[1]) > AT_THR;
      if (__builtin_expect(__any((int)grew), 0)) {
#pragma unroll
        for (int q = 0; q < 2; ++q) {
          const float dl = max2f(c.rmax[q], 0.f);
          c.mhat[q] = fadd_s(c.mhat[q], dl); c.fct[q] = __builtin_amdgcn_exp2f(-dl);
#pragma unroll
          for (int kb = 0; kb < 2; ++kb)
#pragma unroll
            for (int e = 0; e < 16; ++e) c.S[X][kb][q][e] = fsub_s(c.S[X][kb][q][e], dl);
          negm_bcast(c.NEGM[q], -c.mhat[q]);
        }
        c.pend = true;
      }
    }
    c.rs0[0] = c.rs0[1] = c.rs1[0] = c.rs1[1] = 0.f;
  }
  if constexpr (M >= 18) {
    constexpr int g = M - 18, e_lo = (32 * g) / 14, e_hi = (32 * (g + 1)) / 14;
    constexpr int p_lo = g ? (32 * (g - 1)) / 14 : 0, p_hi = g ? (32 * g) / 14 : 0;
#pragma unroll
    for (int e = e_lo; e < e_hi; ++e) c.S[X][0][e >> 4][e & 15] = __builtin_amdgcn_exp2f(c.S[X][0][e >> 4][e & 15]);
#pragma unroll
    for (int e = p_lo; e < p_hi; ++e) {
      if (e & 1) c.rs1[e >> 4] = fadd_s(c.rs1[e >> 4], c.S[X][0][e >> 4][e & 15]);
      else c.rs0[e >> 4] = fadd_s(c.rs0[e >> 4], c.S[X][0][e >> 4][e & 15]);
    }
  }
  if constexpr (M == 31) asm volatile("" ::"v"(c.S[X][0][0]), "v"(c.S[X][0][1]), "v"(c.NEGM[0]), "v"(c.NEGM[1]), "v"(c.rs0[0]), "v"(c.rs0[1]), "v"(c.rs1[0]), "v"(c.rs1[1]));
  GAP();
}
template <int I> DI void o_scale1(float f) {
  float t;
  asm volatile("v_accvgpr_read_b32 %0, a[%c2]\n\tv_mul_f32 %0, %1, %0\n\tv_accvgpr_write_b32 a[%c2], %0" : "=&v"(t) : "v"(f), "i"(I) : ACLOB_LO);
}
template <int... Is> DI void o_scale(const AC& c, iseq<Is...>) { (o_scale1<Is>(c.fct[(Is >> 4) & 1]), ...); }
template <int X, bool PEEL, int... Ms> DI void phaseB_(AC& c, iseq<Ms...>) {
  (gapB<X, PEEL, Ms>(c), ...);
#pragma unroll
  for (int e = (32 * 13) / 14; e < 32; ++e) {
    if (e & 1) c.rs1[e >> 4] = fadd_s(c.rs1[e >> 4], c.S[X][0][e >> 4][e & 15]);
    else c.rs0[e >> 4] = fadd_s(c.rs0[e >> 4], c.S[X][0][e >> 4][e & 15]);
  }
  if constexpr (!PEEL) {
    if (__builtin_expect(c.pend, 0)) {
      asm volatile("s_nop 7\n\ts_nop 7" ::: "memory");
      o_scale(c, make_iseq<128>{});
      asm volatile("s_nop 1" ::: "memory");
      c.lsum[0] = fmul_s(c.lsum[0], c.fct[0]); c.lsum[1] = fmul_s(c.lsum[1], c.fct[1]); c.pend = false;
    }
  }
  c.dd += 2;
  c.mk ^= 65536u; c.mv ^= 65536u;
  asm volatile("s_waitcnt lgkmcnt(0)\n\tv_xor_b32_e32 %0, 0x10000, %0" : "+v"(c.rb)::"memory");
  GAP();
}
template <int X, bool PEEL> DI void phaseB(AC& c) { phaseB_<X, PEEL>(c, make_iseq<32>{}); }
template <int... Is> DI void k_reads(unsigned rb, iseq<Is...>) { (k_read<Is>(rb), ...); }
template <int... Is> DI void v_reads(AC& c, iseq<Is...>) { (v_read<Is>(c), ...); }
template <int... Is> DI void q_loads(const char* qp, unsigned voff, iseq<Is...>) { (q_load<Is>(qp, voff), ...); }
template <int X, int... Cs> DI void packs(AC& c, iseq<Cs...>) { (pack1<X, ((Cs >> 3) & 1), 2 * (Cs >> 4) + ((Cs >> 2) & 1), (Cs & 3)>(c), ...); }
template <int X, int... Js> DI void rowsums(AC& c, iseq<Js...>) { (rowsum1<X, Js>(c), ...); }
template <int M> DI void pv_drain1(AC& c, const char* qn) {
  pv_mfma<M>(c);
  if constexpr (M & 1) q_load<(M >> 1)>(qn, c.voff);
  GAP();
}
template <int... Ms> DI void pv_drain(AC& c, const char* qn, iseq<Ms...>) { (pv_drain1<Ms>(c, qn), ...); }
template <int X> DI f32x4 at_finish(AC& c, char* mlp, int wave, const char* qn) {
  asm volatile("" : "+v"(c.S[X][0][0]), "+v"(c.S[X][0][1]), "+v"(c.S[X][1][0]), "+v"(c.S[X][1][1]));
  v_reads(c, make_iseq<16>{});
#pragma unroll
  for (int e = 0; e < 32; ++e) c.S[X][1][e >> 4][e & 15] = __builtin_amdgcn_exp2f(c.S[X][1][e >> 4][e & 15]);
  packs<X>(c, make_iseq<32>{});
  rowsums<X>(c, iseq<8, 9, 10, 11, 12, 13, 14, 15, 24, 25, 26, 27, 28, 29, 30, 31>{});
  lsum_merge(c);
  *reinterpret_cast<f32x4*>(mlp + wave * 1024 + c.voff) = f32x4{c.mhat[0], c.lsum[0], c.mhat[1], c.lsum[1]};
  asm volatile("s_waitcnt lgkmcnt(0)\n\ts_barrier" : AT_VF::"memory");
  const f32x4 ml = *reinterpret_cast<const f32x4*>(mlp + (wave ^ 2) * 1024 + c.voff);
  GAP();
  pv_drain(c, qn, make_iseq<32>{});
  return ml;
}
template <int I> DI float o_read() { float r; asm volatile("v_accvgpr_read_b32 %0, a[%c1]" : "=v"(r) : "i"(I)); return r; }
template <int Q> DI f32x4 o_quad(float f) {
  return f32x4{fmul_s(o_read<4 * Q>(), f), fmul_s(o_read<4 * Q + 1>(), f), fmul_s(o_read<4 * Q + 2>(), f), fmul_s(o_read<4 * Q + 3>(), f)};
}
template <int Q0, int... Is> DI void o_send(char* dst, const float (&f)[2], iseq<Is...>) {
  ((*reinterpret_cast<f32x4*>(dst + Is * 1024) = o_quad<Q0 + Is>(f[(Is >> 2) & 1])), ...);
}
template <int Q0, int I> DI void o_fin1(char* crow, const char* src, const float (&f)[2]) {
  constexpr int Q = Q0 + I, db = Q >> 3, qb = (Q >> 2) & 1, qd = Q & 3;
  const f32x4 in = *reinterpret_cast<const f32x4*>(src + I * 1024), own = o_quad<Q>(f[qb]);
  u32x2 w;
  w[0] = pack2(fadd_s(own[0], in[0]), fadd_s(own[1], in[1]));
  w[1] = pack2(fadd_s(own[2], in[2]), fadd_s(own[3], in[3]));
  *reinterpret_cast<u32x2*>(crow + qb * (2 * 128 * 1024) + db * 1024 + qd * 256) = w;
}
template <int Q0, int... Is> DI void o_fin(char* crow, const char* src, const float (&f)[2], iseq<Is...>) { (o_fin1<Q0, Is>(crow, src, f), ...); }

__global__ __launch_bounds__(256, 1) void k_attn(const char* __restrict__ Qf, const char* __restrict__ Ki,
                                                 const char* __restrict__ Vi, char* __restrict__ ctx,
                                                 const u16* __restrict__ wo, const u16* __restrict__ win,
                                                 const u16* __restrict__ wsp, const u16* __restrict__ wout) {
  __shared__ __attribute__((aligned(16))) char smem[AT_MISC + 4096 + 256];
  const int b = blockIdx.x, xcd = b & 7, j = b >> 3;
  const int head = xcd * 4 + (j & 3), pair = j >> 2;
  const int wave = __builtin_amdgcn_readfirstlane(threadIdx.x >> 6), kg = wave >> 1, rg = wave & 1;
  AC c;
  const unsigned ldsb = lds_addr(smem);
  c.voff = (threadIdx.x & 63) * 16u;
  c.rb = ldsb + kg * 16384 + c.voff;
  c.mk = ldsb + wave * 8192;
  c.mv = ldsb + 32768 + wave * 8192;
  c.Kh = Ki + (size_t)head * (32 * 16384) + wave * 8192;
  c.Vh = Vi + (size_t)head * (32 * 16384) + wave * 8192;
  c.end0 = 16 - pair;
  const char* Qh = Qf + (size_t)head * (64 * 8192) + rg * 16384;
  STAMP(5, 0);
  {
    dmas(c.Kh, c.voff, c.mk, make_iseq<8>{});
    q_loads(Qh + (15 - pair) * 32768, c.voff, make_iseq<16>{});
    dmas(c.Vh, c.voff, c.mv, make_iseq<8>{});
    dmas(c.Kh + 32768, c.voff, c.mk + 65536, make_iseq<8>{});
    c.kt = 2; c.vt = 1;
    asm volatile("s_waitcnt vmcnt(32)\n\ts_barrier" ::: "memory");
    k_reads(c.rb, make_iseq<16>{});
    asm volatile("s_waitcnt vmcnt(16) lgkmcnt(0)\n\tv_xor_b32_e32 %0, 0x10000, %0" : "+v"(c.rb)::"memory");
    c.mv ^= 65536u;
  }
  for (int it = 0; it < 2; ++it) {
    const int qb = it ? pair : 15 - pair, T = qb + 1;
    c.dd = kg - 2 * qb - rg;
    c.pend = false;
    STAMP(5, it * 4 + 1);
    if (T & 1) {
      phaseA<1, true>(c, it);
      phaseB<1, true>(c);
    } else {
      phaseA<0, true>(c, it);
      phaseB<0, true>(c);
      phaseA<1, false>(c);
      phaseB<1, false>(c);
    }
    for (int t = (T - 1) >> 1; t > 0; --t) {
      if (t == 3) STAMP(4, 0);
      phaseA<0, false>(c);
      if (t == 3) STAMP(4, 1);
      phaseB<0, false>(c);
      if (t == 3) STAMP(4, 2);
      phaseA<1, false>(c);
      if (t == 3) STAMP(4, 3);
      phaseB<1, false>(c);
      if (t == 3) STAMP(4, 4);
    }
    const f32x4 ml = at_finish<1>(c, smem + AT_MISC, wave, Qh + pair * 32768);
    STAMP(5, it * 4 + 2);
    const unsigned ln = lane_id();
    float f[2];
#pragma unroll
    for (int q = 0; q < 2; ++q) {
      const float mt = fmaxf(c.mhat[q], ml[2 * q]);
      const float a0 = __builtin_amdgcn_exp2f(c.mhat[q] - mt), a1 = __builtin_amdgcn_exp2f(ml[2 * q] - mt);
      f[q] = a0 / fadd_s(fmul_s(c.lsum[q], a0), fmul_s(ml[2 * q + 1], a1));
    }
    asm volatile("s_nop 7\n\ts_nop 7" ::: "memory");
    const unsigned so = (c.rb - ldsb) & 65536u;
    char* a0p = smem + (so ^ 65536u) + rg * 16384 + ln * 16;
    char* a1p = smem + so + 32768 + rg * 16384 + ln * 16;
    char* crow = ctx + ((size_t)(qb * 8 + rg * 4 + ((ln >> 4) & 1)) * 128 + head * 4) * 1024 + (ln & 15) * 16 + (ln >> 5) * 8;
    if (kg == 0) o_send<16>(a0p, f, make_iseq<16>{});
    else o_send<0>(a1p, f, make_iseq<16>{});
    barrier();
    if (kg == 0) o_fin<0>(crow, a1p, f, make_iseq<16>{});
    else o_fin<16>(crow, a0p, f, make_iseq<16>{});
    asm volatile("s_waitcnt vmcnt(16)" ::: "memory");
    if (it == 0) {
#pragma unroll
      for (int u = 0; u < 2; ++u) {
        const int sl = (b * 4 + wave) * 2 + u;
        const u16* base = sl < 1024 ? wsp : sl < 1536 ? wo : sl < 1708 ? win : wout;
        const int nb = sl < 1024 ? S * S * 2 : sl < 1536 ? HDM * DM * 2 : sl < 1708 ? DM * FF * 2 : FH * DM * 2;
        const int first = sl < 1024 ? sl : sl < 1536 ? sl - 1024 : sl < 1708 ? sl - 1536 : sl - 1708;
        pf_slice(base, nb, first * 64, ln, smem + AT_MISC + 4096);
      }
      GAP();
    }
    STAMP(5, it * 4 + 3);
  }
  asm volatile("s_waitcnt vmcnt(0)" ::: "memory");
}

constexpr int SK_AREA = 16384;
template <int NT> struct SkOwn { static constexpr int N = (2 * NT + 3) / 4; };
template <int NT>
DI void splitk_reduce(const f32x4 (&acc)[4][NT], f32x4 (&own)[SkOwn<NT>::N], char* smem, int wave, int lane) {
  const int hi = wave >> 2, wq = wave & 3;
  char* mine = smem + wave * SK_AREA + lane * 16;
  f32x4 r[2 * NT];
#pragma unroll
  for (int f = 0; f < 2 * NT; ++f) {
    const f32x4 lo = acc[f / NT][f % NT], up = acc[2 + f / NT][f % NT];
    f32x4 snd, kp;
#pragma unroll
    for (int e = 0; e < 4; ++e) { snd[e] = hi ? lo[e] : up[e]; kp[e] = hi ? up[e] : lo[e]; }
    *reinterpret_cast<f32x4*>(mine + f * 1024) = snd;
    r[f] = kp;
  }
  barrier();
  {
    const char* part = smem + (wave ^ 4) * SK_AREA + lane * 16;
#pragma unroll
    for (int f = 0; f < 2 * NT; ++f) {
      const f32x4 o = *reinterpret_cast<const f32x4*>(part + f * 1024);
      r[f] += o;
    }
  }
  barrier();
#pragma unroll
  for (int f = 0; f < 2 * NT; ++f) *reinterpret_cast<f32x4*>(mine + f * 1024) = r[f];
  barrier();
#pragma unroll
  for (int i = 0; i < SkOwn<NT>::N; ++i) {
    const int f = wq + 4 * i;
    const char* p = smem + hi * 4 * SK_AREA + lane * 16 + (f < 2 * NT ? f : 0) * 1024;
    const f32x4 a = *reinterpret_cast<const f32x4*>(p), b = *reinterpret_cast<const f32x4*>(p + SK_AREA);
    const f32x4 c = *reinterpret_cast<const f32x4*>(p + 2 * SK_AREA), d = *reinterpret_cast<const f32x4*>(p + 3 * SK_AREA);
    own[i] = (a + b) + (c + d);
  }
}
DI void sk_bfrags(const char* img, bf16x8 (&bfr)[4], int lane) {
  const int fr = lane & 15, fg = lane >> 4, q = fr >> 2, p = lane & 3, sw = (fg & 1) << 2;
  const char* base = img + fg * 1024 + p * 8;
#pragma unroll
  for (int nt = 0; nt < 4; ++nt) {
    s16x4 lo = tr_read(base + nt * 256 + (q ^ sw) * 32);
    s16x4 hi = tr_read(base + nt * 256 + ((4 + q) ^ sw) * 32);
    bfr[nt] = __builtin_shufflevector(lo, hi, 0, 1, 2, 3, 4, 5, 6, 7);
  }
}

constexpr int OP_NS = 16, OP_DB = 4, OP_DA = 6;
constexpr int op_wait(int t) {
  int pos = 0, bpos = -1;
  for (int i = 0; i < OP_DA; ++i) {
    if (i < OP_DB) { pos += 4; if (i == t) bpos = pos; }
    pos += 4;
  }
  for (int s = 0; s < t; ++s) {
    if (s + OP_DB < OP_NS) { pos += 4; if (s + OP_DB == t) bpos = pos; }
    if (s + OP_DA < OP_NS) pos += 4;
  }
  return pos - bpos;
}
template <int T> struct OpWait { static constexpr int v = op_wait(T); };
template <int T> DI void op_step(f32x4 (&acc)[4][4], u32x4 (&areg)[OP_DA][4], char* ring, const char* asrc,
                                 const u16* bs0, const u16* bs1, int g0, int lane) {
  wait_vm<OpWait<T>::v>();
  if constexpr (T == 0) STAMP(2, 2);
  if constexpr (T == 8) STAMP(2, 3);
  bf16x8 bfr[4];
  sk_bfrags(ring + (T % OP_DB) * 4096, bfr, lane);
#pragma unroll
  for (int mt = 0; mt < 4; ++mt)
#pragma unroll
    for (int nt = 0; nt < 4; ++nt)
      acc[mt][nt] = __builtin_amdgcn_mfma_f32_16x16x32_bf16(bfr[nt], __builtin_bit_cast(bf16x8, areg[T % OP_DA][mt]), acc[mt][nt], 0, 0, 0);
  if constexpr (T + OP_DB < OP_NS) {
    asm volatile("s_waitcnt lgkmcnt(0)" ::: "memory");
    const int g = (g0 + T + OP_DB) & 127;
#pragma unroll
    for (int i = 0; i < 4; ++i)
      glds16(((i & 1) ? bs1 : bs0) + (size_t)(g * 32 + i * 8) * DM, ring + (T % OP_DB) * 4096 + i * 1024);
  }
  if constexpr (T + OP_DA < OP_NS) {
    const int g = (g0 + T + OP_DA) & 127;
#pragma unroll
    for (int mt = 0; mt < 4; ++mt)
      areg[T % OP_DA][mt] = *reinterpret_cast<const u32x4*>(asrc + (size_t)mt * (128 * 1024) + g * 1024);
  }
  if constexpr (T + 1 < OP_NS) op_step<T + 1>(acc, areg, ring, asrc, bs0, bs1, g0, lane);
}
__global__ __launch_bounds__(512, 2) void k_oproj(const char* __restrict__ ctxf, const u16* __restrict__ wo,
                                                  const u16* __restrict__ x, const float* __restrict__ ga,
                                                  u16* __restrict__ x1) {
  __shared__ __attribute__((aligned(16))) char smem[8 * SK_AREA + 256];
  const int b = blockIdx.x, xcd = b & 7, j = b >> 3;
  const int mtile = xcd * 4 + (j >> 3), ntile = j & 7;
  const int tid = threadIdx.x, wave = __builtin_amdgcn_readfirstlane(tid >> 6), lane = tid & 63;
  const int m0 = mtile * 64, n0 = ntile * 64;
  char* ring = smem + wave * SK_AREA;
  const char* asrc = ctxf + (size_t)(mtile * 4) * 128 * 1024 + lane * 16;
  const int g0 = wave * 16;
  const int rw = (lane & 15) >> 1;
  const u16* bs0 = wo + (size_t)rw * DM + n0 + (lane >> 4) * 16 + (lane & 1) * 8;
  const u16* bs1 = wo + (size_t)(rw ^ 4) * DM + n0 + (lane >> 4) * 16 + (lane & 1) * 8;
  f32x4 acc[4][4];
#pragma unroll
  for (int i = 0; i < 4; ++i)
#pragma unroll
    for (int jj = 0; jj < 4; ++jj) acc[i][jj] = f32x4{0.f, 0.f, 0.f, 0.f};
  u32x4 areg[OP_DA][4];
  STAMP(2, 0);
#pragma unroll
  for (int i = 0; i < OP_DA; ++i) {
    const int g = (g0 + i) & 127;
    if (i < OP_DB) {
#pragma unroll
      for (int ii = 0; ii < 4; ++ii) glds16(((ii & 1) ? bs1 : bs0) + (size_t)(g * 32 + ii * 8) * DM, ring + i * 4096 + ii * 1024);
    }
#pragma unroll
    for (int mt = 0; mt < 4; ++mt)
      areg[i][mt] = *reinterpret_cast<const u32x4*>(asrc + (size_t)mt * (128 * 1024) + g * 1024);
  }
  STAMP(2, 1);
  op_step<0>(acc, areg, ring, asrc, bs0, bs1, g0, lane);
  STAMP(2, 4);
  const int fr = lane & 15, fg = lane >> 4, hi = wave >> 2, wq = wave & 3;
  u32x2 xv[2];
  f32x4 gv;
  {
    gv = *reinterpret_cast<const f32x4*>(ga + n0 + wq * 16 + fg * 4);
#pragma unroll
    for (int i = 0; i < 2; ++i)
      xv[i] = *reinterpret_cast<const u32x2*>(x + (size_t)(m0 + (2 * hi + i) * 16 + fr) * DM + n0 + wq * 16 + fg * 4);
  }
  f32x4 own[2];
  splitk_reduce<4>(acc, own, smem, wave, lane);
#pragma unroll
  for (int i = 0; i < 2; ++i) {
    u32x2 w;
    w[0] = pack2(bflo(xv[i][0]) + gv[0] * own[i][0], bfhi(xv[i][0]) + gv[1] * own[i][1]);
    w[1] = pack2(bflo(xv[i][1]) + gv[2] * own[i][2], bfhi(xv[i][1]) + gv[3] * own[i][3]);
    *reinterpret_cast<u32x2*>(x1 + (size_t)(m0 + (2 * hi + i) * 16 + fr) * DM + n0 + wq * 16 + fg * 4) = w;
  }
  STAMP(2, 5);
}

constexpr int NBLK = 43;
using G4 = Gemm<64, 192, 1, 2, 4>;
DI float gelu_tanh(float x) {
  constexpr float K1 = -2.f * 0.7978845608028654f * 1.4426950408889634f, K2 = K1 * 0.044715f;
  const float e = __builtin_amdgcn_exp2f(x * fmaf(x * x, K2, K1));
  return x * __builtin_amdgcn_rcpf(1.f + e);
}
DI u32x4 gload_asm(const void* p) {
  u32x4 r;
  asm volatile("global_load_dwordx4 %0, %1, off" : "=v"(r) : "v"(p) : "memory");
  return r;
}
template <bool Z2>
DI void win_body(char* smem, const u16* __restrict__ x1, const float* __restrict__ sb, const float* __restrict__ cb,
                 const u16* __restrict__ win, char* __restrict__ zf, float* __restrict__ stats, int mtile, int t4) {
  char* sB = smem + 65536;
  const int tid = threadIdx.x, wave = __builtin_amdgcn_readfirstlane(tid >> 6), lane = tid & 63;
  const int wm = wave >> 2, wn = wave & 3;
  const int m0 = mtile * 64, n0 = (Z2 ? FH : 0) + t4 * 176;
  STAMP(1, 0);
  u32x4 c0 = gload_asm(cb + lane * 8), c1 = gload_asm(cb + lane * 8 + 4);
  u32x4 s0 = gload_asm(sb + lane * 8), s1 = gload_asm(sb + lane * 8 + 4);
  {
    const int rr = lane >> 3, ch = (lane & 7) ^ rr;
    const u16* src = x1 + (size_t)(m0 + wave * 8 + rr) * DM + ch * 8;
#pragma unroll
    for (int sub = 0; sub < 8; ++sub) glds16(src + sub * 64, smem + sub * 8192 + wave * 1024);
  }
#pragma unroll
  for (int st = 0; st < 4; ++st) G4::stageB(win, FF, st * 64, n0, FF - 8, sB + st * G4::B_BYTES, wave, lane);
  u32x4 breg[4][3];
  {
    const int pos = lane & 15, rw = pos >> 1, hf = pos & 1;
    const u16* src[3];
#pragma unroll
    for (int ii = 0; ii < 3; ++ii) {
      const int blk = (ii * 8 + wave) * 4 + (lane >> 4), kb = blk / G4::NB, nb = blk % G4::NB;
      int n = n0 + nb * 16 + hf * 8;
      if (n > FF - 8) n = FF - 8;
      src[ii] = win + (size_t)(256 + kb * 8 + (rw ^ ((kb & 1) << 2))) * FF + n;
    }
#pragma unroll
    for (int st = 0; st < 4; ++st) {
#pragma unroll
      for (int ii = 0; ii < 3; ++ii) breg[st][ii] = *reinterpret_cast<const u32x4*>(src[ii] + (size_t)st * 64 * FF);
      __builtin_amdgcn_sched_barrier(0);
    }
  }
  asm volatile("s_waitcnt vmcnt(24)" : "+v"(c0), "+v"(c1), "+v"(s0), "+v"(s1)::"memory");
  STAMP(1, 1);
  {
    float cbv[8], sbv[8];
#pragma unroll
    for (int jj = 0; jj < 4; ++jj) {
      cbv[jj] = 1.f + __uint_as_float(c0[jj]); cbv[4 + jj] = 1.f + __uint_as_float(c1[jj]);
      sbv[jj] = __uint_as_float(s0[jj]); sbv[4 + jj] = __uint_as_float(s1[jj]);
    }
    u32x4 xr[8];
#pragma unroll
    for (int i = 0; i < 8; ++i)
      xr[i] = *reinterpret_cast<const u32x4*>(smem + (lane >> 3) * 8192 + (wave * 8 + i) * 128 + (((lane & 7) ^ i) << 4));
#pragma unroll
    for (int i = 0; i < 8; ++i) {
      float f[8];
#pragma unroll
      for (int jj = 0; jj < 4; ++jj) { f[2 * jj] = bflo(xr[i][jj]); f[2 * jj + 1] = bfhi(xr[i][jj]); }
      float s = 0.f;
#pragma unroll
      for (int jj = 0; jj < 8; ++jj) s += f[jj];
      const float mean = wave_sum(s) * (1.f / DM);
      float v = 0.f;
#pragma unroll
      for (int jj = 0; jj < 8; ++jj) { f[jj] -= mean; v += f[jj] * f[jj]; }
      const float rstd = rsqrtf(wave_sum(v) * (1.f / DM) + 1e-5f);
#pragma unroll
      for (int jj = 0; jj < 4; ++jj)
        xr[i][jj] = pack2(f[2 * jj] * rstd * cbv[2 * jj] + sbv[2 * jj], f[2 * jj + 1] * rstd * cbv[2 * jj + 1] + sbv[2 * jj + 1]);
    }
#pragma unroll
    for (int i = 0; i < 8; ++i)
      *reinterpret_cast<u32x4*>(smem + (lane >> 3) * 8192 + (wave * 8 + i) * 128 + (((lane & 7) ^ i) << 4)) = xr[i];
  }
  STAMP(1, 2);
  f32x4 acc[G4::MT][G4::NT];
#pragma unroll
  for (int i = 0; i < G4::MT; ++i)
#pragma unroll
    for (int jj = 0; jj < G4::NT; ++jj) acc[i][jj] = f32x4{0.f, 0.f, 0.f, 0.f};
#pragma unroll
  for (int kt = 0; kt < 8; ++kt) {
    if (kt == 0) wait_vm<21>(); else if (kt == 1) wait_vm<18>(); else if (kt == 2) wait_vm<15>(); else if (kt == 3) wait_vm<12>();
    barrier();
    if (kt == 0) STAMP(1, 3);
    if (kt == 1) STAMP(1, 4);
    if (kt == 3) STAMP(1, 5);
    if (kt == 5) STAMP(1, 6);
    if (kt >= 1 && kt + 3 < 8) {
#pragma unroll
      for (int ii = 0; ii < 3; ++ii)
        *reinterpret_cast<u32x4*>(sB + ((kt + 3) & 3) * G4::B_BYTES + (ii * 8 + wave) * 1024 + lane * 16) = breg[kt - 1][ii];
    }
    G4::compute<!Z2>(smem + kt * 8192, sB + (kt & 3) * G4::B_BYTES, acc, wm, wn, lane);
  }
  STAMP(1, 7);
  const int fr = lane & 15, fg = lane >> 4;
  if constexpr (!Z2) {
#pragma unroll
    for (int nt = 0; nt < G4::NT; ++nt) {
      const int nbl = wn * 3 + nt, nb = t4 * 11 + nbl;
      if (nbl < 11 && nb < NBLK) {
#pragma unroll
        for (int mt = 0; mt < G4::MT; ++mt) {
          u32x2 w;
          w[0] = pack2(gelu_tanh(acc[mt][nt][0]), gelu_tanh(acc[mt][nt][1]));
          w[1] = pack2(gelu_tanh(acc[mt][nt][2]), gelu_tanh(acc[mt][nt][3]));
          *reinterpret_cast<u32x2*>(zf + ((size_t)(mtile * 4 + wm * 2 + mt) * NBLK + nb) * 512 + lane * 8) = w;
        }
      }
    }
  } else {
    float p1[G4::MT][4], p2[G4::MT][4];
#pragma unroll
    for (int mt = 0; mt < G4::MT; ++mt)
#pragma unroll
      for (int i = 0; i < 4; ++i) { p1[mt][i] = 0.f; p2[mt][i] = 0.f; }
#pragma unroll
    for (int nt = 0; nt < G4::NT; ++nt) {
      const int nbl = wn * 3 + nt, nb = t4 * 11 + nbl;
      if (nbl < 11 && nb < NBLK) {
#pragma unroll
        for (int mt = 0; mt < G4::MT; ++mt) {
          float gz[4];
#pragma unroll
          for (int i = 0; i < 4; ++i) {
            gz[i] = rbf(gelu_tanh(acc[mt][nt][i]));
            p1[mt][i] += gz[i]; p2[mt][i] += gz[i] * gz[i];
          }
          u32x2 w;
          w[0] = pack2(gz[0], gz[1]);
          w[1] = pack2(gz[2], gz[3]);
          *reinterpret_cast<u32x2*>(zf + ((size_t)(mtile * 2 + wm) * NBLK + nb) * 1024 + (fr + 16 * (mt * 2 + (fg >> 1))) * 16 + (fg & 1) * 8) = w;
        }
      }
    }
    barrier();
    float* red = reinterpret_cast<float*>(smem);
#pragma unroll
    for (int mt = 0; mt < G4::MT; ++mt)
#pragma unroll
      for (int i = 0; i < 4; ++i) {
        float a = p1[mt][i], c = p2[mt][i];
        a = row16_sum(a); c = row16_sum(c);
        if (fr == 0) {
          const int rowl = wm * 32 + mt * 16 + fg * 4 + i;
          red[(wn * 64 + rowl) * 2] = a; red[(wn * 64 + rowl) * 2 + 1] = c;
        }
      }
    barrier();
    if (tid < 128) {
      const float v = (red[tid] + red[128 + tid]) + (red[256 + tid] + red[384 + tid]);
      stats[((size_t)t4 * S + m0) * 2 + tid] = v;
    }
  }
}
__global__ __launch_bounds__(512, 2) void k_win(const u16* __restrict__ x1, const float* __restrict__ sb,
                                                const float* __restrict__ cb, const u16* __restrict__ win,
                                                char* __restrict__ z1f, char* __restrict__ z2f,
                                                float* __restrict__ stats) {
  __shared__ __attribute__((aligned(16))) char smem[65536 + 4 * G4::B_BYTES];
  const int b = blockIdx.x, xcd = b & 7, j = b >> 3;
  const int mtile = xcd * 4 + (j >> 3), ntile = j & 7;
  if (ntile >= 4) win_body<true>(smem, x1, sb, cb, win, z2f, stats, mtile, ntile - 4);
  else win_body<false>(smem, x1, sb, cb, win, z1f, stats, mtile, ntile);
}

constexpr int UKS = 22;
template <int NT>
DI void spatial_body(char* smem, const u16* __restrict__ wsp, const char* __restrict__ z2f,
                     const float* __restrict__ stats, const u16* __restrict__ bsp, const char* __restrict__ z1f,
                     char* __restrict__ uf, int mtile, int nb0, int rot) {
  const int tid = threadIdx.x, wave = __builtin_amdgcn_readfirstlane(tid >> 6), lane = tid & 63;
  const int fr = lane & 15, fg = lane >> 4;
  char* ring = smem + wave * SK_AREA;
  float* rs = reinterpret_cast<float*>(smem + 8 * SK_AREA + wave * 2048);
  float* ms = rs + 256;
  const int m0 = mtile * 64, g0 = wave * 4 + rot;
  STAMP(3, 0);
  f32x4 sp[4][2];
#pragma unroll
  for (int t = 0; t < 4; ++t) {
    const float* p = stats + ((size_t)t * S + ((g0 + (lane >> 4)) & 31) * 64 + (lane & 15) * 4) * 2;
    sp[t][0] = *reinterpret_cast<const f32x4*>(p);
    sp[t][1] = *reinterpret_cast<const f32x4*>(p + 4);
  }
  const int rr = lane >> 3, ch = (lane & 7) ^ rr;
  const u16* asrc = wsp + (size_t)(m0 + rr) * S + ch * 8;
  auto stageA = [&](int s) {
#pragma unroll
    for (int i = 0; i < 8; ++i) glds16(asrc + (size_t)i * 8 * S + ((g0 + s) & 31) * 64, ring + (s & 1) * 8192 + i * 1024);
  };
  const char* bsrc = z2f + (size_t)nb0 * 1024 + lane * 16;
  u32x4 breg[3][NT];
  auto loadB = [&](int t) {
#pragma unroll
    for (int nt = 0; nt < NT; ++nt) breg[t % 3][nt] = *reinterpret_cast<const u32x4*>(bsrc + (((((g0 + (t >> 1)) & 31) * 2 + (t & 1)) * NBLK) + nt) * 1024);
  };
  stageA(0); loadB(0); loadB(1); stageA(1); loadB(2);
  STAMP(3, 1);
  {
    f32x4 rv, mv;
#pragma unroll
    for (int i = 0; i < 4; ++i) {
      float s1 = 0.f, s2 = 0.f;
#pragma unroll
      for (int t = 0; t < 4; ++t) { s1 += sp[t][i >> 1][(i & 1) * 2]; s2 += sp[t][i >> 1][(i & 1) * 2 + 1]; }
      const float mean = s1 * (1.f / FH);
      const float var = fmaxf(s2 * (1.f / FH) - mean * mean, 0.f);
      const float rstd = rsqrtf(var + 1e-5f);
      rv[i] = rstd; mv[i] = -mean * rstd;
    }
    *reinterpret_cast<f32x4*>(rs + lane * 4) = rv;
    *reinterpret_cast<f32x4*>(ms + lane * 4) = mv;
  }
  f32x4 acc[4][NT];
#pragma unroll
  for (int i = 0; i < 4; ++i)
#pragma unroll
    for (int jj = 0; jj < NT; ++jj) acc[i][jj] = f32x4{0.f, 0.f, 0.f, 0.f};
#pragma unroll
  for (int s = 0; s < 4; ++s) {
    if (s < 2) wait_vm<3 * NT + 8>(); else if (s == 2) wait_vm<2 * NT + 8>(); else wait_vm<NT>();
    if (s == 0) STAMP(3, 2);
    if (s == 2) STAMP(3, 3);
#pragma unroll
    for (int kk = 0; kk < 2; ++kk) {
      const int t = 2 * s + kk;
      bf16x8 af[4];
#pragma unroll
      for (int mt = 0; mt < 4; ++mt)
        af[mt] = *reinterpret_cast<const bf16x8*>(ring + (s & 1) * 8192 + (mt * 16 + fr) * 128 + (((kk * 4 + fg) ^ (fr & 7)) << 4));
      f32x4 rv[2], mv[2];
#pragma unroll
      for (int e = 0; e < 2; ++e) {
        rv[e] = *reinterpret_cast<const f32x4*>(rs + t * 32 + fg * 8 + e * 4);
        mv[e] = *reinterpret_cast<const f32x4*>(ms + t * 32 + fg * 8 + e * 4);
      }
#pragma unroll
      for (int nt = 0; nt < NT; ++nt) {
        const u32x4 raw = breg[t % 3][nt];
        u32x4 o4;
#pragma unroll
        for (int jj = 0; jj < 4; ++jj)
          o4[jj] = pack2(fmaf(bflo(raw[jj]), rv[jj >> 1][(jj & 1) * 2], mv[jj >> 1][(jj & 1) * 2]),
                         fmaf(bfhi(raw[jj]), rv[jj >> 1][(jj & 1) * 2 + 1], mv[jj >> 1][(jj & 1) * 2 + 1]));
        const bf16x8 bv = __builtin_bit_cast(bf16x8, o4);
#pragma unroll
        for (int mt = 0; mt < 4; ++mt)
          acc[mt][nt] = __builtin_amdgcn_mfma_f32_16x16x32_bf16(bv, af[mt], acc[mt][nt], 0, 0, 0);
      }
      if (t + 3 < 8) loadB(t + 3);
    }
    if (s + 2 < 4) {
      asm volatile("s_waitcnt lgkmcnt(0)" ::: "memory");
      stageA(s + 2);
    }
  }
  STAMP(3, 4);
  constexpr int NO = SkOwn<NT>::N;
  const int hi = wave >> 2, wq = wave & 3;
  u32x2 zv[NO];
  float bs[NO];
#pragma unroll
  for (int i = 0; i < NO; ++i) {
    int f = wq + 4 * i;
    if (f >= 2 * NT) f = 0;
    const int mt = 2 * hi + f / NT, nb = nb0 + f % NT;
    zv[i] = *reinterpret_cast<const u32x2*>(z1f + ((size_t)(mtile * 4 + mt) * NBLK + nb) * 512 + lane * 8);
    bs[i] = bf2f(bsp[m0 + mt * 16 + fr]);
  }
  f32x4 own[NO];
  splitk_reduce<NT>(acc, own, smem, wave, lane);
#pragma unroll
  for (int i = 0; i < NO; ++i) {
    const int f = wq + 4 * i;
    if (f < 2 * NT) {
      const int mt = 2 * hi + f / NT, nb = nb0 + f % NT;
      u32x2 w;
      w[0] = pack2(bflo(zv[i][0]) * (own[i][0] + bs[i]), bfhi(zv[i][0]) * (own[i][1] + bs[i]));
      w[1] = pack2(bflo(zv[i][1]) * (own[i][2] + bs[i]), bfhi(zv[i][1]) * (own[i][3] + bs[i]));
      char* dst = uf + ((size_t)(mtile * 4 + mt) * UKS + (nb >> 1)) * 1024 + (fr + 16 * (fg >> 1)) * 16 + (fg & 1) * 8;
      *reinterpret_cast<u32x2*>(dst + (nb & 1) * 512) = w;
      if (nb == NBLK - 1) *reinterpret_cast<u32x2*>(dst + 512) = u32x2{0u, 0u};
    }
  }
  STAMP(3, 5);
}
__global__ __launch_bounds__(512, 2) void k_spatial(const u16* __restrict__ wsp, const char* __restrict__ z2f,
                                                    const float* __restrict__ stats, const u16* __restrict__ bsp,
                                                    const char* __restrict__ z1f, char* __restrict__ uf) {
  __shared__ __attribute__((aligned(16))) char smem[8 * SK_AREA + 8 * 2048 + 256];
  const int b = blockIdx.x, xcd = b & 7, j = b >> 3;
  const int mtile = xcd * 4 + (j >> 3), ntile = j & 7;
  const int rot = 0;
  if (ntile < 5) spatial_body<5>(smem, wsp, z2f, stats, bsp, z1f, uf, mtile, ntile * 5, rot);
  else spatial_body<6>(smem, wsp, z2f, stats, bsp, z1f, uf, mtile, 25 + (ntile - 5) * 6, rot);
}

__global__ __launch_bounds__(512, 2) void k_wout(const char* __restrict__ uf, const u16* __restrict__ wout,
                                                 const u16* __restrict__ x1, const float* __restrict__ gb,
                                                 u16* __restrict__ out) {
  __shared__ __attribute__((aligned(16))) char smem[8 * SK_AREA];
  const int b = blockIdx.x, xcd = b & 7, j = b >> 3;
  const int mtile = xcd * 4 + (j >> 3), ntile = j & 7;
  const int tid = threadIdx.x, wave = __builtin_amdgcn_readfirstlane(tid >> 6), lane = tid & 63;
  const int m0 = mtile * 64, n0 = ntile * 64;
  char* ring = smem + wave * SK_AREA;
  const int ks0 = wave < 6 ? wave * 3 : 18 + (wave - 6) * 2, nks = wave < 6 ? 3 : 2;
  const int rw = (lane & 15) >> 1;
  u32x4 areg[3][4];
#pragma unroll
  for (int t = 0; t < 3; ++t) {
    const int ks = ks0 + (t < nks ? t : 0);
#pragma unroll
    for (int i = 0; i < 4; ++i) {
      int row = ks * 32 + i * 8 + ((i & 1) ? (rw ^ 4) : rw);
      if (row > FH - 1) row = FH - 1;
      glds16(wout + (size_t)row * DM + n0 + (lane >> 4) * 16 + (lane & 1) * 8, ring + t * 4096 + i * 1024);
    }
#pragma unroll
    for (int mt = 0; mt < 4; ++mt)
      areg[t][mt] = *reinterpret_cast<const u32x4*>(uf + ((size_t)(mtile * 4 + mt) * UKS + ks) * 1024 + lane * 16);
  }
  const int fr = lane & 15, fg = lane >> 4, hi = wave >> 2, wq = wave & 3;
  u32x2 xv[2];
  const f32x4 gv = *reinterpret_cast<const f32x4*>(gb + n0 + wq * 16 + fg * 4);
#pragma unroll
  for (int i = 0; i < 2; ++i)
    xv[i] = *reinterpret_cast<const u32x2*>(x1 + (size_t)(m0 + (2 * hi + i) * 16 + fr) * DM + n0 + wq * 16 + fg * 4);
  f32x4 acc[4][4];
#pragma unroll
  for (int i = 0; i < 4; ++i)
#pragma unroll
    for (int jj = 0; jj < 4; ++jj) acc[i][jj] = f32x4{0.f, 0.f, 0.f, 0.f};
  wait_vm<0>();
#pragma unroll
  for (int t = 0; t < 3; ++t) {
    if (t < nks) {
      bf16x8 bfr[4];
      sk_bfrags(ring + t * 4096, bfr, lane);
#pragma unroll
      for (int mt = 0; mt < 4; ++mt)
#pragma unroll
        for (int nt = 0; nt < 4; ++nt)
          acc[mt][nt] = __builtin_amdgcn_mfma_f32_16x16x32_bf16(bfr[nt], __builtin_bit_cast(bf16x8, areg[t][mt]), acc[mt][nt], 0, 0, 0);
    }
  }
  f32x4 own[2];
  splitk_reduce<4>(acc, own, smem, wave, lane);
#pragma unroll
  for (int i = 0; i < 2; ++i) {
    u32x2 w;
    w[0] = pack2(bflo(xv[i][0]) + gv[0] * own[i][0], bfhi(xv[i][0]) + gv[1] * own[i][1]);
    w[1] = pack2(bflo(xv[i][1]) + gv[2] * own[i][2], bfhi(xv[i][1]) + gv[3] * own[i][3]);
    *reinterpret_cast<u32x2*>(out + (size_t)(m0 + (2 * hi + i) * 16 + fr) * DM + n0 + wq * 16 + fg * 4) = w;
  }
}

extern "C" void kernel_launch(void* const* d_in, const int* in_sizes, int n_in, void* d_out, int out_size,
                              void* d_ws, size_t ws_size, hipStream_t stream) {
  const u16* x = (const u16*)d_in[0];
  const u16* ckv = (const u16*)d_in[1];
  const u16* wq = (const u16*)d_in[2];
  const u16* wk = (const u16*)d_in[3];
  const u16* wv = (const u16*)d_in[4];
  const u16* wo = (const u16*)d_in[5];
  const u16* win = (const u16*)d_in[7];
  const u16* wsp = (const u16*)d_in[8];
  const u16* bsp = (const u16*)d_in[9];
  const u16* wout = (const u16*)d_in[10];
  const u16* c = (const u16*)d_in[11];
  const u16* wmod = (const u16*)d_in[12];
  char* ws = (char*)d_ws;
  float* mod = (float*)(ws + WS_MOD);
  u16* h1 = (u16*)(ws + WS_H1);
  char* Qf = ws + WS_QF;
  char* Ki = ws + WS_KI;
  char* Vi = ws + WS_VI;
  char* ctx = ws + WS_CTX;
  u16* x1 = (u16*)(ws + WS_X1);
  char* z1 = ws + WS_Z1;
  char* z2 = ws + WS_Z2;
  float* stats = (float*)(ws + WS_ST);
  char* u = ws + WS_U;
  u16* out = (u16*)d_out;
  unsigned* flags = (unsigned*)(ws + WS_FLAG);

  k_kv<<<256, 512, 0, stream>>>(ckv, wk, wv, Ki, Vi, c, wmod, mod);
  k_ln<<<S / 4, 256, 0, stream>>>(x, mod + 0 * DM, mod + 1 * DM, h1);
  k_qkv<<<512, 256, 0, stream>>>(h1, wq, Qf, flags);
  k_attn<<<256, 256, 0, stream>>>(Qf, Ki, Vi, ctx, wo, win, wsp, wout);
  k_oproj<<<256, 512, 0, stream>>>(ctx, wo, x, mod + 2 * DM, x1);
  k_win<<<256, 512, 0, stream>>>(x1, mod + 3 * DM, mod + 4 * DM, win, z1, z2, stats);
  k_spatial<<<256, 512, 0, stream>>>(wsp, z2, stats, bsp, z1, u);
  k_wout<<<256, 512, 0, stream>>>(u, wout, x1, mod + 5 * DM, out);
}
```

```cpp
#include <hip/hip_runtime.h>
#include <hip/hip_bf16.h>
#include <stdint.h>
#include <utility>

constexpr int S = 2048;
constexpr int DM = 512;
constexpr int NH = 32;
constexpr int HD = 128;
constexpr int HDM = NH * HD;
constexpr int FH = 688;
constexpr int FF = 1376;

typedef __attribute__((ext_vector_type(8))) short bf16x8;
typedef __attribute__((ext_vector_type(4))) short s16x4;
typedef __attribute__((ext_vector_type(4))) float f32x4;
typedef __attribute__((ext_vector_type(16))) float f32x16;
typedef __attribute__((ext_vector_type(4))) unsigned u32x4;
typedef __attribute__((ext_vector_type(2))) unsigned u32x2;
typedef __attribute__((address_space(3))) s16x4* lds_s16x4_ptr;
typedef unsigned short u16;

#define DI __device__ __forceinline__

DI float bf2f(u16 v) { return __uint_as_float(((unsigned)v) << 16); }
DI float bflo(unsigned v) { return __uint_as_float(v << 16); }
DI float bfhi(unsigned v) { return __uint_as_float(v & 0xffff0000u); }
typedef __attribute__((ext_vector_type(2))) __bf16 bf16x2_t;
typedef __attribute__((ext_vector_type(2))) float f32x2;
DI unsigned pack2(float lo, float hi) {
  f32x2 v = {lo, hi};
  return __builtin_bit_cast(unsigned, __builtin_convertvector(v, bf16x2_t));
}
template <int N> DI void wait_vm() { asm volatile("s_waitcnt vmcnt(%0)" ::"n"(N) : "memory"); }
DI void barrier() { asm volatile("s_waitcnt lgkmcnt(0)" ::: "memory"); __builtin_amdgcn_s_barrier(); }
DI float rbf(float x) { return bflo(pack2(x, 0.f)); }

DI void glds16(const void* g, const void* lds_wave_base) {
  const unsigned a = __builtin_amdgcn_readfirstlane((unsigned)(size_t)lds_wave_base);
  unsigned keep;
  asm volatile("s_mov_b32 %0, m0\n\ts_mov_b32 m0, %2\n\ts_nop 0\n\tglobal_load_lds_dwordx4 %1, off\n\ts_mov_b32 m0, %0"
               : "=&s"(keep) : "v"(g), "s"(a) : "memory");
}
template <int OFF> DI void glds16s(const void* sbase, unsigned voff, unsigned lds_base) {
  asm volatile("s_mov_b32 m0, %2\n\ts_nop 0\n\tglobal_load_lds_dwordx4 %0, %1 offset:%3"
               : : "v"(voff), "s"(sbase), "s"(lds_base), "n"(OFF) : "memory");
}
DI unsigned lds_addr(const void* p) { return __builtin_amdgcn_readfirstlane((unsigned)(size_t)p); }
DI void pf_line(const void* g, const void* lds_dummy) {
  const unsigned a = __builtin_amdgcn_readfirstlane((unsigned)(size_t)lds_dummy);
  unsigned keep;
  asm volatile("s_mov_b32 %0, m0\n\ts_mov_b32 m0, %2\n\ts_nop 0\n\tglobal_load_lds_dword %1, off\n\ts_mov_b32 m0, %0"
               : "=&s"(keep) : "v"(g), "s"(a) : "memory");
}
DI void pf_slice(const void* base, int nbytes, int first, int lane, const void* lds_dummy) {
  int l = first + lane;
  const int last = (nbytes >> 7) - 1;
  if (l > last) l = last;
  pf_line((const char*)base + (size_t)l * 128, lds_dummy);
}
#define STAMP(k, i)
template <int CTRL> DI float dpp_add(float v) {
  return v + __uint_as_float(__builtin_amdgcn_update_dpp(0u, __float_as_uint(v), CTRL, 0xf, 0xf, true));
}
DI float row16_sum(float v) {
  v = dpp_add<0xB1>(v); v = dpp_add<0x4E>(v); v = dpp_add<0x141>(v); v = dpp_add<0x140>(v);
  return v;
}
DI float wave_sum(float v) {
  v = row16_sum(v);
  auto a = __builtin_amdgcn_permlane16_swap(__float_as_uint(v), __float_as_uint(v), false, false);
  v = __uint_as_float(a[0]) + __uint_as_float(a[1]);
  auto b = __builtin_amdgcn_permlane32_swap(__float_as_uint(v), __float_as_uint(v), false, false);
  return __uint_as_float(b[0]) + __uint_as_float(b[1]);
}
DI s16x4 tr_read(const char* p) {
  return __builtin_amdgcn_ds_read_tr16_b64_v4i16((lds_s16x4_ptr)(p));
}

constexpr size_t WS_MOD = 0;
constexpr size_t WS_H1 = 16384;
constexpr size_t WS_QF = WS_H1 + (size_t)S * DM * 2;
constexpr size_t WS_KI = WS_QF + (size_t)S * HDM * 2;
constexpr size_t WS_VI = WS_KI + (size_t)S * HDM * 2;
constexpr size_t WS_CTX = WS_VI + (size_t)S * HDM * 2;
constexpr size_t WS_X1 = WS_CTX + (size_t)S * HDM * 2;
constexpr size_t WS_Z1 = WS_X1 + (size_t)S * DM * 2;
constexpr size_t WS_Z2 = WS_Z1 + (size_t)S * FH * 2;
constexpr size_t WS_ST = WS_Z2 + (size_t)S * FH * 2;
constexpr size_t WS_U = WS_ST + (size_t)8 * S * 2 * 4;
constexpr size_t WS_SLAB = WS_U + (size_t)S * 704 * 2;
constexpr size_t WS_FLAG = WS_SLAB + (size_t)96 * 4 * 33 * 1024;
constexpr int LDU = 704;

template <int BM, int BN, int KS, int NWM, int NWN>
struct Gemm {
  static constexpr int NW = NWM * NWN;
  static constexpr int WM = BM / NWM, WN = BN / NWN;
  static constexpr int MT = WM / 16, NT = WN / 16;
  static constexpr int BK = 64 * KS;
  static constexpr int A_BYTES = BM * BK * 2;
  static constexpr int B_BYTES = BK * BN * 2;
  static constexpr int STAGE_BYTES = A_BYTES + B_BYTES;
  static constexpr int NB = BN / 16;

  static DI void stageA(const u16* __restrict__ A, int lda, int m0, int k0, char* lds, int wave, int lane) {
    constexpr int NI = A_BYTES / 1024;
    const int rr = lane >> 3, ch = (lane & 7) ^ rr;
#pragma unroll
    for (int ii = 0; ii < NI / NW; ++ii) {
      const int i = ii * NW + wave;
      const int sub = i / (BM / 8), j = i % (BM / 8);
      const u16* src = A + (size_t)(m0 + j * 8 + rr) * lda + k0 + sub * 64 + ch * 8;
      glds16(src, lds + i * 1024);
    }
  }
  static DI void stageB(const u16* __restrict__ B, int ldb, int k0, int n0, int nmax, char* lds, int wave, int lane) {
    constexpr int NI = B_BYTES / 1024;
    const int pos = lane & 15, rw = pos >> 1, hf = pos & 1;
#pragma unroll
    for (int ii = 0; ii < NI / NW; ++ii) {
      const int i = ii * NW + wave;
      const int blk = i * 4 + (lane >> 4);
      const int kb = blk / NB, nb = blk % NB;
      const int row = rw ^ ((kb & 1) << 2);
      int n = n0 + nb * 16 + hf * 8;
      if (n > nmax) n = nmax;
      const u16* src = B + (size_t)(k0 + kb * 8 + row) * ldb + n;
      glds16(src, lds + i * 1024);
    }
  }
  template <bool SWAP>
  static DI void compute(const char* ldsA, const char* ldsB, f32x4 (&acc)[MT][NT], int wm, int wn, int lane) {
    const int fr = lane & 15, fg = lane >> 4;
    const int q = fr >> 2, p = lane & 3;
#pragma unroll
    for (int sub = 0; sub < KS; ++sub) {
#pragma unroll
      for (int kk = 0; kk < 2; ++kk) {
        bf16x8 af[MT], bfr[NT];
#pragma unroll
        for (int mt = 0; mt < MT; ++mt) {
          const int row = wm * WM + mt * 16 + fr;
          af[mt] = *reinterpret_cast<const bf16x8*>(ldsA + sub * (BM * 128) + row * 128 + (((kk * 4 + fg) ^ (row & 7)) << 4));
        }
#pragma unroll
        for (int nt = 0; nt < NT; ++nt) {
          const int kb = sub * 8 + kk * 4 + fg, nb = wn * NT + nt;
          const char* base = ldsB + (kb * NB + nb) * 256 + p * 8;
          const int sw = (fg & 1) << 2;
          s16x4 lo = tr_read(base + ((q) ^ sw) * 32);
          s16x4 hi = tr_read(base + ((4 + q) ^ sw) * 32);
          bfr[nt] = __builtin_shufflevector(lo, hi, 0, 1, 2, 3, 4, 5, 6, 7);
        }
#pragma unroll
        for (int mt = 0; mt < MT; ++mt)
#pragma unroll
          for (int nt = 0; nt < NT; ++nt) {
            if constexpr (SWAP)
              acc[mt][nt] = __builtin_amdgcn_mfma_f32_16x16x32_bf16(bfr[nt], af[mt], acc[mt][nt], 0, 0, 0);
            else
              acc[mt][nt] = __builtin_amdgcn_mfma_f32_16x16x32_bf16(af[mt], bfr[nt], acc[mt][nt], 0, 0, 0);
          }
      }
    }
  }
};

#define KV_PD 2
constexpr int KV_NS = 4, KV_STAGE = 32768;
template <bool isV>
DI void kv_body(char* smem, const u16* __restrict__ ckv, const u16* __restrict__ B, char* __restrict__ dstb,
                const u16* __restrict__ cvec, const u16* __restrict__ wmod, float* __restrict__ mod,
                const u16* __restrict__ x, char* __restrict__ xf, const u16* __restrict__ wq) {
  const int b = blockIdx.x, xcd = b & 7, j = b >> 3;
  const int ntile = xcd * 4 + (j & 3), mtile = j >> 2;
  const int tid = threadIdx.x, wave = __builtin_amdgcn_readfirstlane(tid >> 6), lane = tid & 63;
  const int wm = wave >> 2, wn = wave & 3, r = lane & 31, h = lane >> 5;
  const int m0 = mtile * 256, n0 = (ntile & 15) * 256;
  float* modred = reinterpret_cast<float*>(smem + KV_NS * KV_STAGE);
  STAMP(0, 0);

  const u16* asrc[2];
  const u16* bsrc[2];
#pragma unroll
  for (int ii = 0; ii < 2; ++ii) {
    const int i = ii * 8 + wave;
    const int row = 16 * i + (lane >> 2), ch = (lane & 3) ^ ((lane >> 4) & 3);
    asrc[ii] = ckv + (size_t)(m0 + row) * DM + ch * 8;
    const int blk = i * 4 + (lane >> 4), kb = blk >> 4, nb = blk & 15;
    const int pos = lane & 15, rw = (pos >> 1) ^ ((nb & 1) << 2), hf = pos & 1;
    bsrc[ii] = B + (size_t)(kb * 8 + rw) * HDM + n0 + nb * 16 + hf * 8;
  }
  auto stage = [&](int kt0, int slot) {
    char* dst = smem + slot * KV_STAGE;
    const int kt = (kt0 + 2 * mtile) & 15;
#pragma unroll
    for (int ii = 0; ii < 2; ++ii) {
      glds16(asrc[ii] + kt * 32, dst + (ii * 8 + wave) * 1024);
      glds16(bsrc[ii] + (size_t)kt * 32 * HDM, dst + 16384 + (ii * 8 + wave) * 1024);
    }
  };
  constexpr int NK = DM / 32;
#pragma unroll
  for (int st = 0; st < KV_PD; ++st) stage(st, st);

  if (b < 192) {
    const u16* p = wmod + (size_t)tid * 3072 + b * 16;
    const u32x4 w0 = *reinterpret_cast<const u32x4*>(p), w1 = *reinterpret_cast<const u32x4*>(p + 8);
    const float c = bf2f(cvec[tid]);
    const float sv = rbf(c / (1.f + __expf(-c)));
    float v[16];
#pragma unroll
    for (int jj = 0; jj < 4; ++jj) {
      v[2 * jj] = sv * bflo(w0[jj]); v[2 * jj + 1] = sv * bfhi(w0[jj]);
      v[8 + 2 * jj] = sv * bflo(w1[jj]); v[8 + 2 * jj + 1] = sv * bfhi(w1[jj]);
    }
#pragma unroll
    for (int st = 0; st < 4; ++st) {
      const int half = 8 >> st, m = 32 >> st;
      const bool up = lane & m;
#pragma unroll
      for (int e = 0; e < half; ++e) {
        const float send = up ? v[e] : v[half + e];
        const float keep = up ? v[half + e] : v[e];
        v[e] = keep + __shfl_xor(send, m);
      }
    }
    v[0] += __shfl_xor(v[0], 2);
    v[0] += __shfl_xor(v[0], 1);
    if ((lane & 3) == 0) {
      const int col = ((lane >> 5) & 1) * 8 + ((lane >> 4) & 1) * 4 + ((lane >> 3) & 1) * 2 + ((lane >> 2) & 1);
      modred[wave * 16 + col] = v[0];
    }
  } else {
    const int row0 = (b - 192) * 32 + wave * 4;
    u32x4 xv[4];
#pragma unroll
    for (int i = 0; i < 4; ++i) xv[i] = *reinterpret_cast<const u32x4*>(x + (size_t)(row0 + i) * DM + lane * 8);
#pragma unroll
    for (int i = 0; i < 4; ++i) {
      float f[8];
#pragma unroll
      for (int jj = 0; jj < 4; ++jj) { f[2 * jj] = bflo(xv[i][jj]); f[2 * jj + 1] = bfhi(xv[i][jj]); }
      float sm = 0.f;
#pragma unroll
      for (int jj = 0; jj < 8; ++jj) sm += f[jj];
      const float mean = wave_sum(sm) * (1.f / DM);
      float vr = 0.f;
#pragma unroll
      for (int jj = 0; jj < 8; ++jj) { f[jj] -= mean; vr += f[jj] * f[jj]; }
      const float rstd = rsqrtf(wave_sum(vr) * (1.f / DM) + 1e-5f);
      u32x4 o;
#pragma unroll
      for (int jj = 0; jj < 4; ++jj) o[jj] = pack2(f[2 * jj] * rstd, f[2 * jj + 1] * rstd);
      const int row = row0 + i;
      *reinterpret_cast<u32x4*>(xf + ((size_t)(row >> 5) * 32 + (lane >> 1)) * 1024 + ((row & 31) + 32 * (lane & 1)) * 16) = o;
    }
  }

  STAMP(0, 1);
  f32x16 acc[4][2];
#pragma unroll
  for (int i = 0; i < 4; ++i)
#pragma unroll
    for (int jj = 0; jj < 2; ++jj)
#pragma unroll
      for (int e = 0; e < 16; ++e) acc[i][jj][e] = 0.f;
  const int G = lane >> 4, q4 = (lane & 15) >> 2, p4 = lane & 3;
  int aoff[2];
#pragma unroll
  for (int u = 0; u < 2; ++u) aoff[u] = (wm * 128 + r) * 64 + (((2 * u + h) ^ ((r >> 2) & 3)) << 4);
  const int nh = G & 1;
  const int boff = 16384 + (h * 16 + wn * 4 + nh) * 256 + p4 * 8;
  const int brow0 = (q4 ^ (nh << 2)) * 32, brow1 = ((4 + q4) ^ (nh << 2)) * 32;
  for (int kt = 0; kt < NK; ++kt) {
    if (kt + KV_PD - 1 <= NK - 1) wait_vm<(KV_PD - 1) * 4>(); else wait_vm<0>();
    barrier();
    const bool pf = kt + KV_PD < NK;
    const int ktn = (kt + KV_PD + 2 * mtile) & 15;
    char* dst = smem + ((kt + KV_PD) & (KV_NS - 1)) * KV_STAGE;
    const char* cur = smem + (kt & (KV_NS - 1)) * KV_STAGE;
    bf16x8 af[2][4], bfr[2][2];
#pragma unroll
    for (int u = 0; u < 2; ++u) {
#pragma unroll
      for (int nt = 0; nt < 2; ++nt) {
        const char* bb = cur + boff + u * (2 * 16 * 256) + nt * 512;
        s16x4 lo = tr_read(bb + brow0), hi = tr_read(bb + brow1);
        bfr[u][nt] = __builtin_shufflevector(lo, hi, 0, 1, 2, 3, 4, 5, 6, 7);
      }
#pragma unroll
      for (int mt = 0; mt < 4; ++mt) af[u][mt] = *reinterpret_cast<const bf16x8*>(cur + aoff[u] + mt * 2048);
    }
#pragma unroll
    for (int g4 = 0; g4 < 4; ++g4) {
      const int u = g4 >> 1;
      __builtin_amdgcn_sched_barrier(0);
      if (pf) {
        const int ii = g4 >> 1;
        if (g4 & 1) glds16(bsrc[ii] + (size_t)ktn * 32 * HDM, dst + 16384 + (ii * 8 + wave) * 1024);
        else glds16(asrc[ii] + ktn * 32, dst + (ii * 8 + wave) * 1024);
      }
      __builtin_amdgcn_sched_barrier(0);
#pragma unroll
      for (int m2 = 0; m2 < 2; ++m2)
#pragma unroll
        for (int nt = 0; nt < 2; ++nt) {
          const int mt = (g4 & 1) * 2 + m2;
          if constexpr (isV)
            acc[mt][nt] = __builtin_amdgcn_mfma_f32_32x32x16_bf16(af[u][mt], bfr[u][nt], acc[mt][nt], 0, 0, 0);
          else
            acc[mt][nt] = __builtin_amdgcn_mfma_f32_32x32x16_bf16(bfr[u][nt], af[u][mt], acc[mt][nt], 0, 0, 0);
        }
    }
  }
  STAMP(0, 2);
  if (wave < 2) pf_slice(wq, DM * HDM * 2, (b * 2 + wave) * 64, lane, smem + KV_NS * KV_STAGE + 512);
  {
    const int hd = (ntile & 15) * 2 + (wn >> 1);
    char* base = dstb + ((size_t)hd * 32 + mtile * 4 + wm * 2) * 16384 + lane * 16;
#pragma unroll
    for (int mt = 0; mt < 4; ++mt)
#pragma unroll
      for (int nt = 0; nt < 2; ++nt)
#pragma unroll
        for (int u = 0; u < 2; ++u) {
          u32x4 v;
#pragma unroll
          for (int e = 0; e < 4; ++e) v[e] = pack2(acc[mt][nt][8 * u + 2 * e], acc[mt][nt][8 * u + 2 * e + 1]);
          const int db = (wn & 1) * 2 + nt;
          int off;
          if constexpr (isV) off = (mt >> 1) * 16384 + ((mt & 1) * 2 + u) * 4096 + db * 1024;
          else off = (mt >> 1) * 16384 + (mt & 1) * 8192 + (db * 2 + u) * 1024;
          *reinterpret_cast<u32x4*>(base + off) = v;
        }
  }
  barrier();
  STAMP(0, 3);
  if (b < 192 && tid < 16) {
    float sum = 0.f;
#pragma unroll
    for (int i = 0; i < 8; ++i) sum += modred[i * 16 + tid];
    mod[b * 16 + tid] = rbf(sum);
  }
}
__global__ __launch_bounds__(512, 2) void k_kv(const u16* __restrict__ ckv, const u16* __restrict__ wk,
                                               const u16* __restrict__ wv, char* __restrict__ Ki,
                                               char* __restrict__ Vi, const u16* __restrict__ cvec,
                                               const u16* __restrict__ wmod, float* __restrict__ mod,
                                               const u16* __restrict__ x, char* __restrict__ xf,
                                               const u16* __restrict__ wq) {
  __shared__ __attribute__((aligned(16))) char smem[KV_NS * KV_STAGE + 512 + 256];
  if ((blockIdx.x & 7) >= 4) kv_body<true>(smem, ckv, wv, Vi, cvec, wmod, mod, x, xf, wq);
  else kv_body<false>(smem, ckv, wk, Ki, cvec, wmod, mod, x, xf, wq);
}

#define ACLOB_LO "a0","a1","a2","a3","a4","a5","a6","a7","a8","a9","a10","a11","a12","a13","a14","a15","a16","a17","a18","a19","a20","a21","a22","a23","a24","a25","a26","a27","a28","a29","a30","a31","a32","a33","a34","a35","a36","a37","a38","a39","a40","a41","a42","a43","a44","a45","a46","a47","a48","a49","a50","a51","a52","a53","a54","a55","a56","a57","a58","a59","a60","a61","a62","a63","a64","a65","a66","a67","a68","a69","a70","a71","a72","a73","a74","a75","a76","a77","a78","a79","a80","a81","a82","a83","a84","a85","a86","a87","a88","a89","a90","a91","a92","a93","a94","a95","a96","a97","a98","a99","a100","a101","a102","a103","a104","a105","a106","a107","a108","a109","a110","a111","a112","a113","a114","a115","a116","a117","a118","a119","a120","a121","a122","a123","a124","a125","a126","a127"
#define ACLOB_HI "a128","a129","a130","a131","a132","a133","a134","a135","a136","a137","a138","a139","a140","a141","a142","a143","a144","a145","a146","a147","a148","a149","a150","a151","a152","a153","a154","a155","a156","a157","a158","a159","a160","a161","a162","a163","a164","a165","a166","a167","a168","a169","a170","a171","a172","a173","a174","a175","a176","a177","a178","a179","a180","a181","a182","a183","a184","a185","a186","a187","a188","a189","a190","a191","a192","a193","a194","a195","a196","a197","a198","a199","a200","a201","a202","a203","a204","a205","a206","a207","a208","a209","a210","a211","a212","a213","a214","a215","a216","a217","a218","a219","a220","a221","a222","a223","a224","a225","a226","a227","a228","a229","a230","a231","a232","a233","a234","a235","a236","a237","a238","a239","a240","a241","a242","a243","a244","a245","a246","a247","a248","a249","a250","a251","a252","a253","a254","a255"
template <int... Is> using iseq = std::integer_sequence<int, Is...>;
template <int N> using make_iseq = std::make_integer_sequence<int, N>;
#define GAP() __builtin_amdgcn_sched_barrier(0)
constexpr float AT_THR = 8.f;
constexpr int AT_MISC = 131072;
DI float max3f(float a, float b, float c) { float r; asm("v_max3_f32 %0, %1, %2, %3" : "=v"(r) : "v"(a), "v"(b), "v"(c)); return r; }
DI float max2f(float a, float b) { float r; asm("v_max_f32_e32 %0, %1, %2" : "=v"(r) : "v"(a), "v"(b)); return r; }
DI float fadd_s(float a, float b) { float r; asm("v_add_f32_e32 %0, %1, %2" : "=v"(r) : "v"(a), "v"(b)); return r; }
DI float fmul_s(float a, float b) { float r; asm("v_mul_f32_e32 %0, %1, %2" : "=v"(r) : "v"(a), "v"(b)); return r; }
DI float fsub_s(float a, float b) { float r; asm("v_sub_f32_e32 %0, %1, %2" : "=v"(r) : "v"(a), "v"(b)); return r; }
DI unsigned lane_id() { unsigned l; asm volatile("v_mbcnt_lo_u32_b32 %0, -1, 0\n\tv_mbcnt_hi_u32_b32 %0, -1, %0" : "=v"(l)); return l; }
DI void negm_bcast(f32x16& negm, float neg_mhat) {
  const float aone = lane_id() < 32u ? 1.f : 0.f;
  asm volatile("s_nop 1\n\tv_mfma_f32_32x32x2_f32 %0, %1, %2, 0" : "+v"(negm) : "v"(aone), "v"(neg_mhat));
}
struct AC {
  unsigned rb, voff;
  unsigned mk, mv;
  const char *Kh, *Vh;
  const char *ka, *va;
  int kt, vt, end0;
  int dd;
  f32x16 S[2][2][2];
  f32x16 NEGM[2];
  bf16x8 Vf[4][4];
  bf16x8 Pk[2][4];
  float mhat[2], lsum[2], fct[2]; bool pend;
  float rs0[2], rs1[2], pmx[2][4], rmax[2];
};
constexpr int AO_(int db, int qb) { return 16 * (2 * db + qb); }
constexpr int AQ_(int qb, int d0) { return 128 + 32 * qb + 4 * d0; }
constexpr int AK_(int kb, int d0) { return 192 + 32 * kb + 4 * d0; }
template <int I> DI void v_read(AC& c) {
  asm volatile("ds_read_b128 %0, %1 offset:%2" : "=v"(c.Vf[I & 3][I >> 2]) : "v"(c.rb), "i"(32768 + I * 1024) : "memory");
}
template <int I> DI void k_read(unsigned rb) {
  asm volatile("ds_read_b128 a[%c1:%c2], %0 offset:%c3" ::"v"(rb), "i"(192 + 4 * I), "i"(195 + 4 * I), "i"(I * 1024) : "memory", ACLOB_HI);
}
template <int I> DI void q_load(const char* qp, unsigned voff) {
  asm volatile("global_load_dwordx4 a[%c2:%c3], %0, %1 offset:%c4" ::"v"(voff), "s"(qp + (I >> 2) * 4096), "i"(128 + 4 * I), "i"(131 + 4 * I), "i"((I & 3) * 1024) : "memory", ACLOB_HI);
}
template <int I> DI void dma(const char* src, unsigned voff, unsigned lds) {
  asm volatile("s_mov_b32 m0, %2\n\ts_nop 0\n\tglobal_load_lds_dwordx4 %0, %1 offset:%3" ::"v"(voff), "s"(src + (I >> 2) * 4096), "s"(lds + (I >> 2) * 4096), "n"((I & 3) * 1024) : "memory");
}
template <int... Is> DI void dmas(const char* src, unsigned voff, unsigned lds, iseq<Is...>) { (dma<Is>(src, voff, lds), ...); }
template <int X, bool FIRST, int M> DI void qk_mfma(AC& c) {
  constexpr int d0 = M >> 2, kb = (M >> 1) & 1, qb = M & 1, ak = AK_(kb, d0), aq = AQ_(qb, d0);
  if constexpr (d0 == 0) {
    if constexpr (FIRST) asm volatile("v_mfma_f32_32x32x16_bf16 %0, a[%c1:%c2], a[%c3:%c4], 0" : "=&v"(c.S[X][kb][qb]) : "i"(ak), "i"(ak + 3), "i"(aq), "i"(aq + 3) : ACLOB_LO, ACLOB_HI);
    else asm volatile("v_mfma_f32_32x32x16_bf16 %0, a[%c2:%c3], a[%c4:%c5], %1" : "=&v"(c.S[X][kb][qb]) : "v"(c.NEGM[qb]), "i"(ak), "i"(ak + 3), "i"(aq), "i"(aq + 3) : ACLOB_LO, ACLOB_HI);
  } else asm volatile("v_mfma_f32_32x32x16_bf16 %0, a[%c1:%c2], a[%c3:%c4], %0" : "+v"(c.S[X][kb][qb]) : "i"(ak), "i"(ak + 3), "i"(aq), "i"(aq + 3) : ACLOB_LO, ACLOB_HI);
}
template <int M> DI void pv_mfma(AC& c) {
  constexpr int ks = M >> 3, db = (M >> 1) & 3, qb = M & 1, ao = AO_(db, qb);
  asm volatile("v_mfma_f32_32x32x16_bf16 a[%c2:%c3], %0, %1, a[%c2:%c3]" ::"v"(c.Vf[db][ks]), "v"(c.Pk[qb][ks]), "i"(ao), "i"(ao + 15) : ACLOB_LO);
}
template <int M, int P, int R> DI void pv_mfma_ld(AC& c) {
  constexpr int ks = M >> 3, db = (M >> 1) & 3, qb = M & 1, ao = AO_(db, qb), I = P & 7;
  const char* src = (P < 8 ? c.ka : c.va) + (I >> 2) * 4096;
  const unsigned dst = (P < 8 ? c.mk : c.mv) + (I >> 2) * 4096;
  if constexpr (P >= 0 && R >= 0)
    asm volatile("s_mov_b32 m0, %4\n\tv_mfma_f32_32x32x16_bf16 a[%c6:%c7], %0, %1, a[%c6:%c7]\n\tglobal_load_lds_dwordx4 %2, %3 offset:%c8\n\tds_read_b128 a[%c9:%c10], %5 offset:%c11"
                 ::"v"(c.Vf[db][ks]), "v"(c.Pk[qb][ks]), "v"(c.voff), "s"(src), "s"(dst), "v"(c.rb), "i"(ao), "i"(ao + 15), "i"((I & 3) * 1024),
                   "i"(192 + 4 * R), "i"(195 + 4 * R), "i"(R * 1024) : "memory", ACLOB_LO, ACLOB_HI);
  else if constexpr (P >= 0)
    asm volatile("s_mov_b32 m0, %4\n\tv_mfma_f32_32x32x16_bf16 a[%c5:%c6], %0, %1, a[%c5:%c6]\n\tglobal_load_lds_dwordx4 %2, %3 offset:%c7"
                 ::"v"(c.Vf[db][ks]), "v"(c.Pk[qb][ks]), "v"(c.voff), "s"(src), "s"(dst), "i"(ao), "i"(ao + 15), "i"((I & 3) * 1024) : "memory", ACLOB_LO);
  else if constexpr (R >= 0)
    asm volatile("v_mfma_f32_32x32x16_bf16 a[%c3:%c4], %0, %1, a[%c3:%c4]\n\tds_read_b128 a[%c5:%c6], %2 offset:%c7"
                 ::"v"(c.Vf[db][ks]), "v"(c.Pk[qb][ks]), "v"(c.rb), "i"(ao), "i"(ao + 15),
                   "i"(192 + 4 * R), "i"(195 + 4 * R), "i"(R * 1024) : "memory", ACLOB_LO, ACLOB_HI);
  else pv_mfma<M>(c);
}
template <int I> DI void o_zero4() {
  asm volatile("v_accvgpr_write_b32 a[%c0], 0\n\tv_accvgpr_write_b32 a[%c1], 0\n\tv_accvgpr_write_b32 a[%c2], 0\n\tv_accvgpr_write_b32 a[%c3], 0"
               ::"i"(4 * I), "i"(4 * I + 1), "i"(4 * I + 2), "i"(4 * I + 3) : ACLOB_LO);
}
template <int X, int QB, int KS, int P> DI void pack1(AC& c) {
  u32x4 w = __builtin_bit_cast(u32x4, c.Pk[QB][KS]);
  unsigned d = pack2(c.S[X][KS >> 1][QB][8 * (KS & 1) + 2 * P], c.S[X][KS >> 1][QB][8 * (KS & 1) + 2 * P + 1]);
  asm volatile("" : "+v"(d));
  w[P] = d;
  c.Pk[QB][KS] = __builtin_bit_cast(bf16x8, w);
}
template <int X, int J> DI void rowsum1(AC& c) {
  constexpr int qb = J >> 4, ks = (J >> 2) & 3, p = J & 3;
  c.rs0[qb] = fadd_s(c.rs0[qb], c.S[X][ks >> 1][qb][8 * (ks & 1) + 2 * p]);
  c.rs1[qb] = fadd_s(c.rs1[qb], c.S[X][ks >> 1][qb][8 * (ks & 1) + 2 * p + 1]);
}
DI void lsum_merge(AC& c) {
#pragma unroll
  for (int qb = 0; qb < 2; ++qb) {
    const float rs = fadd_s(c.rs0[qb], c.rs1[qb]);
    auto pr = __builtin_amdgcn_permlane32_swap(__float_as_uint(rs), __float_as_uint(rs), false, false);
    c.lsum[qb] = fadd_s(c.lsum[qb], fadd_s(__uint_as_float(pr[0]), __uint_as_float(pr[1])));
  }
  asm volatile("" : "+v"(c.lsum[0]), "+v"(c.lsum[1]));
}
#define AT_VF "+v"(c.Vf[0][0]), "+v"(c.Vf[0][1]), "+v"(c.Vf[0][2]), "+v"(c.Vf[0][3]), "+v"(c.Vf[1][0]), "+v"(c.Vf[1][1]), "+v"(c.Vf[1][2]), "+v"(c.Vf[1][3]), \
              "+v"(c.Vf[2][0]), "+v"(c.Vf[2][1]), "+v"(c.Vf[2][2]), "+v"(c.Vf[2][3]), "+v"(c.Vf[3][0]), "+v"(c.Vf[3][1]), "+v"(c.Vf[3][2]), "+v"(c.Vf[3][3])
template <int X, bool FIRST, int M> DI void gapA(AC& c) {
  constexpr int Xp = X ^ 1;
  qk_mfma<X, FIRST, M>(c);
  GAP();
  if constexpr (M == 26) {
    c.ka = c.Kh + (size_t)(unsigned)c.kt * 32768; asm volatile("" : "+s"(c.ka));
    c.kt = c.kt + 1 == c.end0 ? 0 : c.kt + 1; asm volatile("" : "+s"(c.kt));
  }
  if constexpr (M == 28) {
    c.va = c.Vh + (size_t)(unsigned)c.vt * 32768; asm volatile("" : "+s"(c.va));
    c.vt = c.vt + 1 == c.end0 ? 0 : c.vt + 1; asm volatile("" : "+s"(c.vt));
  }
  if constexpr (FIRST) o_zero4<M>();
  if constexpr (!FIRST && M == 8) asm volatile("" ::"v"(c.NEGM[0]), "v"(c.NEGM[1]));
  if constexpr (!FIRST) {
    if constexpr (M >= 10 && M < 26) v_read<M - 10>(c);
    if constexpr (M < 16) {
      constexpr int e0 = 2 * M, e1 = 2 * M + 1;
      c.S[Xp][1][e0 >> 4][e0 & 15] = __builtin_amdgcn_exp2f(c.S[Xp][1][e0 >> 4][e0 & 15]);
      c.S[Xp][1][e1 >> 4][e1 & 15] = __builtin_amdgcn_exp2f(c.S[Xp][1][e1 >> 4][e1 & 15]);
      pack1<Xp, (M >> 3), ((M >> 2) & 1), (M & 3)>(c);
    } else {
      constexpr int cc = M - 16;
      pack1<Xp, (cc >> 3), 2 + ((cc >> 2) & 1), (cc & 3)>(c);
      rowsum1<Xp, 16 * (cc >> 3) + 8 + 4 * ((cc >> 2) & 1) + (cc & 3)>(c);
    }
  }
  GAP();
}
template <int X, bool FIRST, int... Ms> DI void phaseA_(AC& c, bool pf, iseq<Ms...>) {
  (gapA<X, FIRST, Ms>(c), ...);
  if constexpr (FIRST) {
    if (pf) asm volatile("s_waitcnt vmcnt(2)" ::: "memory");
    else asm volatile("s_waitcnt vmcnt(0)" ::: "memory");
    asm volatile("s_waitcnt lgkmcnt(0)\n\ts_barrier" ::: "memory");
  } else {
    lsum_merge(c);
    asm volatile("s_waitcnt vmcnt(0) lgkmcnt(0)\n\ts_barrier" : AT_VF::"memory");
  }
  GAP();
}
template <int X, bool FIRST> DI void phaseA(AC& c, bool pf = false) { phaseA_<X, FIRST>(c, pf, make_iseq<32>{}); }

template <int KK> DI void mask1(float& x, int lim, float ninf) {
  asm volatile("v_cmp_gt_i32_e32 vcc, %c3, %1\n\tv_cndmask_b32_e32 %0, %0, %2, vcc" : "+v"(x) : "v"(lim), "v"(ninf), "i"(KK) : "vcc");
}
template <int X, int... Es> DI void mask_all(AC& c, int lim, float ninf, iseq<Es...>) {
  (mask1<32 * ((Es >> 4) & 1) + (Es & 3) + 8 * ((Es >> 2) & 3) - 32 * (Es >> 5)>(reinterpret_cast<float*>(&c.S[X][(Es >> 4) & 1][Es >> 5])[Es & 15], lim, ninf), ...);
}
template <int X> DI void at_mask(AC& c) {
  if (c.dd >= 0) {
    const unsigned l = lane_id();
    const int lim = (int)(l & 31u) - (int)((l >> 5) << 2) - 64 * c.dd;
    mask_all<X>(c, lim, -__builtin_inff(), make_iseq<64>{});
  }
}
template <int X, bool PEEL, int M> DI void gapB(AC& c) {
  if constexpr (PEEL) {
    if constexpr (M < 8) dma<M>(c.ka, c.voff, c.mk);
    else if constexpr (M < 16) dma<M - 8>(c.va, c.voff, c.mv);
    if constexpr (M < 16) k_read<M>(c.rb);
  } else {
    pv_mfma_ld<M, (M & 1) ? -1 : M / 2, M < 16 ? M : -1>(c);
  }
  GAP();
  if constexpr (M == 4) at_mask<X>(c);
  if constexpr (M >= 4 && M < 15) {
    constexpr int s0 = 3 * (M - 4);
#pragma unroll
    for (int s = s0; s < s0 + 3; ++s) {
      if (s < 32) {
        const int q = s >> 4, i = s & 15, ch = i & 3, kb = ch >> 1, e = (ch & 1) * 8 + (i >> 2) * 2;
        if (i < 4) c.pmx[q][ch] = max2f(c.S[X][kb][q][e], c.S[X][kb][q][e + 1]);
        else c.pmx[q][ch] = max3f(c.pmx[q][ch], c.S[X][kb][q][e], c.S[X][kb][q][e + 1]);
      }
    }
  }
  if constexpr (M == 15 || M == 16) {
    constexpr int q = M - 15;
    const float mm = max2f(max3f(c.pmx[q][0], c.pmx[q][1], c.pmx[q][2]), c.pmx[q][3]);
    auto pr = __builtin_amdgcn_permlane32_swap(__float_as_uint(mm), __float_as_uint(mm), false, false);
    c.rmax[q] = max2f(__uint_as_float(pr[0]), __uint_as_float(pr[1]));
  }
  if constexpr (M == 17) {
    if constexpr (PEEL) {
#pragma unroll
      for (int q = 0; q < 2; ++q) {
        c.mhat[q] = max2f(c.rmax[q], -1e30f); c.lsum[q] = 0.f; c.fct[q] = 1.f;
#pragma unroll
        for (int kb = 0; kb < 2; ++kb)
#pragma unroll
          for (int e = 0; e < 16; ++e) c.S[X][kb][q][e] = fsub_s(c.S[X][kb][q][e], c.mhat[q]);
        negm_bcast(c.NEGM[q], -c.mhat[q]);
      }
    } else {
      const bool grew = max2f(c.rmax[0], c.rmax[1]) > AT_THR;
      if (__builtin_expect(__any((int)grew), 0)) {
#pragma unroll
        for (int q = 0; q < 2; ++q) {
          const float dl = max2f(c.rmax[q], 0.f);
          c.mhat[q] = fadd_s(c.mhat[q], dl); c.fct[q] = __builtin_amdgcn_exp2f(-dl);
#pragma unroll
          for (int kb = 0; kb < 2; ++kb)
#pragma unroll
            for (int e = 0; e < 16; ++e) c.S[X][kb][q][e] = fsub_s(c.S[X][kb][q][e], dl);
          negm_bcast(c.NEGM[q], -c.mhat[q]);
        }
        c.pend = true;
      }
    }
    c.rs0[0] = c.rs0[1] = c.rs1[0] = c.rs1[1] = 0.f;
  }
  if constexpr (M >= 18) {
    constexpr int g = M - 18, e_lo = (32 * g) / 14, e_hi = (32 * (g + 1)) / 14;
    constexpr int p_lo = g ? (32 * (g - 1)) / 14 : 0, p_hi = g ? (32 * g) / 14 : 0;
#pragma unroll
    for (int e = e_lo; e < e_hi; ++e) c.S[X][0][e >> 4][e & 15] = __builtin_amdgcn_exp2f(c.S[X][0][e >> 4][e & 15]);
#pragma unroll
    for (int e = p_lo; e < p_hi; ++e) {
      if (e & 1) c.rs1[e >> 4] = fadd_s(c.rs1[e >> 4], c.S[X][0][e >> 4][e & 15]);
      else c.rs0[e >> 4] = fadd_s(c.rs0[e >> 4], c.S[X][0][e >> 4][e & 15]);
    }
  }
  if constexpr (M == 31) asm volatile("" ::"v"(c.S[X][0][0]), "v"(c.S[X][0][1]), "v"(c.NEGM[0]), "v"(c.NEGM[1]), "v"(c.rs0[0]), "v"(c.rs0[1]), "v"(c.rs1[0]), "v"(c.rs1[1]));
  GAP();
}
template <int I> DI void o_scale1(float f) {
  float t;
  asm volatile("v_accvgpr_read_b32 %0, a[%c2]\n\tv_mul_f32 %0, %1, %0\n\tv_accvgpr_write_b32 a[%c2], %0" : "=&v"(t) : "v"(f), "i"(I) : ACLOB_LO);
}
template <int... Is> DI void o_scale(const AC& c, iseq<Is...>) { (o_scale1<Is>(c.fct[(Is >> 4) & 1]), ...); }
template <int X, bool PEEL, int... Ms> DI void phaseB_(AC& c, iseq<Ms...>) {
  (gapB<X, PEEL, Ms>(c), ...);
#pragma unroll
  for (int e = (32 * 13) / 14; e < 32; ++e) {
    if (e & 1) c.rs1[e >> 4] = fadd_s(c.rs1[e >> 4], c.S[X][0][e >> 4][e & 15]);
    else c.rs0[e >> 4] = fadd_s(c.rs0[e >> 4], c.S[X][0][e >> 4][e & 15]);
  }
  if constexpr (!PEEL) {
    if (__builtin_expect(c.pend, 0)) {
      asm volatile("s_nop 7\n\ts_nop 7" ::: "memory");
      o_scale(c, make_iseq<128>{});
      asm volatile("s_nop 1" ::: "memory");
      c.lsum[0] = fmul_s(c.lsum[0], c.fct[0]); c.lsum[1] = fmul_s(c.lsum[1], c.fct[1]); c.pend = false;
    }
  }
  c.dd += 2;
  c.mk ^= 65536u; c.mv ^= 65536u;
  asm volatile("s_waitcnt lgkmcnt(0)\n\tv_xor_b32_e32 %0, 0x10000, %0" : "+v"(c.rb)::"memory");
  GAP();
}
template <int X, bool PEEL> DI void phaseB(AC& c) { phaseB_<X, PEEL>(c, make_iseq<32>{}); }
template <int... Is> DI void k_reads(unsigned rb, iseq<Is...>) { (k_read<Is>(rb), ...); }
template <int... Is> DI void v_reads(AC& c, iseq<Is...>) { (v_read<Is>(c), ...); }
template <int... Is> DI void q_loads(const char* qp, unsigned voff, iseq<Is...>) { (q_load<Is>(qp, voff), ...); }
template <int X, int... Cs> DI void packs(AC& c, iseq<Cs...>) { (pack1<X, ((Cs >> 3) & 1), 2 * (Cs >> 4) + ((Cs >> 2) & 1), (Cs & 3)>(c), ...); }
template <int X, int... Js> DI void rowsums(AC& c, iseq<Js...>) { (rowsum1<X, Js>(c), ...); }
template <int M> DI void pv_drain1(AC& c, const char* qn) {
  pv_mfma<M>(c);
  if constexpr (M & 1) q_load<(M >> 1)>(qn, c.voff);
  GAP();
}
template <int... Ms> DI void pv_drain(AC& c, const char* qn, iseq<Ms...>) { (pv_drain1<Ms>(c, qn), ...); }
template <int X> DI f32x4 at_finish(AC& c, char* mlp, int wave, const char* qn) {
  asm volatile("" : "+v"(c.S[X][0][0]), "+v"(c.S[X][0][1]), "+v"(c.S[X][1][0]), "+v"(c.S[X][1][1]));
  v_reads(c, make_iseq<16>{});
#pragma unroll
  for (int e = 0; e < 32; ++e) c.S[X][1][e >> 4][e & 15] = __builtin_amdgcn_exp2f(c.S[X][1][e >> 4][e & 15]);
  packs<X>(c, make_iseq<32>{});
  rowsums<X>(c, iseq<8, 9, 10, 11, 12, 13, 14, 15, 24, 25, 26, 27, 28, 29, 30, 31>{});
  lsum_merge(c);
  *reinterpret_cast<f32x4*>(mlp + wave * 1024 + c.voff) = f32x4{c.mhat[0], c.lsum[0], c.mhat[1], c.lsum[1]};
  asm volatile("s_waitcnt lgkmcnt(0)\n\ts_barrier" : AT_VF::"memory");
  const f32x4 ml = *reinterpret_cast<const f32x4*>(mlp + (wave ^ 2) * 1024 + c.voff);
  GAP();
  pv_drain(c, qn, make_iseq<32>{});
  return ml;
}
template <int I> DI float o_read() { float r; asm volatile("v_accvgpr_read_b32 %0, a[%c1]" : "=v"(r) : "i"(I)); return r; }
template <int Q> DI f32x4 o_quad(float f) {
  return f32x4{fmul_s(o_read<4 * Q>(), f), fmul_s(o_read<4 * Q + 1>(), f), fmul_s(o_read<4 * Q + 2>(), f), fmul_s(o_read<4 * Q + 3>(), f)};
}
template <int Q0, int... Is> DI void o_send(char* dst, const float (&f)[2], iseq<Is...>) {
  ((*reinterpret_cast<f32x4*>(dst + Is * 1024) = o_quad<Q0 + Is>(f[(Is >> 2) & 1])), ...);
}
template <int Q0, int I> DI void o_fin1(char* crow, const char* src, const float (&f)[2]) {
  constexpr int Q = Q0 + I, db = Q >> 3, qb = (Q >> 2) & 1, qd = Q & 3;
  const f32x4 in = *reinterpret_cast<const f32x4*>(src + I * 1024), own = o_quad<Q>(f[qb]);
  u32x2 w;
  w[0] = pack2(fadd_s(own[0], in[0]), fadd_s(own[1], in[1]));
  w[1] = pack2(fadd_s(own[2], in[2]), fadd_s(own[3], in[3]));
  *reinterpret_cast<u32x2*>(crow + qb * (2 * 128 * 1024) + db * 1024 + qd * 256) = w;
}
template <int Q0, int... Is> DI void o_fin(char* crow, const char* src, const float (&f)[2], iseq<Is...>) { (o_fin1<Q0, Is>(crow, src, f), ...); }

constexpr float QSCALE = 0.08838834764831845f * 1.4426950408889634f;
template <int B> DI void qp_mfma(const bf16x8& w, const bf16x8& h) {
  asm volatile("v_mfma_f32_32x32x16_bf16 a[%c2:%c3], %0, %1, a[%c2:%c3]" ::"v"(w), "v"(h), "i"(16 * B), "i"(16 * B + 15) : ACLOB_LO);
}
template <int I> DI void x_load(u32x4& r, const char* p, unsigned voff) {
  asm volatile("global_load_dwordx4 %0, %1, %2 offset:%c3" : "=v"(r) : "v"(voff), "s"(p + (I >> 3) * 32768 + ((I >> 2) & 1) * 4096), "i"((I & 3) * 1024) : "memory");
}
template <int... Is> DI void x_loads(u32x4 (&x)[16], const char* p, unsigned voff, iseq<Is...>) { (x_load<Is>(x[Is], p, voff), ...); }
#define AT_X(o) "+v"(o[0]), "+v"(o[1]), "+v"(o[2]), "+v"(o[3]), "+v"(o[4]), "+v"(o[5]), "+v"(o[6]), "+v"(o[7]), \
                "+v"(o[8]), "+v"(o[9]), "+v"(o[10]), "+v"(o[11]), "+v"(o[12]), "+v"(o[13]), "+v"(o[14]), "+v"(o[15])
DI void w_stage(const char* src, unsigned voffw, unsigned lds) {
#pragma unroll
  for (int ii = 0; ii < 8; ++ii) dma<0>(src + (ii >> 1) * 65536 + (ii & 1) * 128, voffw, lds + ii * 1024);
}
struct QpFrag { bf16x8 w[4], h[2]; };
DI void qp_fetch(QpFrag& f, const char* wb0, const char* wb1, const char* tab, const u32x4 (&x)[16], int s) {
#pragma unroll
  for (int db = 0; db < 4; ++db) {
    const s16x4 lo = tr_read(wb0 + s * 4096 + db * 512), hi = tr_read(wb1 + s * 4096 + db * 512);
    f.w[db] = __builtin_shufflevector(lo, hi, 0, 1, 2, 3, 4, 5, 6, 7);
  }
  f32x4 t[4];
#pragma unroll
  for (int e = 0; e < 4; ++e) t[e] = *reinterpret_cast<const f32x4*>(tab + s * 128 + e * 16);
#pragma unroll
  for (int qb = 0; qb < 2; ++qb) {
    u32x4 o;
#pragma unroll
    for (int e = 0; e < 4; ++e) {
      const unsigned v = x[8 * qb + s][e];
      o[e] = pack2(fmaf(bflo(v), t[e >> 1][(e & 1) * 2], t[2 + (e >> 1)][(e & 1) * 2]),
                   fmaf(bfhi(v), t[e >> 1][(e & 1) * 2 + 1], t[2 + (e >> 1)][(e & 1) * 2 + 1]));
    }
    f.h[qb] = __builtin_bit_cast(bf16x8, o);
  }
}
DI void qp_quarter(const char* wb0, const char* wb1, const char* tab, const u32x4 (&x)[16]) {
  QpFrag f[2];
  qp_fetch(f[0], wb0, wb1, tab, x, 0);
#pragma unroll
  for (int s = 0; s < 8; ++s) {
    if (s + 1 < 8) qp_fetch(f[(s + 1) & 1], wb0, wb1, tab, x, s + 1);
    const QpFrag& g = f[s & 1];
    asm volatile("s_nop 1" ::"v"(g.h[0]), "v"(g.h[1]));
    qp_mfma<0>(g.w[0], g.h[0]); qp_mfma<1>(g.w[0], g.h[1]); qp_mfma<2>(g.w[1], g.h[0]); qp_mfma<3>(g.w[1], g.h[1]);
    qp_mfma<4>(g.w[2], g.h[0]); qp_mfma<5>(g.w[2], g.h[1]); qp_mfma<6>(g.w[3], g.h[0]); qp_mfma<7>(g.w[3], g.h[1]);
  }
}
template <int I> DI u32x4 q_pack() {
  constexpr int r = 16 * (2 * ((I & 7) >> 1) + (I >> 3)) + 8 * (I & 1);
  return u32x4{pack2(o_read<r>(), o_read<r + 1>()), pack2(o_read<r + 2>(), o_read<r + 3>()),
               pack2(o_read<r + 4>(), o_read<r + 5>()), pack2(o_read<r + 6>(), o_read<r + 7>())};
}
template <int I> DI void q_keep(char* xch) {
  const u32x4 w = q_pack<I>();
  *reinterpret_cast<u32x4*>(xch + I * 1024) = w;
  asm volatile("v_accvgpr_write_b32 a[%c4], %0\n\tv_accvgpr_write_b32 a[%c5], %1\n\tv_accvgpr_write_b32 a[%c6], %2\n\tv_accvgpr_write_b32 a[%c7], %3"
               ::"v"(w[0]), "v"(w[1]), "v"(w[2]), "v"(w[3]), "i"(128 + 4 * I), "i"(129 + 4 * I), "i"(130 + 4 * I), "i"(131 + 4 * I) : ACLOB_HI);
}
template <int... Is> DI void q_keeps(char* xch, iseq<Is...>) { (q_keep<Is>(xch), ...); }
template <int... Is> DI void q_saves(char* dst, iseq<Is...>) { ((*reinterpret_cast<u32x4*>(dst + Is * 1024) = q_pack<Is>()), ...); }
template <int I> DI void q_fetch(unsigned a) {
  asm volatile("ds_read_b128 a[%c1:%c2], %0 offset:%c3" ::"v"(a), "i"(128 + 4 * I), "i"(131 + 4 * I), "i"(I * 1024) : "memory", ACLOB_HI);
}
template <int... Is> DI void q_fetches(unsigned a, iseq<Is...>) { (q_fetch<Is>(a), ...); }
template <int... Is> DI void o_zeros(iseq<Is...>) { (o_zero4<Is>(), ...); }

__global__ __launch_bounds__(256, 1) void k_attn(const char* __restrict__ xf, const u16* __restrict__ wq,
                                                 const float* __restrict__ mod, char* __restrict__ Qf,
                                                 const char* __restrict__ Ki,
                                                 const char* __restrict__ Vi, char* __restrict__ ctx,
                                                 const u16* __restrict__ wo, const u16* __restrict__ win,
                                                 const u16* __restrict__ wsp, const u16* __restrict__ wout) {
  __shared__ __attribute__((aligned(16))) char smem[AT_MISC + 4096 + 256];
  const int b = blockIdx.x, xcd = b & 7, j = b >> 3;
  const int head = xcd * 4 + (j & 3), pair = j >> 2;
  const int wave = __builtin_amdgcn_readfirstlane(threadIdx.x >> 6), kg = wave >> 1, rg = wave & 1;
  AC c;
  const unsigned ldsb = lds_addr(smem);
  c.voff = (threadIdx.x & 63) * 16u;
  c.rb = ldsb + kg * 16384 + c.voff;
  c.mk = ldsb + wave * 8192;
  c.mv = ldsb + 32768 + wave * 8192;
  c.Kh = Ki + (size_t)head * (32 * 16384) + wave * 8192;
  c.Vh = Vi + (size_t)head * (32 * 16384) + wave * 8192;
  c.end0 = 16 - pair;
  char* Qh = Qf + (size_t)head * (64 * 8192) + rg * 16384;
  STAMP(5, 0);
  {
    const int lane = threadIdx.x & 63, G = lane >> 4, pos = lane & 15;
    const unsigned voffw = (unsigned)(((pos >> 1) ^ ((G & 1) << 2)) * 8192 + G * 32 + (pos & 1) * 16);
    const char* wsrc = reinterpret_cast<const char*>(wq) + head * 256 + wave * 262144;
    const char* xsrc = xf + (size_t)((kg ? pair : 15 - pair) * 4 + rg * 2) * 32768;
    const unsigned wl = ldsb + wave * 8192;
    constexpr int WB0 = 98304, WB1 = 32768, WB2 = 65536;
    f32x2 cav, sav;
    asm volatile("global_load_dwordx2 %0, %2, %3 offset:2048\n\tglobal_load_dwordx2 %1, %2, %3" : "=&v"(cav), "=&v"(sav) : "v"(threadIdx.x * 8u), "s"(mod) : "memory");
    u32x4 xa[16], xb[16];
    w_stage(wsrc, voffw, wl + WB0);
    x_loads(xa, xsrc, c.voff, make_iseq<16>{});
    w_stage(wsrc + 1048576, voffw, wl + WB1);
    x_loads(xb, xsrc + 8192, c.voff, make_iseq<16>{});
    asm volatile("s_waitcnt vmcnt(48)" : "+v"(cav), "+v"(sav)::"memory");
    w_stage(wsrc + 2 * 1048576, voffw, wl + WB2);
    dmas(c.Kh, c.voff, c.mk, make_iseq<8>{});
    {
      float* tp = reinterpret_cast<float*>(smem + AT_MISC) + (threadIdx.x >> 2) * 16 + (threadIdx.x & 3) * 2;
      *reinterpret_cast<f32x2*>(tp) = f32x2{(1.f + cav[0]) * QSCALE, (1.f + cav[1]) * QSCALE};
      *reinterpret_cast<f32x2*>(tp + 8) = f32x2{sav[0] * QSCALE, sav[1] * QSCALE};
    }
    o_zeros(make_iseq<32>{});
    const int nh = G & 1, h = G >> 1, q4 = pos >> 2, p4 = lane & 3;
    const char* wb0 = smem + (h * 8 + nh) * 256 + p4 * 8 + (q4 ^ (nh << 2)) * 32;
    const char* wb1 = smem + (h * 8 + nh) * 256 + p4 * 8 + ((4 + q4) ^ (nh << 2)) * 32;
    const char* tab = smem + AT_MISC + h * 64;
    asm volatile("s_waitcnt vmcnt(40) lgkmcnt(0)\n\ts_barrier" : AT_X(xa)::"memory");
    qp_quarter(wb0 + WB0, wb1 + WB0, tab, xa);
    asm volatile("s_waitcnt vmcnt(16) lgkmcnt(0)\n\ts_barrier" : AT_X(xb)::"memory");
    w_stage(wsrc + 3 * 1048576, voffw, wl + WB0);
    x_loads(xa, xsrc + 2 * 8192, c.voff, make_iseq<16>{});
    qp_quarter(wb0 + WB1, wb1 + WB1, tab + 1024, xb);
    asm volatile("s_waitcnt vmcnt(0) lgkmcnt(0)\n\ts_barrier" : AT_X(xa)::"memory");
    x_loads(xb, xsrc + 3 * 8192, c.voff, make_iseq<16>{});
    dmas(c.Vh, c.voff, c.mv, make_iseq<8>{});
    k_reads(c.rb, make_iseq<16>{});
    qp_quarter(wb0 + WB2, wb1 + WB2, tab + 2048, xa);
    asm volatile("s_waitcnt vmcnt(8)" : AT_X(xb)::"memory");
    qp_quarter(wb0 + WB0, wb1 + WB0, tab + 3072, xb);
    asm volatile("s_waitcnt lgkmcnt(0)\n\ts_barrier\n\ts_nop 7\n\ts_nop 7" ::: "memory");
    dmas(c.Kh + 32768, c.voff, c.mk + 65536, make_iseq<8>{});
    c.kt = 2; c.vt = 1;
    if (kg == 0) q_keeps(smem + WB0 + rg * 16384 + c.voff, make_iseq<16>{});
    else q_saves(Qh + pair * 32768 + c.voff, make_iseq<16>{});
    barrier();
    if (kg) q_fetches(ldsb + WB0 + rg * 16384 + c.voff, make_iseq<16>{});
    asm volatile("s_waitcnt lgkmcnt(0)\n\tv_xor_b32_e32 %0, 0x10000, %0" : "+v"(c.rb)::"memory");
    c.mv ^= 65536u;
  }
  for (int it = 0; it < 2; ++it) {
    const int qb = it ? pair : 15 - pair, T = qb + 1;
    c.dd = kg - 2 * qb - rg;
    c.pend = false;
    STAMP(5, it * 4 + 1);
    if (T & 1) {
      phaseA<1, true>(c, it);
      phaseB<1, true>(c);
    } else {
      phaseA<0, true>(c, it);
      phaseB<0, true>(c);
      phaseA<1, false>(c);
      phaseB<1, false>(c);
    }
    for (int t = (T - 1) >> 1; t > 0; --t) {
      if (t == 3) STAMP(4, 0);
      phaseA<0, false>(c);
      if (t == 3) STAMP(4, 1);
      phaseB<0, false>(c);
      if (t == 3) STAMP(4, 2);
      phaseA<1, false>(c);
      if (t == 3) STAMP(4, 3);
      phaseB<1, false>(c);
      if (t == 3) STAMP(4, 4);
    }
    const f32x4 ml = at_finish<1>(c, smem + AT_MISC, wave, Qh + pair * 32768);
    STAMP(5, it * 4 + 2);
    const unsigned ln = lane_id();
    float f[2];
#pragma unroll
    for (int q = 0; q < 2; ++q) {
      const float mt = fmaxf(c.mhat[q], ml[2 * q]);
      const float a0 = __builtin_amdgcn_exp2f(c.mhat[q] - mt), a1 = __builtin_amdgcn_exp2f(ml[2 * q] - mt);
      f[q] = a0 / fadd_s(fmul_s(c.lsum[q], a0), fmul_s(ml[2 * q + 1], a1));
    }
    asm volatile("s_nop 7\n\ts_nop 7" ::: "memory");
    const unsigned so = (c.rb - ldsb) & 65536u;
    char* a0p = smem + (so ^ 65536u) + rg * 16384 + ln * 16;
    char* a1p = smem + so + 32768 + rg * 16384 + ln * 16;
    char* crow = ctx + ((size_t)(qb * 8 + rg * 4 + ((ln >> 4) & 1)) * 128 + head * 4) * 1024 + (ln & 15) * 16 + (ln >> 5) * 8;
    if (kg == 0) o_send<16>(a0p, f, make_iseq<16>{});
    else o_send<0>(a1p, f, make_iseq<16>{});
    barrier();
    if (kg == 0) o_fin<0>(crow, a1p, f, make_iseq<16>{});
    else o_fin<16>(crow, a0p, f, make_iseq<16>{});
    asm volatile("s_waitcnt vmcnt(16)" ::: "memory");
    if (it == 0) {
#pragma unroll
      for (int u = 0; u < 2; ++u) {
        const int sl = (b * 4 + wave) * 2 + u;
        const u16* base = sl < 1024 ? wsp : sl < 1536 ? wo : sl < 1708 ? win : wout;
        const int nb = sl < 1024 ? S * S * 2 : sl < 1536 ? HDM * DM * 2 : sl < 1708 ? DM * FF * 2 : FH * DM * 2;
        const int first = sl < 1024 ? sl : sl < 1536 ? sl - 1024 : sl < 1708 ? sl - 1536 : sl - 1708;
        pf_slice(base, nb, first * 64, ln, smem + AT_MISC + 4096);
      }
      GAP();
    }
    STAMP(5, it * 4 + 3);
  }
  asm volatile("s_waitcnt vmcnt(0)" ::: "memory");
}

constexpr int SK_AREA = 16384;
template <int NT> struct SkOwn { static constexpr int N = (2 * NT + 3) / 4; };
template <int NT>
DI void splitk_reduce(const f32x4 (&acc)[4][NT], f32x4 (&own)[SkOwn<NT>::N], char* smem, int wave, int lane) {
  const int hi = wave >> 2, wq = wave & 3;
  char* mine = smem + wave * SK_AREA + lane * 16;
  f32x4 r[2 * NT];
#pragma unroll
  for (int f = 0; f < 2 * NT; ++f) {
    const f32x4 lo = acc[f / NT][f % NT], up = acc[2 + f / NT][f % NT];
    f32x4 snd, kp;
#pragma unroll
    for (int e = 0; e < 4; ++e) { snd[e] = hi ? lo[e] : up[e]; kp[e] = hi ? up[e] : lo[e]; }
    *reinterpret_cast<f32x4*>(mine + f * 1024) = snd;
    r[f] = kp;
  }
  barrier();
  {
    const char* part = smem + (wave ^ 4) * SK_AREA + lane * 16;
#pragma unroll
    for (int f = 0; f < 2 * NT; ++f) {
      const f32x4 o = *reinterpret_cast<const f32x4*>(part + f * 1024);
      r[f] += o;
    }
  }
  barrier();
#pragma unroll
  for (int f = 0; f < 2 * NT; ++f) *reinterpret_cast<f32x4*>(mine + f * 1024) = r[f];
  barrier();
#pragma unroll
  for (int i = 0; i < SkOwn<NT>::N; ++i) {
    const int f = wq + 4 * i;
    const char* p = smem + hi * 4 * SK_AREA + lane * 16 + (f < 2 * NT ? f : 0) * 1024;
    const f32x4 a = *reinterpret_cast<const f32x4*>(p), b = *reinterpret_cast<const f32x4*>(p + SK_AREA);
    const f32x4 c = *reinterpret_cast<const f32x4*>(p + 2 * SK_AREA), d = *reinterpret_cast<const f32x4*>(p + 3 * SK_AREA);
    own[i] = (a + b) + (c + d);
  }
}
DI void sk_bfrags(const char* img, bf16x8 (&bfr)[4], int lane) {
  const int fr = lane & 15, fg = lane >> 4, q = fr >> 2, p = lane & 3, sw = (fg & 1) << 2;
  const char* base = img + fg * 1024 + p * 8;
#pragma unroll
  for (int nt = 0; nt < 4; ++nt) {
    s16x4 lo = tr_read(base + nt * 256 + (q ^ sw) * 32);
    s16x4 hi = tr_read(base + nt * 256 + ((4 + q) ^ sw) * 32);
    bfr[nt] = __builtin_shufflevector(lo, hi, 0, 1, 2, 3, 4, 5, 6, 7);
  }
}

constexpr int OP_NS = 16, OP_DB = 4, OP_DA = 6;
constexpr int op_wait(int t) {
  int pos = 0, bpos = -1;
  for (int i = 0; i < OP_DA; ++i) {
    if (i < OP_DB) { pos += 4; if (i == t) bpos = pos; }
    pos += 4;
  }
  for (int s = 0; s < t; ++s) {
    if (s + OP_DB < OP_NS) { pos += 4; if (s + OP_DB == t) bpos = pos; }
    if (s + OP_DA < OP_NS) pos += 4;
  }
  return pos - bpos;
}
template <int T> struct OpWait { static constexpr int v = op_wait(T); };
template <int T> DI void op_step(f32x4 (&acc)[4][4], u32x4 (&areg)[OP_DA][4], char* ring, const char* asrc,
                                 const u16* bs0, const u16* bs1, int g0, int lane) {
  wait_vm<OpWait<T>::v>();
  if constexpr (T == 0) STAMP(2, 2);
  if constexpr (T == 8) STAMP(2, 3);
  bf16x8 bfr[4];
  sk_bfrags(ring + (T % OP_DB) * 4096, bfr, lane);
#pragma unroll
  for (int mt = 0; mt < 4; ++mt)
#pragma unroll
    for (int nt = 0; nt < 4; ++nt)
      acc[mt][nt] = __builtin_amdgcn_mfma_f32_16x16x32_bf16(bfr[nt], __builtin_bit_cast(bf16x8, areg[T % OP_DA][mt]), acc[mt][nt], 0, 0, 0);
  if constexpr (T + OP_DB < OP_NS) {
    asm volatile("s_waitcnt lgkmcnt(0)" ::: "memory");
    const int g = (g0 + T + OP_DB) & 127;
#pragma unroll
    for (int i = 0; i < 4; ++i)
      glds16(((i & 1) ? bs1 : bs0) + (size_t)(g * 32 + i * 8) * DM, ring + (T % OP_DB) * 4096 + i * 1024);
  }
  if constexpr (T + OP_DA < OP_NS) {
    const int g = (g0 + T + OP_DA) & 127;
#pragma unroll
    for (int mt = 0; mt < 4; ++mt)
      areg[T % OP_DA][mt] = *reinterpret_cast<const u32x4*>(asrc + (size_t)mt * (128 * 1024) + g * 1024);
  }
  if constexpr (T + 1 < OP_NS) op_step<T + 1>(acc, areg, ring, asrc, bs0, bs1, g0, lane);
}
__global__ __launch_bounds__(512, 2) void k_oproj(const char* __restrict__ ctxf, const u16* __restrict__ wo,
                                                  const u16* __restrict__ x, const float* __restrict__ ga,
                                                  u16* __restrict__ x1) {
  __shared__ __attribute__((aligned(16))) char smem[8 * SK_AREA + 256];
  const int b = blockIdx.x, xcd = b & 7, j = b >> 3;
  const int mtile = xcd * 4 + (j >> 3), ntile = j & 7;
  const int tid = threadIdx.x, wave = __builtin_amdgcn_readfirstlane(tid >> 6), lane = tid & 63;
  const int m0 = mtile * 64, n0 = ntile * 64;
  char* ring = smem + wave * SK_AREA;
  const char* asrc = ctxf + (size_t)(mtile * 4) * 128 * 1024 + lane * 16;
  const int g0 = wave * 16;
  const int rw = (lane & 15) >> 1;
  const u16* bs0 = wo + (size_t)rw * DM + n0 + (lane >> 4) * 16 + (lane & 1) * 8;
  const u16* bs1 = wo + (size_t)(rw ^ 4) * DM + n0 + (lane >> 4) * 16 + (lane & 1) * 8;
  f32x4 acc[4][4];
#pragma unroll
  for (int i = 0; i < 4; ++i)
#pragma unroll
    for (int jj = 0; jj < 4; ++jj) acc[i][jj] = f32x4{0.f, 0.f, 0.f, 0.f};
  u32x4 areg[OP_DA][4];
  STAMP(2, 0);
#pragma unroll
  for (int i = 0; i < OP_DA; ++i) {
    const int g = (g0 + i) & 127;
    if (i < OP_DB) {
#pragma unroll
      for (int ii = 0; ii < 4; ++ii) glds16(((ii & 1) ? bs1 : bs0) + (size_t)(g * 32 + ii * 8) * DM, ring + i * 4096 + ii * 1024);
    }
#pragma unroll
    for (int mt = 0; mt < 4; ++mt)
      areg[i][mt] = *reinterpret_cast<const u32x4*>(asrc + (size_t)mt * (128 * 1024) + g * 1024);
  }
  STAMP(2, 1);
  op_step<0>(acc, areg, ring, asrc, bs0, bs1, g0, lane);
  STAMP(2, 4);
  const int fr = lane & 15, fg = lane >> 4, hi = wave >> 2, wq = wave & 3;
  u32x2 xv[2];
  f32x4 gv;
  {
    gv = *reinterpret_cast<const f32x4*>(ga + n0 + wq * 16 + fg * 4);
#pragma unroll
    for (int i = 0; i < 2; ++i)
      xv[i] = *reinterpret_cast<const u32x2*>(x + (size_t)(m0 + (2 * hi + i) * 16 + fr) * DM + n0 + wq * 16 + fg * 4);
  }
  f32x4 own[2];
  splitk_reduce<4>(acc, own, smem, wave, lane);
#pragma unroll
  for (int i = 0; i < 2; ++i) {
    u32x2 w;
    w[0] = pack2(bflo(xv[i][0]) + gv[0] * own[i][0], bfhi(xv[i][0]) + gv[1] * own[i][1]);
    w[1] = pack2(bflo(xv[i][1]) + gv[2] * own[i][2], bfhi(xv[i][1]) + gv[3] * own[i][3]);
    *reinterpret_cast<u32x2*>(x1 + (size_t)(m0 + (2 * hi + i) * 16 + fr) * DM + n0 + wq * 16 + fg * 4) = w;
  }
  STAMP(2, 5);
}

constexpr int NBLK = 43;
using G4 = Gemm<64, 192, 1, 2, 4>;
DI float gelu_tanh(float x) {
  constexpr float K1 = -2.f * 0.7978845608028654f * 1.4426950408889634f, K2 = K1 * 0.044715f;
  const float e = __builtin_amdgcn_exp2f(x * fmaf(x * x, K2, K1));
  return x * __builtin_amdgcn_rcpf(1.f + e);
}
DI u32x4 gload_asm(const void* p) {
  u32x4 r;
  asm volatile("global_load_dwordx4 %0, %1, off" : "=v"(r) : "v"(p) : "memory");
  return r;
}
template <bool Z2>
DI void win_body(char* smem, const u16* __restrict__ x1, const float* __restrict__ sb, const float* __restrict__ cb,
                 const u16* __restrict__ win, char* __restrict__ zf, float* __restrict__ stats, int mtile, int t4) {
  char* sB = smem + 65536;
  const int tid = threadIdx.x, wave = __builtin_amdgcn_readfirstlane(tid >> 6), lane = tid & 63;
  const int wm = wave >> 2, wn = wave & 3;
  const int m0 = mtile * 64, n0 = (Z2 ? FH : 0) + t4 * 176;
  STAMP(1, 0);
  u32x4 c0 = gload_asm(cb + lane * 8), c1 = gload_asm(cb + lane * 8 + 4);
  u32x4 s0 = gload_asm(sb + lane * 8), s1 = gload_asm(sb + lane * 8 + 4);
  {
    const int rr = lane >> 3, ch = (lane & 7) ^ rr;
    const u16* src = x1 + (size_t)(m0 + wave * 8 + rr) * DM + ch * 8;
#pragma unroll
    for (int sub = 0; sub < 8; ++sub) glds16(src + sub * 64, smem + sub * 8192 + wave * 1024);
  }
#pragma unroll
  for (int st = 0; st < 4; ++st) G4::stageB(win, FF, st * 64, n0, FF - 8, sB + st * G4::B_BYTES, wave, lane);
  u32x4 breg[4][3];
  {
    const int pos = lane & 15, rw = pos >> 1, hf = pos & 1;
    const u16* src[3];
#pragma unroll
    for (int ii = 0; ii < 3; ++ii) {
      const int blk = (ii * 8 + wave) * 4 + (lane >> 4), kb = blk / G4::NB, nb = blk % G4::NB;
      int n = n0 + nb * 16 + hf * 8;
      if (n > FF - 8) n = FF - 8;
      src[ii] = win + (size_t)(256 + kb * 8 + (rw ^ ((kb & 1) << 2))) * FF + n;
    }
#pragma unroll
    for (int st = 0; st < 4; ++st) {
#pragma unroll
      for (int ii = 0; ii < 3; ++ii) breg[st][ii] = *reinterpret_cast<const u32x4*>(src[ii] + (size_t)st * 64 * FF);
      __builtin_amdgcn_sched_barrier(0);
    }
  }
  asm volatile("s_waitcnt vmcnt(24)" : "+v"(c0), "+v"(c1), "+v"(s0), "+v"(s1)::"memory");
  STAMP(1, 1);
  {
    float cbv[8], sbv[8];
#pragma unroll
    for (int jj = 0; jj < 4; ++jj) {
      cbv[jj] = 1.f + __uint_as_float(c0[jj]); cbv[4 + jj] = 1.f + __uint_as_float(c1[jj]);
      sbv[jj] = __uint_as_float(s0[jj]); sbv[4 + jj] = __uint_as_float(s1[jj]);
    }
    u32x4 xr[8];
#pragma unroll
    for (int i = 0; i < 8; ++i)
      xr[i] = *reinterpret_cast<const u32x4*>(smem + (lane >> 3) * 8192 + (wave * 8 + i) * 128 + (((lane & 7) ^ i) << 4));
#pragma unroll
    for (int i = 0; i < 8; ++i) {
      float f[8];
#pragma unroll
      for (int jj = 0; jj < 4; ++jj) { f[2 * jj] = bflo(xr[i][jj]); f[2 * jj + 1] = bfhi(xr[i][jj]); }
      float s = 0.f;
#pragma unroll
      for (int jj = 0; jj < 8; ++jj) s += f[jj];
      const float mean = wave_sum(s) * (1.f / DM);
      float v = 0.f;
#pragma unroll
      for (int jj = 0; jj < 8; ++jj) { f[jj] -= mean; v += f[jj] * f[jj]; }
      const float rstd = rsqrtf(wave_sum(v) * (1.f / DM) + 1e-5f);
#pragma unroll
      for (int jj = 0; jj < 4; ++jj)
        xr[i][jj] = pack2(f[2 * jj] * rstd * cbv[2 * jj] + sbv[2 * jj], f[2 * jj + 1] * rstd * cbv[2 * jj + 1] + sbv[2 * jj + 1]);
    }
#pragma unroll
    for (int i = 0; i < 8; ++i)
      *reinterpret_cast<u32x4*>(smem + (lane >> 3) * 8192 + (wave * 8 + i) * 128 + (((lane & 7) ^ i) << 4)) = xr[i];
  }
  STAMP(1, 2);
  f32x4 acc[G4::MT][G4::NT];
#pragma unroll
  for (int i = 0; i < G4::MT; ++i)
#pragma unroll
    for (int jj = 0; jj < G4::NT; ++jj) acc[i][jj] = f32x4{0.f, 0.f, 0.f, 0.f};
#pragma unroll
  for (int kt = 0; kt < 8; ++kt) {
    if (kt == 0) wait_vm<21>(); else if (kt == 1) wait_vm<18>(); else if (kt == 2) wait_vm<15>(); else if (kt == 3) wait_vm<12>();
    barrier();
    if (kt == 0) STAMP(1, 3);
    if (kt == 1) STAMP(1, 4);
    if (kt == 3) STAMP(1, 5);
    if (kt == 5) STAMP(1, 6);
    if (kt >= 1 && kt + 3 < 8) {
#pragma unroll
      for (int ii = 0; ii < 3; ++ii)
        *reinterpret_cast<u32x4*>(sB + ((kt + 3) & 3) * G4::B_BYTES + (ii * 8 + wave) * 1024 + lane * 16) = breg[kt - 1][ii];
    }
    G4::compute<!Z2>(smem + kt * 8192, sB + (kt & 3) * G4::B_BYTES, acc, wm, wn, lane);
  }
  STAMP(1, 7);
  const int fr = lane & 15, fg = lane >> 4;
  if constexpr (!Z2) {
#pragma unroll
    for (int nt = 0; nt < G4::NT; ++nt) {
      const int nbl = wn * 3 + nt, nb = t4 * 11 + nbl;
      if (nbl < 11 && nb < NBLK) {
#pragma unroll
        for (int mt = 0; mt < G4::MT; ++mt) {
          u32x2 w;
          w[0] = pack2(gelu_tanh(acc[mt][nt][0]), gelu_tanh(acc[mt][nt][1]));
          w[1] = pack2(gelu_tanh(acc[mt][nt][2]), gelu_tanh(acc[mt][nt][3]));
          *reinterpret_cast<u32x2*>(zf + ((size_t)(mtile * 4 + wm * 2 + mt) * NBLK + nb) * 512 + lane * 8) = w;
        }
      }
    }
  } else {
    float p1[G4::MT][4], p2[G4::MT][4];
#pragma unroll
    for (int mt = 0; mt < G4::MT; ++mt)
#pragma unroll
      for (int i = 0; i < 4; ++i) { p1[mt][i] = 0.f; p2[mt][i] = 0.f; }
#pragma unroll
    for (int nt = 0; nt < G4::NT; ++nt) {
      const int nbl = wn * 3 + nt, nb = t4 * 11 + nbl;
      if (nbl < 11 && nb < NBLK) {
#pragma unroll
        for (int mt = 0; mt < G4::MT; ++mt) {
          float gz[4];
#pragma unroll
          for (int i = 0; i < 4; ++i) {
            gz[i] = rbf(gelu_tanh(acc[mt][nt][i]));
            p1[mt][i] += gz[i]; p2[mt][i] += gz[i] * gz[i];
          }
          u32x2 w;
          w[0] = pack2(gz[0], gz[1]);
          w[1] = pack2(gz[2], gz[3]);
          *reinterpret_cast<u32x2*>(zf + ((size_t)(mtile * 2 + wm) * NBLK + nb) * 1024 + (fr + 16 * (mt * 2 + (fg >> 1))) * 16 + (fg & 1) * 8) = w;
        }
      }
    }
    barrier();
    float* red = reinterpret_cast<float*>(smem);
#pragma unroll
    for (int mt = 0; mt < G4::MT; ++mt)
#pragma unroll
      for (int i = 0; i < 4; ++i) {
        float a = p1[mt][i], c = p2[mt][i];
        a = row16_sum(a); c = row16_sum(c);
        if (fr == 0) {
          const int rowl = wm * 32 + mt * 16 + fg * 4 + i;
          red[(wn * 64 + rowl) * 2] = a; red[(wn * 64 + rowl) * 2 + 1] = c;
        }
      }
    barrier();
    if (tid < 128) {
      const float v = (red[tid] + red[128 + tid]) + (red[256 + tid] + red[384 + tid]);
      stats[((size_t)t4 * S + m0) * 2 + tid] = v;
    }
  }
}
__global__ __launch_bounds__(512, 2) void k_win(const u16* __restrict__ x1, const float* __restrict__ sb,
                                                const float* __restrict__ cb, const u16* __restrict__ win,
                                                char* __restrict__ z1f, char* __restrict__ z2f,
                                                float* __restrict__ stats) {
  __shared__ __attribute__((aligned(16))) char smem[65536 + 4 * G4::B_BYTES];
  const int b = blockIdx.x, xcd = b & 7, j = b >> 3;
  const int mtile = xcd * 4 + (j >> 3), ntile = j & 7;
  if (ntile >= 4) win_body<true>(smem, x1, sb, cb, win, z2f, stats, mtile, ntile - 4);
  else win_body<false>(smem, x1, sb, cb, win, z1f, stats, mtile, ntile);
}

constexpr int UKS = 22;
template <int NT>
DI void spatial_body(char* smem, const u16* __restrict__ wsp, const char* __restrict__ z2f,
                     const float* __restrict__ stats, const u16* __restrict__ bsp, const char* __restrict__ z1f,
                     char* __restrict__ uf, int mtile, int nb0, int rot) {
  const int tid = threadIdx.x, wave = __builtin_amdgcn_readfirstlane(tid >> 6), lane = tid & 63;
  const int fr = lane & 15, fg = lane >> 4;
  char* ring = smem + wave * SK_AREA;
  float* rs = reinterpret_cast<float*>(smem + 8 * SK_AREA + wave * 2048);
  float* ms = rs + 256;
  const int m0 = mtile * 64, g0 = wave * 4 + rot;
  STAMP(3, 0);
  f32x4 sp[4][2];
#pragma unroll
  for (int t = 0; t < 4; ++t) {
    const float* p = stats + ((size_t)t * S + ((g0 + (lane >> 4)) & 31) * 64 + (lane & 15) * 4) * 2;
    sp[t][0] = *reinterpret_cast<const f32x4*>(p);
    sp[t][1] = *reinterpret_cast<const f32x4*>(p + 4);
  }
  const int rr = lane >> 3, ch = (lane & 7) ^ rr;
  const u16* asrc = wsp + (size_t)(m0 + rr) * S + ch * 8;
  auto stageA = [&](int s) {
#pragma unroll
    for (int i = 0; i < 8; ++i) glds16(asrc + (size_t)i * 8 * S + ((g0 + s) & 31) * 64, ring + (s & 1) * 8192 + i * 1024);
  };
  const char* bsrc = z2f + (size_t)nb0 * 1024 + lane * 16;
  u32x4 breg[3][NT];
  auto loadB = [&](int t) {
#pragma unroll
    for (int nt = 0; nt < NT; ++nt) breg[t % 3][nt] = *reinterpret_cast<const u32x4*>(bsrc + (((((g0 + (t >> 1)) & 31) * 2 + (t & 1)) * NBLK) + nt) * 1024);
  };
  stageA(0); loadB(0); loadB(1); stageA(1); loadB(2);
  STAMP(3, 1);
  {
    f32x4 rv, mv;
#pragma unroll
    for (int i = 0; i < 4; ++i) {
      float s1 = 0.f, s2 = 0.f;
#pragma unroll
      for (int t = 0; t < 4; ++t) { s1 += sp[t][i >> 1][(i & 1) * 2]; s2 += sp[t][i >> 1][(i & 1) * 2 + 1]; }
      const float mean = s1 * (1.f / FH);
      const float var = fmaxf(s2 * (1.f / FH) - mean * mean, 0.f);
      const float rstd = rsqrtf(var + 1e-5f);
      rv[i] = rstd; mv[i] = -mean * rstd;
    }
    *reinterpret_cast<f32x4*>(rs + lane * 4) = rv;
    *reinterpret_cast<f32x4*>(ms + lane * 4) = mv;
  }
  f32x4 acc[4][NT];
#pragma unroll
  for (int i = 0; i < 4; ++i)
#pragma unroll
    for (int jj = 0; jj < NT; ++jj) acc[i][jj] = f32x4{0.f, 0.f, 0.f, 0.f};
#pragma unroll
  for (int s = 0; s < 4; ++s) {
    if (s < 2) wait_vm<3 * NT + 8>(); else if (s == 2) wait_vm<2 * NT + 8>(); else wait_vm<NT>();
    if (s == 0) STAMP(3, 2);
    if (s == 2) STAMP(3, 3);
#pragma unroll
    for (int kk = 0; kk < 2; ++kk) {
      const int t = 2 * s + kk;
      bf16x8 af[4];
#pragma unroll
      for (int mt = 0; mt < 4; ++mt)
        af[mt] = *reinterpret_cast<const bf16x8*>(ring + (s & 1) * 8192 + (mt * 16 + fr) * 128 + (((kk * 4 + fg) ^ (fr & 7)) << 4));
      f32x4 rv[2], mv[2];
#pragma unroll
      for (int e = 0; e < 2; ++e) {
        rv[e] = *reinterpret_cast<const f32x4*>(rs + t * 32 + fg * 8 + e * 4);
        mv[e] = *reinterpret_cast<const f32x4*>(ms + t * 32 + fg * 8 + e * 4);
      }
#pragma unroll
      for (int nt = 0; nt < NT; ++nt) {
        const u32x4 raw = breg[t % 3][nt];
        u32x4 o4;
#pragma unroll
        for (int jj = 0; jj < 4; ++jj)
          o4[jj] = pack2(fmaf(bflo(raw[jj]), rv[jj >> 1][(jj & 1) * 2], mv[jj >> 1][(jj & 1) * 2]),
                         fmaf(bfhi(raw[jj]), rv[jj >> 1][(jj & 1) * 2 + 1], mv[jj >> 1][(jj & 1) * 2 + 1]));
        const bf16x8 bv = __builtin_bit_cast(bf16x8, o4);
#pragma unroll
        for (int mt = 0; mt < 4; ++mt)
          acc[mt][nt] = __builtin_amdgcn_mfma_f32_16x16x32_bf16(bv, af[mt], acc[mt][nt], 0, 0, 0);
      }
      if (t + 3 < 8) loadB(t + 3);
    }
    if (s + 2 < 4) {
      asm volatile("s_waitcnt lgkmcnt(0)" ::: "memory");
      stageA(s + 2);
    }
  }
  STAMP(3, 4);
  constexpr int NO = SkOwn<NT>::N;
  const int hi = wave >> 2, wq = wave & 3;
  u32x2 zv[NO];
  float bs[NO];
#pragma unroll
  for (int i = 0; i < NO; ++i) {
    int f = wq + 4 * i;
    if (f >= 2 * NT) f = 0;
    const int mt = 2 * hi + f / NT, nb = nb0 + f % NT;
    zv[i] = *reinterpret_cast<const u32x2*>(z1f + ((size_t)(mtile * 4 + mt) * NBLK + nb) * 512 + lane * 8);
    bs[i] = bf2f(bsp[m0 + mt * 16 + fr]);
  }
  f32x4 own[NO];
  splitk_reduce<NT>(acc, own, smem, wave, lane);
#pragma unroll
  for (int i = 0; i < NO; ++i) {
    const int f = wq + 4 * i;
    if (f < 2 * NT) {
      const int mt = 2 * hi + f / NT, nb = nb0 + f % NT;
      u32x2 w;
      w[0] = pack2(bflo(zv[i][0]) * (own[i][0] + bs[i]), bfhi(zv[i][0]) * (own[i][1] + bs[i]));
      w[1] = pack2(bflo(zv[i][1]) * (own[i][2] + bs[i]), bfhi(zv[i][1]) * (own[i][3] + bs[i]));
      char* dst = uf + ((size_t)(mtile * 4 + mt) * UKS + (nb >> 1)) * 1024 + (fr + 16 * (fg >> 1)) * 16 + (fg & 1) * 8;
      *reinterpret_cast<u32x2*>(dst + (nb & 1) * 512) = w;
      if (nb == NBLK - 1) *reinterpret_cast<u32x2*>(dst + 512) = u32x2{0u, 0u};
    }
  }
  STAMP(3, 5);
}
__global__ __launch_bounds__(512, 2) void k_spatial(const u16* __restrict__ wsp, const char* __restrict__ z2f,
                                                    const float* __restrict__ stats, const u16* __restrict__ bsp,
                                                    const char* __restrict__ z1f, char* __restrict__ uf) {
  __shared__ __attribute__((aligned(16))) char smem[8 * SK_AREA + 8 * 2048 + 256];
  const int b = blockIdx.x, xcd = b & 7, j = b >> 3;
  const int mtile = xcd * 4 + (j >> 3), ntile = j & 7;
  const int rot = 0;
  if (ntile < 5) spatial_body<5>(smem, wsp, z2f, stats, bsp, z1f, uf, mtile, ntile * 5, rot);
  else spatial_body<6>(smem, wsp, z2f, stats, bsp, z1f, uf, mtile, 25 + (ntile - 5) * 6, rot);
}

__global__ __launch_bounds__(512, 2) void k_wout(const char* __restrict__ uf, const u16* __restrict__ wout,
                                                 const u16* __restrict__ x1, const float* __restrict__ gb,
                                                 u16* __restrict__ out) {
  __shared__ __attribute__((aligned(16))) char smem[8 * SK_AREA];
  const int b = blockIdx.x, xcd = b & 7, j = b >> 3;
  const int mtile = xcd * 4 + (j >> 3), ntile = j & 7;
  const int tid = threadIdx.x, wave = __builtin_amdgcn_readfirstlane(tid >> 6), lane = tid & 63;
  const int m0 = mtile * 64, n0 = ntile * 64;
  char* ring = smem + wave * SK_AREA;
  const int ks0 = wave < 6 ? wave * 3 : 18 + (wave - 6) * 2, nks = wave < 6 ? 3 : 2;
  const int rw = (lane & 15) >> 1;
  u32x4 areg[3][4];
#pragma unroll
  for (int t = 0; t < 3; ++t) {
    const int ks = ks0 + (t < nks ? t : 0);
#pragma unroll
    for (int i = 0; i < 4; ++i) {
      int row = ks * 32 + i * 8 + ((i & 1) ? (rw ^ 4) : rw);
      if (row > FH - 1) row = FH - 1;
      glds16(wout + (size_t)row * DM + n0 + (lane >> 4) * 16 + (lane & 1) * 8, ring + t * 4096 + i * 1024);
    }
#pragma unroll
    for (int mt = 0; mt < 4; ++mt)
      areg[t][mt] = *reinterpret_cast<const u32x4*>(uf + ((size_t)(mtile * 4 + mt) * UKS + ks) * 1024 + lane * 16);
  }
  const int fr = lane & 15, fg = lane >> 4, hi = wave >> 2, wq = wave & 3;
  u32x2 xv[2];
  const f32x4 gv = *reinterpret_cast<const f32x4*>(gb + n0 + wq * 16 + fg * 4);
#pragma unroll
  for (int i = 0; i < 2; ++i)
    xv[i] = *reinterpret_cast<const u32x2*>(x1 + (size_t)(m0 + (2 * hi + i) * 16 + fr) * DM + n0 + wq * 16 + fg * 4);
  f32x4 acc[4][4];
#pragma unroll
  for (int i = 0; i < 4; ++i)
#pragma unroll
    for (int jj = 0; jj < 4; ++jj) acc[i][jj] = f32x4{0.f, 0.f, 0.f, 0.f};
  wait_vm<0>();
#pragma unroll
  for (int t = 0; t < 3; ++t) {
    if (t < nks) {
      bf16x8 bfr[4];
      sk_bfrags(ring + t * 4096, bfr, lane);
#pragma unroll
      for (int mt = 0; mt < 4; ++mt)
#pragma unroll
        for (int nt = 0; nt < 4; ++nt)
          acc[mt][nt] = __builtin_amdgcn_mfma_f32_16x16x32_bf16(bfr[nt], __builtin_bit_cast(bf16x8, areg[t][mt]), acc[mt][nt], 0, 0, 0);
    }
  }
  f32x4 own[2];
  splitk_reduce<4>(acc, own, smem, wave, lane);
#pragma unroll
  for (int i = 0; i < 2; ++i) {
    u32x2 w;
    w[0] = pack2(bflo(xv[i][0]) + gv[0] * own[i][0], bfhi(xv[i][0]) + gv[1] * own[i][1]);
    w[1] = pack2(bflo(xv[i][1]) + gv[2] * own[i][2], bfhi(xv[i][1]) + gv[3] * own[i][3]);
    *reinterpret_cast<u32x2*>(out + (size_t)(m0 + (2 * hi + i) * 16 + fr) * DM + n0 + wq * 16 + fg * 4) = w;
  }
}

extern "C" void kernel_launch(void* const* d_in, const int* in_sizes, int n_in, void* d_out, int out_size,
                              void* d_ws, size_t ws_size, hipStream_t stream) {
  const u16* x = (const u16*)d_in[0];
  const u16* ckv = (const u16*)d_in[1];
  const u16* wq = (const u16*)d_in[2];
  const u16* wk = (const u16*)d_in[3];
  const u16* wv = (const u16*)d_in[4];
  const u16* wo = (const u16*)d_in[5];
  const u16* win = (const u16*)d_in[7];
  const u16* wsp = (const u16*)d_in[8];
  const u16* bsp = (const u16*)d_in[9];
  const u16* wout = (const u16*)d_in[10];
  const u16* c = (const u16*)d_in[11];
  const u16* wmod = (const u16*)d_in[12];
  char* ws = (char*)d_ws;
  float* mod = (float*)(ws + WS_MOD);
  char* xf = ws + WS_H1;
  char* Qf = ws + WS_QF;
  char* Ki = ws + WS_KI;
  char* Vi = ws + WS_VI;
  char* ctx = ws + WS_CTX;
  u16* x1 = (u16*)(ws + WS_X1);
  char* z1 = ws + WS_Z1;
  char* z2 = ws + WS_Z2;
  float* stats = (float*)(ws + WS_ST);
  char* u = ws + WS_U;
  u16* out = (u16*)d_out;

  k_kv<<<256, 512, 0, stream>>>(ckv, wk, wv, Ki, Vi, c, wmod, mod, x, xf, wq);
  k_attn<<<256, 256, 0, stream>>>(xf, wq, mod, Qf, Ki, Vi, ctx, wo, win, wsp, wout);
  k_oproj<<<256, 512, 0, stream>>>(ctx, wo, x, mod + 2 * DM, x1);
  k_win<<<256, 512, 0, stream>>>(x1, mod + 3 * DM, mod + 4 * DM, win, z1, z2, stats);
  k_spatial<<<256, 512, 0, stream>>>(wsp, z2, stats, bsp, z1, u);
  k_wout<<<256, 512, 0, stream>>>(u, wout, x1, mod + 5 * DM, out);
}
```

```cpp
#include <hip/hip_runtime.h>
#include <hip/hip_bf16.h>
#include <stdint.h>
#include <utility>

constexpr int S = 2048;
constexpr int DM = 512;
constexpr int NH = 32;
constexpr int HD = 128;
constexpr int HDM = NH * HD;
constexpr int FH = 688;
constexpr int FF = 1376;

typedef __attribute__((ext_vector_type(8))) short bf16x8;
typedef __attribute__((ext_vector_type(4))) short s16x4;
typedef __attribute__((ext_vector_type(4))) float f32x4;
typedef __attribute__((ext_vector_type(16))) float f32x16;
typedef __attribute__((ext_vector_type(4))) unsigned u32x4;
typedef __attribute__((ext_vector_type(2))) unsigned u32x2;
typedef __attribute__((address_space(3))) s16x4* lds_s16x4_ptr;
typedef unsigned short u16;

#define DI __device__ __forceinline__

DI float bf2f(u16 v) { return __uint_as_float(((unsigned)v) << 16); }
DI float bflo(unsigned v) { return __uint_as_float(v << 16); }
DI float bfhi(unsigned v) { return __uint_as_float(v & 0xffff0000u); }
typedef __attribute__((ext_vector_type(2))) __bf16 bf16x2_t;
typedef __attribute__((ext_vector_type(2))) float f32x2;
DI unsigned pack2(float lo, float hi) {
  f32x2 v = {lo, hi};
  return __builtin_bit_cast(unsigned, __builtin_convertvector(v, bf16x2_t));
}
template <int N> DI void wait_vm() { asm volatile("s_waitcnt vmcnt(%0)" ::"n"(N) : "memory"); }
DI void barrier() { asm volatile("s_waitcnt lgkmcnt(0)" ::: "memory"); __builtin_amdgcn_s_barrier(); }
DI float rbf(float x) { return bflo(pack2(x, 0.f)); }

DI void glds16(const void* g, const void* lds_wave_base) {
  const unsigned a = __builtin_amdgcn_readfirstlane((unsigned)(size_t)lds_wave_base);
  unsigned keep;
  asm volatile("s_mov_b32 %0, m0\n\ts_mov_b32 m0, %2\n\ts_nop 0\n\tglobal_load_lds_dwordx4 %1, off\n\ts_mov_b32 m0, %0"
               : "=&s"(keep) : "v"(g), "s"(a) : "memory");
}
template <int OFF> DI void glds16s(const void* sbase, unsigned voff, unsigned lds_base) {
  asm volatile("s_mov_b32 m0, %2\n\ts_nop 0\n\tglobal_load_lds_dwordx4 %0, %1 offset:%3"
               : : "v"(voff), "s"(sbase), "s"(lds_base), "n"(OFF) : "memory");
}
DI unsigned lds_addr(const void* p) { return __builtin_amdgcn_readfirstlane((unsigned)(size_t)p); }
DI void pf_line(const void* g, const void* lds_dummy) {
  const unsigned a = __builtin_amdgcn_readfirstlane((unsigned)(size_t)lds_dummy);
  unsigned keep;
  asm volatile("s_mov_b32 %0, m0\n\ts_mov_b32 m0, %2\n\ts_nop 0\n\tglobal_load_lds_dword %1, off\n\ts_mov_b32 m0, %0"
               : "=&s"(keep) : "v"(g), "s"(a) : "memory");
}
DI void pf_slice(const void* base, int nbytes, int first, int lane, const void* lds_dummy) {
  int l = first + lane;
  const int last = (nbytes >> 7) - 1;
  if (l > last) l = last;
  pf_line((const char*)base + (size_t)l * 128, lds_dummy);
}
#define STAMP(k, i)
#define STAMPN(k, i)
#define STAMPF(k)
template <int CTRL> DI float dpp_add(float v) {
  return v + __uint_as_float(__builtin_amdgcn_update_dpp(0u, __float_as_uint(v), CTRL, 0xf, 0xf, true));
}
DI float row16_sum(float v) {
  v = dpp_add<0xB1>(v); v = dpp_add<0x4E>(v); v = dpp_add<0x141>(v); v = dpp_add<0x140>(v);
  return v;
}
DI float wave_sum(float v) {
  v = row16_sum(v);
  auto a = __builtin_amdgcn_permlane16_swap(__float_as_uint(v), __float_as_uint(v), false, false);
  v = __uint_as_float(a[0]) + __uint_as_float(a[1]);
  auto b = __builtin_amdgcn_permlane32_swap(__float_as_uint(v), __float_as_uint(v), false, false);
  return __uint_as_float(b[0]) + __uint_as_float(b[1]);
}
DI s16x4 tr_read(const char* p) {
  return __builtin_amdgcn_ds_read_tr16_b64_v4i16((lds_s16x4_ptr)(p));
}

constexpr size_t WS_MOD = 0;
constexpr size_t WS_H1 = 16384;
constexpr size_t WS_QF = WS_H1 + (size_t)S * DM * 2;
constexpr size_t WS_KI = WS_QF + (size_t)S * HDM * 2;
constexpr size_t WS_VI = WS_KI + (size_t)S * HDM * 2;
constexpr size_t WS_CTX = WS_VI + (size_t)S * HDM * 2;
constexpr size_t WS_X1 = WS_CTX + (size_t)S * HDM * 2;
constexpr size_t WS_Z1 = WS_X1 + (size_t)S * DM * 2;
constexpr size_t WS_Z2 = WS_Z1 + (size_t)S * FH * 2;
constexpr size_t WS_ST = WS_Z2 + (size_t)S * FH * 2;
constexpr size_t WS_U = WS_ST + (size_t)8 * S * 2 * 4;
constexpr size_t WS_SLAB = WS_U + (size_t)S * 704 * 2;
constexpr size_t WS_FLAG = WS_SLAB + (size_t)96 * 4 * 33 * 1024;
constexpr int LDU = 704;

template <int BM, int BN, int KS, int NWM, int NWN>
struct Gemm {
  static constexpr int NW = NWM * NWN;
  static constexpr int WM = BM / NWM, WN = BN / NWN;
  static constexpr int MT = WM / 16, NT = WN / 16;
  static constexpr int BK = 64 * KS;
  static constexpr int A_BYTES = BM * BK * 2;
  static constexpr int B_BYTES = BK * BN * 2;
  static constexpr int STAGE_BYTES = A_BYTES + B_BYTES;
  static constexpr int NB = BN / 16;

  static DI void stageA(const u16* __restrict__ A, int lda, int m0, int k0, char* lds, int wave, int lane) {
    constexpr int NI = A_BYTES / 1024;
    const int rr = lane >> 3, ch = (lane & 7) ^ rr;
#pragma unroll
    for (int ii = 0; ii < NI / NW; ++ii) {
      const int i = ii * NW + wave;
      const int sub = i / (BM / 8), j = i % (BM / 8);
      const u16* src = A + (size_t)(m0 + j * 8 + rr) * lda + k0 + sub * 64 + ch * 8;
      glds16(src, lds + i * 1024);
    }
  }
  static DI void stageB(const u16* __restrict__ B, int ldb, int k0, int n0, int nmax, char* lds, int wave, int lane) {
    constexpr int NI = B_BYTES / 1024;
    const int pos = lane & 15, rw = pos >> 1, hf = pos & 1;
#pragma unroll
    for (int ii = 0; ii < NI / NW; ++ii) {
      const int i = ii * NW + wave;
      const int blk = i * 4 + (lane >> 4);
      const int kb = blk / NB, nb = blk % NB;
      const int row = rw ^ ((kb & 1) << 2);
      int n = n0 + nb * 16 + hf * 8;
      if (n > nmax) n = nmax;
      const u16* src = B + (size_t)(k0 + kb * 8 + row) * ldb + n;
      glds16(src, lds + i * 1024);
    }
  }
  template <bool SWAP>
  static DI void compute(const char* ldsA, const char* ldsB, f32x4 (&acc)[MT][NT], int wm, int wn, int lane) {
    const int fr = lane & 15, fg = lane >> 4;
    const int q = fr >> 2, p = lane & 3;
#pragma unroll
    for (int sub = 0; sub < KS; ++sub) {
#pragma unroll
      for (int kk = 0; kk < 2; ++kk) {
        bf16x8 af[MT], bfr[NT];
#pragma unroll
        for (int mt = 0; mt < MT; ++mt) {
          const int row = wm * WM + mt * 16 + fr;
          af[mt] = *reinterpret_cast<const bf16x8*>(ldsA + sub * (BM * 128) + row * 128 + (((kk * 4 + fg) ^ (row & 7)) << 4));
        }
#pragma unroll
        for (int nt = 0; nt < NT; ++nt) {
          const int kb = sub * 8 + kk * 4 + fg, nb = wn * NT + nt;
          const char* base = ldsB + (kb * NB + nb) * 256 + p * 8;
          const int sw = (fg & 1) << 2;
          s16x4 lo = tr_read(base + ((q) ^ sw) * 32);
          s16x4 hi = tr_read(base + ((4 + q) ^ sw) * 32);
          bfr[nt] = __builtin_shufflevector(lo, hi, 0, 1, 2, 3, 4, 5, 6, 7);
        }
#pragma unroll
        for (int mt = 0; mt < MT; ++mt)
#pragma unroll
          for (int nt = 0; nt < NT; ++nt) {
            if constexpr (SWAP)
              acc[mt][nt] = __builtin_amdgcn_mfma_f32_16x16x32_bf16(bfr[nt], af[mt], acc[mt][nt], 0, 0, 0);
            else
              acc[mt][nt] = __builtin_amdgcn_mfma_f32_16x16x32_bf16(af[mt], bfr[nt], acc[mt][nt], 0, 0, 0);
          }
      }
    }
  }
};

#define KV_PD 2
constexpr int KV_NS = 4, KV_STAGE = 32768;
template <bool isV>
DI void kv_body(char* smem, const u16* __restrict__ ckv, const u16* __restrict__ B, char* __restrict__ dstb,
                const u16* __restrict__ cvec, const u16* __restrict__ wmod, float* __restrict__ mod,
                const u16* __restrict__ x, char* __restrict__ xf, const u16* __restrict__ wq) {
  const int b = blockIdx.x, xcd = b & 7, j = b >> 3;
  const int ntile = xcd * 4 + (j & 3), mtile = j >> 2;
  const int tid = threadIdx.x, wave = __builtin_amdgcn_readfirstlane(tid >> 6), lane = tid & 63;
  const int wm = wave >> 2, wn = wave & 3, r = lane & 31, h = lane >> 5;
  const int m0 = mtile * 256, n0 = (ntile & 15) * 256;
  float* modred = reinterpret_cast<float*>(smem + KV_NS * KV_STAGE);
  STAMP(0, 0);

  const u16* asrc[2];
  const u16* bsrc[2];
#pragma unroll
  for (int ii = 0; ii < 2; ++ii) {
    const int i = ii * 8 + wave;
    const int row = 16 * i + (lane >> 2), ch = (lane & 3) ^ ((lane >> 4) & 3);
    asrc[ii] = ckv + (size_t)(m0 + row) * DM + ch * 8;
    const int blk = i * 4 + (lane >> 4), kb = blk >> 4, nb = blk & 15;
    const int pos = lane & 15, rw = (pos >> 1) ^ ((nb & 1) << 2), hf = pos & 1;
    bsrc[ii] = B + (size_t)(kb * 8 + rw) * HDM + n0 + nb * 16 + hf * 8;
  }
  auto stage = [&](int kt0, int slot) {
    char* dst = smem + slot * KV_STAGE;
    const int kt = (kt0 + 2 * mtile) & 15;
#pragma unroll
    for (int ii = 0; ii < 2; ++ii) {
      glds16(asrc[ii] + kt * 32, dst + (ii * 8 + wave) * 1024);
      glds16(bsrc[ii] + (size_t)kt * 32 * HDM, dst + 16384 + (ii * 8 + wave) * 1024);
    }
  };
  constexpr int NK = DM / 32;
#pragma unroll
  for (int st = 0; st < KV_PD; ++st) stage(st, st);

  if (b < 192) {
    const u16* p = wmod + (size_t)tid * 3072 + b * 16;
    const u32x4 w0 = *reinterpret_cast<const u32x4*>(p), w1 = *reinterpret_cast<const u32x4*>(p + 8);
    const float c = bf2f(cvec[tid]);
    const float sv = rbf(c / (1.f + __expf(-c)));
    float v[16];
#pragma unroll
    for (int jj = 0; jj < 4; ++jj) {
      v[2 * jj] = sv * bflo(w0[jj]); v[2 * jj + 1] = sv * bfhi(w0[jj]);
      v[8 + 2 * jj] = sv * bflo(w1[jj]); v[8 + 2 * jj + 1] = sv * bfhi(w1[jj]);
    }
#pragma unroll
    for (int st = 0; st < 4; ++st) {
      const int half = 8 >> st, m = 32 >> st;
      const bool up = lane & m;
#pragma unroll
      for (int e = 0; e < half; ++e) {
        const float send = up ? v[e] : v[half + e];
        const float keep = up ? v[half + e] : v[e];
        v[e] = keep + __shfl_xor(send, m);
      }
    }
    v[0] += __shfl_xor(v[0], 2);
    v[0] += __shfl_xor(v[0], 1);
    if ((lane & 3) == 0) {
      const int col = ((lane >> 5) & 1) * 8 + ((lane >> 4) & 1) * 4 + ((lane >> 3) & 1) * 2 + ((lane >> 2) & 1);
      modred[wave * 16 + col] = v[0];
    }
  } else {
    const int row0 = (b - 192) * 32 + wave * 4;
    u32x4 xv[4];
#pragma unroll
    for (int i = 0; i < 4; ++i) xv[i] = *reinterpret_cast<const u32x4*>(x + (size_t)(row0 + i) * DM + lane * 8);
#pragma unroll
    for (int i = 0; i < 4; ++i) {
      float f[8];
#pragma unroll
      for (int jj = 0; jj < 4; ++jj) { f[2 * jj] = bflo(xv[i][jj]); f[2 * jj + 1] = bfhi(xv[i][jj]); }
      float sm = 0.f;
#pragma unroll
      for (int jj = 0; jj < 8; ++jj) sm += f[jj];
      const float mean = wave_sum(sm) * (1.f / DM);
      float vr = 0.f;
#pragma unroll
      for (int jj = 0; jj < 8; ++jj) { f[jj] -= mean; vr += f[jj] * f[jj]; }
      const float rstd = rsqrtf(wave_sum(vr) * (1.f / DM) + 1e-5f);
      u32x4 o;
#pragma unroll
      for (int jj = 0; jj < 4; ++jj) {
        typedef __attribute__((ext_vector_type(2))) _Float16 f16x2;
        const f16x2 hv = {(_Float16)(f[2 * jj] * rstd), (_Float16)(f[2 * jj + 1] * rstd)};
        o[jj] = __builtin_bit_cast(unsigned, hv);
      }
      const int row = row0 + i;
      *reinterpret_cast<u32x4*>(xf + ((size_t)(row >> 5) * 32 + (lane >> 1)) * 1024 + ((row & 31) + 32 * (lane & 1)) * 16) = o;
    }
  }

  STAMP(0, 1);
  f32x16 acc[4][2];
#pragma unroll
  for (int i = 0; i < 4; ++i)
#pragma unroll
    for (int jj = 0; jj < 2; ++jj)
#pragma unroll
      for (int e = 0; e < 16; ++e) acc[i][jj][e] = 0.f;
  const int G = lane >> 4, q4 = (lane & 15) >> 2, p4 = lane & 3;
  int aoff[2];
#pragma unroll
  for (int u = 0; u < 2; ++u) aoff[u] = (wm * 128 + r) * 64 + (((2 * u + h) ^ ((r >> 2) & 3)) << 4);
  const int nh = G & 1;
  const int boff = 16384 + (h * 16 + wn * 4 + nh) * 256 + p4 * 8;
  const int brow0 = (q4 ^ (nh << 2)) * 32, brow1 = ((4 + q4) ^ (nh << 2)) * 32;
  for (int kt = 0; kt < NK; ++kt) {
    if (kt + KV_PD - 1 <= NK - 1) wait_vm<(KV_PD - 1) * 4>(); else wait_vm<0>();
    barrier();
    const bool pf = kt + KV_PD < NK;
    const int ktn = (kt + KV_PD + 2 * mtile) & 15;
    char* dst = smem + ((kt + KV_PD) & (KV_NS - 1)) * KV_STAGE;
    const char* cur = smem + (kt & (KV_NS - 1)) * KV_STAGE;
    bf16x8 af[2][4], bfr[2][2];
#pragma unroll
    for (int u = 0; u < 2; ++u) {
#pragma unroll
      for (int nt = 0; nt < 2; ++nt) {
        const char* bb = cur + boff + u * (2 * 16 * 256) + nt * 512;
        s16x4 lo = tr_read(bb + brow0), hi = tr_read(bb + brow1);
        bfr[u][nt] = __builtin_shufflevector(lo, hi, 0, 1, 2, 3, 4, 5, 6, 7);
      }
#pragma unroll
      for (int mt = 0; mt < 4; ++mt) af[u][mt] = *reinterpret_cast<const bf16x8*>(cur + aoff[u] + mt * 2048);
    }
#pragma unroll
    for (int g4 = 0; g4 < 4; ++g4) {
      const int u = g4 >> 1;
      __builtin_amdgcn_sched_barrier(0);
      if (pf) {
        const int ii = g4 >> 1;
        if (g4 & 1) glds16(bsrc[ii] + (size_t)ktn * 32 * HDM, dst + 16384 + (ii * 8 + wave) * 1024);
        else glds16(asrc[ii] + ktn * 32, dst + (ii * 8 + wave) * 1024);
      }
      __builtin_amdgcn_sched_barrier(0);
#pragma unroll
      for (int m2 = 0; m2 < 2; ++m2)
#pragma unroll
        for (int nt = 0; nt < 2; ++nt) {
          const int mt = (g4 & 1) * 2 + m2;
          if constexpr (isV)
            acc[mt][nt] = __builtin_amdgcn_mfma_f32_32x32x16_bf16(af[u][mt], bfr[u][nt], acc[mt][nt], 0, 0, 0);
          else
            acc[mt][nt] = __builtin_amdgcn_mfma_f32_32x32x16_bf16(bfr[u][nt], af[u][mt], acc[mt][nt], 0, 0, 0);
        }
    }
  }
  STAMP(0, 2);
  if (wave < 2) pf_slice(wq, DM * HDM * 2, (b * 2 + wave) * 64, lane, smem + KV_NS * KV_STAGE + 512);
  {
    const int hd = (ntile & 15) * 2 + (wn >> 1);
    char* base = dstb + ((size_t)hd * 32 + mtile * 4 + wm * 2) * 16384 + lane * 16;
#pragma unroll
    for (int mt = 0; mt < 4; ++mt)
#pragma unroll
      for (int nt = 0; nt < 2; ++nt)
#pragma unroll
        for (int u = 0; u < 2; ++u) {
          u32x4 v;
#pragma unroll
          for (int e = 0; e < 4; ++e) v[e] = pack2(acc[mt][nt][8 * u + 2 * e], acc[mt][nt][8 * u + 2 * e + 1]);
          const int db = (wn & 1) * 2 + nt;
          int off;
          if constexpr (isV) off = (mt >> 1) * 16384 + ((mt & 1) * 2 + u) * 4096 + db * 1024;
          else off = (mt >> 1) * 16384 + (mt & 1) * 8192 + (db * 2 + u) * 1024;
          *reinterpret_cast<u32x4*>(base + off) = v;
        }
  }
  barrier();
  STAMP(0, 3);
  if (b < 192 && tid < 16) {
    float sum = 0.f;
#pragma unroll
    for (int i = 0; i < 8; ++i) sum += modred[i * 16 + tid];
    mod[b * 16 + tid] = rbf(sum);
  }
}
__global__ __launch_bounds__(512, 2) void k_kv(const u16* __restrict__ ckv, const u16* __restrict__ wk,
                                               const u16* __restrict__ wv, char* __restrict__ Ki,
                                               char* __restrict__ Vi, const u16* __restrict__ cvec,
                                               const u16* __restrict__ wmod, float* __restrict__ mod,
                                               const u16* __restrict__ x, char* __restrict__ xf,
                                               const u16* __restrict__ wq) {
  __shared__ __attribute__((aligned(16))) char smem[KV_NS * KV_STAGE + 512 + 256];
  if ((blockIdx.x & 7) >= 4) kv_body<true>(smem, ckv, wv, Vi, cvec, wmod, mod, x, xf, wq);
  else kv_body<false>(smem, ckv, wk, Ki, cvec, wmod, mod, x, xf, wq);
}

#define ACLOB_LO "a0","a1","a2","a3","a4","a5","a6","a7","a8","a9","a10","a11","a12","a13","a14","a15","a16","a17","a18","a19","a20","a21","a22","a23","a24","a25","a26","a27","a28","a29","a30","a31","a32","a33","a34","a35","a36","a37","a38","a39","a40","a41","a42","a43","a44","a45","a46","a47","a48","a49","a50","a51","a52","a53","a54","a55","a56","a57","a58","a59","a60","a61","a62","a63","a64","a65","a66","a67","a68","a69","a70","a71","a72","a73","a74","a75","a76","a77","a78","a79","a80","a81","a82","a83","a84","a85","a86","a87","a88","a89","a90","a91","a92","a93","a94","a95","a96","a97","a98","a99","a100","a101","a102","a103","a104","a105","a106","a107","a108","a109","a110","a111","a112","a113","a114","a115","a116","a117","a118","a119","a120","a121","a122","a123","a124","a125","a126","a127"
#define ACLOB_HI "a128","a129","a130","a131","a132","a133","a134","a135","a136","a137","a138","a139","a140","a141","a142","a143","a144","a145","a146","a147","a148","a149","a150","a151","a152","a153","a154","a155","a156","a157","a158","a159","a160","a161","a162","a163","a164","a165","a166","a167","a168","a169","a170","a171","a172","a173","a174","a175","a176","a177","a178","a179","a180","a181","a182","a183","a184","a185","a186","a187","a188","a189","a190","a191","a192","a193","a194","a195","a196","a197","a198","a199","a200","a201","a202","a203","a204","a205","a206","a207","a208","a209","a210","a211","a212","a213","a214","a215","a216","a217","a218","a219","a220","a221","a222","a223","a224","a225","a226","a227","a228","a229","a230","a231","a232","a233","a234","a235","a236","a237","a238","a239","a240","a241","a242","a243","a244","a245","a246","a247","a248","a249","a250","a251","a252","a253","a254","a255"
template <int... Is> using iseq = std::integer_sequence<int, Is...>;
template <int N> using make_iseq = std::make_integer_sequence<int, N>;
#define GAP() __builtin_amdgcn_sched_barrier(0)
constexpr float AT_THR = 8.f;
constexpr int AT_MISC = 131072;
DI float max3f(float a, float b, float c) { float r; asm("v_max3_f32 %0, %1, %2, %3" : "=v"(r) : "v"(a), "v"(b), "v"(c)); return r; }
DI float max2f(float a, float b) { float r; asm("v_max_f32_e32 %0, %1, %2" : "=v"(r) : "v"(a), "v"(b)); return r; }
DI float fadd_s(float a, float b) { float r; asm("v_add_f32_e32 %0, %1, %2" : "=v"(r) : "v"(a), "v"(b)); return r; }
DI float fmul_s(float a, float b) { float r; asm("v_mul_f32_e32 %0, %1, %2" : "=v"(r) : "v"(a), "v"(b)); return r; }
DI float fsub_s(float a, float b) { float r; asm("v_sub_f32_e32 %0, %1, %2" : "=v"(r) : "v"(a), "v"(b)); return r; }
DI unsigned lane_id() { unsigned l; asm volatile("v_mbcnt_lo_u32_b32 %0, -1, 0\n\tv_mbcnt_hi_u32_b32 %0, -1, %0" : "=v"(l)); return l; }
DI void negm_bcast(f32x16& negm, float neg_mhat) {
  const float aone = lane_id() < 32u ? 1.f : 0.f;
  asm volatile("s_nop 1\n\tv_mfma_f32_32x32x2_f32 %0, %1, %2, 0" : "+v"(negm) : "v"(aone), "v"(neg_mhat));
}
struct AC {
  unsigned rb, voff;
  unsigned mk, mv;
  const char *Kh, *Vh;
  const char *ka, *va;
  int kt, vt, end0;
  int dd;
  f32x16 S[2][2][2];
  f32x16 NEGM[2];
  bf16x8 Vf[4][4];
  bf16x8 Pk[2][4];
  float mhat[2], lsum[2], fct[2]; bool pend;
  float rs0[2], rs1[2], pmx[2][4], rmax[2];
};
constexpr int AO_(int db, int qb) { return 16 * (2 * db + qb); }
constexpr int AQ_(int qb, int d0) { return 128 + 32 * qb + 4 * d0; }
constexpr int AK_(int kb, int d0) { return 192 + 32 * kb + 4 * d0; }
template <int I> DI void v_read(AC& c) {
  asm volatile("ds_read_b128 %0, %1 offset:%2" : "=v"(c.Vf[I & 3][I >> 2]) : "v"(c.rb), "i"(32768 + I * 1024) : "memory");
}
template <int I> DI void k_read(unsigned rb) {
  asm volatile("ds_read_b128 a[%c1:%c2], %0 offset:%c3" ::"v"(rb), "i"(192 + 4 * I), "i"(195 + 4 * I), "i"(I * 1024) : "memory", ACLOB_HI);
}
template <int I> DI void q_load(const char* qp, unsigned voff) {
  asm volatile("global_load_dwordx4 a[%c2:%c3], %0, %1 offset:%c4" ::"v"(voff), "s"(qp + (I >> 2) * 4096), "i"(128 + 4 * I), "i"(131 + 4 * I), "i"((I & 3) * 1024) : "memory", ACLOB_HI);
}
template <int I> DI void dma(const char* src, unsigned voff, unsigned lds) {
  asm volatile("s_mov_b32 m0, %2\n\ts_nop 0\n\tglobal_load_lds_dwordx4 %0, %1 offset:%3" ::"v"(voff), "s"(src + (I >> 2) * 4096), "s"(lds + (I >> 2) * 4096), "n"((I & 3) * 1024) : "memory");
}
template <int... Is> DI void dmas(const char* src, unsigned voff, unsigned lds, iseq<Is...>) { (dma<Is>(src, voff, lds), ...); }
template <int X, bool FIRST, int M> DI void qk_mfma(AC& c) {
  constexpr int d0 = M >> 2, kb = (M >> 1) & 1, qb = M & 1, ak = AK_(kb, d0), aq = AQ_(qb, d0);
  if constexpr (d0 == 0) {
    if constexpr (FIRST) asm volatile("v_mfma_f32_32x32x16_bf16 %0, a[%c1:%c2], a[%c3:%c4], 0" : "=&v"(c.S[X][kb][qb]) : "i"(ak), "i"(ak + 3), "i"(aq), "i"(aq + 3) : ACLOB_LO, ACLOB_HI);
    else asm volatile("v_mfma_f32_32x32x16_bf16 %0, a[%c2:%c3], a[%c4:%c5], %1" : "=&v"(c.S[X][kb][qb]) : "v"(c.NEGM[qb]), "i"(ak), "i"(ak + 3), "i"(aq), "i"(aq + 3) : ACLOB_LO, ACLOB_HI);
  } else asm volatile("v_mfma_f32_32x32x16_bf16 %0, a[%c1:%c2], a[%c3:%c4], %0" : "+v"(c.S[X][kb][qb]) : "i"(ak), "i"(ak + 3), "i"(aq), "i"(aq + 3) : ACLOB_LO, ACLOB_HI);
}
template <int M> DI void pv_mfma(AC& c) {
  constexpr int ks = M >> 3, db = (M >> 1) & 3, qb = M & 1, ao = AO_(db, qb);
  asm volatile("v_mfma_f32_32x32x16_bf16 a[%c2:%c3], %0, %1, a[%c2:%c3]" ::"v"(c.Vf[db][ks]), "v"(c.Pk[qb][ks]), "i"(ao), "i"(ao + 15) : ACLOB_LO);
}
template <int M, int P, int R> DI void pv_mfma_ld(AC& c) {
  constexpr int ks = M >> 3, db = (M >> 1) & 3, qb = M & 1, ao = AO_(db, qb), I = P & 7;
  const char* src = (P < 8 ? c.ka : c.va) + (I >> 2) * 4096;
  const unsigned dst = (P < 8 ? c.mk : c.mv) + (I >> 2) * 4096;
  if constexpr (P >= 0 && R >= 0)
    asm volatile("s_mov_b32 m0, %4\n\tv_mfma_f32_32x32x16_bf16 a[%c6:%c7], %0, %1, a[%c6:%c7]\n\tglobal_load_lds_dwordx4 %2, %3 offset:%c8\n\tds_read_b128 a[%c9:%c10], %5 offset:%c11"
                 ::"v"(c.Vf[db][ks]), "v"(c.Pk[qb][ks]), "v"(c.voff), "s"(src), "s"(dst), "v"(c.rb), "i"(ao), "i"(ao + 15), "i"((I & 3) * 1024),
                   "i"(192 + 4 * R), "i"(195 + 4 * R), "i"(R * 1024) : "memory", ACLOB_LO, ACLOB_HI);
  else if constexpr (P >= 0)
    asm volatile("s_mov_b32 m0, %4\n\tv_mfma_f32_32x32x16_bf16 a[%c5:%c6], %0, %1, a[%c5:%c6]\n\tglobal_load_lds_dwordx4 %2, %3 offset:%c7"
                 ::"v"(c.Vf[db][ks]), "v"(c.Pk[qb][ks]), "v"(c.voff), "s"(src), "s"(dst), "i"(ao), "i"(ao + 15), "i"((I & 3) * 1024) : "memory", ACLOB_LO);
  else if constexpr (R >= 0)
    asm volatile("v_mfma_f32_32x32x16_bf16 a[%c3:%c4], %0, %1, a[%c3:%c4]\n\tds_read_b128 a[%c5:%c6], %2 offset:%c7"
                 ::"v"(c.Vf[db][ks]), "v"(c.Pk[qb][ks]), "v"(c.rb), "i"(ao), "i"(ao + 15),
                   "i"(192 + 4 * R), "i"(195 + 4 * R), "i"(R * 1024) : "memory", ACLOB_LO, ACLOB_HI);
  else pv_mfma<M>(c);
}
template <int I> DI void o_zero4() {
  asm volatile("v_accvgpr_write_b32 a[%c0], 0\n\tv_accvgpr_write_b32 a[%c1], 0\n\tv_accvgpr_write_b32 a[%c2], 0\n\tv_accvgpr_write_b32 a[%c3], 0"
               ::"i"(4 * I), "i"(4 * I + 1), "i"(4 * I + 2), "i"(4 * I + 3) : ACLOB_LO);
}
template <int X, int QB, int KS, int P> DI void pack1(AC& c) {
  u32x4 w = __builtin_bit_cast(u32x4, c.Pk[QB][KS]);
  unsigned d = pack2(c.S[X][KS >> 1][QB][8 * (KS & 1) + 2 * P], c.S[X][KS >> 1][QB][8 * (KS & 1) + 2 * P + 1]);
  asm volatile("" : "+v"(d));
  w[P] = d;
  c.Pk[QB][KS] = __builtin_bit_cast(bf16x8, w);
}
template <int X, int J> DI void rowsum1(AC& c) {
  constexpr int qb = J >> 4, ks = (J >> 2) & 3, p = J & 3;
  c.rs0[qb] = fadd_s(c.rs0[qb], c.S[X][ks >> 1][qb][8 * (ks & 1) + 2 * p]);
  c.rs1[qb] = fadd_s(c.rs1[qb], c.S[X][ks >> 1][qb][8 * (ks & 1) + 2 * p + 1]);
}
DI void lsum_merge(AC& c) {
#pragma unroll
  for (int qb = 0; qb < 2; ++qb) {
    const float rs = fadd_s(c.rs0[qb], c.rs1[qb]);
    auto pr = __builtin_amdgcn_permlane32_swap(__float_as_uint(rs), __float_as_uint(rs), false, false);
    c.lsum[qb] = fadd_s(c.lsum[qb], fadd_s(__uint_as_float(pr[0]), __uint_as_float(pr[1])));
  }
  asm volatile("" : "+v"(c.lsum[0]), "+v"(c.lsum[1]));
}
#define AT_VF "+v"(c.Vf[0][0]), "+v"(c.Vf[0][1]), "+v"(c.Vf[0][2]), "+v"(c.Vf[0][3]), "+v"(c.Vf[1][0]), "+v"(c.Vf[1][1]), "+v"(c.Vf[1][2]), "+v"(c.Vf[1][3]), \
              "+v"(c.Vf[2][0]), "+v"(c.Vf[2][1]), "+v"(c.Vf[2][2]), "+v"(c.Vf[2][3]), "+v"(c.Vf[3][0]), "+v"(c.Vf[3][1]), "+v"(c.Vf[3][2]), "+v"(c.Vf[3][3])
template <int X, bool FIRST, int M> DI void gapA(AC& c) {
  constexpr int Xp = X ^ 1;
  qk_mfma<X, FIRST, M>(c);
  GAP();
  if constexpr (M == 26) {
    c.ka = c.Kh + (size_t)(unsigned)c.kt * 32768; asm volatile("" : "+s"(c.ka));
    c.kt = c.kt + 1 == c.end0 ? 0 : c.kt + 1; asm volatile("" : "+s"(c.kt));
  }
  if constexpr (M == 28) {
    c.va = c.Vh + (size_t)(unsigned)c.vt * 32768; asm volatile("" : "+s"(c.va));
    c.vt = c.vt + 1 == c.end0 ? 0 : c.vt + 1; asm volatile("" : "+s"(c.vt));
  }
  if constexpr (FIRST) o_zero4<M>();
  if constexpr (!FIRST && M == 8) asm volatile("" ::"v"(c.NEGM[0]), "v"(c.NEGM[1]));
  if constexpr (!FIRST) {
    if constexpr (M >= 10 && M < 26) v_read<M - 10>(c);
    if constexpr (M < 16) {
      constexpr int e0 = 2 * M, e1 = 2 * M + 1;
      c.S[Xp][1][e0 >> 4][e0 & 15] = __builtin_amdgcn_exp2f(c.S[Xp][1][e0 >> 4][e0 & 15]);
      c.S[Xp][1][e1 >> 4][e1 & 15] = __builtin_amdgcn_exp2f(c.S[Xp][1][e1 >> 4][e1 & 15]);
      pack1<Xp, (M >> 3), ((M >> 2) & 1), (M & 3)>(c);
    } else {
      constexpr int cc = M - 16;
      pack1<Xp, (cc >> 3), 2 + ((cc >> 2) & 1), (cc & 3)>(c);
      rowsum1<Xp, 16 * (cc >> 3) + 8 + 4 * ((cc >> 2) & 1) + (cc & 3)>(c);
    }
  }
  GAP();
}
template <int X, bool FIRST, int... Ms> DI void phaseA_(AC& c, bool pf, iseq<Ms...>) {
  (gapA<X, FIRST, Ms>(c), ...);
  if constexpr (FIRST) {
    if (pf) asm volatile("s_waitcnt vmcnt(2)" ::: "memory");
    else asm volatile("s_waitcnt vmcnt(0)" ::: "memory");
    asm volatile("s_waitcnt lgkmcnt(0)\n\ts_barrier" ::: "memory");
  } else {
    lsum_merge(c);
    asm volatile("s_waitcnt vmcnt(0) lgkmcnt(0)\n\ts_barrier" : AT_VF::"memory");
  }
  GAP();
}
template <int X, bool FIRST> DI void phaseA(AC& c, bool pf = false) { phaseA_<X, FIRST>(c, pf, make_iseq<32>{}); }

template <int KK> DI void mask1(float& x, int lim, float ninf) {
  asm volatile("v_cmp_gt_i32_e32 vcc, %c3, %1\n\tv_cndmask_b32_e32 %0, %0, %2, vcc" : "+v"(x) : "v"(lim), "v"(ninf), "i"(KK) : "vcc");
}
template <int X, int... Es> DI void mask_all(AC& c, int lim, float ninf, iseq<Es...>) {
  (mask1<32 * ((Es >> 4) & 1) + (Es & 3) + 8 * ((Es >> 2) & 3) - 32 * (Es >> 5)>(reinterpret_cast<float*>(&c.S[X][(Es >> 4) & 1][Es >> 5])[Es & 15], lim, ninf), ...);
}
template <int X> DI void at_mask(AC& c) {
  if (c.dd >= 0) {
    const unsigned l = lane_id();
    const int lim = (int)(l & 31u) - (int)((l >> 5) << 2) - 64 * c.dd;
    mask_all<X>(c, lim, -__builtin_inff(), make_iseq<64>{});
  }
}
template <int X, bool PEEL, int M> DI void gapB(AC& c) {
  if constexpr (PEEL) {
    if constexpr (M < 8) dma<M>(c.ka, c.voff, c.mk);
    else if constexpr (M < 16) dma<M - 8>(c.va, c.voff, c.mv);
    if constexpr (M < 16) k_read<M>(c.rb);
  } else {
    pv_mfma_ld<M, (M & 1) ? -1 : M / 2, M < 16 ? M : -1>(c);
  }
  GAP();
  if constexpr (M == 4) at_mask<X>(c);
  if constexpr (M >= 4 && M < 15) {
    constexpr int s0 = 3 * (M - 4);
#pragma unroll
    for (int s = s0; s < s0 + 3; ++s) {
      if (s < 32) {
        const int q = s >> 4, i = s & 15, ch = i & 3, kb = ch >> 1, e = (ch & 1) * 8 + (i >> 2) * 2;
        if (i < 4) c.pmx[q][ch] = max2f(c.S[X][kb][q][e], c.S[X][kb][q][e + 1]);
        else c.pmx[q][ch] = max3f(c.pmx[q][ch], c.S[X][kb][q][e], c.S[X][kb][q][e + 1]);
      }
    }
  }
  if constexpr (M == 15 || M == 16) {
    constexpr int q = M - 15;
    const float mm = max2f(max3f(c.pmx[q][0], c.pmx[q][1], c.pmx[q][2]), c.pmx[q][3]);
    auto pr = __builtin_amdgcn_permlane32_swap(__float_as_uint(mm), __float_as_uint(mm), false, false);
    c.rmax[q] = max2f(__uint_as_float(pr[0]), __uint_as_float(pr[1]));
  }
  if constexpr (M == 17) {
    if constexpr (PEEL) {
#pragma unroll
      for (int q = 0; q < 2; ++q) {
        c.mhat[q] = max2f(c.rmax[q], -1e30f); c.lsum[q] = 0.f; c.fct[q] = 1.f;
#pragma unroll
        for (int kb = 0; kb < 2; ++kb)
#pragma unroll
          for (int e = 0; e < 16; ++e) c.S[X][kb][q][e] = fsub_s(c.S[X][kb][q][e], c.mhat[q]);
        negm_bcast(c.NEGM[q], -c.mhat[q]);
      }
    } else {
      const bool grew = max2f(c.rmax[0], c.rmax[1]) > AT_THR;
      if (__builtin_expect(__any((int)grew), 0)) {
#pragma unroll
        for (int q = 0; q < 2; ++q) {
          const float dl = max2f(c.rmax[q], 0.f);
          c.mhat[q] = fadd_s(c.mhat[q], dl); c.fct[q] = __builtin_amdgcn_exp2f(-dl);
#pragma unroll
          for (int kb = 0; kb < 2; ++kb)
#pragma unroll
            for (int e = 0; e < 16; ++e) c.S[X][kb][q][e] = fsub_s(c.S[X][kb][q][e], dl);
          negm_bcast(c.NEGM[q], -c.mhat[q]);
        }
        c.pend = true;
      }
    }
    c.rs0[0] = c.rs0[1] = c.rs1[0] = c.rs1[1] = 0.f;
  }
  if constexpr (M >= 18) {
    constexpr int g = M - 18, e_lo = (32 * g) / 14, e_hi = (32 * (g + 1)) / 14;
    constexpr int p_lo = g ? (32 * (g - 1)) / 14 : 0, p_hi = g ? (32 * g) / 14 : 0;
#pragma unroll
    for (int e = e_lo; e < e_hi; ++e) c.S[X][0][e >> 4][e & 15] = __builtin_amdgcn_exp2f(c.S[X][0][e >> 4][e & 15]);
#pragma unroll
    for (int e = p_lo; e < p_hi; ++e) {
      if (e & 1) c.rs1[e >> 4] = fadd_s(c.rs1[e >> 4], c.S[X][0][e >> 4][e & 15]);
      else c.rs0[e >> 4] = fadd_s(c.rs0[e >> 4], c.S[X][0][e >> 4][e & 15]);
    }
  }
  if constexpr (M == 31) asm volatile("" ::"v"(c.S[X][0][0]), "v"(c.S[X][0][1]), "v"(c.NEGM[0]), "v"(c.NEGM[1]), "v"(c.rs0[0]), "v"(c.rs0[1]), "v"(c.rs1[0]), "v"(c.rs1[1]));
  GAP();
}
template <int I> DI void o_scale1(float f) {
  float t;
  asm volatile("v_accvgpr_read_b32 %0, a[%c2]\n\tv_mul_f32 %0, %1, %0\n\tv_accvgpr_write_b32 a[%c2], %0" : "=&v"(t) : "v"(f), "i"(I) : ACLOB_LO);
}
template <int... Is> DI void o_scale(const AC& c, iseq<Is...>) { (o_scale1<Is>(c.fct[(Is >> 4) & 1]), ...); }
template <int X, bool PEEL, int... Ms> DI void phaseB_(AC& c, iseq<Ms...>) {
  (gapB<X, PEEL, Ms>(c), ...);
#pragma unroll
  for (int e = (32 * 13) / 14; e < 32; ++e) {
    if (e & 1) c.rs1[e >> 4] = fadd_s(c.rs1[e >> 4], c.S[X][0][e >> 4][e & 15]);
    else c.rs0[e >> 4] = fadd_s(c.rs0[e >> 4], c.S[X][0][e >> 4][e & 15]);
  }
  if constexpr (!PEEL) {
    if (__builtin_expect(c.pend, 0)) {
      asm volatile("s_nop 7\n\ts_nop 7" ::: "memory");
      o_scale(c, make_iseq<128>{});
      asm volatile("s_nop 1" ::: "memory");
      c.lsum[0] = fmul_s(c.lsum[0], c.fct[0]); c.lsum[1] = fmul_s(c.lsum[1], c.fct[1]); c.pend = false;
    }
  }
  c.dd += 2;
  c.mk ^= 65536u; c.mv ^= 65536u;
  asm volatile("s_waitcnt lgkmcnt(0)\n\tv_xor_b32_e32 %0, 0x10000, %0" : "+v"(c.rb)::"memory");
  GAP();
}
template <int X, bool PEEL> DI void phaseB(AC& c) { phaseB_<X, PEEL>(c, make_iseq<32>{}); }
template <int... Is> DI void k_reads(unsigned rb, iseq<Is...>) { (k_read<Is>(rb), ...); }
template <int... Is> DI void v_reads(AC& c, iseq<Is...>) { (v_read<Is>(c), ...); }
template <int... Is> DI void q_loads(const char* qp, unsigned voff, iseq<Is...>) { (q_load<Is>(qp, voff), ...); }
template <int X, int... Cs> DI void packs(AC& c, iseq<Cs...>) { (pack1<X, ((Cs >> 3) & 1), 2 * (Cs >> 4) + ((Cs >> 2) & 1), (Cs & 3)>(c), ...); }
template <int X, int... Js> DI void rowsums(AC& c, iseq<Js...>) { (rowsum1<X, Js>(c), ...); }
template <int M> DI void pv_drain1(AC& c, const char* qn) {
  pv_mfma<M>(c);
  if constexpr (M & 1) q_load<(M >> 1)>(qn, c.voff);
  GAP();
}
template <int... Ms> DI void pv_drain(AC& c, const char* qn, iseq<Ms...>) { (pv_drain1<Ms>(c, qn), ...); }
template <int X> DI f32x4 at_finish(AC& c, char* mlp, int wave, const char* qn) {
  asm volatile("" : "+v"(c.S[X][0][0]), "+v"(c.S[X][0][1]), "+v"(c.S[X][1][0]), "+v"(c.S[X][1][1]));
  v_reads(c, make_iseq<16>{});
#pragma unroll
  for (int e = 0; e < 32; ++e) c.S[X][1][e >> 4][e & 15] = __builtin_amdgcn_exp2f(c.S[X][1][e >> 4][e & 15]);
  packs<X>(c, make_iseq<32>{});
  rowsums<X>(c, iseq<8, 9, 10, 11, 12, 13, 14, 15, 24, 25, 26, 27, 28, 29, 30, 31>{});
  lsum_merge(c);
  *reinterpret_cast<f32x4*>(mlp + wave * 1024 + c.voff) = f32x4{c.mhat[0], c.lsum[0], c.mhat[1], c.lsum[1]};
  asm volatile("s_waitcnt lgkmcnt(0)\n\ts_barrier" : AT_VF::"memory");
  const f32x4 ml = *reinterpret_cast<const f32x4*>(mlp + (wave ^ 2) * 1024 + c.voff);
  GAP();
  pv_drain(c, qn, make_iseq<32>{});
  return ml;
}
template <int I> DI float o_read() { float r; asm volatile("v_accvgpr_read_b32 %0, a[%c1]" : "=v"(r) : "i"(I)); return r; }
template <int Q> DI f32x4 o_quad(float f) {
  return f32x4{fmul_s(o_read<4 * Q>(), f), fmul_s(o_read<4 * Q + 1>(), f), fmul_s(o_read<4 * Q + 2>(), f), fmul_s(o_read<4 * Q + 3>(), f)};
}
template <int Q0, int... Is> DI void o_send(char* dst, const float (&f)[2], iseq<Is...>) {
  ((*reinterpret_cast<f32x4*>(dst + Is * 1024) = o_quad<Q0 + Is>(f[(Is >> 2) & 1])), ...);
}
template <int Q0, int I> DI void o_fin1(char* crow, const char* src, const float (&f)[2]) {
  constexpr int Q = Q0 + I, db = Q >> 3, qb = (Q >> 2) & 1, qd = Q & 3;
  const f32x4 in = *reinterpret_cast<const f32x4*>(src + I * 1024), own = o_quad<Q>(f[qb]);
  u32x2 w;
  w[0] = pack2(fadd_s(own[0], in[0]), fadd_s(own[1], in[1]));
  w[1] = pack2(fadd_s(own[2], in[2]), fadd_s(own[3], in[3]));
  *reinterpret_cast<u32x2*>(crow + qb * (2 * 128 * 1024) + db * 1024 + qd * 256) = w;
}
template <int Q0, int... Is> DI void o_fin(char* crow, const char* src, const float (&f)[2], iseq<Is...>) { (o_fin1<Q0, Is>(crow, src, f), ...); }

constexpr float QSCALE = 0.08838834764831845f * 1.4426950408889634f;
DI void w_stage(const char* src, unsigned voffw, unsigned lds) {
#pragma unroll
  for (int ii = 0; ii < 8; ++ii) dma<0>(src + (ii >> 1) * 65536 + (ii & 1) * 128, voffw, lds + ii * 1024);
}
constexpr int QP_WB[4] = {98304, 32768, 65536, 98304};
struct QP {
  u32x4 x[16][2];
  u32x2 w[3][4][2];
  f32x4 t[2][4];
  u32x4 h[2][2];
  unsigned wa0, wa1, ta, voff, voffw, wl;
  const char *xsrc, *wsrc;
};
template <int S, int QB> DI void x_load(QP& q) {
  asm volatile("global_load_dwordx4 %0, %1, %2 offset:%c3" : "=v"(q.x[S & 15][QB]) : "v"(q.voff), "s"(q.xsrc + QB * 32768 + (S >> 2) * 4096), "i"((S & 3) * 1024) : "memory");
}
template <int S0, int... Is> DI void x_loads(QP& q, iseq<Is...>) { (x_load<S0 + (Is >> 1), Is & 1>(q), ...); }
template <int S, int G> DI void w_read(QP& q) {
  constexpr int off = (QP_WB[S >> 3] & 65535) + (S & 7) * 4096 + (G >> 1) * 512;
  const unsigned a = ((G & 1) ? q.wa1 : q.wa0) + (QP_WB[S >> 3] & 65536);
  asm volatile("ds_read_b64_tr_b16 %0, %1 offset:%c2" : "=v"(q.w[S % 3][G >> 1][G & 1]) : "v"(a), "i"(off) : "memory");
}
template <int S, int E> DI void t_read(QP& q) {
  asm volatile("ds_read_b128 %0, %1 offset:%c2" : "=v"(q.t[S & 1][E]) : "v"(q.ta), "i"(S * 128 + E * 16) : "memory");
}
template <int S, int G> DI void h_make(QP& q) {
  constexpr int qb = G >> 2, e = G & 3;
  float lo, hi;
  asm volatile("v_fma_mix_f32 %0, %2, %3, %5 op_sel_hi:[1,0,0]\n\tv_fma_mix_f32 %1, %2, %4, %6 op_sel:[1,0,0] op_sel_hi:[1,0,0]"
               : "=&v"(lo), "=&v"(hi) : "v"(q.x[S & 15][qb][e]), "v"(q.t[S & 1][e >> 1][(e & 1) * 2]), "v"(q.t[S & 1][e >> 1][(e & 1) * 2 + 1]),
                 "v"(q.t[S & 1][2 + (e >> 1)][(e & 1) * 2]), "v"(q.t[S & 1][2 + (e >> 1)][(e & 1) * 2 + 1]));
  unsigned d = pack2(lo, hi);
  asm volatile("" : "+v"(d));
  q.h[S & 1][qb][e] = d;
}
#define QP_T(i) "+v"(q.t[i][0]), "+v"(q.t[i][1]), "+v"(q.t[i][2]), "+v"(q.t[i][3])
#define QP_W(i) "+v"(q.w[i][0][0]), "+v"(q.w[i][0][1]), "+v"(q.w[i][1][0]), "+v"(q.w[i][1][1]), "+v"(q.w[i][2][0]), "+v"(q.w[i][2][1]), "+v"(q.w[i][3][0]), "+v"(q.w[i][3][1])
#define QP_X(i) "+v"(q.x[i][0]), "+v"(q.x[i][1]), "+v"(q.x[i + 1][0]), "+v"(q.x[i + 1][1]), "+v"(q.x[i + 2][0]), "+v"(q.x[i + 2][1]), "+v"(q.x[i + 3][0]), "+v"(q.x[i + 3][1]), \
                "+v"(q.x[i + 4][0]), "+v"(q.x[i + 4][1]), "+v"(q.x[i + 5][0]), "+v"(q.x[i + 5][1]), "+v"(q.x[i + 6][0]), "+v"(q.x[i + 6][1]), "+v"(q.x[i + 7][0]), "+v"(q.x[i + 7][1])
template <int S, int G> DI void qp_gap(QP& q, AC& c) {
  constexpr int db = G >> 1, qb = G & 1;
  const u32x4 wf = {q.w[S % 3][db][0][0], q.w[S % 3][db][0][1], q.w[S % 3][db][1][0], q.w[S % 3][db][1][1]};
  asm volatile("v_mfma_f32_32x32x16_bf16 a[%c2:%c3], %0, %1, a[%c2:%c3]" ::"v"(wf), "v"(q.h[S & 1][qb]), "i"(16 * G), "i"(16 * G + 15) : ACLOB_LO);
  GAP();
  if constexpr (S + 2 < 32) {
    if constexpr (G < 2) { t_read<S + 2, 2 * G>(q); t_read<S + 2, 2 * G + 1>(q); }
    else if constexpr (G < 6) { w_read<S + 2, 2 * (G - 2)>(q); w_read<S + 2, 2 * (G - 2) + 1>(q); }
  }
  if constexpr (S < 16 && G >= 6) x_load<S + 16, G - 6>(q);
  if constexpr (S + 1 < 32) h_make<S + 1, G>(q);
  if constexpr (G == 7) {
    if constexpr (S == 6) { w_stage(q.wsrc + 3 * 1048576, q.voffw, q.wl + QP_WB[3]); dmas(c.Kh, c.voff, c.mk, make_iseq<8>{}); }
    if constexpr (S == 14) dmas(c.Vh, c.voff, c.mv, make_iseq<8>{});
    if constexpr (S == 22) { dmas(c.Kh + 32768, c.voff, c.mk + 65536, make_iseq<8>{}); k_reads(c.rb, make_iseq<16>{}); }
  }
  GAP();
}
template <int S, int... Gs> DI void qp_step(QP& q, AC& c, iseq<Gs...>) {
  if constexpr (S == 6) asm volatile("s_waitcnt vmcnt(20) lgkmcnt(0)\n\ts_barrier" : QP_T((S + 1) & 1), QP_W(S % 3), QP_X(8)::"memory");
  else if constexpr (S == 14) asm volatile("s_waitcnt vmcnt(30) lgkmcnt(0)\n\ts_barrier" : QP_T((S + 1) & 1), QP_W(S % 3), QP_X(0)::"memory");
  else if constexpr (S == 22) { asm volatile("s_waitcnt vmcnt(0) lgkmcnt(0)\n\ts_barrier" : QP_T((S + 1) & 1), QP_W(S % 3), QP_X(8)::"memory"); asm volatile("" : QP_X(0)); }
  else if constexpr (S == 23) asm volatile("s_waitcnt lgkmcnt(15)" : QP_T((S + 1) & 1), QP_W(S % 3)::"memory");
  else if constexpr (S >= 30) asm volatile("s_waitcnt lgkmcnt(0)" : QP_T((S + 1) & 1), QP_W(S % 3)::"memory");
  else asm volatile("s_waitcnt lgkmcnt(8)" : QP_T((S + 1) & 1), QP_W(S % 3)::"memory");
  GAP();
  (qp_gap<S, Gs>(q, c), ...);
}
template <int... Ss> DI void qp_steps(QP& q, AC& c, iseq<Ss...>) { (qp_step<Ss>(q, c, make_iseq<8>{}), ...); }
template <int S, int... Gs> DI void qp_pre(QP& q, iseq<Gs...>) { (w_read<S, Gs>(q), ...); }
template <int... Gs> DI void qp_h0(QP& q, iseq<Gs...>) { (h_make<0, Gs>(q), ...); }
template <int I> DI u32x4 q_pack() {
  constexpr int r = 16 * (2 * ((I & 7) >> 1) + (I >> 3)) + 8 * (I & 1);
  return u32x4{pack2(o_read<r>(), o_read<r + 1>()), pack2(o_read<r + 2>(), o_read<r + 3>()),
               pack2(o_read<r + 4>(), o_read<r + 5>()), pack2(o_read<r + 6>(), o_read<r + 7>())};
}
template <int I> DI void q_keep(char* xch) {
  const u32x4 w = q_pack<I>();
  *reinterpret_cast<u32x4*>(xch + I * 1024) = w;
  asm volatile("v_accvgpr_write_b32 a[%c4], %0\n\tv_accvgpr_write_b32 a[%c5], %1\n\tv_accvgpr_write_b32 a[%c6], %2\n\tv_accvgpr_write_b32 a[%c7], %3"
               ::"v"(w[0]), "v"(w[1]), "v"(w[2]), "v"(w[3]), "i"(128 + 4 * I), "i"(129 + 4 * I), "i"(130 + 4 * I), "i"(131 + 4 * I) : ACLOB_HI);
}
template <int... Is> DI void q_keeps(char* xch, iseq<Is...>) { (q_keep<Is>(xch), ...); }
template <int... Is> DI void q_saves(char* dst, iseq<Is...>) { ((*reinterpret_cast<u32x4*>(dst + Is * 1024) = q_pack<Is>()), ...); }
template <int I> DI void q_fetch(unsigned a) {
  asm volatile("ds_read_b128 a[%c1:%c2], %0 offset:%c3" ::"v"(a), "i"(128 + 4 * I), "i"(131 + 4 * I), "i"(I * 1024) : "memory", ACLOB_HI);
}
template <int... Is> DI void q_fetches(unsigned a, iseq<Is...>) { (q_fetch<Is>(a), ...); }
template <int... Is> DI void o_zeros(iseq<Is...>) { (o_zero4<Is>(), ...); }

__global__ __launch_bounds__(256, 1) void k_attn(const char* __restrict__ xf, const u16* __restrict__ wq,
                                                 const float* __restrict__ mod, char* __restrict__ Qf,
                                                 const char* __restrict__ Ki,
                                                 const char* __restrict__ Vi, char* __restrict__ ctx,
                                                 const u16* __restrict__ wo, const u16* __restrict__ win,
                                                 const u16* __restrict__ wsp, const u16* __restrict__ wout) {
  __shared__ __attribute__((aligned(16))) char smem[AT_MISC + 4096 + 256];
  const int b = blockIdx.x, xcd = b & 7, j = b >> 3;
  const int head = xcd * 4 + (j & 3), pair = j >> 2;
  const int wave = __builtin_amdgcn_readfirstlane(threadIdx.x >> 6), kg = wave >> 1, rg = wave & 1;
  AC c;
  const unsigned ldsb = lds_addr(smem);
  c.voff = (threadIdx.x & 63) * 16u;
  c.rb = ldsb + kg * 16384 + c.voff;
  c.mk = ldsb + wave * 8192;
  c.mv = ldsb + 32768 + wave * 8192;
  c.Kh = Ki + (size_t)head * (32 * 16384) + wave * 8192;
  c.Vh = Vi + (size_t)head * (32 * 16384) + wave * 8192;
  c.end0 = 16 - pair;
  char* Qh = Qf + (size_t)head * (64 * 8192) + rg * 16384;
  STAMP(5, 0);
  STAMP(6, 0);
  {
    const int lane = threadIdx.x & 63, G = lane >> 4, pos = lane & 15;
    const int nh = G & 1, h = G >> 1, q4 = pos >> 2, p4 = lane & 3;
    constexpr int WB0 = QP_WB[0];
    QP q;
    q.voff = c.voff;
    q.voffw = (unsigned)(((pos >> 1) ^ (nh << 2)) * 8192 + G * 32 + (pos & 1) * 16);
    q.wsrc = reinterpret_cast<const char*>(wq) + head * 256 + wave * 262144;
    q.xsrc = xf + (size_t)((kg ? pair : 15 - pair) * 4 + rg * 2) * 32768;
    q.wl = ldsb + wave * 8192;
    q.wa0 = ldsb + (h * 8 + nh) * 256 + p4 * 8 + (q4 ^ (nh << 2)) * 32;
    q.wa1 = q.wa0 ^ 128u;
    q.ta = ldsb + AT_MISC + h * 64;
    f32x2 cav, sav;
    asm volatile("global_load_dwordx2 %0, %2, %3 offset:2048\n\tglobal_load_dwordx2 %1, %2, %3" : "=&v"(cav), "=&v"(sav) : "v"(threadIdx.x * 8u), "s"(mod) : "memory");
    w_stage(q.wsrc, q.voffw, q.wl + QP_WB[0]);
    x_loads<0>(q, make_iseq<16>{});
    w_stage(q.wsrc + 1048576, q.voffw, q.wl + QP_WB[1]);
    x_loads<8>(q, make_iseq<16>{});
    w_stage(q.wsrc + 2 * 1048576, q.voffw, q.wl + QP_WB[2]);
    o_zeros(make_iseq<32>{});
    asm volatile("s_waitcnt vmcnt(56)" : "+v"(cav), "+v"(sav)::"memory");
    {
      float* tp = reinterpret_cast<float*>(smem + AT_MISC) + (threadIdx.x >> 2) * 16 + (threadIdx.x & 3) * 2;
      *reinterpret_cast<f32x2*>(tp) = f32x2{(1.f + cav[0]) * QSCALE, (1.f + cav[1]) * QSCALE};
      *reinterpret_cast<f32x2*>(tp + 8) = f32x2{sav[0] * QSCALE, sav[1] * QSCALE};
    }
    STAMPN(6, 1);
    asm volatile("s_waitcnt vmcnt(32) lgkmcnt(0)\n\ts_barrier" : QP_X(0)::"memory");
    STAMPN(6, 2);
    t_read<0, 0>(q); t_read<0, 1>(q); t_read<0, 2>(q); t_read<0, 3>(q);
    qp_pre<0>(q, make_iseq<8>{});
    t_read<1, 0>(q); t_read<1, 1>(q); t_read<1, 2>(q); t_read<1, 3>(q);
    qp_pre<1>(q, make_iseq<8>{});
    asm volatile("s_waitcnt lgkmcnt(15)" : QP_T(0)::"memory");
    qp_h0(q, make_iseq<8>{});
    asm volatile("s_nop 1" ::: "memory");
    STAMPN(6, 3);
    STAMPN(6, 4);
    STAMPN(6, 5);
    qp_steps(q, c, make_iseq<32>{});
    STAMPN(6, 6);
    asm volatile("s_barrier\n\ts_nop 7\n\ts_nop 7" ::: "memory");
    c.kt = 2; c.vt = 1;
    if (kg == 0) q_keeps(smem + WB0 + rg * 16384 + c.voff, make_iseq<16>{});
    else q_saves(Qh + pair * 32768 + c.voff, make_iseq<16>{});
    barrier();
    if (kg) q_fetches(ldsb + WB0 + rg * 16384 + c.voff, make_iseq<16>{});
    asm volatile("s_waitcnt lgkmcnt(0)\n\tv_xor_b32_e32 %0, 0x10000, %0" : "+v"(c.rb)::"memory");
    c.mv ^= 65536u;
    STAMPN(6, 7);
    STAMPF(6);
  }
  for (int it = 0; it < 2; ++it) {
    const int qb = it ? pair : 15 - pair, T = qb + 1;
    c.dd = kg - 2 * qb - rg;
    c.pend = false;
    STAMP(5, it * 4 + 1);
    if (T & 1) {
      phaseA<1, true>(c, it);
      phaseB<1, true>(c);
    } else {
      phaseA<0, true>(c, it);
      phaseB<0, true>(c);
      phaseA<1, false>(c);
      phaseB<1, false>(c);
    }
    for (int t = (T - 1) >> 1; t > 0; --t) {
      if (t == 3) STAMP(4, 0);
      phaseA<0, false>(c);
      if (t == 3) STAMP(4, 1);
      phaseB<0, false>(c);
      if (t == 3) STAMP(4, 2);
      phaseA<1, false>(c);
      if (t == 3) STAMP(4, 3);
      phaseB<1, false>(c);
      if (t == 3) STAMP(4, 4);
    }
    const f32x4 ml = at_finish<1>(c, smem + AT_MISC, wave, Qh + pair * 32768);
    STAMP(5, it * 4 + 2);
    const unsigned ln = lane_id();
    float f[2];
#pragma unroll
    for (int q = 0; q < 2; ++q) {
      const float mt = fmaxf(c.mhat[q], ml[2 * q]);
      const float a0 = __builtin_amdgcn_exp2f(c.mhat[q] - mt), a1 = __builtin_amdgcn_exp2f(ml[2 * q] - mt);
      f[q] = a0 / fadd_s(fmul_s(c.lsum[q], a0), fmul_s(ml[2 * q + 1], a1));
    }
    asm volatile("s_nop 7\n\ts_nop 7" ::: "memory");
    const unsigned so = (c.rb - ldsb) & 65536u;
    char* a0p = smem + (so ^ 65536u) + rg * 16384 + ln * 16;
    char* a1p = smem + so + 32768 + rg * 16384 + ln * 16;
    char* crow = ctx + ((size_t)(qb * 8 + rg * 4 + ((ln >> 4) & 1)) * 128 + head * 4) * 1024 + (ln & 15) * 16 + (ln >> 5) * 8;
    if (kg == 0) o_send<16>(a0p, f, make_iseq<16>{});
    else o_send<0>(a1p, f, make_iseq<16>{});
    barrier();
    if (kg == 0) o_fin<0>(crow, a1p, f, make_iseq<16>{});
    else o_fin<16>(crow, a0p, f, make_iseq<16>{});
    asm volatile("s_waitcnt vmcnt(16)" ::: "memory");
    if (it == 0) {
#pragma unroll
      for (int u = 0; u < 2; ++u) {
        const int sl = (b * 4 + wave) * 2 + u;
        const u16* base = sl < 1024 ? wsp : sl < 1536 ? wo : sl < 1708 ? win : wout;
        const int nb = sl < 1024 ? S * S * 2 : sl < 1536 ? HDM * DM * 2 : sl < 1708 ? DM * FF * 2 : FH * DM * 2;
        const int first = sl < 1024 ? sl : sl < 1536 ? sl - 1024 : sl < 1708 ? sl - 1536 : sl - 1708;
        pf_slice(base, nb, first * 64, ln, smem + AT_MISC + 4096);
      }
      GAP();
    }
    STAMP(5, it * 4 + 3);
  }
  asm volatile("s_waitcnt vmcnt(0)" ::: "memory");
}

constexpr int SK_AREA = 16384;
template <int NT> struct SkOwn { static constexpr int N = (2 * NT + 3) / 4; };
template <int NT>
DI void splitk_reduce(const f32x4 (&acc)[4][NT], f32x4 (&own)[SkOwn<NT>::N], char* smem, int wave, int lane) {
  const int hi = wave >> 2, wq = wave & 3;
  char* mine = smem + wave * SK_AREA + lane * 16;
  f32x4 r[2 * NT];
#pragma unroll
  for (int f = 0; f < 2 * NT; ++f) {
    const f32x4 lo = acc[f / NT][f % NT], up = acc[2 + f / NT][f % NT];
    f32x4 snd, kp;
#pragma unroll
    for (int e = 0; e < 4; ++e) { snd[e] = hi ? lo[e] : up[e]; kp[e] = hi ? up[e] : lo[e]; }
    *reinterpret_cast<f32x4*>(mine + f * 1024) = snd;
    r[f] = kp;
  }
  barrier();
  {
    const char* part = smem + (wave ^ 4) * SK_AREA + lane * 16;
#pragma unroll
    for (int f = 0; f < 2 * NT; ++f) {
      const f32x4 o = *reinterpret_cast<const f32x4*>(part + f * 1024);
      r[f] += o;
    }
  }
  barrier();
#pragma unroll
  for (int f = 0; f < 2 * NT; ++f) *reinterpret_cast<f32x4*>(mine + f * 1024) = r[f];
  barrier();
#pragma unroll
  for (int i = 0; i < SkOwn<NT>::N; ++i) {
    const int f = wq + 4 * i;
    const char* p = smem + hi * 4 * SK_AREA + lane * 16 + (f < 2 * NT ? f : 0) * 1024;
    const f32x4 a = *reinterpret_cast<const f32x4*>(p), b = *reinterpret_cast<const f32x4*>(p + SK_AREA);
    const f32x4 c = *reinterpret_cast<const f32x4*>(p + 2 * SK_AREA), d = *reinterpret_cast<const f32x4*>(p + 3 * SK_AREA);
    own[i] = (a + b) + (c + d);
  }
}
DI void sk_bfrags(const char* img, bf16x8 (&bfr)[4], int lane) {
  const int fr = lane & 15, fg = lane >> 4, q = fr >> 2, p = lane & 3, sw = (fg & 1) << 2;
  const char* base = img + fg * 1024 + p * 8;
#pragma unroll
  for (int nt = 0; nt < 4; ++nt) {
    s16x4 lo = tr_read(base + nt * 256 + (q ^ sw) * 32);
    s16x4 hi = tr_read(base + nt * 256 + ((4 + q) ^ sw) * 32);
    bfr[nt] = __builtin_shufflevector(lo, hi, 0, 1, 2, 3, 4, 5, 6, 7);
  }
}

constexpr int OP_NS = 16, OP_DB = 4, OP_DA = 6;
constexpr int op_wait(int t) {
  int pos = 0, bpos = -1;
  for (int i = 0; i < OP_DA; ++i) {
    if (i < OP_DB) { pos += 4; if (i == t) bpos = pos; }
    pos += 4;
  }
  for (int s = 0; s < t; ++s) {
    if (s + OP_DB < OP_NS) { pos += 4; if (s + OP_DB == t) bpos = pos; }
    if (s + OP_DA < OP_NS) pos += 4;
  }
  return pos - bpos;
}
template <int T> struct OpWait { static constexpr int v = op_wait(T); };
template <int T> DI void op_step(f32x4 (&acc)[4][4], u32x4 (&areg)[OP_DA][4], char* ring, const char* asrc,
                                 const u16* bs0, const u16* bs1, int g0, int lane) {
  wait_vm<OpWait<T>::v>();
  if constexpr (T == 0) STAMP(2, 2);
  if constexpr (T == 8) STAMP(2, 3);
  bf16x8 bfr[4];
  sk_bfrags(ring + (T % OP_DB) * 4096, bfr, lane);
#pragma unroll
  for (int mt = 0; mt < 4; ++mt)
#pragma unroll
    for (int nt = 0; nt < 4; ++nt)
      acc[mt][nt] = __builtin_amdgcn_mfma_f32_16x16x32_bf16(bfr[nt], __builtin_bit_cast(bf16x8, areg[T % OP_DA][mt]), acc[mt][nt], 0, 0, 0);
  if constexpr (T + OP_DB < OP_NS) {
    asm volatile("s_waitcnt lgkmcnt(0)" ::: "memory");
    const int g = (g0 + T + OP_DB) & 127;
#pragma unroll
    for (int i = 0; i < 4; ++i)
      glds16(((i & 1) ? bs1 : bs0) + (size_t)(g * 32 + i * 8) * DM, ring + (T % OP_DB) * 4096 + i * 1024);
  }
  if constexpr (T + OP_DA < OP_NS) {
    const int g = (g0 + T + OP_DA) & 127;
#pragma unroll
    for (int mt = 0; mt < 4; ++mt)
      areg[T % OP_DA][mt] = *reinterpret_cast<const u32x4*>(asrc + (size_t)mt * (128 * 1024) + g * 1024);
  }
  if constexpr (T + 1 < OP_NS) op_step<T + 1>(acc, areg, ring, asrc, bs0, bs1, g0, lane);
}
__global__ __launch_bounds__(512, 2) void k_oproj(const char* __restrict__ ctxf, const u16* __restrict__ wo,
                                                  const u16* __restrict__ x, const float* __restrict__ ga,
                                                  u16* __restrict__ x1) {
  __shared__ __attribute__((aligned(16))) char smem[8 * SK_AREA + 256];
  const int b = blockIdx.x, xcd = b & 7, j = b >> 3;
  const int mtile = xcd * 4 + (j >> 3), ntile = j & 7;
  const int tid = threadIdx.x, wave = __builtin_amdgcn_readfirstlane(tid >> 6), lane = tid & 63;
  const int m0 = mtile * 64, n0 = ntile * 64;
  char* ring = smem + wave * SK_AREA;
  const char* asrc = ctxf + (size_t)(mtile * 4) * 128 * 1024 + lane * 16;
  const int g0 = wave * 16;
  const int rw = (lane & 15) >> 1;
  const u16* bs0 = wo + (size_t)rw * DM + n0 + (lane >> 4) * 16 + (lane & 1) * 8;
  const u16* bs1 = wo + (size_t)(rw ^ 4) * DM + n0 + (lane >> 4) * 16 + (lane & 1) * 8;
  f32x4 acc[4][4];
#pragma unroll
  for (int i = 0; i < 4; ++i)
#pragma unroll
    for (int jj = 0; jj < 4; ++jj) acc[i][jj] = f32x4{0.f, 0.f, 0.f, 0.f};
  u32x4 areg[OP_DA][4];
  STAMP(2, 0);
#pragma unroll
  for (int i = 0; i < OP_DA; ++i) {
    const int g = (g0 + i) & 127;
    if (i < OP_DB) {
#pragma unroll
      for (int ii = 0; ii < 4; ++ii) glds16(((ii & 1) ? bs1 : bs0) + (size_t)(g * 32 + ii * 8) * DM, ring + i * 4096 + ii * 1024);
    }
#pragma unroll
    for (int mt = 0; mt < 4; ++mt)
      areg[i][mt] = *reinterpret_cast<const u32x4*>(asrc + (size_t)mt * (128 * 1024) + g * 1024);
  }
  STAMP(2, 1);
  op_step<0>(acc, areg, ring, asrc, bs0, bs1, g0, lane);
  STAMP(2, 4);
  const int fr = lane & 15, fg = lane >> 4, hi = wave >> 2, wq = wave & 3;
  u32x2 xv[2];
  f32x4 gv;
  {
    gv = *reinterpret_cast<const f32x4*>(ga + n0 + wq * 16 + fg * 4);
#pragma unroll
    for (int i = 0; i < 2; ++i)
      xv[i] = *reinterpret_cast<const u32x2*>(x + (size_t)(m0 + (2 * hi + i) * 16 + fr) * DM + n0 + wq * 16 + fg * 4);
  }
  f32x4 own[2];
  splitk_reduce<4>(acc, own, smem, wave, lane);
#pragma unroll
  for (int i = 0; i < 2; ++i) {
    u32x2 w;
    w[0] = pack2(bflo(xv[i][0]) + gv[0] * own[i][0], bfhi(xv[i][0]) + gv[1] * own[i][1]);
    w[1] = pack2(bflo(xv[i][1]) + gv[2] * own[i][2], bfhi(xv[i][1]) + gv[3] * own[i][3]);
    *reinterpret_cast<u32x2*>(x1 + (size_t)(m0 + (2 * hi + i) * 16 + fr) * DM + n0 + wq * 16 + fg * 4) = w;
  }
  STAMP(2, 5);
}

constexpr int NBLK = 43;
using G4 = Gemm<64, 192, 1, 2, 4>;
DI float gelu_tanh(float x) {
  constexpr float K1 = -2.f * 0.7978845608028654f * 1.4426950408889634f, K2 = K1 * 0.044715f;
  const float e = __builtin_amdgcn_exp2f(x * fmaf(x * x, K2, K1));
  return x * __builtin_amdgcn_rcpf(1.f + e);
}
DI u32x4 gload_asm(const void* p) {
  u32x4 r;
  asm volatile("global_load_dwordx4 %0, %1, off" : "=v"(r) : "v"(p) : "memory");
  return r;
}
template <bool Z2>
DI void win_body(char* smem, const u16* __restrict__ x1, const float* __restrict__ sb, const float* __restrict__ cb,
                 const u16* __restrict__ win, char* __restrict__ zf, float* __restrict__ stats, int mtile, int t4) {
  char* sB = smem + 65536;
  const int tid = threadIdx.x, wave = __builtin_amdgcn_readfirstlane(tid >> 6), lane = tid & 63;
  const int wm = wave >> 2, wn = wave & 3;
  const int m0 = mtile * 64, n0 = (Z2 ? FH : 0) + t4 * 176;
  STAMP(1, 0);
  u32x4 c0 = gload_asm(cb + lane * 8), c1 = gload_asm(cb + lane * 8 + 4);
  u32x4 s0 = gload_asm(sb + lane * 8), s1 = gload_asm(sb + lane * 8 + 4);
  {
    const int rr = lane >> 3, ch = (lane & 7) ^ rr;
    const u16* src = x1 + (size_t)(m0 + wave * 8 + rr) * DM + ch * 8;
#pragma unroll
    for (int sub = 0; sub < 8; ++sub) glds16(src + sub * 64, smem + sub * 8192 + wave * 1024);
  }
#pragma unroll
  for (int st = 0; st < 4; ++st) G4::stageB(win, FF, st * 64, n0, FF - 8, sB + st * G4::B_BYTES, wave, lane);
  u32x4 breg[4][3];
  {
    const int pos = lane & 15, rw = pos >> 1, hf = pos & 1;
    const u16* src[3];
#pragma unroll
    for (int ii = 0; ii < 3; ++ii) {
      const int blk = (ii * 8 + wave) * 4 + (lane >> 4), kb = blk / G4::NB, nb = blk % G4::NB;
      int n = n0 + nb * 16 + hf * 8;
      if (n > FF - 8) n = FF - 8;
      src[ii] = win + (size_t)(256 + kb * 8 + (rw ^ ((kb & 1) << 2))) * FF + n;
    }
#pragma unroll
    for (int st = 0; st < 4; ++st) {
#pragma unroll
      for (int ii = 0; ii < 3; ++ii) breg[st][ii] = *reinterpret_cast<const u32x4*>(src[ii] + (size_t)st * 64 * FF);
      __builtin_amdgcn_sched_barrier(0);
    }
  }
  asm volatile("s_waitcnt vmcnt(24)" : "+v"(c0), "+v"(c1), "+v"(s0), "+v"(s1)::"memory");
  STAMP(1, 1);
  {
    float cbv[8], sbv[8];
#pragma unroll
    for (int jj = 0; jj < 4; ++jj) {
      cbv[jj] = 1.f + __uint_as_float(c0[jj]); cbv[4 + jj] = 1.f + __uint_as_float(c1[jj]);
      sbv[jj] = __uint_as_float(s0[jj]); sbv[4 + jj] = __uint_as_float(s1[jj]);
    }
    u32x4 xr[8];
#pragma unroll
    for (int i = 0; i < 8; ++i)
      xr[i] = *reinterpret_cast<const u32x4*>(smem + (lane >> 3) * 8192 + (wave * 8 + i) * 128 + (((lane & 7) ^ i) << 4));
#pragma unroll
    for (int i = 0; i < 8; ++i) {
      float f[8];
#pragma unroll
      for (int jj = 0; jj < 4; ++jj) { f[2 * jj] = bflo(xr[i][jj]); f[2 * jj + 1] = bfhi(xr[i][jj]); }
      float s = 0.f;
#pragma unroll
      for (int jj = 0; jj < 8; ++jj) s += f[jj];
      const float mean = wave_sum(s) * (1.f / DM);
      float v = 0.f;
#pragma unroll
      for (int jj = 0; jj < 8; ++jj) { f[jj] -= mean; v += f[jj] * f[jj]; }
      const float rstd = rsqrtf(wave_sum(v) * (1.f / DM) + 1e-5f);
#pragma unroll
      for (int jj = 0; jj < 4; ++jj)
        xr[i][jj] = pack2(f[2 * jj] * rstd * cbv[2 * jj] + sbv[2 * jj], f[2 * jj + 1] * rstd * cbv[2 * jj + 1] + sbv[2 * jj + 1]);
    }
#pragma unroll
    for (int i = 0; i < 8; ++i)
      *reinterpret_cast<u32x4*>(smem + (lane >> 3) * 8192 + (wave * 8 + i) * 128 + (((lane & 7) ^ i) << 4)) = xr[i];
  }
  STAMP(1, 2);
  f32x4 acc[G4::MT][G4::NT];
#pragma unroll
  for (int i = 0; i < G4::MT; ++i)
#pragma unroll
    for (int jj = 0; jj < G4::NT; ++jj) acc[i][jj] = f32x4{0.f, 0.f, 0.f, 0.f};
#pragma unroll
  for (int kt = 0; kt < 8; ++kt) {
    if (kt == 0) wait_vm<21>(); else if (kt == 1) wait_vm<18>(); else if (kt == 2) wait_vm<15>(); else if (kt == 3) wait_vm<12>();
    barrier();
    if (kt == 0) STAMP(1, 3);
    if (kt == 1) STAMP(1, 4);
    if (kt == 3) STAMP(1, 5);
    if (kt == 5) STAMP(1, 6);
    if (kt >= 1 && kt + 3 < 8) {
#pragma unroll
      for (int ii = 0; ii < 3; ++ii)
        *reinterpret_cast<u32x4*>(sB + ((kt + 3) & 3) * G4::B_BYTES + (ii * 8 + wave) * 1024 + lane * 16) = breg[kt - 1][ii];
    }
    G4::compute<!Z2>(smem + kt * 8192, sB + (kt & 3) * G4::B_BYTES, acc, wm, wn, lane);
  }
  STAMP(1, 7);
  const int fr = lane & 15, fg = lane >> 4;
  if constexpr (!Z2) {
#pragma unroll
    for (int nt = 0; nt < G4::NT; ++nt) {
      const int nbl = wn * 3 + nt, nb = t4 * 11 + nbl;
      if (nbl < 11 && nb < NBLK) {
#pragma unroll
        for (int mt = 0; mt < G4::MT; ++mt) {
          u32x2 w;
          w[0] = pack2(gelu_tanh(acc[mt][nt][0]), gelu_tanh(acc[mt][nt][1]));
          w[1] = pack2(gelu_tanh(acc[mt][nt][2]), gelu_tanh(acc[mt][nt][3]));
          *reinterpret_cast<u32x2*>(zf + ((size_t)(mtile * 4 + wm * 2 + mt) * NBLK + nb) * 512 + lane * 8) = w;
        }
      }
    }
  } else {
    float p1[G4::MT][4], p2[G4::MT][4];
#pragma unroll
    for (int mt = 0; mt < G4::MT; ++mt)
#pragma unroll
      for (int i = 0; i < 4; ++i) { p1[mt][i] = 0.f; p2[mt][i] = 0.f; }
#pragma unroll
    for (int nt = 0; nt < G4::NT; ++nt) {
      const int nbl = wn * 3 + nt, nb = t4 * 11 + nbl;
      if (nbl < 11 && nb < NBLK) {
#pragma unroll
        for (int mt = 0; mt < G4::MT; ++mt) {
          float gz[4];
#pragma unroll
          for (int i = 0; i < 4; ++i) {
            gz[i] = rbf(gelu_tanh(acc[mt][nt][i]));
            p1[mt][i] += gz[i]; p2[mt][i] += gz[i] * gz[i];
          }
          u32x2 w;
          w[0] = pack2(gz[0], gz[1]);
          w[1] = pack2(gz[2], gz[3]);
          *reinterpret_cast<u32x2*>(zf + ((size_t)(mtile * 2 + wm) * NBLK + nb) * 1024 + (fr + 16 * (mt * 2 + (fg >> 1))) * 16 + (fg & 1) * 8) = w;
        }
      }
    }
    barrier();
    float* red = reinterpret_cast<float*>(smem);
#pragma unroll
    for (int mt = 0; mt < G4::MT; ++mt)
#pragma unroll
      for (int i = 0; i < 4; ++i) {
        float a = p1[mt][i], c = p2[mt][i];
        a = row16_sum(a); c = row16_sum(c);
        if (fr == 0) {
          const int rowl = wm * 32 + mt * 16 + fg * 4 + i;
          red[(wn * 64 + rowl) * 2] = a; red[(wn * 64 + rowl) * 2 + 1] = c;
        }
      }
    barrier();
    if (tid < 128) {
      const float v = (red[tid] + red[128 + tid]) + (red[256 + tid] + red[384 + tid]);
      stats[((size_t)t4 * S + m0) * 2 + tid] = v;
    }
  }
}
__global__ __launch_bounds__(512, 2) void k_win(const u16* __restrict__ x1, const float* __restrict__ sb,
                                                const float* __restrict__ cb, const u16* __restrict__ win,
                                                char* __restrict__ z1f, char* __restrict__ z2f,
                                                float* __restrict__ stats) {
  __shared__ __attribute__((aligned(16))) char smem[65536 + 4 * G4::B_BYTES];
  const int b = blockIdx.x, xcd = b & 7, j = b >> 3;
  const int mtile = xcd * 4 + (j >> 3), ntile = j & 7;
  if (ntile >= 4) win_body<true>(smem, x1, sb, cb, win, z2f, stats, mtile, ntile - 4);
  else win_body<false>(smem, x1, sb, cb, win, z1f, stats, mtile, ntile);
}

constexpr int UKS = 22;
template <int NT>
DI void spatial_body(char* smem, const u16* __restrict__ wsp, const char* __restrict__ z2f,
                     const float* __restrict__ stats, const u16* __restrict__ bsp, const char* __restrict__ z1f,
                     char* __restrict__ uf, int mtile, int nb0, int rot) {
  const int tid = threadIdx.x, wave = __builtin_amdgcn_readfirstlane(tid >> 6), lane = tid & 63;
  const int fr = lane & 15, fg = lane >> 4;
  char* ring = smem + wave * SK_AREA;
  float* rs = reinterpret_cast<float*>(smem + 8 * SK_AREA + wave * 2048);
  float* ms = rs + 256;
  const int m0 = mtile * 64, g0 = wave * 4 + rot;
  STAMP(3, 0);
  f32x4 sp[4][2];
#pragma unroll
  for (int t = 0; t < 4; ++t) {
    const float* p = stats + ((size_t)t * S + ((g0 + (lane >> 4)) & 31) * 64 + (lane & 15) * 4) * 2;
    sp[t][0] = *reinterpret_cast<const f32x4*>(p);
    sp[t][1] = *reinterpret_cast<const f32x4*>(p + 4);
  }
  const int rr = lane >> 3, ch = (lane & 7) ^ rr;
  const u16* asrc = wsp + (size_t)(m0 + rr) * S + ch * 8;
  auto stageA = [&](int s) {
#pragma unroll
    for (int i = 0; i < 8; ++i) glds16(asrc + (size_t)i * 8 * S + ((g0 + s) & 31) * 64, ring + (s & 1) * 8192 + i * 1024);
  };
  const char* bsrc = z2f + (size_t)nb0 * 1024 + lane * 16;
  u32x4 breg[3][NT];
  auto loadB = [&](int t) {
#pragma unroll
    for (int nt = 0; nt < NT; ++nt) breg[t % 3][nt] = *reinterpret_cast<const u32x4*>(bsrc + (((((g0 + (t >> 1)) & 31) * 2 + (t & 1)) * NBLK) + nt) * 1024);
  };
  stageA(0); loadB(0); loadB(1); stageA(1); loadB(2);
  STAMP(3, 1);
  {
    f32x4 rv, mv;
#pragma unroll
    for (int i = 0; i < 4; ++i) {
      float s1 = 0.f, s2 = 0.f;
#pragma unroll
      for (int t = 0; t < 4; ++t) { s1 += sp[t][i >> 1][(i & 1) * 2]; s2 += sp[t][i >> 1][(i & 1) * 2 + 1]; }
      const float mean = s1 * (1.f / FH);
      const float var = fmaxf(s2 * (1.f / FH) - mean * mean, 0.f);
      const float rstd = rsqrtf(var + 1e-5f);
      rv[i] = rstd; mv[i] = -mean * rstd;
    }
    *reinterpret_cast<f32x4*>(rs + lane * 4) = rv;
    *reinterpret_cast<f32x4*>(ms + lane * 4) = mv;
  }
  f32x4 acc[4][NT];
#pragma unroll
  for (int i = 0; i < 4; ++i)
#pragma unroll
    for (int jj = 0; jj < NT; ++jj) acc[i][jj] = f32x4{0.f, 0.f, 0.f, 0.f};
#pragma unroll
  for (int s = 0; s < 4; ++s) {
    if (s < 2) wait_vm<3 * NT + 8>(); else if (s == 2) wait_vm<2 * NT + 8>(); else wait_vm<NT>();
    if (s == 0) STAMP(3, 2);
    if (s == 2) STAMP(3, 3);
#pragma unroll
    for (int kk = 0; kk < 2; ++kk) {
      const int t = 2 * s + kk;
      bf16x8 af[4];
#pragma unroll
      for (int mt = 0; mt < 4; ++mt)
        af[mt] = *reinterpret_cast<const bf16x8*>(ring + (s & 1) * 8192 + (mt * 16 + fr) * 128 + (((kk * 4 + fg) ^ (fr & 7)) << 4));
      f32x4 rv[2], mv[2];
#pragma unroll
      for (int e = 0; e < 2; ++e) {
        rv[e] = *reinterpret_cast<const f32x4*>(rs + t * 32 + fg * 8 + e * 4);
        mv[e] = *reinterpret_cast<const f32x4*>(ms + t * 32 + fg * 8 + e * 4);
      }
#pragma unroll
      for (int nt = 0; nt < NT; ++nt) {
        const u32x4 raw = breg[t % 3][nt];
        u32x4 o4;
#pragma unroll
        for (int jj = 0; jj < 4; ++jj)
          o4[jj] = pack2(fmaf(bflo(raw[jj]), rv[jj >> 1][(jj & 1) * 2], mv[jj >> 1][(jj & 1) * 2]),
                         fmaf(bfhi(raw[jj]), rv[jj >> 1][(jj & 1) * 2 + 1], mv[jj >> 1][(jj & 1) * 2 + 1]));
        const bf16x8 bv = __builtin_bit_cast(bf16x8, o4);
#pragma unroll
        for (int mt = 0; mt < 4; ++mt)
          acc[mt][nt] = __builtin_amdgcn_mfma_f32_16x16x32_bf16(bv, af[mt], acc[mt][nt], 0, 0, 0);
      }
      if (t + 3 < 8) loadB(t + 3);
    }
    if (s + 2 < 4) {
      asm volatile("s_waitcnt lgkmcnt(0)" ::: "memory");
      stageA(s + 2);
    }
  }
  STAMP(3, 4);
  constexpr int NO = SkOwn<NT>::N;
  const int hi = wave >> 2, wq = wave & 3;
  u32x2 zv[NO];
  float bs[NO];
#pragma unroll
  for (int i = 0; i < NO; ++i) {
    int f = wq + 4 * i;
    if (f >= 2 * NT) f = 0;
    const int mt = 2 * hi + f / NT, nb = nb0 + f % NT;
    zv[i] = *reinterpret_cast<const u32x2*>(z1f + ((size_t)(mtile * 4 + mt) * NBLK + nb) * 512 + lane * 8);
    bs[i] = bf2f(bsp[m0 + mt * 16 + fr]);
  }
  f32x4 own[NO];
  splitk_reduce<NT>(acc, own, smem, wave, lane);
#pragma unroll
  for (int i = 0; i < NO; ++i) {
    const int f = wq + 4 * i;
    if (f < 2 * NT) {
      const int mt = 2 * hi + f / NT, nb = nb0 + f % NT;
      u32x2 w;
      w[0] = pack2(bflo(zv[i][0]) * (own[i][0] + bs[i]), bfhi(zv[i][0]) * (own[i][1] + bs[i]));
      w[1] = pack2(bflo(zv[i][1]) * (own[i][2] + bs[i]), bfhi(zv[i][1]) * (own[i][3] + bs[i]));
      char* dst = uf + ((size_t)(mtile * 4 + mt) * UKS + (nb >> 1)) * 1024 + (fr + 16 * (fg >> 1)) * 16 + (fg & 1) * 8;
      *reinterpret_cast<u32x2*>(dst + (nb & 1) * 512) = w;
      if (nb == NBLK - 1) *reinterpret_cast<u32x2*>(dst + 512) = u32x2{0u, 0u};
    }
  }
  STAMP(3, 5);
}
__global__ __launch_bounds__(512, 2) void k_spatial(const u16* __restrict__ wsp, const char* __restrict__ z2f,
                                                    const float* __restrict__ stats, const u16* __restrict__ bsp,
                                                    const char* __restrict__ z1f, char* __restrict__ uf) {
  __shared__ __attribute__((aligned(16))) char smem[8 * SK_AREA + 8 * 2048 + 256];
  const int b = blockIdx.x, xcd = b & 7, j = b >> 3;
  const int mtile = xcd * 4 + (j >> 3), ntile = j & 7;
  const int rot = 0;
  if (ntile < 5) spatial_body<5>(smem, wsp, z2f, stats, bsp, z1f, uf, mtile, ntile * 5, rot);
  else spatial_body<6>(smem, wsp, z2f, stats, bsp, z1f, uf, mtile, 25 + (ntile - 5) * 6, rot);
}

__global__ __launch_bounds__(512, 2) void k_wout(const char* __restrict__ uf, const u16* __restrict__ wout,
                                                 const u16* __restrict__ x1, const float* __restrict__ gb,
                                                 u16* __restrict__ out) {
  __shared__ __attribute__((aligned(16))) char smem[8 * SK_AREA];
  const int b = blockIdx.x, xcd = b & 7, j = b >> 3;
  const int mtile = xcd * 4 + (j >> 3), ntile = j & 7;
  const int tid = threadIdx.x, wave = __builtin_amdgcn_readfirstlane(tid >> 6), lane = tid & 63;
  const int m0 = mtile * 64, n0 = ntile * 64;
  char* ring = smem + wave * SK_AREA;
  const int ks0 = wave < 6 ? wave * 3 : 18 + (wave - 6) * 2, nks = wave < 6 ? 3 : 2;
  const int rw = (lane & 15) >> 1;
  u32x4 areg[3][4];
#pragma unroll
  for (int t = 0; t < 3; ++t) {
    const int ks = ks0 + (t < nks ? t : 0);
#pragma unroll
    for (int i = 0; i < 4; ++i) {
      int row = ks * 32 + i * 8 + ((i & 1) ? (rw ^ 4) : rw);
      if (row > FH - 1) row = FH - 1;
      glds16(wout + (size_t)row * DM + n0 + (lane >> 4) * 16 + (lane & 1) * 8, ring + t * 4096 + i * 1024);
    }
#pragma unroll
    for (int mt = 0; mt < 4; ++mt)
      areg[t][mt] = *reinterpret_cast<const u32x4*>(uf + ((size_t)(mtile * 4 + mt) * UKS + ks) * 1024 + lane * 16);
  }
  const int fr = lane & 15, fg = lane >> 4, hi = wave >> 2, wq = wave & 3;
  u32x2 xv[2];
  const f32x4 gv = *reinterpret_cast<const f32x4*>(gb + n0 + wq * 16 + fg * 4);
#pragma unroll
  for (int i = 0; i < 2; ++i)
    xv[i] = *reinterpret_cast<const u32x2*>(x1 + (size_t)(m0 + (2 * hi + i) * 16 + fr) * DM + n0 + wq * 16 + fg * 4);
  f32x4 acc[4][4];
#pragma unroll
  for (int i = 0; i < 4; ++i)
#pragma unroll
    for (int jj = 0; jj < 4; ++jj) acc[i][jj] = f32x4{0.f, 0.f, 0.f, 0.f};
  wait_vm<0>();
#pragma unroll
  for (int t = 0; t < 3; ++t) {
    if (t < nks) {
      bf16x8 bfr[4];
      sk_bfrags(ring + t * 4096, bfr, lane);
#pragma unroll
      for (int mt = 0; mt < 4; ++mt)
#pragma unroll
        for (int nt = 0; nt < 4; ++nt)
          acc[mt][nt] = __builtin_amdgcn_mfma_f32_16x16x32_bf16(bfr[nt], __builtin_bit_cast(bf16x8, areg[t][mt]), acc[mt][nt], 0, 0, 0);
    }
  }
  f32x4 own[2];
  splitk_reduce<4>(acc, own, smem, wave, lane);
#pragma unroll
  for (int i = 0; i < 2; ++i) {
    u32x2 w;
    w[0] = pack2(bflo(xv[i][0]) + gv[0] * own[i][0], bfhi(xv[i][0]) + gv[1] * own[i][1]);
    w[1] = pack2(bflo(xv[i][1]) + gv[2] * own[i][2], bfhi(xv[i][1]) + gv[3] * own[i][3]);
    *reinterpret_cast<u32x2*>(out + (size_t)(m0 + (2 * hi + i) * 16 + fr) * DM + n0 + wq * 16 + fg * 4) = w;
  }
}

extern "C" void kernel_launch(void* const* d_in, const int* in_sizes, int n_in, void* d_out, int out_size,
                              void* d_ws, size_t ws_size, hipStream_t stream) {
  const u16* x = (const u16*)d_in[0];
  const u16* ckv = (const u16*)d_in[1];
  const u16* wq = (const u16*)d_in[2];
  const u16* wk = (const u16*)d_in[3];
  const u16* wv = (const u16*)d_in[4];
  const u16* wo = (const u16*)d_in[5];
  const u16* win = (const u16*)d_in[7];
  const u16* wsp = (const u16*)d_in[8];
  const u16* bsp = (const u16*)d_in[9];
  const u16* wout = (const u16*)d_in[10];
  const u16* c = (const u16*)d_in[11];
  const u16* wmod = (const u16*)d_in[12];
  char* ws = (char*)d_ws;
  float* mod = (float*)(ws + WS_MOD);
  char* xf = ws + WS_H1;
  char* Qf = ws + WS_QF;
  char* Ki = ws + WS_KI;
  char* Vi = ws + WS_VI;
  char* ctx = ws + WS_CTX;
  u16* x1 = (u16*)(ws + WS_X1);
  char* z1 = ws + WS_Z1;
  char* z2 = ws + WS_Z2;
  float* stats = (float*)(ws + WS_ST);
  char* u = ws + WS_U;
  u16* out = (u16*)d_out;

  k_kv<<<256, 512, 0, stream>>>(ckv, wk, wv, Ki, Vi, c, wmod, mod, x, xf, wq);
  k_attn<<<256, 256, 0, stream>>>(xf, wq, mod, Qf, Ki, Vi, ctx, wo, win, wsp, wout);
  k_oproj<<<256, 512, 0, stream>>>(ctx, wo, x, mod + 2 * DM, x1);
  k_win<<<256, 512, 0, stream>>>(x1, mod + 3 * DM, mod + 4 * DM, win, z1, z2, stats);
  k_spatial<<<256, 512, 0, stream>>>(wsp, z2, stats, bsp, z1, u);
  k_wout<<<256, 512, 0, stream>>>(u, wout, x1, mod + 5 * DM, out);
}
```

```cpp
#include <hip/hip_runtime.h>
#include <utility>

constexpr int S = 2048;
constexpr int DM = 512;
constexpr int NH = 32;
constexpr int HD = 128;
constexpr int HDM = NH * HD;
constexpr int FH = 688;
constexpr int FF = 1376;
constexpr int UKS = 22;

typedef __attribute__((ext_vector_type(8))) short bf16x8;
typedef __attribute__((ext_vector_type(4))) float f32x4;
typedef __attribute__((ext_vector_type(16))) float f32x16;
typedef __attribute__((ext_vector_type(4))) unsigned u32x4;
typedef __attribute__((ext_vector_type(2))) unsigned u32x2;
typedef unsigned short u16;

#define DI __device__ __forceinline__

DI float bflo(unsigned v) { return __uint_as_float(v << 16); }
DI float bfhi(unsigned v) { return __uint_as_float(v & 0xffff0000u); }
typedef __attribute__((ext_vector_type(2))) __bf16 bf16x2_t;
typedef __attribute__((ext_vector_type(2))) float f32x2;
typedef __attribute__((ext_vector_type(2))) _Float16 f16x2;
DI unsigned pack2(float lo, float hi) {
  f32x2 v = {lo, hi};
  return __builtin_bit_cast(unsigned, __builtin_convertvector(v, bf16x2_t));
}
DI void barrier() { asm volatile("s_waitcnt lgkmcnt(0)" ::: "memory"); __builtin_amdgcn_s_barrier(); }
DI void st_wt(void* p, u32x4 v) { asm volatile("global_store_dwordx4 %0, %1, off sc1\n\ts_nop 1" ::"v"(p), "v"(v) : "memory"); }
DI void st_wt(void* p, float v) { asm volatile("global_store_dword %0, %1, off sc1" ::"v"(p), "v"(v) : "memory"); }
DI void arrive(unsigned* cnt, unsigned v) { __hip_atomic_fetch_add(cnt, v, __ATOMIC_RELAXED, __HIP_MEMORY_SCOPE_AGENT); }
DI unsigned peek(const unsigned* cnt) { return __builtin_amdgcn_readfirstlane(__hip_atomic_load(cnt, __ATOMIC_RELAXED, __HIP_MEMORY_SCOPE_AGENT)); }
DI void wait_count(const unsigned* cnt, unsigned mask, unsigned want) {
#pragma nounroll
  for (int spin = 0; spin < (1 << 18); ++spin) {
    if ((peek(cnt) & mask) == want) break;
    __builtin_amdgcn_s_sleep(1);
  }
}
DI float rbf(float x) { return bflo(pack2(x, 0.f)); }
DI unsigned lane_id() { unsigned l; asm volatile("v_mbcnt_lo_u32_b32 %0, -1, 0\n\tv_mbcnt_hi_u32_b32 %0, -1, %0" : "=v"(l)); return l; }
DI unsigned lds_addr(const void* p) { return __builtin_amdgcn_readfirstlane((unsigned)(size_t)p); }
DI void pf_line(const void* g, const void* lds_dummy) {
  const unsigned a = __builtin_amdgcn_readfirstlane((unsigned)(size_t)lds_dummy);
  unsigned keep;
  asm volatile("s_mov_b32 %0, m0\n\ts_mov_b32 m0, %2\n\ts_nop 0\n\tglobal_load_lds_dword %1, off\n\ts_mov_b32 m0, %0"
               : "=&s"(keep) : "v"(g), "s"(a) : "memory");
}
#define CODE_PC(pc, A) asm volatile("s_getpc_b64 %0\n.Lct_" A ":" : "=s"(pc))
#define CODE_LEN(len, A, B) asm volatile("s_mov_b32 %0, .Lct_" B "-.Lct_" A : "=s"(len))
DI void code_touch(const char* pc, unsigned len, int chunk, int lane, unsigned lds_dummy) {
  unsigned off = (unsigned)(chunk * 64 + lane) * 128u;
  off = (len < 262144u && off + 4u < len) ? off : 0u;
  asm volatile("s_mov_b32 m0, %2\n\ts_nop 0\n\tglobal_load_lds_dword %0, %1" ::"v"(off), "s"(pc), "s"(lds_dummy) : "memory");
}
#define CODE_TOUCH(pc, A, B, chunk, lane, lds_dummy) do { unsigned len_; CODE_LEN(len_, A, B); code_touch(pc, len_, chunk, lane, lds_dummy); } while (0)
template <int CTRL> DI float dpp_add(float v) {
  return v + __uint_as_float(__builtin_amdgcn_update_dpp(0u, __float_as_uint(v), CTRL, 0xf, 0xf, true));
}
DI float row16_sum(float v) {
  v = dpp_add<0xB1>(v); v = dpp_add<0x4E>(v); v = dpp_add<0x141>(v); v = dpp_add<0x140>(v);
  return v;
}
DI float wave_sum(float v) {
  v = row16_sum(v);
  auto a = __builtin_amdgcn_permlane16_swap(__float_as_uint(v), __float_as_uint(v), false, false);
  v = __uint_as_float(a[0]) + __uint_as_float(a[1]);
  auto b = __builtin_amdgcn_permlane32_swap(__float_as_uint(v), __float_as_uint(v), false, false);
  return __uint_as_float(b[0]) + __uint_as_float(b[1]);
}

constexpr size_t WS_MOD = 0;
constexpr size_t WS_H1 = 16384;
constexpr size_t WS_SP = WS_H1 + (size_t)S * DM * 2;
constexpr size_t WS_SS = WS_SP + (size_t)S * S * 2;
constexpr size_t WS_WU = WS_SS + (size_t)S * 64;
constexpr size_t WS_KI = WS_WU + (size_t)(1 << 20);
constexpr int HS = S * HD * 2;
constexpr size_t WS_VI = WS_KI + (size_t)NH * HS;
constexpr size_t WS_CTX = WS_VI + (size_t)NH * HS;
constexpr size_t WS_X1 = WS_CTX + (size_t)S * HDM * 2;
constexpr size_t WS_Z2 = WS_X1 + (size_t)S * DM * 2;
constexpr size_t WS_ST = WS_Z2 + (size_t)S * FH * 2;
constexpr size_t WS_U = WS_ST + (size_t)8 * S * 2 * 4;
constexpr size_t WS_WF = WS_U + (size_t)S * 704 * 2;
constexpr size_t WS_UV = WS_WF + (size_t)DM * FF * 2;
constexpr size_t WS_CNT = WS_UV + 90112;
constexpr int NCNT = 72, CNT_Z2 = 128, CNT_PE = CNT_Z2 * 64 + 32, CNT_PL = 384 * 64, CNT_CX = CNT_PL + 32;
constexpr size_t WS_WO = WS_CNT + 163840;
constexpr size_t WS_PT = WS_WO + (size_t)HDM * DM * 2;

#define ACLOB_LO "a0","a1","a2","a3","a4","a5","a6","a7","a8","a9","a10","a11","a12","a13","a14","a15","a16","a17","a18","a19","a20","a21","a22","a23","a24","a25","a26","a27","a28","a29","a30","a31","a32","a33","a34","a35","a36","a37","a38","a39","a40","a41","a42","a43","a44","a45","a46","a47","a48","a49","a50","a51","a52","a53","a54","a55","a56","a57","a58","a59","a60","a61","a62","a63","a64","a65","a66","a67","a68","a69","a70","a71","a72","a73","a74","a75","a76","a77","a78","a79","a80","a81","a82","a83","a84","a85","a86","a87","a88","a89","a90","a91","a92","a93","a94","a95","a96","a97","a98","a99","a100","a101","a102","a103","a104","a105","a106","a107","a108","a109","a110","a111","a112","a113","a114","a115","a116","a117","a118","a119","a120","a121","a122","a123","a124","a125","a126","a127"
#define ACLOB_HI "a128","a129","a130","a131","a132","a133","a134","a135","a136","a137","a138","a139","a140","a141","a142","a143","a144","a145","a146","a147","a148","a149","a150","a151","a152","a153","a154","a155","a156","a157","a158","a159","a160","a161","a162","a163","a164","a165","a166","a167","a168","a169","a170","a171","a172","a173","a174","a175","a176","a177","a178","a179","a180","a181","a182","a183","a184","a185","a186","a187","a188","a189","a190","a191","a192","a193","a194","a195","a196","a197","a198","a199","a200","a201","a202","a203","a204","a205","a206","a207","a208","a209","a210","a211","a212","a213","a214","a215","a216","a217","a218","a219","a220","a221","a222","a223","a224","a225","a226","a227","a228","a229","a230","a231","a232","a233","a234","a235","a236","a237","a238","a239","a240","a241","a242","a243","a244","a245","a246","a247","a248","a249","a250","a251","a252","a253","a254","a255"
template <int... Is> using iseq = std::integer_sequence<int, Is...>;
template <int N> using make_iseq = std::make_integer_sequence<int, N>;
#define GAP() __builtin_amdgcn_sched_barrier(0)

constexpr int KV_SLOTS = 9, KV_PRE = 6, KV_MISC = KV_SLOTS * 16384, KV_NSTEP = 64, KV_ALL = 43, KV_PF = KV_ALL + 1, KV_SIDE = 20, KV_SC = 36, KV_W0 = 5, KV_WO = 16;
constexpr int kv_needU(int u) { return u < 8 ? 8 * (u & 3) - 1 : u < 12 ? 31 + 4 * ((u & 3) >> 1) : KV_ALL; }
constexpr bool kv_bar(int s) { return (s < 32 && (s & 7) == 7) || s == 35 || s == KV_ALL || s == KV_SC + 3; }
constexpr int kv_stg(int s) { return s < 31 ? -1 : s == 31 ? 0 : s < 48 ? 1 + ((s - 32) >> 2) : (s & 1) ? 5 + ((s - 48) >> 3) : -1; }
constexpr int kv_sti(int s) { return s == 31 ? 0 : s < 48 ? s & 3 : (s >> 1) & 3; }
constexpr int kv_nst(int s) { return kv_stg(s) < 0 ? 0 : s == 31 ? 4 : 1; }
struct KvSched {
  int isA[32], num[32], slot[32], last[32];
  int slotA[16], slotB[16];
  int ibeg[KV_NSTEP + 2];
  int nvm[KV_NSTEP + 1];
  bool ok;
};
constexpr KvSched kv_sched() {
  KvSched q{};
  int n = 0;
  for (int t = -2; t <= KV_ALL; ++t) {
    for (int u = 0; u < 16; ++u) if (kv_needU(u) == (t == -2 ? -1 : t == -1 ? -9 : t)) { q.isA[n] = 1; q.num[n++] = u; }
    for (int b = 0; b < 16; ++b) if (2 * b - 2 == t) { q.isA[n] = 0; q.num[n++] = b; }
  }
  int freed[KV_SLOTS] = {};
  for (int i = 0; i < KV_SLOTS; ++i) freed[i] = -1;
  int pos = 0, it = 0, side = 0;
  q.ok = n == 32;
  for (int s = -1; s < KV_NSTEP; ++s) {
    if (s >= 0) {
      int w = -1;
      for (int i = 0; i < 32; ++i) {
        if ((q.isA[i] ? kv_needU(q.num[i]) : 2 * q.num[i] - 2) == s) {
          if (i >= it) { q.ok = false; continue; }
          const int c = pos - q.last[i] - 1;
          if (w < 0 || c < w) w = c;
        }
      }
      q.nvm[s] = w > 63 ? 63 : w;
    }
    q.ibeg[s + 1] = it;
    for (int c = 0; c < (s < 0 ? KV_PRE : s == 31 ? 0 : 2) && it < 32; ++c) {
      int sl = -1;
      for (int i = 0; i < KV_SLOTS; ++i) if (sl < 0 && freed[i] <= s) sl = i;
      if (sl < 0) break;
      q.slot[it] = sl;
      (q.isA[it] ? q.slotA : q.slotB)[q.num[it]] = sl;
      freed[sl] = q.isA[it] ? (q.num[it] < 8 ? 8 * (q.num[it] & 3) + 7 : 1000) : 2 * q.num[it];
      pos += 4;
      q.last[it] = pos - 1;
      ++it;
    }
    if (s >= 0) pos += kv_nst(s);
    if (s == KV_PF) pos += 1;
    if (s == KV_W0 || s == KV_WO) pos += 4;
    if (s == KV_SIDE) { pos += 8; side = pos; }
    if (s == KV_SC - 1) q.nvm[KV_NSTEP] = pos - side > 63 ? 63 : pos - side;
  }
  q.ibeg[KV_NSTEP + 1] = it;
  if (it != 32) q.ok = false;
  return q;
}
constexpr KvSched KS = kv_sched();
static_assert(KS.ok && KS.isA[0] && KS.isA[1] && !KS.isA[2] && KS.num[2] == 0, "kv schedule");
constexpr int KV_WOW = 4 * (KS.ibeg[KV_WO + 1] - KS.ibeg[KV_W0 + 2]);
static_assert(KV_WO <= KV_SIDE && KV_WOW < 64, "w_o copy");
struct KV {
  unsigned ra[3][4], rb[3][4];
  unsigned va[4], vb[4], vs;
  unsigned ldsw, ko[4];
  const char *ab, *bb;
  char *sb, *sb2;
  f32x4 acc[4][2][4];
  u32x4 af[2][4], pk;
  u32x4 sx[2], sw[4];
  u32x2 sc;
  const char *px, *pw, *pc_, *po, *pf1;
  unsigned vo;
};
template <int IT, int J> DI void kv_dma(KV& k) {
  constexpr int n = KS.num[IT], dst = KS.slot[IT] * 16384 + J * 1024;
  const char* src = KS.isA[IT] ? k.ab + (k.ko[n & 3] + (unsigned)((n >> 2) * 65536)) : k.bb + (k.ko[n >> 2] * 4096u + (unsigned)((n & 3) * 262144));
  asm volatile("" : "+s"(src));
  asm volatile("s_add_u32 m0, %2, %c3\n\ts_nop 0\n\tglobal_load_lds_dwordx4 %0, %1" ::"v"(KS.isA[IT] ? k.va[J] : k.vb[J]), "s"(src), "s"(k.ldsw), "i"(dst) : "memory", "scc");
}
template <int... Is> DI void kv_dmas(KV& k, iseq<Is...>) { (kv_dma<(Is >> 2), (Is & 3)>(k), ...); }
constexpr int kv_ks(int s, int i) { return s < 32 ? s >> 1 : 2 * (s & 7) + (i >> 1); }
template <int S, int I> DI void kv_aread(KV& k) {
  constexpr int ks = kv_ks(S, I), r = S < 32 ? S & 1 : 2 + ((S - 32) >> 4), sl = KS.slotA[4 * r + (ks >> 2)];
  constexpr int blk = S < 32 ? I : ((S >> 3) & 1) * 2 + (I & 1);
  asm volatile("ds_read_b128 %0, %1 offset:%c2" : "=v"(k.af[S & 1][I]) : "v"(k.ra[sl >> 2][ks & 3]), "i"((sl & 3) * 16384 + blk * 4096) : "memory");
}
template <int KSTEP, int G> DI void kv_bread(KV& k) {
  constexpr int sl = KS.slotB[KSTEP], r = 16 * KSTEP + 2 * G;
  asm volatile("ds_read_b64_tr_b16 a[%c1:%c2], %0 offset:%c3" ::"v"(k.rb[sl >> 2][G >> 1]), "i"(r), "i"(r + 1), "i"((sl & 3) * 16384 + (G & 1) * 512) : "memory", ACLOB_LO, ACLOB_HI);
}
template <int S, int... Is> DI void kv_areads(KV& k, iseq<Is...>) { (kv_aread<S, Is>(k), ...); }
template <int S, int... Gs> DI void kv_breads(KV& k, iseq<Gs...>) { (kv_bread<S, Gs>(k), ...); }
template <bool isV, int R, int I, int PART> DI void kv_store(KV& k) {
  constexpr int mt = isV ? 0 : I >> 1, nt = isV ? I : (I & 1) * 2;
  if constexpr (PART < 2) {
    const f32x4 v = PART == 0 ? k.acc[R & 3][mt][nt] : isV ? k.acc[R & 3][1][nt] : k.acc[R & 3][mt][(nt + 1) & 3];
    asm volatile("v_cvt_pk_bf16_f32 %0, %2, %3\n\tv_cvt_pk_bf16_f32 %1, %4, %5" : "=&v"(k.pk[2 * PART]), "=&v"(k.pk[2 * PART + 1]) : "v"(v[0]), "v"(v[1]), "v"(v[2]), "v"(v[3]));
  } else {
    constexpr int off = isV ? (nt >> 1) * 1024 + (nt & 1) * 256 : (nt >> 1) * 2048 + mt * 256;
    char* dst = (isV && (R & 2) ? k.sb2 : k.sb) + R * 8192;
    asm volatile("" : "+s"(dst));
    asm volatile("global_store_dwordx4 %0, %1, %2 offset:%c3 sc1\n\ts_nop 1" ::"v"(k.vs), "v"(k.pk), "s"(dst), "i"(off) : "memory");
  }
}
template <bool isV, int S, int M> DI void kv_gap(KV& k) {
  constexpr int ks = kv_ks(S, M >> 2), g = S < 32 ? 2 * (S & 1) + (M >> 3) : (S - 32) >> 3, nt = M & 3, br = 16 * ks + 4 * nt;
  f32x4& acc = k.acc[g][(M >> 2) & 1][nt];
  const u32x4& a = k.af[S & 1][M >> 2];
  if constexpr (ks == 0) {
    if constexpr (isV) asm volatile("v_mfma_f32_16x16x32_bf16 %0, %1, a[%c2:%c3], 0" : "=&v"(acc) : "v"(a), "i"(br), "i"(br + 3));
    else asm volatile("v_mfma_f32_16x16x32_bf16 %0, a[%c2:%c3], %1, 0" : "=&v"(acc) : "v"(a), "i"(br), "i"(br + 3));
  } else {
    if constexpr (isV) asm volatile("v_mfma_f32_16x16x32_bf16 %0, %1, a[%c2:%c3], %0" : "+v"(acc) : "v"(a), "i"(br), "i"(br + 3));
    else asm volatile("v_mfma_f32_16x16x32_bf16 %0, a[%c2:%c3], %1, %0" : "+v"(acc) : "v"(a), "i"(br), "i"(br + 3));
  }
  GAP();
  if constexpr (S + 1 < KV_NSTEP && M < 4) kv_aread<S + 1, M>(k);
  if constexpr (S < 30 && M >= 4 && M < 8) kv_bread<(S >> 1) + 1, (S & 1) * 4 + M - 4>(k);
  constexpr int nd = 4 * (KS.ibeg[S + 2] - KS.ibeg[S + 1]), d = M - 4;
  if constexpr (d >= 0 && d < nd) kv_dma<KS.ibeg[S + 1] + (d >> 2), d & 3>(k);
  if constexpr (kv_nst(S) == 4 && (M & 3) < 3) kv_store<isV, kv_stg(S), (M >> 2), (M & 3)>(k);
  if constexpr (kv_nst(S) == 1 && M >= 12 && M < 15) kv_store<isV, kv_stg(S), kv_sti(S), M - 12>(k);
  if constexpr (S == KV_PF && M == 15)
    asm volatile("s_add_u32 m0, %1, %c2\n\ts_nop 0\n\tglobal_load_lds_dword %0, off" ::"v"(k.pf1), "s"(k.ldsw), "i"(KV_MISC) : "memory", "scc");
  if constexpr (S == KV_W0 && M >= 12)
    asm volatile("s_add_u32 m0, %2, %c3\n\ts_nop 0\n\tglobal_load_lds_dwordx4 %0, %1" ::"v"(k.vo), "s"(k.po + (M - 12) * 4096), "s"(k.ldsw), "i"(KV_MISC + (M - 12) * 1024) : "memory", "scc");
  if constexpr (S == KV_SIDE && M == 15) {
    asm volatile("global_load_dwordx4 %0, %2, off\n\tglobal_load_dwordx4 %1, %2, off offset:1024" : "=&v"(k.sx[0]), "=&v"(k.sx[1]) : "v"(k.px) : "memory");
    const char* pw2 = k.pw + 2 * 6144;
    asm volatile("global_load_dwordx4 %0, %4, off offset:-3072\n\tglobal_load_dwordx4 %1, %4, off offset:3072\n\tglobal_load_dwordx4 %2, %5, off offset:-3072\n\tglobal_load_dwordx4 %3, %5, off offset:3072"
                 : "=&v"(k.sw[0]), "=&v"(k.sw[1]), "=&v"(k.sw[2]), "=&v"(k.sw[3]) : "v"(k.pw), "v"(pw2) : "memory");
    asm volatile("global_load_dwordx2 %0, %1, off" : "=v"(k.sc) : "v"(k.pc_) : "memory");
  }
  if constexpr (S == KV_SIDE && M == 14)
    asm volatile("s_add_u32 m0, %2, %c3\n\ts_nop 0\n\tglobal_load_lds_dwordx4 %0, %1" ::"v"(k.vo), "s"(k.po), "s"(k.ldsw), "i"(KV_MISC + 1024) : "memory", "scc");
  GAP();
}
template <bool isV, int S, int... Ms> DI void kv_step_(KV& k, iseq<Ms...>) {
  constexpr int n = KS.nvm[S];
  if constexpr (n >= 0) asm volatile("s_waitcnt vmcnt(%c0) lgkmcnt(0)" ::"i"(n) : "memory");
  else asm volatile("s_waitcnt lgkmcnt(0)" ::: "memory");
  if constexpr (kv_bar(S)) asm volatile("s_barrier" ::: "memory");
  GAP();
  (kv_gap<isV, S, Ms>(k), ...);
}
template <bool isV, int S0, int... Ss> DI void kv_steps(KV& k, iseq<Ss...>) { (kv_step_<isV, S0 + Ss>(k, make_iseq<16>{}), ...); }
template <bool isV, int... Is> DI void kv_last(KV& k, iseq<Is...>) { (kv_store<isV, 7, Is / 3, Is % 3>(k), ...); }

template <bool isV>
DI void kv_body(char* smem, const u16* __restrict__ ckv, const u16* __restrict__ B, char* __restrict__ dstb,
                const u16* __restrict__ cvec, const u16* __restrict__ wmod, float* __restrict__ mod,
                const u16* __restrict__ x, char* __restrict__ xf, const u16* __restrict__ wq, const u16* __restrict__ wo,
                const u16* __restrict__ wout, unsigned* __restrict__ cnt, const char* pc) {
  const int b = blockIdx.x, xcd = b & 7, j = b >> 3;
  const int ntile = xcd * 4 + (j & 3), mtile = j >> 2;
  const int tid = threadIdx.x, wave = __builtin_amdgcn_readfirstlane(tid >> 6), lane = tid & 63;
  const int fr = lane & 15, fg = lane >> 4, q4 = fr >> 2, p4 = lane & 3;
  const unsigned ldsb = lds_addr(smem);
  KV k;
  k.ldsw = ldsb + wave * 4096;
#pragma unroll
  for (int i = 0; i < 4; ++i) {
    k.ko[i] = ((i + mtile) & 3) * 256;
    const int row = isV ? 32 * (wave >> 1) + 8 * (wave & 1) + 16 * (i >> 1) + 4 * (i & 1) + fg : 16 * wave + 4 * i + fg;
    k.va[i] = row * 1024 + ((fr ^ fg ^ (4 * i)) << 4);
    k.vb[i] = (i * 8 + (lane >> 3)) * 8192 + ((lane & 7) ^ ((lane >> 3) & 2) ^ ((i & 1) << 2)) * 16;
  }
  k.ab = reinterpret_cast<const char*>(ckv) + mtile * 262144;
  k.bb = reinterpret_cast<const char*>(B) + (ntile & 15) * 512 + wave * 128;
  kv_dmas(k, make_iseq<4 * KV_PRE>{});
#pragma unroll
  for (int g = 0; g < 3; ++g) {
#pragma unroll
    for (int i = 0; i < 4; ++i) k.ra[g][i] = ldsb + g * 65536 + fr * 256 + (((4 * i + fg) ^ fr) << 4);
#pragma unroll
    for (int i = 0; i < 4; ++i) k.rb[g][i] = k.ldsw + g * 65536 + fg * 1024 + q4 * 128 + p4 * 8 + (i ^ (q4 >> 1) ^ ((fg & 1) << 1)) * 32;
  }
  k.vs = (fr + 32 * (fg & 1)) * 16 + (fg >> 1) * (isV ? 4096 : 1024);
  k.sb = dstb + ((ntile & 15) * 2 + (wave >> 1)) * HS + mtile * 65536 + (wave & 1) * (isV ? 2048 : 4096);
  k.sb2 = k.sb + ((wave & 1) ? -2048 : 2048);
  const int bu = b < 176 ? b : b - 176;
  const int mcol = (xcd * 24 + (j < 24 ? j : 0)) * 16;
  k.px = reinterpret_cast<const char*>(x) + (b * 8 + wave * 2) * 1024 + lane * 16;
  k.pw = reinterpret_cast<const char*>(wmod) + (tid >> 1) * 24576 + 3072 + mcol * 2 + (tid & 1) * 16;
  k.pc_ = reinterpret_cast<const char*>(cvec) + (tid >> 1) * 8;
  k.po = reinterpret_cast<const char*>(wo) + b * 16384 + wave * 256;
  k.vo = fg * 1024 + (fr ^ (fg << 2)) * 16;
  {
    unsigned len, off;
    CODE_LEN(len, "kv", "end");
    CODE_LEN(off, "kv", "at");
    off += (unsigned)(((j & 15) * 2 + (wave & 1)) * 64 + lane) * 128u;
    off = (len < 262144u && off + 4u < len) ? off : 0u;
    k.pf1 = wave < 2 ? reinterpret_cast<const char*>(wq) + ((b * 2 + wave) * 64 + lane) * 128 : pc + off;
  }
  asm volatile("s_waitcnt vmcnt(%c0)\n\ts_barrier" ::"i"(4 * KV_PRE - 12) : "memory");
  kv_areads<0>(k, make_iseq<4>{});
  kv_breads<0>(k, make_iseq<8>{});
  kv_steps<isV, 0>(k, make_iseq<KV_WO>{});
  {
    u32x2 wt[4][2];
    const unsigned l = lane_id(), q = (l >> 2) & 3u, g = l >> 4;
    const unsigned ra = k.ldsw + KV_MISC + (g >> 1) * 2048 + q * 256 + ((l & 1u) * 2 + ((l >> 1) & 1u)) * 8;
    asm volatile("s_waitcnt vmcnt(%c0)" ::"i"(KV_WOW) : "memory");
#pragma unroll
    for (int cb = 0; cb < 4; ++cb)
      asm volatile("ds_read_b64_tr_b16 %0, %2\n\tds_read_b64_tr_b16 %1, %2 offset:1024" : "=&v"(wt[cb][0]), "=&v"(wt[cb][1]) : "v"(ra + ((2 * cb + (g & 1u)) ^ (2 * q)) * 32) : "memory");
    kv_steps<isV, KV_WO>(k, make_iseq<1>{});
    char* dst = reinterpret_cast<char*>(cnt) + (WS_WO - WS_CNT) + (((b >> 5) * 4 + wave) * 128 + (b & 31)) * 1024;
#pragma unroll
    for (int cb = 0; cb < 4; ++cb) {
      asm volatile("" : "+v"(wt[cb][0]), "+v"(wt[cb][1]));
      const u32x4 w4 = {wt[cb][0][0], wt[cb][0][1], wt[cb][1][0], wt[cb][1][1]};
      asm volatile("global_store_dwordx4 %0, %1, %2 sc1\n\ts_nop 1" ::"v"(l * 16u), "v"(w4), "s"(dst + cb * 32768) : "memory");
    }
    k.vo = ((l >> 1) & (bu >= 168 ? 15u : 31u)) * 1024u + (l & 1u) * 16u;
    k.po = reinterpret_cast<const char*>(wout) + (bu >> 3) * 32768 + (bu & 7) * 128 + wave * 32;
    asm volatile("" : "+v"(k.vo));
  }
  kv_steps<isV, KV_WO + 1>(k, make_iseq<31 - KV_WO>{});
  kv_steps<isV, 32>(k, make_iseq<KV_SC - 32>{});
  asm volatile("s_waitcnt vmcnt(%c7) lgkmcnt(0)" : "+v"(k.sx[0]), "+v"(k.sx[1]), "+v"(k.sw[0]), "+v"(k.sw[1]), "+v"(k.sw[2]), "+v"(k.sw[3]), "+v"(k.sc) : "i"(KS.nvm[KV_NSTEP]) : "memory");
#pragma unroll
  for (int i = 0; i < 2; ++i) {
    const u32x4 xv = k.sx[i];
    float f[8];
#pragma unroll
    for (int jj = 0; jj < 4; ++jj) { f[2 * jj] = bflo(xv[jj]); f[2 * jj + 1] = bfhi(xv[jj]); }
    float sm = 0.f;
#pragma unroll
    for (int jj = 0; jj < 8; ++jj) sm += f[jj];
    const float mean = wave_sum(sm) * (1.f / DM);
    float vr = 0.f;
#pragma unroll
    for (int jj = 0; jj < 8; ++jj) { f[jj] -= mean; vr += f[jj] * f[jj]; }
    const float rstd = rsqrtf(wave_sum(vr) * (1.f / DM) + 1e-5f);
    u32x4 o;
#pragma unroll
    for (int jj = 0; jj < 4; ++jj) {
      const f16x2 hv = {(_Float16)(f[2 * jj] * rstd), (_Float16)(f[2 * jj + 1] * rstd)};
      o[jj] = __builtin_bit_cast(unsigned, hv);
    }
    const int row = b * 8 + wave * 2 + i;
    *reinterpret_cast<u32x4*>(xf + ((size_t)(row >> 5) * 32 + (lane >> 1)) * 1024 + ((row & 31) + 32 * (lane & 1)) * 16) = o;
  }
  if (b == 255 && tid < NCNT) cnt[tid * 64] = 0u;
  if (tid >= 252) cnt[(CNT_Z2 + (tid & 2) * 128 + b) * 64 + (tid & 1) * 32] = 0u;
  if (tid >= 128 && tid < 160) (cnt - (WS_CNT - WS_ST) / 4)[b * 32 + tid - 128] = 0u;
  float* modred = reinterpret_cast<float*>(smem + KV_MISC + 16384 - 256);
  if (b < 192) {
    float v[8];
#pragma unroll
    for (int i = 0; i < 4; ++i) {
      const float c0 = i & 1 ? bfhi(k.sc[i >> 1]) : bflo(k.sc[i >> 1]), s0 = rbf(c0 / (1.f + __expf(-c0)));
#pragma unroll
      for (int jj = 0; jj < 4; ++jj) {
        v[2 * jj] = i ? fmaf(s0, bflo(k.sw[i][jj]), v[2 * jj]) : s0 * bflo(k.sw[i][jj]);
        v[2 * jj + 1] = i ? fmaf(s0, bfhi(k.sw[i][jj]), v[2 * jj + 1]) : s0 * bfhi(k.sw[i][jj]);
      }
    }
#pragma unroll
    for (int st = 0; st < 3; ++st) {
      const int half = 4 >> st, m = 32 >> st;
      const bool up = lane & m;
#pragma unroll
      for (int e = 0; e < half; ++e) {
        const float send = up ? v[e] : v[half + e];
        const float keep = up ? v[half + e] : v[e];
        v[e] = keep + __shfl_xor(send, m);
      }
    }
    v[0] += __shfl_xor(v[0], 4);
    v[0] += __shfl_xor(v[0], 2);
    if (!(lane & 6)) modred[wave * 16 + (lane & 1) * 8 + (lane >> 3)] = v[0];
  }
  u32x2 wt[2];
  {
    const unsigned l = lane_id();
    asm volatile("ds_read_b64_tr_b16 %0, %2\n\tds_read_b64_tr_b16 %1, %2 offset:128" : "=&v"(wt[0]), "=&v"(wt[1]) : "v"(k.ldsw + KV_MISC + 1024 + (l >> 4) * 256 + (l & 15u) * 8) : "memory");
  }
  GAP();
  kv_steps<isV, KV_SC>(k, make_iseq<4>{});
  asm volatile("s_waitcnt lgkmcnt(0)" : "+v"(wt[0]), "+v"(wt[1])::"memory");
  if (b < 176) st_wt(reinterpret_cast<char*>(cnt) - (WS_CNT - WS_WU) + ((b & 7) * UKS + (b >> 3)) * 4096 + tid * 16, u32x4{wt[0][0], wt[0][1], wt[1][0], wt[1][1]});
  if (b < 192 && tid < 16) mod[mcol + tid] = rbf((modred[tid] + modred[16 + tid]) + (modred[32 + tid] + modred[48 + tid]));
  GAP();
  kv_steps<isV, KV_SC + 4>(k, make_iseq<KV_NSTEP - KV_SC - 4>{});
  asm volatile("s_nop 7" ::: "memory");
  kv_last<isV>(k, make_iseq<12>{});
}
__global__ __launch_bounds__(256, 1) void k_kv(const u16* __restrict__ ckv, const u16* __restrict__ wk,
                                               const u16* __restrict__ wv, char* __restrict__ Ki,
                                               char* __restrict__ Vi, const u16* __restrict__ cvec,
                                               const u16* __restrict__ wmod, float* __restrict__ mod,
                                               const u16* __restrict__ x, char* __restrict__ xf,
                                               const u16* __restrict__ wq, const u16* __restrict__ wo,
                                               const u16* __restrict__ wout, unsigned* __restrict__ cnt) {
  __shared__ __attribute__((aligned(16))) char smem[KV_MISC + 16384];
  const char* pc;
  CODE_PC(pc, "kv");
  CODE_TOUCH(pc, "kv", "at", (threadIdx.x >> 6) + ((blockIdx.x >> 3) & 1) * 4, threadIdx.x & 63, lds_addr(smem) + KV_MISC);
  if ((blockIdx.x & 7) >= 4) kv_body<true>(smem, ckv, wv, Vi, cvec, wmod, mod, x, xf, wq, wo, wout, cnt, pc);
  else kv_body<false>(smem, ckv, wk, Ki, cvec, wmod, mod, x, xf, wq, wo, wout, cnt, pc);
}

constexpr float AT_THR = 8.f;
constexpr int AT_MISC = 131072;
DI float max3f(float a, float b, float c) { float r; asm("v_max3_f32 %0, %1, %2, %3" : "=v"(r) : "v"(a), "v"(b), "v"(c)); return r; }
DI float max2f(float a, float b) { float r; asm("v_max_f32_e32 %0, %1, %2" : "=v"(r) : "v"(a), "v"(b)); return r; }
DI float fadd_s(float a, float b) { float r; asm("v_add_f32_e32 %0, %1, %2" : "=v"(r) : "v"(a), "v"(b)); return r; }
DI float fmul_s(float a, float b) { float r; asm("v_mul_f32_e32 %0, %1, %2" : "=v"(r) : "v"(a), "v"(b)); return r; }
DI float fsub_s(float a, float b) { float r; asm("v_sub_f32_e32 %0, %1, %2" : "=v"(r) : "v"(a), "v"(b)); return r; }
template <bool DEF> DI void negm_bcast(f32x16& negm, float neg_mhat) {
  const float aone = lane_id() < 32u ? 1.f : 0.f;
  if constexpr (DEF) asm volatile("s_nop 1\n\tv_mfma_f32_32x32x2_f32 %0, %1, %2, 0" : "=&v"(negm) : "v"(aone), "v"(neg_mhat));
  else asm volatile("s_nop 1\n\tv_mfma_f32_32x32x2_f32 %0, %1, %2, 0" : "+v"(negm) : "v"(aone), "v"(neg_mhat));
}
struct AC {
  unsigned rb, voff;
  unsigned mk, mv;
  const char *Kh, *Vh;
  const char *ka, *va;
  int kt, vt, end0;
  int dd, ddq, dds;
  unsigned* sig;
  unsigned pe, qa;
  const char *wi, *wp;
  f32x16 S[2][2][2];
  f32x16 NEGM[2];
  bf16x8 Vf[4][4];
  bf16x8 Pk[2][4];
  float mhat[2], lsum[2], fct[2]; bool pend;
  float rs0[2], rs1[2], pmx[2][4], rmax[2];
};
constexpr int AO_(int db, int qb) { return 16 * (2 * db + qb); }
constexpr int AQ_(int qb, int d0) { return 128 + 32 * qb + 4 * d0; }
constexpr int AK_(int kb, int d0) { return 192 + 32 * kb + 4 * d0; }
template <int I> DI void v_read(AC& c) {
  asm volatile("ds_read_b128 %0, %1 offset:%2" : "=v"(c.Vf[I & 3][I >> 2]) : "v"(c.rb), "i"(32768 + I * 1024) : "memory");
}
template <int I> DI void k_read(unsigned rb) {
  asm volatile("ds_read_b128 a[%c1:%c2], %0 offset:%c3" ::"v"(rb), "i"(192 + 4 * I), "i"(195 + 4 * I), "i"(I * 1024) : "memory", ACLOB_HI);
}
template <int I> DI void q_load(const char* qp, unsigned voff) {
  asm volatile("global_load_dwordx4 a[%c2:%c3], %0, %1 offset:%c4 sc1" ::"v"(voff), "s"(qp + (I >> 2) * 4096), "i"(128 + 4 * I), "i"(131 + 4 * I), "i"((I & 3) * 1024) : "memory", ACLOB_HI);
}
template <int I> DI void dma(const char* src, unsigned voff, unsigned lds) {
  asm volatile("s_mov_b32 m0, %2\n\ts_nop 0\n\tglobal_load_lds_dwordx4 %0, %1 offset:%3" ::"v"(voff), "s"(src + (I >> 2) * 4096), "s"(lds + (I >> 2) * 4096), "n"((I & 3) * 1024) : "memory");
}
template <int X, bool FIRST, int M> DI void qk_mfma(AC& c) {
  constexpr int d0 = M >> 2, kb = (M >> 1) & 1, qb = M & 1, ak = AK_(kb, d0), aq = AQ_(qb, d0);
  if constexpr (d0 == 0) {
    if constexpr (FIRST) asm volatile("v_mfma_f32_32x32x16_bf16 %0, a[%c1:%c2], a[%c3:%c4], 0" : "=&v"(c.S[X][kb][qb]) : "i"(ak), "i"(ak + 3), "i"(aq), "i"(aq + 3) : ACLOB_LO, ACLOB_HI);
    else asm volatile("v_mfma_f32_32x32x16_bf16 %0, a[%c2:%c3], a[%c4:%c5], %1" : "=&v"(c.S[X][kb][qb]) : "v"(c.NEGM[qb]), "i"(ak), "i"(ak + 3), "i"(aq), "i"(aq + 3) : ACLOB_LO, ACLOB_HI);
  } else asm volatile("v_mfma_f32_32x32x16_bf16 %0, a[%c1:%c2], a[%c3:%c4], %0" : "+v"(c.S[X][kb][qb]) : "i"(ak), "i"(ak + 3), "i"(aq), "i"(aq + 3) : ACLOB_LO, ACLOB_HI);
}
template <int M> DI void pv_mfma(AC& c) {
  constexpr int ks = M >> 3, db = (M >> 1) & 3, qb = M & 1, ao = AO_(db, qb);
  asm volatile("v_mfma_f32_32x32x16_bf16 a[%c2:%c3], %0, %1, a[%c2:%c3]" ::"v"(c.Vf[db][ks]), "v"(c.Pk[qb][ks]), "i"(ao), "i"(ao + 15) : ACLOB_LO);
}
template <int M, int P, int R> DI void pv_mfma_ld(AC& c) {
  constexpr int ks = M >> 3, db = (M >> 1) & 3, qb = M & 1, ao = AO_(db, qb), I = P & 7;
  const char* src = (P < 8 ? c.ka : c.va) + (I >> 2) * 4096;
  const unsigned dst = (P < 8 ? c.mk : c.mv) + (I >> 2) * 4096;
  if constexpr (P >= 0 && R >= 0)
    asm volatile("s_mov_b32 m0, %4\n\tv_mfma_f32_32x32x16_bf16 a[%c6:%c7], %0, %1, a[%c6:%c7]\n\tglobal_load_lds_dwordx4 %2, %3 offset:%c8\n\tds_read_b128 a[%c9:%c10], %5 offset:%c11"
                 ::"v"(c.Vf[db][ks]), "v"(c.Pk[qb][ks]), "v"(c.voff), "s"(src), "s"(dst), "v"(c.rb), "i"(ao), "i"(ao + 15), "i"((I & 3) * 1024),
                   "i"(192 + 4 * R), "i"(195 + 4 * R), "i"(R * 1024) : "memory", ACLOB_LO, ACLOB_HI);
  else if constexpr (P >= 0)
    asm volatile("s_mov_b32 m0, %4\n\tv_mfma_f32_32x32x16_bf16 a[%c5:%c6], %0, %1, a[%c5:%c6]\n\tglobal_load_lds_dwordx4 %2, %3 offset:%c7"
                 ::"v"(c.Vf[db][ks]), "v"(c.Pk[qb][ks]), "v"(c.voff), "s"(src), "s"(dst), "i"(ao), "i"(ao + 15), "i"((I & 3) * 1024) : "memory", ACLOB_LO);
  else if constexpr (R >= 0)
    asm volatile("v_mfma_f32_32x32x16_bf16 a[%c3:%c4], %0, %1, a[%c3:%c4]\n\tds_read_b128 a[%c5:%c6], %2 offset:%c7"
                 ::"v"(c.Vf[db][ks]), "v"(c.Pk[qb][ks]), "v"(c.rb), "i"(ao), "i"(ao + 15),
                   "i"(192 + 4 * R), "i"(195 + 4 * R), "i"(R * 1024) : "memory", ACLOB_LO, ACLOB_HI);
  else pv_mfma<M>(c);
}
template <int X, int QB, int KS, int P> DI void pack1(AC& c) {
  u32x4 w = __builtin_bit_cast(u32x4, c.Pk[QB][KS]);
  unsigned d = pack2(c.S[X][KS >> 1][QB][8 * (KS & 1) + 2 * P], c.S[X][KS >> 1][QB][8 * (KS & 1) + 2 * P + 1]);
  asm volatile("" : "+v"(d));
  w[P] = d;
  c.Pk[QB][KS] = __builtin_bit_cast(bf16x8, w);
}
template <int X, int J> DI void rowsum1(AC& c) {
  constexpr int qb = J >> 4, ks = (J >> 2) & 3, p = J & 3;
  c.rs0[qb] = fadd_s(c.rs0[qb], c.S[X][ks >> 1][qb][8 * (ks & 1) + 2 * p]);
  c.rs1[qb] = fadd_s(c.rs1[qb], c.S[X][ks >> 1][qb][8 * (ks & 1) + 2 * p + 1]);
}
DI void lsum_merge(AC& c) {
#pragma unroll
  for (int qb = 0; qb < 2; ++qb) {
    const float rs = fadd_s(c.rs0[qb], c.rs1[qb]);
    auto pr = __builtin_amdgcn_permlane32_swap(__float_as_uint(rs), __float_as_uint(rs), false, false);
    c.lsum[qb] = fadd_s(c.lsum[qb], fadd_s(__uint_as_float(pr[0]), __uint_as_float(pr[1])));
  }
  asm volatile("" : "+v"(c.lsum[0]), "+v"(c.lsum[1]));
}
#define AT_VF "+v"(c.Vf[0][0]), "+v"(c.Vf[0][1]), "+v"(c.Vf[0][2]), "+v"(c.Vf[0][3]), "+v"(c.Vf[1][0]), "+v"(c.Vf[1][1]), "+v"(c.Vf[1][2]), "+v"(c.Vf[1][3]), \
              "+v"(c.Vf[2][0]), "+v"(c.Vf[2][1]), "+v"(c.Vf[2][2]), "+v"(c.Vf[2][3]), "+v"(c.Vf[3][0]), "+v"(c.Vf[3][1]), "+v"(c.Vf[3][2]), "+v"(c.Vf[3][3])
template <int I> DI float o_read() { float r; asm volatile("v_accvgpr_read_b32 %0, a[%c1]" : "=v"(r) : "i"(I)); return r; }
struct Fin {
  unsigned ra, so;
  unsigned rx, ro, mb;
  char* cb[2];
  f32x4 in[16], own[16], ml, mo;
  float os[16][4];
  f32x2 ff[2];
  u32x4 w[2];
};
template <int I> DI void fin_rd(Fin& f) { asm volatile("ds_read_b128 %0, %1 offset:%c2" : "=v"(f.in[I]) : "v"(f.ra), "i"(I * 1024) : "memory"); }
template <bool PK, int I> DI void fin_own(Fin& f) {
  if constexpr (PK) f.own[I] = f32x4{o_read<4 * I>(), o_read<4 * I + 1>(), o_read<4 * I + 2>(), o_read<4 * I + 3>()};
  else { f.os[I][0] = o_read<4 * I>(); f.os[I][1] = o_read<4 * I + 1>(); f.os[I][2] = o_read<4 * I + 2>(); f.os[I][3] = o_read<4 * I + 3>(); }
}
template <int PR> DI void fin_swap(Fin& f) {
  asm volatile("v_permlane32_swap_b32 %0, %2\n\tv_permlane32_swap_b32 %1, %3" : "+v"(f.w[PR & 1][0]), "+v"(f.w[PR & 1][1]), "+v"(f.w[PR & 1][2]), "+v"(f.w[PR & 1][3]));
}
template <int PR> DI void fin_store(Fin& f) {
  asm volatile("global_store_dwordx4 %0, %1, %2 offset:%c3 sc1" ::"v"(f.so), "v"(f.w[PR & 1]), "s"(f.cb[(PR >> 1) & 1]), "i"((PR >> 2) * 2048 + (PR & 1) * 1024) : "memory");
}
template <bool PK, int U> DI void fin_unit(Fin& f) {
  constexpr int I = U >> 1, pr = I >> 1, qb = (pr >> 1) & 1, pp = (pr + 1) & 1;
  constexpr bool PREV = !(I & 1) && pr > 0;
  float* e = reinterpret_cast<float*>(&f.in[I & 15]);
  unsigned* w = reinterpret_cast<unsigned*>(&f.w[pr & 1]) + 2 * (I & 1);
  if constexpr (I == 16) {
    if constexpr (U & 1) { fin_store<7>(f); asm volatile("s_nop 1"); } else { asm volatile("s_nop 1"); fin_swap<7>(f); }
  } else if constexpr (PK) {
    f32x2* t = reinterpret_cast<f32x2*>(&f.in[I]);
    const f32x2* o = reinterpret_cast<const f32x2*>(&f.own[I]);
    if constexpr (!(U & 1)) {
      asm volatile("v_pk_mul_f32 %0, %0, %4 op_sel_hi:[1,0]\n\tv_pk_mul_f32 %1, %1, %4 op_sel_hi:[1,0]\n\t"
                   "v_pk_fma_f32 %0, %2, %4, %0 op_sel:[0,1,0] op_sel_hi:[1,1,1]\n\tv_pk_fma_f32 %1, %3, %4, %1 op_sel:[0,1,0] op_sel_hi:[1,1,1]"
                   : "+v"(t[0]), "+v"(t[1]) : "v"(o[0]), "v"(o[1]), "v"(f.ff[qb]));
      if constexpr (PREV) fin_swap<pr - 1>(f);
    } else if constexpr (PREV) {
      fin_store<pr - 1>(f);
      asm volatile("v_cvt_pk_bf16_f32 %0, %2, %3\n\tv_cvt_pk_bf16_f32 %1, %4, %5" : "=&v"(w[0]), "=&v"(w[1]) : "v"(e[0]), "v"(e[1]), "v"(e[2]), "v"(e[3]), "v"(f.w[pp]));
    } else {
      asm volatile("v_cvt_pk_bf16_f32 %0, %2, %3\n\tv_cvt_pk_bf16_f32 %1, %4, %5" : "=&v"(w[0]), "=&v"(w[1]) : "v"(e[0]), "v"(e[1]), "v"(e[2]), "v"(e[3]));
    }
  } else {
    float* o = f.os[I];
    const float* ff = reinterpret_cast<const float*>(&f.ff[qb]);
    if constexpr (!(U & 1)) {
      asm volatile("v_mul_f32_e32 %0, %0, %10\n\tv_mul_f32_e32 %1, %1, %10\n\tv_mul_f32_e32 %2, %2, %10\n\tv_mul_f32_e32 %3, %3, %10\n\t"
                   "v_fmac_f32_e32 %0, %6, %11\n\tv_fmac_f32_e32 %1, %7, %11\n\tv_fmac_f32_e32 %2, %8, %11\n\tv_fmac_f32_e32 %3, %9, %11\n\t"
                   "v_cvt_pk_bf16_f32 %4, %0, %1\n\tv_cvt_pk_bf16_f32 %5, %2, %3"
                   : "+v"(o[0]), "+v"(o[1]), "+v"(o[2]), "+v"(o[3]), "=&v"(w[0]), "=&v"(w[1]) : "v"(e[0]), "v"(e[1]), "v"(e[2]), "v"(e[3]), "v"(ff[1]), "v"(ff[0]));
    } else if constexpr (PREV) {
      fin_swap<pr - 1>(f);
      fin_store<pr - 1>(f);
    }
  }
}
template <bool PK, int U0, int... Us> DI void fin_units(Fin& f, iseq<Us...>) { (fin_unit<PK, U0 + Us>(f), ...); }
template <bool PK, int G> DI void fin_gap(Fin& f, const AC& c) {
  if constexpr (G == 0) {
    const unsigned ln = lane_id();
    f.ra = (c.rb ^ f.rx) + f.ro;
    f.so = ln * 16;
    asm volatile("ds_read_b128 %0, %1" : "=v"(f.ml) : "v"(f.mb + ln * 16) : "memory");
    asm volatile("ds_read_b128 %0, %1" : "=v"(f.mo) : "v"((f.mb ^ 2048u) + ln * 16) : "memory");
  }
  if constexpr (G < 2) { fin_rd<8 * G>(f); fin_rd<8 * G + 1>(f); fin_rd<8 * G + 2>(f); fin_rd<8 * G + 3>(f); fin_rd<8 * G + 4>(f); fin_rd<8 * G + 5>(f); fin_rd<8 * G + 6>(f); fin_rd<8 * G + 7>(f); }
  if constexpr (G == 3) {
    asm volatile("s_waitcnt lgkmcnt(15)" : "+v"(f.ml), "+v"(f.mo)::"memory");
#pragma unroll
    for (int q = 0; q < 2; ++q) {
      const float m0 = f.mo[2 * q], l0 = f.mo[2 * q + 1], m1 = f.ml[2 * q], l1 = f.ml[2 * q + 1], mt = max2f(m0, m1);
      const float a0 = __builtin_amdgcn_exp2f(m0 - mt), a1 = __builtin_amdgcn_exp2f(m1 - mt);
      const float inv = __builtin_amdgcn_rcpf(fmaf(l0, a0, l1 * a1));
      f.ff[q] = f32x2{a1 * inv, a0 * inv};
    }
  }
  if constexpr (G == 1) fin_own<PK, 0>(f);
  if constexpr (G == 2) { fin_own<PK, 1>(f); fin_own<PK, 2>(f); }
  if constexpr (G >= 4 && G < 8) { fin_own<PK, 2 * G - 5>(f); fin_own<PK, 2 * G - 4>(f); }
  if constexpr (G >= 11 && G < 24 && G % 3 == 2) fin_own<PK, 11 + (G - 11) / 3>(f);
  if constexpr (G == 8)
    asm volatile("s_waitcnt lgkmcnt(0)" : "+v"(f.in[0]), "+v"(f.in[1]), "+v"(f.in[2]), "+v"(f.in[3]), "+v"(f.in[4]), "+v"(f.in[5]), "+v"(f.in[6]), "+v"(f.in[7]),
                 "+v"(f.in[8]), "+v"(f.in[9]), "+v"(f.in[10]), "+v"(f.in[11]), "+v"(f.in[12]), "+v"(f.in[13]), "+v"(f.in[14]), "+v"(f.in[15])::"memory");
  if constexpr (G >= 8) {
    constexpr int k = (G - 9) / 3, r = (G - 9) % 3;
    if constexpr (G == 8) fin_units<PK, 0>(f, make_iseq<2>{});
    else if constexpr (r == 0 || G == 31) fin_units<PK, 4 * k + 2 + 2 * r>(f, make_iseq<2>{});
    else fin_unit<PK, 4 * k + 3 + r>(f);
  }
}
template <int P> DI void peel_dma(AC& c) {
  if constexpr (P < 8) dma<P>(c.ka, c.voff, c.mk);
  else dma<P - 8>(c.va, c.voff, c.mv);
}
constexpr int a2_piece(int m) {
  for (int p = 0; p < 16; ++p) if (10 + (p * 22) / 16 == m) return p;
  return -1;
}
template <int X, int FIRST, int M> DI void gapA(AC& c, Fin& f) {
  constexpr int Xp = X ^ 1;
  qk_mfma<X, FIRST != 0, M>(c);
  GAP();
  if constexpr (M == (FIRST ? 0 : 26)) {
    c.ka = c.Kh + (unsigned)(((c.kt < 0 ? c.kt + c.end0 : c.kt) & 15) * 32768);
    if constexpr (!FIRST) asm volatile("s_cmp_eq_u32 %1, 17\n\ts_cselect_b64 %0, %2, %0\n\ts_cmp_eq_u32 %1, 18\n\ts_cselect_b64 %0, %3, %0" : "+s"(c.ka) : "s"(c.kt + c.end0), "s"(c.wp), "s"(c.wi) : "scc");
    asm volatile("" : "+s"(c.ka));
    c.kt = c.kt + 1; asm volatile("" : "+s"(c.kt));
  }
  if constexpr (M == (FIRST ? 1 : 28)) {
    c.va = c.Vh + (unsigned)(((c.vt < 0 ? c.vt + c.end0 : c.vt) & 15) * 32768);
    if (!FIRST && c.vt + c.end0 == 17) c.va = c.wi + 32768;
    asm volatile("" : "+s"(c.va));
    c.vt = c.vt + 1; asm volatile("" : "+s"(c.vt));
  }
  if constexpr ((FIRST == 1 && M == 15) || (FIRST == 2 && M == 9)) asm volatile("s_barrier" ::: "memory");
  if constexpr (FIRST == 1 && !(M & 1)) peel_dma<M / 2>(c);
  if constexpr (FIRST == 2 && a2_piece(M) >= 0) peel_dma<a2_piece(M)>(c);
  if constexpr (FIRST == 2) fin_gap<false, M>(f, c);
  if constexpr (!FIRST && M == 8) asm volatile("" ::"v"(c.NEGM[0]), "v"(c.NEGM[1]));
  if constexpr (!FIRST) {
    if constexpr (M >= 10 && M < 26) v_read<M - 10>(c);
    if constexpr (M < 16) {
      constexpr int e0 = 2 * M, e1 = 2 * M + 1;
      c.S[Xp][1][e0 >> 4][e0 & 15] = __builtin_amdgcn_exp2f(c.S[Xp][1][e0 >> 4][e0 & 15]);
      c.S[Xp][1][e1 >> 4][e1 & 15] = __builtin_amdgcn_exp2f(c.S[Xp][1][e1 >> 4][e1 & 15]);
      pack1<Xp, (M >> 3), ((M >> 2) & 1), (M & 3)>(c);
    } else {
      constexpr int cc = M - 16;
      pack1<Xp, (cc >> 3), 2 + ((cc >> 2) & 1), (cc & 3)>(c);
      rowsum1<Xp, 16 * (cc >> 3) + 8 + 4 * ((cc >> 2) & 1) + (cc & 3)>(c);
    }
  }
  GAP();
}
template <int X, int FIRST, int... Ms> DI void phaseA_(AC& c, Fin& f, iseq<Ms...>) {
  (gapA<X, FIRST, Ms>(c, f), ...);
  if constexpr (FIRST) {
    if constexpr (FIRST == 1) asm volatile("s_waitcnt vmcnt(16)" ::: "memory");
    asm volatile("s_waitcnt lgkmcnt(0)\n\ts_barrier" ::: "memory");
  } else {
    lsum_merge(c);
    asm volatile("s_waitcnt vmcnt(0) lgkmcnt(0)\n\ts_barrier" : AT_VF, "+v"(c.pe)::"memory");
  }
  GAP();
}
template <int X, int FIRST> DI void phaseA(AC& c, Fin& f) { phaseA_<X, FIRST>(c, f, make_iseq<32>{}); }

template <int X, int G> DI void mask4(AC& c, int lim, float ninf) {
  float* a = reinterpret_cast<float*>(&c.S[X][0][0]) + 4 * G;
  float* b = reinterpret_cast<float*>(&c.S[X][1][1]) + 4 * G;
  unsigned long long s0, s1, s2;
  asm volatile("v_cmp_gt_i32_e32 vcc, %c13, %11\n\tv_cmp_gt_i32_e64 %8, %c14, %11\n\tv_cmp_gt_i32_e64 %9, %c15, %11\n\tv_cmp_gt_i32_e64 %10, %c16, %11\n\t"
               "v_cndmask_b32_e32 %0, %0, %12, vcc\n\tv_cndmask_b32_e32 %4, %4, %12, vcc\n\tv_cndmask_b32_e64 %1, %1, %12, %8\n\tv_cndmask_b32_e64 %5, %5, %12, %8\n\t"
               "v_cndmask_b32_e64 %2, %2, %12, %9\n\tv_cndmask_b32_e64 %6, %6, %12, %9\n\tv_cndmask_b32_e64 %3, %3, %12, %10\n\tv_cndmask_b32_e64 %7, %7, %12, %10"
               : "+v"(a[0]), "+v"(a[1]), "+v"(a[2]), "+v"(a[3]), "+v"(b[0]), "+v"(b[1]), "+v"(b[2]), "+v"(b[3]), "=&s"(s0), "=&s"(s1), "=&s"(s2)
               : "v"(lim), "v"(ninf), "i"(8 * G), "i"(8 * G + 1), "i"(8 * G + 2), "i"(8 * G + 3) : "vcc");
}
DI void fill16(f32x16& v, float x) {
  float* a = reinterpret_cast<float*>(&v);
#pragma unroll
  for (int g = 0; g < 16; g += 4)
    asm volatile("v_mov_b32 %0, %4\n\tv_mov_b32 %1, %4\n\tv_mov_b32 %2, %4\n\tv_mov_b32 %3, %4" : "+v"(a[g]), "+v"(a[g + 1]), "+v"(a[g + 2]), "+v"(a[g + 3]) : "v"(x));
}
template <int X> DI void at_mask(AC& c) {
  const float ninf = -__builtin_inff();
  if (c.dd >= 0) {
    fill16(c.S[X][1][0], ninf);
    const unsigned l = lane_id();
    const int lim = (int)(l & 31u) - (int)((l >> 5) << 2) - 64 * c.dd;
    mask4<X, 0>(c, lim, ninf); mask4<X, 1>(c, lim, ninf); mask4<X, 2>(c, lim, ninf); mask4<X, 3>(c, lim, ninf);
    if (c.dd > 0) fill16(c.S[X][0][1], ninf);
  }
}
template <int I> DI void q_fetch(unsigned a) {
  asm volatile("ds_read_b128 a[%c1:%c2], %0 offset:%c3" ::"v"(a), "i"(128 + 4 * I), "i"(131 + 4 * I), "i"(I * 1024) : "memory", ACLOB_HI);
}
template <int... Is> DI void q_fetches(unsigned a, iseq<Is...>) { (q_fetch<Is>(a), ...); }
template <int... Is> DI void q_loads(const char* qp, unsigned voff, iseq<Is...>) { (q_load<Is>(qp, voff), ...); }
template <int X, bool PEEL, int M> DI void gapB(AC& c) {
  if constexpr (PEEL) {
    if constexpr (M < 16) k_read<M>(c.rb);
    if constexpr (M >= 20 && M < 28) {
      const u32x4 z = {0u, 0u, 0u, 0u};
      asm volatile("s_nop 1\n\tv_mfma_f32_32x32x16_bf16 a[%c1:%c2], %0, %0, 0" ::"v"(z), "i"(16 * (M - 20)), "i"(16 * (M - 20) + 15) : ACLOB_LO);
    }
  } else {
    pv_mfma_ld<M, (M & 1) ? -1 : M / 2, M < 16 ? M : -1>(c);
  }
  GAP();
  if constexpr (X == 1 && M == 16) {
    if (c.dd == c.ddq) q_fetches(c.qa + c.voff, make_iseq<16>{});
  }
  if constexpr (!PEEL && X == 0 && M == 3) asm volatile("global_load_dword %0, %1, %2 sc1" : "=v"(c.pe) : "v"(0u), "s"(c.sig) : "memory");
  if constexpr (!PEEL && M == 2) {
    if (c.dd == c.dds && lane_id() == 0) arrive(c.sig, 1u);
  }
  if constexpr (M == 4) at_mask<X>(c);
  if constexpr (M >= 4 && M < 15) {
    constexpr int s0 = 3 * (M - 4);
#pragma unroll
    for (int s = s0; s < s0 + 3; ++s) {
      if (s < 32) {
        const int q = s >> 4, i = s & 15, ch = i & 3, kb = ch >> 1, e = (ch & 1) * 8 + (i >> 2) * 2;
        if (i < 4) c.pmx[q][ch] = max2f(c.S[X][kb][q][e], c.S[X][kb][q][e + 1]);
        else c.pmx[q][ch] = max3f(c.pmx[q][ch], c.S[X][kb][q][e], c.S[X][kb][q][e + 1]);
      }
    }
  }
  if constexpr (M == 15 || M == 16) {
    constexpr int q = M - 15;
    const float mm = max2f(max3f(c.pmx[q][0], c.pmx[q][1], c.pmx[q][2]), c.pmx[q][3]);
    auto pr = __builtin_amdgcn_permlane32_swap(__float_as_uint(mm), __float_as_uint(mm), false, false);
    c.rmax[q] = max2f(__uint_as_float(pr[0]), __uint_as_float(pr[1]));
  }
  if constexpr (M == 17) {
    if constexpr (PEEL) {
#pragma unroll
      for (int q = 0; q < 2; ++q) {
        c.mhat[q] = max2f(c.rmax[q], -1e30f);
#pragma unroll
        for (int kb = 0; kb < 2; ++kb)
#pragma unroll
          for (int e = 0; e < 16; ++e) c.S[X][kb][q][e] = fsub_s(c.S[X][kb][q][e], c.mhat[q]);
      }
#pragma unroll
      for (int q = 0; q < 2; ++q) {
        c.lsum[q] = 0.f; c.fct[q] = 1.f;
        negm_bcast<true>(c.NEGM[q], -c.mhat[q]);
      }
    } else {
      const bool grew = max2f(c.rmax[0], c.rmax[1]) > AT_THR;
      if (__builtin_expect(__any((int)grew), 0)) {
#pragma unroll
        for (int q = 0; q < 2; ++q) {
          const float dl = max2f(c.rmax[q], 0.f);
          c.mhat[q] = fadd_s(c.mhat[q], dl); c.fct[q] = __builtin_amdgcn_exp2f(-dl);
#pragma unroll
          for (int kb = 0; kb < 2; ++kb)
#pragma unroll
            for (int e = 0; e < 16; ++e) c.S[X][kb][q][e] = fsub_s(c.S[X][kb][q][e], dl);
          negm_bcast<false>(c.NEGM[q], -c.mhat[q]);
        }
        c.pend = true;
      }
    }
    c.rs0[0] = c.rs0[1] = c.rs1[0] = c.rs1[1] = 0.f;
  }
  if constexpr (M >= 18) {
    constexpr int g = M - 18, e_lo = (32 * g) / 14, e_hi = (32 * (g + 1)) / 14;
    constexpr int p_lo = g ? (32 * (g - 1)) / 14 : 0, p_hi = g ? (32 * g) / 14 : 0;
#pragma unroll
    for (int e = e_lo; e < e_hi; ++e) c.S[X][0][e >> 4][e & 15] = __builtin_amdgcn_exp2f(c.S[X][0][e >> 4][e & 15]);
#pragma unroll
    for (int e = p_lo; e < p_hi; ++e) {
      if (e & 1) c.rs1[e >> 4] = fadd_s(c.rs1[e >> 4], c.S[X][0][e >> 4][e & 15]);
      else c.rs0[e >> 4] = fadd_s(c.rs0[e >> 4], c.S[X][0][e >> 4][e & 15]);
    }
  }
  if constexpr (M == 31) asm volatile("" ::"v"(c.S[X][0][0]), "v"(c.S[X][0][1]), "v"(c.NEGM[0]), "v"(c.NEGM[1]), "v"(c.rs0[0]), "v"(c.rs0[1]), "v"(c.rs1[0]), "v"(c.rs1[1]));
  GAP();
}
template <int I> DI void o_scale1(float f) {
  float t;
  asm volatile("v_accvgpr_read_b32 %0, a[%c2]\n\tv_mul_f32 %0, %1, %0\n\tv_accvgpr_write_b32 a[%c2], %0" : "=&v"(t) : "v"(f), "i"(I) : ACLOB_LO);
}
template <int... Is> DI void o_scale(const AC& c, iseq<Is...>) { (o_scale1<Is>(c.fct[(Is >> 4) & 1]), ...); }
template <int X, bool PEEL, int... Ms> DI void phaseB_(AC& c, iseq<Ms...>) {
  (gapB<X, PEEL, Ms>(c), ...);
#pragma unroll
  for (int e = (32 * 13) / 14; e < 32; ++e) {
    if (e & 1) c.rs1[e >> 4] = fadd_s(c.rs1[e >> 4], c.S[X][0][e >> 4][e & 15]);
    else c.rs0[e >> 4] = fadd_s(c.rs0[e >> 4], c.S[X][0][e >> 4][e & 15]);
  }
  if constexpr (!PEEL) {
    if (__builtin_expect(c.pend, 0)) {
      asm volatile("s_nop 7\n\ts_nop 7" ::: "memory");
      o_scale(c, make_iseq<128>{});
      asm volatile("s_nop 1" ::: "memory");
      c.lsum[0] = fmul_s(c.lsum[0], c.fct[0]); c.lsum[1] = fmul_s(c.lsum[1], c.fct[1]); c.pend = false;
    }
  }
  c.dd += 2;
  c.mk ^= 65536u; c.mv ^= 65536u;
  asm volatile("s_waitcnt lgkmcnt(0)\n\tv_xor_b32_e32 %0, 0x10000, %0" : "+v"(c.rb)::"memory");
  GAP();
}
template <int X, bool PEEL> DI void phaseB(AC& c) { phaseB_<X, PEEL>(c, make_iseq<32>{}); }
template <int... Is> DI void k_reads(unsigned rb, iseq<Is...>) { (k_read<Is>(rb), ...); }
constexpr int dr_ks(int n) { return n < 16 ? n >> 3 : 2 + ((n >> 2) & 1); }
constexpr int dr_db(int n) { return ((n < 16 ? !(n & 4) : n < 24) ? 2 : 0) + ((n >> 1) & 1); }
template <int F> DI void dr_vread(AC& c) { v_read<4 * dr_ks(2 * F) + dr_db(2 * F)>(c); }
template <int... Fs> DI void dr_vreads(AC& c, iseq<Fs...>) { (dr_vread<Fs>(c), ...); }
template <int I> DI void o_put(unsigned a) {
  asm volatile("ds_write_b128 %0, a[%c1:%c2] offset:%c3" ::"v"(a), "i"(64 + 4 * I), "i"(67 + 4 * I), "i"(I * 1024) : "memory");
}
template <int X, bool LAST, int N> DI void drain_gap(AC& c, unsigned sa, unsigned mw, const char* cn) {
  if constexpr (N == 0) asm volatile("s_waitcnt lgkmcnt(14)" : "+v"(c.Vf[2][0]), "+v"(c.Vf[3][0])::"memory");
  if constexpr (N == 4) asm volatile("s_waitcnt lgkmcnt(12)" : "+v"(c.Vf[0][0]), "+v"(c.Vf[1][0])::"memory");
  if constexpr (N == 8) asm volatile("s_waitcnt lgkmcnt(8)" : "+v"(c.Vf[0][1]), "+v"(c.Vf[1][1]), "+v"(c.Vf[2][1]), "+v"(c.Vf[3][1])::"memory");
  if constexpr (N == 16)
    asm volatile("s_waitcnt lgkmcnt(0)\n\ts_barrier" : "+v"(c.Vf[0][2]), "+v"(c.Vf[1][2]), "+v"(c.Vf[2][2]), "+v"(c.Vf[3][2]), "+v"(c.Vf[0][3]), "+v"(c.Vf[1][3]), "+v"(c.Vf[2][3]), "+v"(c.Vf[3][3])::"memory");
  pv_mfma<8 * dr_ks(N) + 2 * dr_db(N) + (N & 1)>(c);
  GAP();
  if constexpr (LAST) q_load<N>(cn, c.voff);
  if constexpr (N < 16) {
    constexpr int qb = (N >> 2) & 1, e = (N & 8) + 2 * (N & 3);
    c.S[X][1][qb][e] = __builtin_amdgcn_exp2f(c.S[X][1][qb][e]);
    c.S[X][1][qb][e + 1] = __builtin_amdgcn_exp2f(c.S[X][1][qb][e + 1]);
    pack1<X, qb, 1 + (N >> 3), (N & 3)>(c);
  } else if constexpr (N < 20) {
    pack1<X, ((N >> 1) & 1), 3, 2 * (N & 1)>(c);
    pack1<X, ((N >> 1) & 1), 3, 2 * (N & 1) + 1>(c);
    rowsum1<X, 16 * (N & 1) + 8 + ((N & 3) >> 1)>(c);
  } else if constexpr (N < 24) {
    constexpr int r = 4 + 3 * (N - 20);
    rowsum1<X, 16 * (r & 1) + 8 + (r >> 1)>(c);
    rowsum1<X, 16 * ((r + 1) & 1) + 8 + ((r + 1) >> 1)>(c);
    rowsum1<X, 16 * ((r + 2) & 1) + 8 + ((r + 2) >> 1)>(c);
  } else {
    o_put<2 * (N - 24)>(sa); o_put<2 * (N - 24) + 1>(sa);
    if constexpr (N == 24) lsum_merge(c);
    if constexpr (N == 25) {
      const f32x4 ml = {c.mhat[0], c.lsum[0], c.mhat[1], c.lsum[1]};
      asm volatile("ds_write_b128 %0, %1" ::"v"(mw), "v"(ml) : "memory");
    }
  }
  GAP();
}
template <int X, bool LAST, int... Ns> DI void at_finish(AC& c, unsigned sa, unsigned mw, const char* cn, iseq<Ns...>) {
  asm volatile("" : "+v"(c.S[X][0][0]), "+v"(c.S[X][0][1]), "+v"(c.S[X][1][0]), "+v"(c.S[X][1][1]));
  dr_vreads(c, make_iseq<16>{});
  pack1<X, 0, 0, 0>(c); pack1<X, 0, 0, 1>(c); pack1<X, 0, 0, 2>(c); pack1<X, 0, 0, 3>(c);
  pack1<X, 1, 0, 0>(c); pack1<X, 1, 0, 1>(c); pack1<X, 1, 0, 2>(c); pack1<X, 1, 0, 3>(c);
  GAP();
  (drain_gap<X, LAST, Ns>(c, sa, mw, cn), ...);
  asm volatile("s_waitcnt vmcnt(%c0) lgkmcnt(0)\n\ts_barrier\n\ts_nop 7\n\ts_nop 3" ::"i"(LAST ? 32 : 0) : "memory");
  GAP();
}

constexpr float QSCALE = 0.08838834764831845f * 1.4426950408889634f;
constexpr int NBLK = 43, Q_SD = 14;
constexpr int Q_NP = 18, Q_MX = 4;
struct QSched { int npre, pre[160], ops[32][5]; bool ok; };
constexpr QSched qp_sched(int L) {
  QSched q{};
  int n = 0;
  q.pre[n++] = 0; q.pre[n++] = 1;
  for (int s = 0; s < L; ++s) {
    if (s == 0 || s == 2 || s == 6) for (int i = 0; i < 4; ++i) q.pre[n++] = 200 + (s + 2) / 4 * 4 + i;
    q.pre[n++] = 100 + 2 * s; q.pre[n++] = 101 + 2 * s;
  }
  q.pre[n++] = 2; q.pre[n++] = 3; q.pre[n++] = 4;
  q.ok = true;
  int due[32] = {};
  int e = 0;
  for (int s = 0; s < 32; ++s)
    for (int i = 0; i < 3; ++i) {
      if (i < 2 && s + L < 32) { q.pre[n++] = 100 + 2 * (s + L) + i; continue; }
      const int code = e < 20 ? 212 + e : e < 44 ? 300 + (e - 20) / 8 * 10 + (e - 20) % 8 : -1;
      if (code >= 216 && code < 300 && s < (code - 200) / 4 * 4 - 14) q.ok = false;
      if (code >= 320 && s < 22) q.ok = false;
      if (code >= 0) q.pre[n++] = code;
      due[s] = n;
      ++e;
    }
  int k = q.npre = Q_NP;
  for (int s = 0; s < 32; ++s) {
    const int m = due[s] - k < Q_MX ? due[s] - k : Q_MX;
    for (int i = 0; i < 5; ++i) q.ops[s][i] = (m > 4 || (m == 4 ? i != 3 : !(i & 1) && i < 2 * m)) ? q.pre[k++] : -1;
  }
  if (e < 44 || k != n) q.ok = false;
  return q;
}
constexpr QSched QS = qp_sched(9);
static_assert(QS.ok, "schedule");
constexpr int qp_younger(int code, int step) {
  int n = 0;
  bool seen = false;
  for (int i = 0; i < QS.npre; ++i) { if (seen) ++n; if (QS.pre[i] == code) seen = true; }
  for (int s = 0; s < step; ++s)
    for (int i = 0; i < 5; ++i)
      if (QS.ops[s][i] >= 0) { if (seen) ++n; if (QS.ops[s][i] == code) seen = true; }
  return seen ? n : -1000;
}
constexpr int imin(int a, int b) { return a < b ? a : b; }
constexpr int qp_wait(int s) {
  int w = 63;
  if (s + 1 < 32) w = imin(w, qp_younger(100 + 2 * (s + 1) + 1, s));
  if ((s & 3) == 2 && s <= 26) w = imin(w, qp_younger(200 + (s + 2) + 3, s));
  return w;
}
static_assert(qp_wait(Q_SD - 4) <= qp_younger(4, Q_SD - 4), "side job");
struct QP {
  u32x4 x[16][2];
  u32x2 w[3][4][2];
  f32x4 t[2][4];
  u32x4 h[2][2];
  unsigned wa[4], ta, voffw, wl;
  const char *xsrc, *wsrc;
  unsigned wo_, mo_, sl;
  const char *sw, *sm;
  char* sd;
  u32x2 wr[4];
  float w0, w1, su, sv;
  f32x4 sc[8];
  u32x4 pk;
  bool side;
};
template <int C> DI void qp_op(QP& q, AC& c) {
  if constexpr (C >= 320) dma<C - 320>(c.Kh + (c.end0 > 1 ? 32768 : 0), c.voff, c.mk + 65536);
  else if constexpr (C >= 310) dma<C - 310>(c.Vh, c.voff, c.mv);
  else if constexpr (C >= 300) dma<C - 300>(c.Kh, c.voff, c.mk);
  else if constexpr (C >= 200) {
    constexpr int e = (C - 200) >> 2, i = C & 3;
    asm volatile("s_add_u32 m0, %2, %c3\n\ts_nop 0\n\tglobal_load_lds_dwordx4 %0, %1" ::"v"(q.voffw), "s"(q.wsrc + e * 524288 + i * 32768),
                 "s"(q.wl), "i"((e & 3) * 16384 + i * 1024) : "memory", "scc");
  } else if constexpr (C >= 100) {
    constexpr int S = (C - 100) >> 1, QB = C & 1;
    asm volatile("global_load_dwordx4 %0, %1, %2 offset:%c3" : "=v"(q.x[S & 15][QB]) : "v"(c.voff), "s"(q.xsrc + QB * 32768 + (S >> 2) * 4096), "i"((S & 3) * 1024) : "memory");
  } else if constexpr (C >= 2) {
    asm volatile("s_add_u32 m0, %2, %c3\n\ts_nop 0\n\tglobal_load_lds_dwordx4 %0, %1" ::"v"(C < 4 ? (c.voff >> 5) * (FF * 2u) + (c.voff & 16u) : (c.voff & 256u) * 8u + (c.voff & 240u)),
                 "s"(C < 4 ? q.sw + (C - 2) * 64 * FF : q.sm), "s"(q.sl), "i"((C - 2) * 1024) : "memory", "scc");
  }
}
template <int I0, int... Is> DI void qp_pre_ops(QP& q, AC& c, iseq<Is...>) { (qp_op<QS.pre[I0 + Is]>(q, c), ...); }
template <int S, int G> DI void w_read(QP& q) {
  asm volatile("ds_read_b64_tr_b16 %0, %1 offset:%c2" : "=v"(q.w[S % 3][G >> 1][G & 1]) : "v"(q.wa[G >> 1]), "i"((S & 15) * 4096 + (G & 1) * 1024) : "memory");
}
template <int S, int E> DI void t_read(QP& q) {
  asm volatile("ds_read_b128 %0, %1 offset:%c2" : "=v"(q.t[S & 1][E]) : "v"(q.ta), "i"(S * 128 + E * 16) : "memory");
}
template <int S, int G> DI void h_make(QP& q) {
  constexpr int qb = G >> 2, e = G & 3;
  float lo, hi;
  asm volatile("v_fma_mix_f32 %0, %2, %3, %5 op_sel_hi:[1,0,0]\n\tv_fma_mix_f32 %1, %2, %4, %6 op_sel:[1,0,0] op_sel_hi:[1,0,0]"
               : "=&v"(lo), "=&v"(hi) : "v"(q.x[S & 15][qb][e]), "v"(q.t[S & 1][e >> 1][(e & 1) * 2]), "v"(q.t[S & 1][e >> 1][(e & 1) * 2 + 1]),
                 "v"(q.t[S & 1][2 + (e >> 1)][(e & 1) * 2]), "v"(q.t[S & 1][2 + (e >> 1)][(e & 1) * 2 + 1]));
  unsigned d = pack2(lo, hi);
  asm volatile("" : "+v"(d));
  q.h[S & 1][qb][e] = d;
}
template <int I> DI void qp_sread(QP& q) {
  constexpr int t = I / 6, r = I % 6, n = 4 * t + r - 2;
  if constexpr (r < 2) asm volatile("ds_read_b64_tr_b16 %0, %1 offset:%c2" : "=v"(q.wr[2 * t + r]) : "v"(q.wo_), "i"(t * 1024 + r * 128) : "memory");
  else asm volatile("ds_read_b128 %0, %1 offset:%c2" : "=v"(q.sc[n]) : "v"(q.mo_), "i"((n & 1) * 256 + t * 128 + (n & 2) * 8) : "memory");
}
template <int P, int G> DI void qp_side(QP& q, unsigned voff) {
  if constexpr (G == 0) {
    asm volatile("" : "+v"(q.wr[P >> 1]), "+v"(q.sc[P & 6]), "+v"(q.sc[P | 1]));
    asm volatile("v_lshlrev_b32_e32 %0, 16, %2\n\tv_and_b32_e32 %1, 0xffff0000, %2" : "=&v"(q.w0), "=&v"(q.w1) : "v"(q.wr[P >> 1][P & 1]));
  } else if constexpr (G == 2) {
    asm volatile("v_fmac_f32_e32 %0, %1, %3\n\tv_fmac_f32_e32 %0, %2, %4" : "+v"(q.sv) : "v"(q.w0), "v"(q.w1), "v"(q.sc[P & 6][2 * (P & 1)]), "v"(q.sc[P & 6][2 * (P & 1) + 1]));
  } else if constexpr (G == 3) {
    asm volatile("v_fmac_f32_e32 %0, %0, %2\n\tv_fmac_f32_e32 %1, %1, %3" : "+v"(q.w0), "+v"(q.w1) : "v"(q.sc[P | 1][2 * (P & 1)]), "v"(q.sc[P | 1][2 * (P & 1) + 1]));
  } else if constexpr (G == 6) {
    asm volatile("v_cvt_pk_bf16_f32 %1, %2, %3\n\tv_dot2c_f32_bf16_e32 %0, 0x3f803f80, %1" : "+v"(q.su), "=&v"(q.pk[P & 3]) : "v"(q.w0), "v"(q.w1));
    if constexpr ((P & 3) == 3) {
      if (q.side) asm volatile("global_store_dwordx4 %0, %1, %2 sc1\n\ts_nop 1" ::"v"(voff), "v"(q.pk), "s"(q.sd + (P >> 2) * (2 * NBLK * 1024)) : "memory");
    }
  }
}
#define QP_T(i) "+v"(q.t[i][0]), "+v"(q.t[i][1]), "+v"(q.t[i][2]), "+v"(q.t[i][3])
#define QP_W(i) "+v"(q.w[i][0][0]), "+v"(q.w[i][0][1]), "+v"(q.w[i][1][0]), "+v"(q.w[i][1][1]), "+v"(q.w[i][2][0]), "+v"(q.w[i][2][1]), "+v"(q.w[i][3][0]), "+v"(q.w[i][3][1])
#define QP_X(i) "+v"(q.x[i][0]), "+v"(q.x[i][1])
template <int S, int G> DI void qp_gap(QP& q, AC& c) {
  constexpr int db = G >> 1, qb = G & 1;
  const u32x4 wf = {q.w[S % 3][db][0][0], q.w[S % 3][db][0][1], q.w[S % 3][db][1][0], q.w[S % 3][db][1][1]};
  if constexpr (S == 0) asm volatile("v_mfma_f32_32x32x16_bf16 a[%c2:%c3], %0, %1, 0" ::"v"(wf), "v"(q.h[S & 1][qb]), "i"(16 * G), "i"(16 * G + 15) : ACLOB_LO);
  else asm volatile("v_mfma_f32_32x32x16_bf16 a[%c2:%c3], %0, %1, a[%c2:%c3]" ::"v"(wf), "v"(q.h[S & 1][qb]), "i"(16 * G), "i"(16 * G + 15) : ACLOB_LO);
  GAP();
  if constexpr (S + 2 < 32) {
    if constexpr (G < 2) { t_read<S + 2, 2 * G>(q); t_read<S + 2, 2 * G + 1>(q); }
    else if constexpr (G < 6) { w_read<S + 2, 2 * (G - 2)>(q); w_read<S + 2, 2 * (G - 2) + 1>(q); }
  }
  if constexpr (G % 3) qp_op<QS.ops[S][G / 3 * 2 + G % 3 - 1]>(q, c);
  if constexpr (S + 1 < 32) h_make<S + 1, G>(q);
  if constexpr (S >= Q_SD - 4 && S < Q_SD + 2 && G >= 6) qp_sread<2 * (S - Q_SD + 4) + G - 6>(q);
  if constexpr (S >= Q_SD && S < Q_SD + 8) qp_side<S - Q_SD, G>(q, c.voff);
  GAP();
}
template <int S, int... Gs> DI void qp_step(QP& q, AC& c, iseq<Gs...>) {
  static_assert(qp_wait(S) >= 0, "schedule");
  if constexpr ((S & 3) == 2 && S <= 26)
  {
    asm volatile("s_waitcnt vmcnt(%c14) lgkmcnt(0)\n\ts_barrier" : QP_T((S + 1) & 1), QP_W(S % 3), QP_X((S + 1) & 15) : "i"(qp_wait(S)) : "memory");
  }
  else
    asm volatile("s_waitcnt vmcnt(%c14) lgkmcnt(%c15)" : QP_T((S + 1) & 1), QP_W(S % 3), QP_X((S + 1) & 15) : "i"(qp_wait(S)), "i"(S == 31 ? 0 : 8) : "memory");
  GAP();
  (qp_gap<S, Gs>(q, c), ...);
}
template <int... Ss> DI void qp_steps(QP& q, AC& c, iseq<Ss...>) { (qp_step<Ss>(q, c, make_iseq<8>{}), ...); }
template <int S, int... Gs> DI void qp_pre(QP& q, iseq<Gs...>) { (w_read<S, Gs>(q), ...); }
template <int... Gs> DI void qp_h0(QP& q, iseq<Gs...>) { (h_make<0, Gs>(q), ...); }
template <int I> DI u32x4 q_pack() {
  constexpr int r = 16 * (2 * ((I & 7) >> 1) + (I >> 3)) + 4 * (I & 1);
  return u32x4{pack2(o_read<r>(), o_read<r + 1>()), pack2(o_read<r + 2>(), o_read<r + 3>()),
               pack2(o_read<r + 8>(), o_read<r + 9>()), pack2(o_read<r + 10>(), o_read<r + 11>())};
}
template <int... Is> DI void q_saves(char* dst, iseq<Is...>) { ((*reinterpret_cast<u32x4*>(dst + Is * 1024) = q_pack<Is>()), ...); }

DI int x1_off(int mblk, int ntile, int wq, int lane) {
  return ((mblk * 16 + ntile * 2 + (wq >> 1)) * 64 + ((wq & 1) * 2 + (lane >> 5)) * 16 + (lane & 15)) * 16 + ((lane >> 4) & 1) * 8;
}

DI u32x4 widen(u32x2 a, u32x2 b) {
  const auto r0 = __builtin_amdgcn_permlane16_swap(a[0], b[0], false, false);
  const auto r1 = __builtin_amdgcn_permlane16_swap(a[1], b[1], false, false);
  return u32x4{r0[0], r1[0], r0[1], r1[1]};
}
DI float gelu_tanh(float x) {
  constexpr float K1 = -2.f * 0.7978845608028654f * 1.4426950408889634f, K2 = K1 * 0.044715f;
  const float e = __builtin_amdgcn_exp2f(x * fmaf(x * x, K2, K1));
  return x * __builtin_amdgcn_rcpf(1.f + e);
}
constexpr int T4_RS = 131072, T4_RED = 147456, T4_Z1 = T4_RED + 2048, T4_ST = T4_Z1 + 12288;
constexpr int o_ks(int H, int N) { return !H ? (N & 63) >> 1 : N < 96 ? N >> 2 : 24 + ((N & 15) >> 1); }
constexpr int o_cb(int H, int N) { return !H ? (N >> 6) * 2 + (N & 1) : N < 96 ? N & 3 : ((N >> 4) & 1) * 2 + (N & 1); }
constexpr int O_LA = 6, O_QA = 10, O_B1 = 56, O_W0 = 40, O_PL = 112, O_B3 = 8, O_XL = 100, O_XE = 88, O_SL = 2, O_SM = 30, O_SX = 62;
constexpr int o_early(int n) { return (n < O_W0 ? 0 : n < O_W0 + 24 ? n - O_W0 : n < O_W0 + 27 ? 2 * (n - O_W0) - 24 : 30) + (n <= O_SL ? 0 : n < O_SL + 16 ? (n - O_SL + 1) / 2 : 8); }
struct OC {
  unsigned voff, ra[2], so, wo_, mo_;
  const char *xs, *gs, *ps, *qs, *wsrc[3], *uvp[3];
  char* pt[2];
  bf16x8 wf[8], wv[8];
  u32x4 pk[2], xv[2], pp[8];
  f32x4 gv[2];
  f32x2 uq[3], vq[3];
  unsigned *cs, *ce;
  const unsigned* cr;
  unsigned pw, pe;
  unsigned t[2], mx, vp, sl;
  const char* sp;
  char *sd, *ss;
};
template <int H, int N> DI void o_read1(OC& o) {
  constexpr int cb = o_cb(H, N);
  asm volatile("ds_read_b128 %0, %1 offset:%c2" : "=v"(o.wf[N & 7]) : "v"(o.ra[cb & 1]), "i"(((cb ^ (cb >> 1)) & 1) * 32768 + o_ks(H, N) * 1024) : "memory");
}
template <int H, int... Ns> DI void o_reads(OC& o, iseq<Ns...>) { (o_read1<H, Ns>(o), ...); }
template <int H, int P> DI void o_part(OC& o) {
  constexpr int pc = P / 5, q = P % 5, r = 64 + 8 * pc + 2 * q;
  if constexpr (q < 4) {
    float t;
    asm volatile("v_accvgpr_read_b32 %0, a[%c2]\n\tv_accvgpr_read_b32 %1, a[%c3]\n\tv_cvt_pk_bf16_f32 %1, %0, %1" : "=&v"(t), "=&v"(o.pk[pc & 1][q]) : "i"(r), "i"(r + 1));
  } else {
    asm volatile("global_store_dwordx4 %0, %1, %2 offset:%c3 sc1\n\ts_nop 1" ::"v"(o.so), "v"(o.pk[pc & 1]), "s"(o.pt[H]), "i"(pc * 512) : "memory");
  }
}
template <int H, int P0, int... Ps> DI void o_parts(OC& o, iseq<Ps...>) { (o_part<H, P0 + Ps>(o), ...); }
constexpr int w_reg(int n) { return n < 16 ? 4 * n : 64 + 4 * n; }
template <int N> DI void w_load(OC& o) {
  const char* src = o.wsrc[N % 3] + (N / 3) * (2 * NBLK * 1024);
  if constexpr (N < 40) asm volatile("global_load_dwordx4 a[%c2:%c3], %0, %1 sc1" ::"v"(o.voff), "s"(src), "i"(w_reg(N)), "i"(w_reg(N) + 3) : "memory", ACLOB_LO, ACLOB_HI);
  else asm volatile("global_load_dwordx4 %0, %1, %2 sc1" : "=v"(o.wv[N - 40]) : "v"(o.voff), "s"(src) : "memory");
}
template <int H, int G> DI void p_load(OC& o) {
  asm volatile("global_load_dwordx4 %0, %1, %2 sc1" : "=v"(o.pp[G]) : "v"(o.voff), "s"(o.ps + G * (S * DM * 2) + H * 32768) : "memory");
}
template <int H, int... Gs> DI void p_loads(OC& o, iseq<Gs...>) { (p_load<H, Gs>(o), ...); }
constexpr int o_late(int n) {
  return (n > O_XL) + imin(n, 32 - O_QA) + (n > 28 ? imin(24, (n - 26) / 3) : 0) + (n < O_SX ? 0 : n < O_SX + 32 ? 1 + (n - O_SX) / 4 : 9);
}
#define PP_ALL "+v"(o.pp[0]), "+v"(o.pp[1]), "+v"(o.pp[2]), "+v"(o.pp[3]), "+v"(o.pp[4]), "+v"(o.pp[5]), "+v"(o.pp[6]), "+v"(o.pp[7])
template <int N> DI void o_side(OC& o) {
  if constexpr (N == O_SM - 16) { o.vp = o.sl + ((o.voff >> 4) & 7u) * 128u + (((o.voff >> 3) ^ o.voff) & 0x70u); asm volatile("" : "+v"(o.vp)); }
  if constexpr (N > O_SM - 16 && N <= O_SM - 8) asm volatile("ds_read_b128 %0, %1 offset:%c2" : "=v"(o.pp[N - O_SM + 15]) : "v"(o.vp), "i"((N - O_SM + 15) * 1024) : "memory");
  if constexpr (N == O_SM) asm volatile("" : PP_ALL);
  if constexpr (constexpr int n = N - O_SM; n >= 0 && n < 16) {
    if constexpr (n == 0) asm volatile("v_pk_max_u16 %0, %2, %3\n\tv_pk_max_i16 %1, %2, %3" : "=&v"(o.t[0]), "=&v"(o.t[1]) : "v"(o.pp[0][0]), "v"(o.pp[0][1]));
    else asm volatile("v_pk_max_u16 %0, %0, %2\n\tv_pk_max_i16 %1, %1, %2\n\tv_pk_max_u16 %0, %0, %3\n\tv_pk_max_i16 %1, %1, %3"
                      : "+v"(o.t[0]), "+v"(o.t[1]) : "v"(o.pp[n >> 1][(n & 1) * 2]), "v"(o.pp[n >> 1][(n & 1) * 2 + 1]));
  } else if constexpr (n >= 16 && N < O_SX) {
    if constexpr (n == 16) { o.t[0] &= 0x7fff7fffu; o.t[1] &= 0x7fff7fffu; asm volatile("v_pk_max_u16 %0, %0, %1" : "+v"(o.t[0]) : "v"(o.t[1])); }
    if constexpr (n == 17) o.t[0] = max(o.t[0] & 0xffffu, o.t[0] >> 16);
    if constexpr (n == 18) o.t[0] = max(o.t[0], (unsigned)__builtin_amdgcn_update_dpp(0, (int)o.t[0], 0x128, 0xf, 0xf, false));
    if constexpr (n == 19) { const auto a = __builtin_amdgcn_permlane16_swap(o.t[0], o.t[0], false, false); o.t[0] = max(a[0], a[1]); }
    if constexpr (n == 20) { const auto a = __builtin_amdgcn_permlane32_swap(o.t[0], o.t[0], false, false); o.t[0] = max(max(a[0], a[1]) >> 7, 30u); }
    if constexpr (n == 21) { o.mx = (o.t[0] - 30u) * 0x00800080u; o.t[0] = (o.t[0] - 15u) << 23; asm volatile("" : "+v"(o.mx)); }
    if constexpr (n == 22) { o.vp = (o.voff >> 9) * 4096u + ((o.voff >> 7) & 3u) * 256u + (o.voff & 127u); asm volatile("" : "+v"(o.vp)); }
    if constexpr (N == O_SX - 1) asm volatile("s_lshr_b64 exec, exec, 32\n\tglobal_store_dword %0, %1, %2 sc1\n\ts_mov_b64 exec, -1" ::"v"(o.voff >> 2), "v"(o.t[0]), "s"(o.ss) : "memory", "scc");
    asm volatile("" : "+v"(o.t[0]));
  } else if constexpr (constexpr int i = (N - O_SX) >> 2, e = (N - O_SX) & 2; N >= O_SX && i < 8) {
    if constexpr (!((N - O_SX) & 1))
      asm volatile("v_and_b32_e32 %0, %5, %2\n\tv_and_b32_e32 %1, %5, %3\n\tv_pk_sub_u16 %0, %0, %4 clamp\n\tv_pk_sub_u16 %1, %1, %4 clamp"
                   : "=&v"(o.t[0]), "=&v"(o.t[1]) : "v"(o.pp[i][e]), "v"(o.pp[i][e + 1]), "v"(o.mx), "s"(0x7fff7fffu));
    else
      asm volatile("v_lshlrev_b32_e32 %2, 3, %2\n\tv_lshlrev_b32_e32 %3, 3, %3\n\tv_bfi_b32 %0, %4, %2, %0\n\tv_bfi_b32 %1, %4, %3, %1"
                   : "+v"(o.pp[i][e]), "+v"(o.pp[i][e + 1]), "+v"(o.t[0]), "+v"(o.t[1]) : "s"(0x7fff7fffu));
    if constexpr (((N - O_SX) & 3) == 3) asm volatile("global_store_dwordx4 %0, %1, %2 sc1\n\ts_nop 1" ::"v"(o.vp), "v"(o.pp[i]), "s"(o.sd + i * 8192) : "memory");
  }
}
static_assert(O_XE > O_B1 && O_XE < O_PL && O_SM >= 16 && O_SM + 23 < O_SX && O_SX + 32 <= O_XL && !(O_SL & 1) && O_SL + 16 <= O_PL && o_late(112) < 64 && O_QA >= 4, "side job");
#define O_WF "+v"(o.wf[0]), "+v"(o.wf[1]), "+v"(o.wf[2]), "+v"(o.wf[3]), "+v"(o.wf[4]), "+v"(o.wf[5]), "+v"(o.wf[6]), "+v"(o.wf[7])
template <int H, int N> DI void o_gap(OC& o) {
  constexpr int ks = o_ks(H, N), d = 64 + 16 * o_cb(H, N), a = 128 + 4 * ks, lg = N + O_LA <= 128 ? O_LA - 1 : 127 - N;
  if constexpr (!H && (N == 0 || N == 32)) asm volatile("s_waitcnt vmcnt(%c0)" ::"i"(N ? 24 + o_early(N) : 40) : "memory");
  if constexpr (!H && N == O_B1) {
    asm volatile("s_waitcnt vmcnt(%c0)\n\ts_barrier" ::"i"(o_early(N)) : "memory");
    if (threadIdx.x == 0) arrive(o.cs, 1u);
  }
  if constexpr (H && (N < 96 ? !(N & 3) : N < 112 && !(N & 1))) asm volatile("s_waitcnt vmcnt(%c0)" ::"i"(o_late(N) - (ks < O_QA ? ks + 1 - O_QA : o_late(ks - O_QA + 1))) : "memory");
  if constexpr (H && N == O_B3) {
    asm volatile("s_barrier" ::: "memory");
    if (threadIdx.x < 8) arrive(o.ce + ((threadIdx.x >> 1) * 8 + (threadIdx.x & 1)) * 64, 1u);
  }
  if constexpr (ks == 0)
    asm volatile("s_waitcnt lgkmcnt(%c5)\n\tv_mfma_f32_32x32x16_bf16 a[%c1:%c2], %0, a[%c3:%c4], 0" : "+v"(o.wf[N & 7]) : "i"(d), "i"(d + 15), "i"(a), "i"(a + 3), "i"(lg) : "memory", ACLOB_LO, ACLOB_HI);
  else
    asm volatile("s_waitcnt lgkmcnt(%c5)\n\tv_mfma_f32_32x32x16_bf16 a[%c1:%c2], %0, a[%c3:%c4], a[%c1:%c2]" : "+v"(o.wf[N & 7]) : "i"(d), "i"(d + 15), "i"(a), "i"(a + 3), "i"(lg) : "memory", ACLOB_LO, ACLOB_HI);
  GAP();
  if constexpr (N + O_LA < 128) o_read1<H, N + O_LA>(o);
  if constexpr (!H && N == O_XE) {
    asm volatile("global_load_dwordx4 %0, %2, %3\n\tglobal_load_dwordx4 %1, %2, %3 offset:16" : "=&v"(o.gv[0]), "=&v"(o.gv[1]) : "v"((o.voff >> 8) * 32u), "s"(o.gs) : "memory");
    asm volatile("global_load_dwordx4 %0, %2, %3\n\tglobal_load_dwordx4 %1, %2, %4" : "=&v"(o.xv[0]), "=&v"(o.xv[1]) : "v"(o.wo_), "s"(o.xs), "s"(o.xs + 1024 * 1024) : "memory");
  }
  if constexpr (!H) {
    if constexpr (N >= O_W0 && N < O_W0 + 24) w_load<(N < O_W0 + 16 ? N - O_W0 : N - O_W0 + 24)>(o);
    if constexpr (N >= O_W0 + 24 && N < O_W0 + 27)
      asm volatile("global_load_dwordx2 %0, %2, %3 sc1\n\tglobal_load_dwordx2 %1, %2, %3 offset:512 sc1" : "=&v"(o.uq[N - O_W0 - 24]), "=&v"(o.vq[N - O_W0 - 24]) : "v"(o.mo_), "s"(o.uvp[N - O_W0 - 24]) : "memory");
    if constexpr (N >= 68 && N < 88) o_part<0, N - 68>(o);
    if constexpr (constexpr int i = (N - O_SL) >> 1; N >= O_SL && i < 8 && !(N & 1))
      asm volatile("s_add_u32 m0, %2, %c3\n\ts_nop 0\n\tglobal_load_lds_dwordx4 %0, %1" ::"v"(o.vp), "s"(o.sp + (i >> 2) * 2048 + (i & 3) * 128), "s"(o.sl), "i"(i * 1024) : "memory", "scc");
    if constexpr (N == O_PL) asm volatile("global_load_dword %0, %1, %2 sc1" : "=v"(o.pw) : "v"(0u), "s"(o.cs) : "memory");
  } else {
    if constexpr (N >= 28 && N < 100 && N % 3 == 1) w_load<16 + (N - 28) / 3>(o);
    o_side<N>(o);
    if constexpr (N == O_XL) asm volatile("global_load_dword %0, %1, %2 sc1" : "=v"(o.pe) : "v"(0u), "s"(o.cr) : "memory");
    if constexpr (O_QA + N < 32) q_load<O_QA + N>(o.qs, o.voff);
    if constexpr (N >= 116 && N < 126) { o_part<1, 2 * (N - 116)>(o); o_part<1, 2 * (N - 116) + 1>(o); }
  }
  GAP();
}
template <int H, int... Ns> DI void o_gaps(OC& o, iseq<Ns...>) { (o_gap<H, Ns>(o), ...); }
template <int G> DI void fin_last(Fin& f, const AC& c, unsigned lw) {
  fin_gap<true, G>(f, c);
  if constexpr (G == 11) asm volatile("s_barrier" ::: "memory");
  if constexpr (G >= 12 && G < 28) dma<((G - 12) & 7)>(c.wi + (2 + ((G - 12) >> 3)) * 32768, c.voff, lw + ((G - 12) >> 3) * 32768);
}
template <int... Gs> DI void fin_all(Fin& f, const AC& c, unsigned lw, iseq<Gs...>) { (fin_last<Gs>(f, c, lw), ...); }
constexpr int s_areg(int i) { return i < 16 ? 64 + 4 * i : i < 24 ? 160 + 4 * i : (4 * i + 64) & 127; }
template <int I> DI void s_aload(const char* src, unsigned vo) {
  asm volatile("global_load_dwordx4 a[%c2:%c3], %0, %1 offset:%c4 sc1" ::"v"(vo), "s"(src + (I >> 3) * 8192 + 4096), "i"(s_areg(I)), "i"(s_areg(I) + 3), "i"((I & 7) * 1024 - 4096) : "memory", ACLOB_LO, ACLOB_HI);
}
template <int I0, int... Is> DI void s_aloads(const char* src, unsigned vo, iseq<Is...>) { (s_aload<I0 + Is>(src, vo), ...); }
template <int... Is> DI void s_aputs(unsigned a, iseq<Is...>) { (o_put<Is>(a), ...); }
DI void reduce4(const f32x4 (&acc)[4][4], f32x4 (&own)[4], char* area, int stride, int wave, int lane) {
#pragma unroll
  for (int f = 0; f < 16; ++f) *reinterpret_cast<f32x4*>(area + wave * stride + f * 1024 + lane * 16) = acc[f >> 2][f & 3];
  barrier();
#pragma unroll
  for (int i = 0; i < 4; ++i) {
    const char* p = area + (wave * 4 + i) * 1024 + lane * 16;
    const f32x4 a = *reinterpret_cast<const f32x4*>(p), b = *reinterpret_cast<const f32x4*>(p + stride);
    const f32x4 c = *reinterpret_cast<const f32x4*>(p + 2 * stride), d = *reinterpret_cast<const f32x4*>(p + 3 * stride);
    own[i] = (a + b) + (c + d);
  }
}
constexpr int W_A0 = 6;
struct WC {
  f32x4 acc[2][3], st[2][4];
  float rs[2][4], ms[2][4];
  bf16x8 af[4][2];
  unsigned ra[2], sa, vo;
  const char* as;
};
template <int H, int T, int L> DI void wi_read(WC& w) {
  asm volatile("ds_read_b128 %0, %1 offset:%c2" : "=v"(w.af[T & 3][L]) : "v"(w.ra[L]), "i"((T >> 3) * 32768 + 16384 + H * 8192 + (T & 7) * 1024) : "memory");
}
template <int H, int T, int M> DI void wi_gap(WC& w, OC& o) {
  constexpr int l = M & 1, nt = M >> 1, n = 3 * T + nt, r = w_reg(n);
  if constexpr (T == 0) asm volatile("v_mfma_f32_16x16x32_bf16 %0, %1, a[%c2:%c3], 0" : "=&v"(w.acc[l][nt]) : "v"(w.af[T & 3][l]), "i"(r), "i"(r + 3) : ACLOB_LO, ACLOB_HI);
  else if constexpr (n < 40) asm volatile("v_mfma_f32_16x16x32_bf16 %0, %1, a[%c2:%c3], %0" : "+v"(w.acc[l][nt]) : "v"(w.af[T & 3][l]), "i"(r), "i"(r + 3) : ACLOB_LO, ACLOB_HI);
  else asm volatile("v_mfma_f32_16x16x32_bf16 %0, %1, %2, %0" : "+v"(w.acc[l][nt]) : "v"(w.af[T & 3][l]), "v"(o.wv[n - 40]));
  GAP();
  if constexpr (M < 2 && T + 3 < 16) wi_read<H, T + 3, M>(w);
  if constexpr (constexpr int i = W_A0 + 2 * T + (M >> 2); (M == 3 || M == 5) && i < (H ? 16 : 24)) s_aload<32 * H + i>(w.as, w.vo);
  if constexpr (H && (M == 3 || M == 5) && T >= 6 && T < 14) s_aload<36 + 2 * T + (M >> 2)>(w.as, w.vo);
  if constexpr (T >= 1 && T < 9 && (M == 2 || M == 4)) {
    constexpr int q = (T - 1) >> 2, i = (T - 1) & 3;
    if constexpr (M == 2) {
      w.ms[q][i] = (w.st[q][0][i] + w.st[q][2][i]) * (-1.f / DM);
      w.rs[q][i] = fmaf(w.st[q][1][i] + w.st[q][3][i], 1.f / DM, -w.ms[q][i] * w.ms[q][i]);
    } else w.rs[q][i] = rsqrtf(fmaxf(w.rs[q][i], 0.f) + 1e-5f);
    asm volatile("" : "+v"(w.ms[q][i]), "+v"(w.rs[q][i]));
  }
  GAP();
}
template <int H, int T, int... Ms> DI void wi_step(WC& w, OC& o, iseq<Ms...>) {
  asm volatile("s_waitcnt lgkmcnt(%c2)" : "+v"(w.af[T & 3][0]), "+v"(w.af[T & 3][1]) : "i"(T < 14 ? 4 : 30 - 2 * T) : "memory");
  if constexpr (T == 0) asm volatile("" : "+v"(w.st[0][0]), "+v"(w.st[0][1]), "+v"(w.st[0][2]), "+v"(w.st[0][3]), "+v"(w.st[1][0]), "+v"(w.st[1][1]), "+v"(w.st[1][2]), "+v"(w.st[1][3]));
  GAP();
  (wi_gap<H, T, Ms>(w, o), ...);
}
template <int H, int... Ts> DI void wi_steps(WC& w, OC& o, iseq<Ts...>) {
#pragma unroll
  for (int i = 0; i < 8; ++i) asm volatile("ds_read_b128 %0, %1 offset:%c2" : "=v"(w.st[i >> 2][i & 3]) : "v"(w.sa), "i"(H * 512 + i * 64) : "memory");
  wi_read<H, 0, 0>(w); wi_read<H, 0, 1>(w); wi_read<H, 1, 0>(w); wi_read<H, 1, 1>(w); wi_read<H, 2, 0>(w); wi_read<H, 2, 1>(w);
  (wi_step<H, Ts>(w, o, make_iseq<6>{}), ...);
  asm volatile("s_nop 7" : "+v"(w.acc[0][0]), "+v"(w.acc[1][0]), "+v"(w.acc[0][1]), "+v"(w.acc[1][1]), "+v"(w.acc[0][2]), "+v"(w.acc[1][2]));
}
template <int H> DI void x_loads(u32x4 (&xr)[8], const char* src, unsigned voff) {
#pragma unroll
  for (int i = 0; i < 8; ++i)
    asm volatile("global_load_dwordx4 %0, %1, %2 offset:%c3 sc1" : "=v"(xr[i]) : "v"(voff), "s"(src + H * 32768 + (i >> 2) * 4096), "i"((i & 3) * 1024) : "memory");
}
template <int H, int Y> DI void x_put(u32x4 (&xr)[8], char* smem, int wave, int lane) {
  f32x4 gram, rsum;
  u32x4 ones = {0x3f803f80u, 0x3f803f80u, 0x3f803f80u, 0x3f803f80u};
  asm volatile("" : "+v"(ones));
#pragma unroll
  for (int i = 0; i < 8; ++i) {
    if (i == 0) asm volatile("s_waitcnt vmcnt(%c4)\n\tv_mfma_f32_16x16x32_bf16 %1, %0, %0, 0\n\tv_mfma_f32_16x16x32_bf16 %2, %3, %0, 0" : "+v"(xr[i]), "=&v"(gram), "=&v"(rsum) : "v"(ones), "i"(Y + 7 - i) : "memory");
    else asm volatile("s_waitcnt vmcnt(%c4)\n\tv_mfma_f32_16x16x32_bf16 %1, %0, %0, %1\n\tv_mfma_f32_16x16x32_bf16 %2, %3, %0, %2" : "+v"(xr[i]), "+v"(gram), "+v"(rsum) : "v"(ones), "i"(Y + 7 - i) : "memory");
    *reinterpret_cast<u32x4*>(smem + wave * 32768 + 16384 + H * 8192 + i * 1024 + lane * 16) = xr[i];
  }
  asm volatile("s_nop 7" : "+v"(gram), "+v"(rsum));
  const int e = lane & 3;
  const float g = e == 0 ? gram[0] : e == 1 ? gram[1] : e == 2 ? gram[2] : gram[3];
  if ((lane >> 4) == ((lane >> 2) & 3)) {
    float* d = reinterpret_cast<float*>(smem + T4_ST + H * 512 + wave * 128) + (lane & 15);
    d[0] = rsum[0]; d[16] = g;
  }
}

constexpr int S_DB = 7, S_TP = 2, S_TC = 4;
constexpr int S_N0 = 8, S_N1 = 16, S_N2 = 24, S_P = 3;
struct SSched { int ld[384], vm[97], vc; };
constexpr SSched s_sched(int NT) {
  SSched q{};
  int at[125] = {}, pos = S_N2, nx = S_N2, na = 0, ps = 0, last = -S_P;
  for (int i = 0; i < nx; ++i) at[i] = i;
  q.vm[0] = pos - 1;
  for (int g = 0; g < 64 * NT; ++g) {
    const int f = (g >> 2) + 1;
    if (!(g & 3) && f < 16 * NT) q.vm[f] = f < nx ? imin(63, pos - at[f] - 1) : -1;
    if (g == S_TC * 4 * NT) q.vc = pos - ps;
    if (g == S_TP * 4 * NT) ps = pos += 4;
    const bool ok = nx < 16 * NT + 29 && g >= 4 * (nx - S_DB * NT) - 1 && (nx < 8 * NT || g >= S_TC * 4 * NT) && g >= last + S_P;
    q.ld[g] = !ok ? -1 : nx == 6 * NT && na < 8 ? 200 + na++ : nx++;
    if (ok) { if (q.ld[g] < 200) at[nx - 1] = pos; ++pos; last = g; }
  }
  q.vm[96] = nx;
  return q;
}
template <int NT> struct SC {
  static constexpr SSched Q = s_sched(NT);
  static_assert(Q.vm[96] >= 16 * NT + 5 && S_TP < S_TC && S_TC <= 5 && S_N2 <= 6 * NT, "schedule");
  u32x4 b[S_DB * NT], o4[2], sp[4], rv[2][2], af[4][4];
  u32x4 wu[24];
  float sc[4];
  unsigned voff, rr, wa, bs;
  const char *bsrc, *st, *ss, *bp, *wp, *ap;
  const unsigned* cz;
  unsigned* rs;
};
template <int NT, int I> DI void s_load(SC<NT>& s) {
  if constexpr (I >= 200) {
    asm volatile("global_load_dwordx4 %0, %1, %2 offset:%c3 sc1" : "=v"(s.af[2 + ((I - 200) >> 2)][I & 3]) : "v"(s.voff), "s"(s.ap), "i"((I - 204) * 1024) : "memory");
  } else if constexpr (constexpr int e = I - 16 * NT; e >= 5) {
    asm volatile("global_load_dwordx4 %0, %1, %2 offset:%c3" : "=v"(s.wu[e - 5]) : "v"(s.voff), "s"(s.wp + ((e - 5) >> 3) * 8192), "i"(((e - 5) & 7) * 1024 - 4096) : "memory");
  } else if constexpr (e == 4) {
    asm volatile("global_load_ushort %0, %1, %2" : "=v"(s.bs) : "v"((s.voff >> 3) & 30u), "s"(s.bp) : "memory");
  } else if constexpr (e >= 0) {
    asm volatile("global_load_dword %0, %1, %2 offset:%c3 sc1" : "=v"(s.sc[e]) : "v"((s.voff & 128u) * 4u + ((s.voff >> 2) & 60u)), "s"(s.ss), "i"(e * 128) : "memory");
  } else if constexpr (I >= 0) {
    constexpr int T = I / NT;
    asm volatile("global_load_dwordx4 %0, %1, %2 offset:%c3 sc1" : "=v"(s.b[I % (S_DB * NT)]) : "v"(s.voff), "s"(s.bsrc + (T + (T >> 3) * 24) * (NBLK * 1024)), "i"((I % NT - 3) * 1024) : "memory");
  }
}
template <int NT, int I0, int... Is> DI void s_loads(SC<NT>& s, iseq<Is...>) { (s_load<NT, I0 + Is>(s), ...); }
DI void st_load(u32x4 (&sp)[4], const char* st, unsigned voff) {
  asm volatile("global_load_dwordx4 %0, %4, %5 sc1\n\tglobal_load_dwordx4 %1, %4, %5 offset:1024 sc1\n\t"
               "global_load_dwordx4 %2, %4, %6 sc1\n\tglobal_load_dwordx4 %3, %4, %6 offset:1024 sc1"
               : "=&v"(sp[0]), "=&v"(sp[1]), "=&v"(sp[2]), "=&v"(sp[3]) : "v"(voff), "s"(st), "s"(st + S * 8) : "memory");
}
DI void st_check(u32x4 (&sp)[4], const char* st, unsigned voff, const unsigned* cz, unsigned mask) {
#pragma nounroll
  for (int spin = 0; spin < (1 << 18); ++spin) {
    const unsigned all = (sp[0][1] & sp[0][3] & sp[1][1] & sp[1][3]) & (sp[2][1] & sp[2][3] & sp[3][1] & sp[3][3]);
    if (!__builtin_amdgcn_ballot_w64((int)all >= 0)) break;
#pragma nounroll
    for (; spin < (1 << 18); ++spin) {
      if (!__builtin_amdgcn_ballot_w64((__hip_atomic_load(cz, __ATOMIC_RELAXED, __HIP_MEMORY_SCOPE_AGENT) & mask) == 0u)) break;
      __builtin_amdgcn_s_sleep(1);
    }
    st_load(sp, st, voff);
    asm volatile("s_waitcnt vmcnt(0)" : "+v"(sp[0]), "+v"(sp[1]), "+v"(sp[2]), "+v"(sp[3])::"memory");
  }
}
DI void st_put(const u32x4 (&sp)[4], unsigned* rs) {
#pragma unroll
  for (int h = 0; h < 2; ++h) {
    f16x2 rv, mv;
#pragma unroll
    for (int i = 0; i < 2; ++i) {
      const float s1 = fmaf((float)sp[h][2 * i], 1.f / 4294967296.f, (float)((int)(sp[h][2 * i + 1] & 0x0fffffffu) - 256));
      const float s2 = fmaf((float)sp[2 + h][2 * i], 1.f / 262144.f, (float)(sp[2 + h][2 * i + 1] & 0x0fffffffu) * 16384.f);
      const float mean = s1 * (1.f / FH);
      const float var = fmaxf(s2 * (1.f / FH) - mean * mean, 0.f);
      rv[i] = (_Float16)rsqrtf(fmaxf(var + 1e-5f, mean * mean * 1e-9f));
      mv[i] = (_Float16)(-mean * (float)rv[i]);
    }
    rs[h * 64] = __builtin_bit_cast(unsigned, rv);
    rs[256 + h * 64] = __builtin_bit_cast(unsigned, mv);
  }
}
template <int NT, int F, int J> DI void s_conv(SC<NT>& s) {
  asm volatile("v_pk_fma_f16 %0, %1, %2, %3" : "=v"(s.o4[F & 1][J]) : "v"(s.b[F % (S_DB * NT)][J]), "v"(s.rv[(F / NT) & 1][0][J]), "v"(s.rv[(F / NT) & 1][1][J]));
}
template <int NT, int T, int I> DI void s_read(SC<NT>& s) {
  if constexpr (I < 2) asm volatile("ds_read_b128 %0, %1 offset:%c2" : "=v"(s.rv[T & 1][I]) : "v"(s.rr), "i"(I * 1024 + T * 64) : "memory");
  else if constexpr (T < 4) asm volatile("ds_read_b128 %0, %1 offset:%c2" : "=v"(s.af[T & 1][I - 2]) : "v"(s.wa), "i"(T * 4096 + (I - 2) * 1024) : "memory");
}
template <int NT, int T, int... Is> DI void s_reads(SC<NT>& s, iseq<Is...>) { (s_read<NT, T, Is>(s), ...); }
template <int NT, int F> DI void s_wait(SC<NT>& s) {
  constexpr int n = SC<NT>::Q.vm[F], q = (F / NT) & 1;
  static_assert(n >= 0, "schedule");
  if constexpr (F % NT) asm volatile("s_waitcnt vmcnt(%c1)" : "+v"(s.b[F % (S_DB * NT)]) : "i"(n) : "memory");
  else asm volatile("s_waitcnt vmcnt(%c3) lgkmcnt(0)" : "+v"(s.b[F % (S_DB * NT)]), "+v"(s.rv[q][0]), "+v"(s.rv[q][1]) : "i"(n) : "memory");
  if constexpr (constexpr int T = F / NT, u = T < 4 ? q : T - 4; !(F % NT) && (T < 4 || T == 6 || T == 7)) asm volatile("" : "+v"(s.af[u][0]), "+v"(s.af[u][1]), "+v"(s.af[u][2]), "+v"(s.af[u][3]));
}
template <int NT, int G> DI void s_gap(SC<NT>& s) {
  constexpr int F = G >> 2, T = F / NT, M = G - 4 * NT * T, mt = G & 3, r = 128 + 4 * (mt * NT + F % NT), a = s_areg(4 * T + mt);
  if constexpr (G == S_TC * 4 * NT) {
    asm volatile("s_waitcnt vmcnt(%c4)" : "+v"(s.sp[0]), "+v"(s.sp[1]), "+v"(s.sp[2]), "+v"(s.sp[3]) : "i"(SC<NT>::Q.vc) : "memory");
    st_check(s.sp, s.st + 8192, s.voff, s.cz, 0x2000u);
    GAP();
  }
  if constexpr (G == (S_TC + 1) * 4 * NT) { st_put(s.sp, s.rs + 128); GAP(); }
  if constexpr (T == 0) asm volatile("v_mfma_f32_16x16x32_f16 a[%c2:%c3], %0, %1, 0" ::"v"(s.o4[F & 1]), "v"(s.af[0][mt]), "i"(r), "i"(r + 3) : ACLOB_HI);
  else if constexpr (T < 4 || T == 6 || T == 7) asm volatile("v_mfma_f32_16x16x32_f16 a[%c2:%c3], %0, %1, a[%c2:%c3]" ::"v"(s.o4[F & 1]), "v"(s.af[T < 4 ? T & 1 : T - 4][mt]), "i"(r), "i"(r + 3) : ACLOB_HI);
  else asm volatile("v_mfma_f32_16x16x32_f16 a[%c1:%c2], %0, a[%c3:%c4], a[%c1:%c2]" ::"v"(s.o4[F & 1]), "i"(r), "i"(r + 3), "i"(a), "i"(a + 3) : ACLOB_LO, ACLOB_HI);
  GAP();
  if constexpr (F + 1 < 16 * NT && mt < 2) {
    if constexpr (mt == 0) s_wait<NT, F + 1>(s);
    s_conv<NT, F + 1, 2 * mt>(s); s_conv<NT, F + 1, 2 * mt + 1>(s);
  }
  if constexpr (T < 15 && M < 12 && mt >= 2) s_read<NT, T + 1, (M >> 2) * 2 + mt - 2>(s);
  if constexpr (G == S_TP * 4 * NT) st_load(s.sp, s.st + 8192, s.voff);
  s_load<NT, SC<NT>::Q.ld[G]>(s);
  GAP();
}
template <int NT, int G0, int... Gs> DI void s_gaps(SC<NT>& s, iseq<Gs...>) { (s_gap<NT, G0 + Gs>(s), ...); }
template <int NT, int... Ts> DI void s_steps(SC<NT>& s, iseq<Ts...>) { (s_gaps<NT, 4 * NT * Ts>(s, make_iseq<4 * NT>{}), ...); }
template <int F> DI void s_accput(unsigned a) { asm volatile("ds_write_b128 %0, a[%c1:%c2] offset:%c3" ::"v"(a), "i"(128 + 4 * F), "i"(131 + 4 * F), "i"(F * 1024) : "memory", ACLOB_HI); }
template <int... Fs> DI void s_accputs(unsigned a, iseq<Fs...>) { (s_accput<Fs>(a), ...); }

struct TP {
  const u16 *x, *wsp, *bsp;
  u16* out;
  char* ws;
};
template <int NT, class F> DI void t4_spatial(char* smem, const TP& p, int wave, int mtile, int ntile, int nb0, const u32x4 (&sp)[4], F&& mid) {
  unsigned* cnt = reinterpret_cast<unsigned*>(p.ws + WS_CNT);
  char* uf = p.ws + WS_U;
  const unsigned ldsb = lds_addr(smem);
  int lane = lane_id(), fr = lane & 15, fg = lane >> 4;
  const int m0 = (wave >> 1) * 1024 + mtile * 32 + (wave & 1) * 16, n0 = ntile * 64;
  const int ks0 = wave < 2 ? wave * 6 : 2 + wave * 5, nks = wave < 2 ? 6 : 5;
  SC<NT> s;
  s.voff = lane * 16u;
  s.bsrc = p.ws + WS_Z2 + (wave * 8 * NBLK + nb0 + 3) * 1024;
  s_loads<NT, 0>(s, make_iseq<S_N0>{});
  mid(0);
  s_loads<NT, S_N0>(s, make_iseq<S_N1 - S_N0>{});
  s.rs = reinterpret_cast<unsigned*>(smem + T4_RS + wave * 2048) + lane;
  st_put(sp, s.rs);
  mid(1);
  s_loads<NT, S_N1>(s, make_iseq<S_N2 - S_N1>{});
  s.st = p.ws + WS_ST + wave * 2048;
  s.cz = cnt + (CNT_Z2 + (wave * 2 + (lane & 1)) * 32 + (blockIdx.x >> 3)) * 64;
  s.ap = p.ws + WS_SP + (mtile * 4 + wave) * 65536 + 28672;
  s.rr = ldsb + T4_RS + wave * 2048 + fg * 16;
  s.wa = ldsb + wave * 32768 + lane * 16;
  s.ss = p.ws + WS_SS + (mtile * 4 + wave) * 1024;
  s.bp = reinterpret_cast<const char*>(p.bsp + m0);
  s.wp = p.ws + WS_WU + (ntile * UKS + ks0 + 1) * 4096;
  s_reads<NT, 0>(s, make_iseq<6>{});
  s_wait<NT, 0>(s);
  s_conv<NT, 0, 0>(s); s_conv<NT, 0, 1>(s); s_conv<NT, 0, 2>(s); s_conv<NT, 0, 3>(s);
  asm volatile("s_nop 1");
  GAP();
  s_steps<NT>(s, make_iseq<16>{});
  s_loads<NT, SC<NT>::Q.vm[96]>(s, make_iseq<16 * NT + 29 - SC<NT>::Q.vm[96]>{});
  asm volatile("s_nop 7\n\ts_nop 1" ::: "memory");
  lane = lane_id(); fr = lane & 15; fg = lane >> 4;
  s_accputs(s.wa, make_iseq<4 * NT>{});
  asm volatile("s_waitcnt vmcnt(24) lgkmcnt(0)\n\ts_barrier" : "+v"(s.bs), "+v"(s.sc[0]), "+v"(s.sc[1]), "+v"(s.sc[2]), "+v"(s.sc[3])::"memory");
  {
    u32x2 w[6];
    const float bs = bflo(s.bs);
#pragma unroll
    for (int i = 0; i < 6; ++i) {
      if (i < NT) {
        const char* q = smem + (wave * NT + i) * 1024 + lane * 16;
        const f32x4 a = *reinterpret_cast<const f32x4*>(q), b = *reinterpret_cast<const f32x4*>(q + 32768);
        const f32x4 c = *reinterpret_cast<const f32x4*>(q + 65536), d = *reinterpret_cast<const f32x4*>(q + 98304);
        f32x4 g;
#pragma unroll
        for (int e = 0; e < 4; ++e) g[e] = fmaf(d[e], s.sc[3], fmaf(c[e], s.sc[2], fmaf(b[e], s.sc[1], fmaf(a[e], s.sc[0], bs))));
        const u32x2 zv = *reinterpret_cast<const u32x2*>(smem + T4_Z1 + (wave * 6 + i) * 512 + lane * 8);
        w[i][0] = pack2(bflo(zv[0]) * g[0], bfhi(zv[0]) * g[1]);
        w[i][1] = pack2(bflo(zv[1]) * g[2], bfhi(zv[1]) * g[3]);
      } else w[i] = u32x2{0u, 0u};
    }
    char* ub = uf + ((mtile * 4 + wave) * UKS * 2 + nb0 + (fg & 1)) * 512 + (fr + 16 * (fg >> 1)) * 16;
    st_wt(ub, widen(w[0], w[1]));
    st_wt(ub + 1024, widen(w[2], w[3]));
    const u32x4 w2 = widen(w[4], w[5]);
    if (NT == 6 || !(fg & 1)) st_wt(ub + 2048, w2);
    if (ntile == 7 && !(fg & 1)) st_wt(ub + 3072, u32x4{0u, 0u, 0u, 0u});
  }
  asm volatile("s_waitcnt vmcnt(0)\n\ts_barrier" ::: "memory");
#pragma unroll
  for (int t = 0; t < 24; t += 8) asm volatile("" : "+v"(s.wu[t]), "+v"(s.wu[t + 1]), "+v"(s.wu[t + 2]), "+v"(s.wu[t + 3]), "+v"(s.wu[t + 4]), "+v"(s.wu[t + 5]), "+v"(s.wu[t + 6]), "+v"(s.wu[t + 7]));
  if (wave == 0 && lane == 0) arrive(cnt + mtile * 64, 1u);
  u32x2 xv[4];
  f32x4 gv[4];
#pragma unroll
  for (int i = 0; i < 4; ++i) {
    gv[i] = *reinterpret_cast<const f32x4*>(reinterpret_cast<const float*>(p.ws + WS_MOD) + 5 * DM + n0 + i * 16 + fg * 4);
    xv[i] = *reinterpret_cast<const u32x2*>(p.ws + WS_X1 + x1_off(mtile * 4 + wave, ntile, i, lane));
  }
  asm volatile("" ::"v"(gv[0]), "v"(gv[1]), "v"(gv[2]), "v"(gv[3]), "v"(xv[0]), "v"(xv[1]), "v"(xv[2]), "v"(xv[3]));
  if (wave == 0) wait_count(cnt + mtile * 64, ~0u, 8u);
  barrier();
  auto uload = [&](int t, int mt) {
    asm volatile("global_load_dwordx4 a[%c2:%c3], %0, %1 sc1" ::"v"(lane * 16u),
                 "s"(uf + ((size_t)(mtile * 4 + mt) * UKS + ks0 + (t < nks ? t : 0)) * 1024), "i"(16 * t + 4 * mt), "i"(16 * t + 4 * mt + 3) : "memory", ACLOB_LO);
  };
#pragma unroll
  for (int i = 0; i < 12; ++i) uload(i >> 2, i & 3);
  f32x4 ac2[4][4];
#pragma unroll
  for (int t = 0; t < 6; ++t) {
    asm volatile("s_waitcnt vmcnt(%c0)" ::"i"(t < 4 ? 8 : 20 - 4 * t) : "memory");
    if (t < nks) {
#pragma unroll
      for (int mt = 0; mt < 4; ++mt)
#pragma unroll
        for (int nt = 0; nt < 4; ++nt) {
          if (t == 0) asm volatile("v_mfma_f32_16x16x32_bf16 %0, %1, a[%c2:%c3], 0" : "=&v"(ac2[mt][nt]) : "v"(s.wu[4 * t + nt]), "i"(16 * t + 4 * mt), "i"(16 * t + 4 * mt + 3) : ACLOB_LO);
          else asm volatile("v_mfma_f32_16x16x32_bf16 %0, %1, a[%c2:%c3], %0" : "+v"(ac2[mt][nt]) : "v"(s.wu[4 * t + nt]), "i"(16 * t + 4 * mt), "i"(16 * t + 4 * mt + 3) : ACLOB_LO);
          if (t < 3 && nt == 0) uload(t + 3, mt);
        }
    }
  }
  asm volatile("s_nop 7\n\ts_nop 1" ::: "memory");
  f32x4 own[4];
  reduce4(ac2, own, smem, 32768, wave, lane);
  u32x2 w[4];
#pragma unroll
  for (int i = 0; i < 4; ++i) {
    w[i][0] = pack2(bflo(xv[i][0]) + gv[i][0] * own[i][0], bfhi(xv[i][0]) + gv[i][1] * own[i][1]);
    w[i][1] = pack2(bflo(xv[i][1]) + gv[i][2] * own[i][2], bfhi(xv[i][1]) + gv[i][3] * own[i][3]);
  }
  u16* op = p.out + (size_t)(m0 + fr) * DM + n0 + (fg & 1) * 16 + (fg >> 1) * 8;
  *reinterpret_cast<u32x4*>(op) = widen(w[0], w[1]);
  *reinterpret_cast<u32x4*>(op + 32) = widen(w[2], w[3]);
}
DI void t4_main(Fin& f, const AC& c, char* smem, const TP& p, int wave, unsigned pw) {
  const int b = blockIdx.x, xcd = b & 7, j = b >> 3, mtile = xcd * 4 + (j >> 3), ntile = j & 7;
  const int nb0 = ntile < 5 ? ntile * 5 : 25 + (ntile - 5) * 6;
  unsigned* cnt = reinterpret_cast<unsigned*>(p.ws + WS_CNT);
  const float* mod = reinterpret_cast<const float*>(p.ws + WS_MOD);
  const unsigned ldsb = lds_addr(smem);
  int lane = lane_id(), fr = lane & 15, fg = lane >> 4;
  asm volatile("" : "+v"(pw));
  if (__builtin_amdgcn_readfirstlane(pw) != (ntile < 5 ? 20u : 24u)) wait_count(cnt + (64 + ntile) * 64, ~0u, ntile < 5 ? 20u : 24u);
  fin_all(f, c, ldsb + 32768 + wave * 8192, make_iseq<32>{});
  OC o;
  const int pair = j >> 2, hq = j & 3;
  o.voff = lane * 16u;
  o.ra[0] = ldsb + o.voff; o.ra[1] = o.ra[0] + 65536;
  o_reads<0>(o, make_iseq<O_LA>{});
  o.so = (fg & 1) * 16384 + (fg >> 1) * 256 + fr * 16;
#pragma unroll
  for (int h = 0; h < 2; ++h) o.pt[h] = p.ws + WS_PT + ((((xcd * 8 + (h ? 7 - pair : pair)) * 4 + wave) * 4 + 2 * h) * 16 + hq * 4) * 1024;
  o.cs = cnt + CNT_CX + (xcd * 16 + 15 - pair) * 64;
  o.ce = cnt + CNT_PE + (pair * 32 + hq * 2) * 64;
  o.cr = cnt + CNT_PE + (mtile * 8 + ntile) * 64;
  const bool ok2 = !(wave & 1) || ntile >= 5;
  const int nl2 = ok2 ? 4 + (wave & 1) : 0;
#pragma unroll
  for (int i = 0; i < 3; ++i) {
    const int blk = nb0 + (i == 0 ? NBLK + wave : i == 1 ? wave : (wave < 2 ? NBLK : 0) + nl2);
    o.wsrc[i] = p.ws + WS_WF + blk * 1024;
    o.uvp[i] = p.ws + WS_UV + blk * 1024;
  }
  o.mo_ = fr * 32u + fg * 8u;
  o.wo_ = fr * 1024u + fg * 16u;
  o.xs = reinterpret_cast<const char*>(p.x + (mtile * 32 + (wave >> 1) * 16) * DM + ntile * 64 + (wave & 1) * 32);
  o.gs = reinterpret_cast<const char*>(mod + 2 * DM + ntile * 64 + (wave & 1) * 32);
  const int r0 = (ntile >> 2) * 1024 + mtile * 32 + (ntile & 3) * 8;
  o.vp = (lane >> 3) * 4096u + ((lane & 7) ^ (lane >> 3)) * 16u;
  o.sl = ldsb + T4_RS + wave * 8192;
  o.sp = reinterpret_cast<const char*>(p.wsp) + r0 * 4096 + wave * 512;
  o.sd = p.ws + WS_SP + ((mtile * 4 + wave) * 64 + (ntile >> 1)) * 1024 + (ntile & 1) * 128;
  o.ss = p.ws + WS_SS + ((mtile * 8 + ntile) * 4 + wave) * 128;
  o_gaps<0>(o, make_iseq<128>{});
  o_reads<1>(o, make_iseq<O_LA>{});
  const int fo = ((mtile * 4 + (wave >> 1)) * 16 + ntile * 2 + (wave & 1)) * 1024;
  o.ps = p.ws + WS_PT + fo;
  asm volatile("s_nop 7\n\ts_nop 3" ::: "memory");
  o_parts<0, 20>(o, make_iseq<20>{});
  asm volatile("s_waitcnt vmcnt(4) lgkmcnt(0)" : O_WF, "+v"(o.pw), "+v"(o.uq[0]), "+v"(o.uq[1]), "+v"(o.uq[2]), "+v"(o.vq[0]), "+v"(o.vq[1]), "+v"(o.vq[2])::"memory");
  asm volatile("" : "+v"(o.wv[0]), "+v"(o.wv[1]), "+v"(o.wv[2]), "+v"(o.wv[3]), "+v"(o.wv[4]), "+v"(o.wv[5]), "+v"(o.wv[6]), "+v"(o.wv[7]));
  if (__builtin_amdgcn_readfirstlane(o.pw) != 4u) wait_count(o.cs, ~0u, 4u);
  o.qs = p.ws + WS_CTX + ((xcd * 16 + 15 - pair) * 4 + wave) * 32768;
  q_loads(o.qs, o.voff, make_iseq<O_QA>{});
  float uu[3], vv[3];
#pragma unroll
  for (int i = 0; i < 3; ++i) {
    const float a = o.uq[i][0] + o.uq[i][1], c = o.vq[i][0] + o.vq[i][1];
    auto s1 = __builtin_amdgcn_permlane16_swap(__float_as_uint(a), __float_as_uint(c), false, false);
    const float m = __uint_as_float(s1[0]) + __uint_as_float(s1[1]);
    auto s2 = __builtin_amdgcn_permlane32_swap(__float_as_uint(m), __float_as_uint(m), false, false);
    const float t = __uint_as_float(s2[0]) + __uint_as_float(s2[1]);
    auto s3 = __builtin_amdgcn_permlane16_swap(__float_as_uint(t), __float_as_uint(t), false, false);
    uu[i] = __uint_as_float(s3[0]); vv[i] = __uint_as_float(s3[1]);
    asm volatile("" : "+v"(uu[i]), "+v"(vv[i]));
  }
  o_gaps<1>(o, make_iseq<128>{});
  asm volatile("s_nop 7\n\ts_nop 7" ::: "memory");
  o_parts<1, 20>(o, make_iseq<20>{});
  lane = lane_id(); fr = lane & 15; fg = lane >> 4;
  const unsigned voff = lane * 16u;
  auto x1out = [&](int h) {
    u32x4 w;
#pragma unroll
    for (int e = 0; e < 4; ++e) {
      const float lo = ((bflo(o.pp[0][e]) + bflo(o.pp[1][e])) + (bflo(o.pp[2][e]) + bflo(o.pp[3][e]))) + ((bflo(o.pp[4][e]) + bflo(o.pp[5][e])) + (bflo(o.pp[6][e]) + bflo(o.pp[7][e])));
      const float hi = ((bfhi(o.pp[0][e]) + bfhi(o.pp[1][e])) + (bfhi(o.pp[2][e]) + bfhi(o.pp[3][e]))) + ((bfhi(o.pp[4][e]) + bfhi(o.pp[5][e])) + (bfhi(o.pp[6][e]) + bfhi(o.pp[7][e])));
      w[e] = pack2(bflo(o.xv[h][e]) + o.gv[e >> 1][(e & 1) * 2] * lo, bfhi(o.xv[h][e]) + o.gv[e >> 1][(e & 1) * 2 + 1] * hi);
    }
    asm volatile("global_store_dwordx4 %0, %1, %2 sc1\n\ts_nop 1" ::"v"(voff), "v"(w), "s"(p.ws + WS_X1 + h * 32768 + fo) : "memory");
  };
  unsigned* cx = cnt + (32 + mtile) * 64;
  const char* asp = p.ws + WS_SP + (mtile * 4 + wave) * 65536;
  const char* xsrc = p.ws + WS_X1 + mtile * 65536 + wave * 8192;
  asm volatile("s_waitcnt vmcnt(8)" : "+v"(o.pe), "+v"(o.gv[0]), "+v"(o.gv[1]), "+v"(o.xv[0]), "+v"(o.xv[1])::"memory");
  if (__builtin_amdgcn_readfirstlane(o.pe) != 8u) wait_count(o.cr, ~0u, 8u);
  p_loads<0>(o, make_iseq<8>{});
  asm volatile("s_waitcnt vmcnt(8)\n\ts_barrier" ::: "memory");
  asm volatile("s_mov_b64 exec, 3\n\tglobal_atomic_add %0, %1, %2\n\ts_mov_b64 exec, -1" ::"v"(lane * 256u), "v"(1u), "s"(cnt + CNT_PL + ((7 - pair) * 32 + hq * 2 + wave * 8) * 64) : "memory");
  asm volatile("s_waitcnt vmcnt(1)" : PP_ALL::"memory");
  x1out(0);
  asm volatile("s_waitcnt vmcnt(0)\n\ts_barrier" ::: "memory");
  if (threadIdx.x == 0) arrive(cx, 1u);
  wait_count(cnt + CNT_PL + (mtile * 8 + ntile) * 64, ~0u, 8u);
  unsigned fl;
  u32x4 xr[8];
  p_loads<1>(o, make_iseq<8>{});
  asm volatile("global_load_dword %0, %1, %2 sc1" : "=v"(fl) : "v"(0u), "s"(cx) : "memory");
  asm volatile("s_waitcnt vmcnt(0)" : PP_ALL, "+v"(fl)::"memory");
  if ((__builtin_amdgcn_readfirstlane(fl) & 255u) != 8u) wait_count(cx, 255u, 8u);
  x1out(1);
  x_loads<0>(xr, xsrc, voff);
  s_aloads<0>(asp, voff, make_iseq<W_A0>{});
  x_put<0, W_A0>(xr, smem, wave, lane);
  barrier();
  if (threadIdx.x == 0) arrive(cx, 256u);
  WC w;
  w.ra[0] = ldsb + voff; w.ra[1] = w.ra[0] + 65536;
  w.sa = ldsb + T4_ST + fg * 16;
  w.vo = voff; w.as = asp;
  float p1[2][4], p2[2][4];
  auto affine = [&](int nt) {
#pragma unroll
    for (int l = 0; l < 2; ++l)
#pragma unroll
      for (int i = 0; i < 4; ++i) w.acc[l][nt][i] = fmaf(w.rs[l][i], fmaf(w.ms[l][i], uu[nt], w.acc[l][nt][i]), vv[nt]);
  };
  auto z2part = [&](int h, int nt, int nl) {
    u32x2 z[2];
    affine(nt);
#pragma unroll
    for (int l = 0; l < 2; ++l) {
      _Float16 gz[4];
#pragma unroll
      for (int i = 0; i < 4; ++i) {
        gz[i] = (_Float16)fminf(gelu_tanh(w.acc[l][nt][i]), 65504.f);
        const float gf = (float)gz[i];
        p1[l][i] = nt ? p1[l][i] + gf : gf; p2[l][i] = nt ? fmaf(gf, gf, p2[l][i]) : gf * gf;
      }
      z[l][0] = __builtin_bit_cast(unsigned, f16x2{gz[0], gz[1]});
      z[l][1] = __builtin_bit_cast(unsigned, f16x2{gz[2], gz[3]});
    }
    st_wt(p.ws + WS_Z2 + ((mtile + h * 32) * NBLK + nb0 + nl) * 1024 + (fr + 16 * ((fg & 1) * 2 + (fg >> 1))) * 16, widen(z[0], z[1]));
  };
  auto z1part = [&](int h, int nt, int nl) {
    affine(nt);
#pragma unroll
    for (int l = 0; l < 2; ++l) {
      const f32x4 a = w.acc[l][nt];
      u16* d = reinterpret_cast<u16*>(smem + T4_Z1 + ((2 * h + l) * 6 + nl) * 512 + (fr >> 2) * 128 + fg * 32 + (fr & 3) * 2);
      const unsigned w0 = pack2(gelu_tanh(a[0]), gelu_tanh(a[1])), w1 = pack2(gelu_tanh(a[2]), gelu_tanh(a[3]));
      d[0] = (u16)w0; d[4] = (u16)(w0 >> 16); d[8] = (u16)w1; d[12] = (u16)(w1 >> 16);
    }
  };
  float* red = reinterpret_cast<float*>(smem + T4_RED);
  auto sums = [&](int h) {
#pragma unroll
    for (int l = 0; l < 2; ++l)
#pragma unroll
      for (int i = 0; i < 4; ++i) {
        const float a = row16_sum(p1[l][i]), c = row16_sum(p2[l][i]);
        if (fr == 0) *reinterpret_cast<f32x2*>(red + h * 256 + (wave * 32 + l * 16 + fg * 4 + i) * 2) = f32x2{a, c};
      }
  };
  auto publish = [&](int h) {
    if (wave == 3) {
      const float* r = red + h * 256 + (lane & 31) * 2 + (lane >> 5);
      const float v = (r[0] + r[64]) + (r[128] + r[192]);
      const long long q = (long long)((double)v * (lane < 32 ? 4294967296.0 : 262144.0)) + (lane < 32 ? (1ll << 60) + (32ll << 32) : 1ll << 60);
      __hip_atomic_fetch_add(reinterpret_cast<long long*>(p.ws + WS_ST) + (lane >> 5) * S + h * 1024 + mtile * 32 + (lane & 31), q, __ATOMIC_RELAXED, __HIP_MEMORY_SCOPE_AGENT);
      if (lane < 32) arrive(cnt + (CNT_Z2 + (mtile >> 2) * 32 + lane) * 64, h ? 256u : 1u);
    }
  };
  wi_steps<0>(w, o, make_iseq<16>{});
  z2part(0, 0, wave);
  if (wave < 2 && ok2) z2part(0, 2, nl2);
  asm volatile("global_load_dword %0, %1, %2 sc1" : "=v"(fl) : "v"(0u), "s"(cx) : "memory");
  sums(0);
  z1part(0, 1, wave);
  asm volatile("s_waitcnt vmcnt(0)" : "+v"(fl)::"memory");
  if (__builtin_amdgcn_readfirstlane(fl) != 0x808u) wait_count(cx, ~0u, 0x808u);
  x_loads<1>(xr, xsrc, voff);
  s_aputs(ldsb + wave * 32768 + voff, make_iseq<16>{});
  asm volatile("s_waitcnt lgkmcnt(0)" ::: "memory");
  s_aloads<32>(asp, voff, make_iseq<W_A0>{});
  if (wave >= 2 && ok2) z1part(0, 2, nl2);
  x_put<1, W_A0>(xr, smem, wave, lane);
  barrier();
  publish(0);
  wi_steps<1>(w, o, make_iseq<16>{});
  z2part(1, 0, wave);
  if (wave < 2 && ok2) z2part(1, 2, nl2);
  u32x4 sp[4];
  st_load(sp, p.ws + WS_ST + wave * 2048, voff);
  sums(1);
  asm volatile("s_waitcnt vmcnt(0) lgkmcnt(0)\n\ts_barrier" : "+v"(sp[0]), "+v"(sp[1]), "+v"(sp[2]), "+v"(sp[3])::"memory");
  publish(1);
  st_check(sp, p.ws + WS_ST + wave * 2048, voff, cnt + (CNT_Z2 + (wave * 2 + (lane & 1)) * 32 + j) * 64, 0x20u);
  auto mid = [&](int i) {
    lane = lane_id(); fr = lane & 15; fg = lane >> 4;
    if (i == 0) z1part(1, 1, wave);
    else if (wave >= 2 && ok2) z1part(1, 2, nl2);
  };
  int nt5 = ntile;
  if (ntile >= 5) {
    t4_spatial<6>(smem, p, wave, mtile, ntile, nb0, sp, mid);
    asm volatile("" : "=v"(sp[0]), "=v"(sp[1]), "=v"(sp[2]), "=v"(sp[3]), "=v"(w.acc[0][1]), "=v"(w.acc[1][1]), "=v"(w.acc[0][2]), "=v"(w.acc[1][2]),
                      "=v"(uu[1]), "=v"(uu[2]), "=v"(vv[1]), "=v"(vv[2]));
#pragma unroll
    for (int i = 0; i < 8; ++i) asm volatile("" : "=v"(w.rs[i >> 2][i & 3]), "=v"(w.ms[i >> 2][i & 3]));
  }
  asm volatile("" : "+s"(nt5));
  if (nt5 < 5) t4_spatial<5>(smem, p, wave, mtile, ntile, nb0, sp, mid);
}
__global__ __launch_bounds__(256, 1) void k_attn(char* ws, const u16* __restrict__ wq, const u16* __restrict__ x,
                                                 const u16* __restrict__ win, const u16* __restrict__ wsp,
                                                 const u16* __restrict__ bsp, u16* __restrict__ out) {
  __shared__ __attribute__((aligned(16))) char smem[AT_MISC + 32768];
  const char *xf = ws + WS_H1, *Ki = ws + WS_KI, *Vi = ws + WS_VI;
  const float* mod = reinterpret_cast<const float*>(ws + WS_MOD);
  char* ctx = ws + WS_CTX;
  const int b = blockIdx.x, xcd = b & 7, j = b >> 3;
  const int head = xcd * 4 + (j & 3), pair = j >> 2, qb0 = pair;
  const int wave = __builtin_amdgcn_readfirstlane(threadIdx.x >> 6), kg = wave >> 1, rg = wave & 1;
  AC c;
  const unsigned ldsb = lds_addr(smem);
  const char* pc;
  CODE_PC(pc, "at");
  c.voff = (threadIdx.x & 63) * 16u;
  c.rb = ldsb + kg * 16384 + c.voff;
  c.mk = ldsb + wave * 8192;
  c.mv = ldsb + 32768 + wave * 8192;
  c.Kh = Ki + head * HS + wave * 8192;
  c.Vh = Vi + head * HS + wave * 8192;
  c.end0 = qb0 + 1;
  {
    const int lane = threadIdx.x & 63, G = lane >> 4, pos = lane & 15;
    const int nh = G & 1, h = G >> 1, q4 = pos >> 2, p4 = lane & 3;
    constexpr int WB0 = 98304;
    QP q;
    q.voffw = (unsigned)(G * 8192 + (pos ^ (G << 2)) * 16);
    q.wsrc = reinterpret_cast<const char*>(wq) + head * 256 + wave * 131072;
    q.xsrc = xf + (size_t)((kg ? 15 - qb0 : qb0) * 4 + rg * 2) * 32768;
    q.wl = ldsb + 65536 + wave * 4096;
#pragma unroll
    for (int db = 0; db < 4; ++db) q.wa[db] = ldsb + 65536 + h * 2048 + q4 * 256 + p4 * 8 + ((2 * db + nh) ^ (2 * q4)) * 32;
    q.ta = ldsb + AT_MISC + h * 64;
    f32x2 cav, sav;
    asm volatile("global_load_dwordx2 %0, %2, %3 offset:2048\n\tglobal_load_dwordx2 %1, %2, %3" : "=&v"(cav), "=&v"(sav) : "v"(threadIdx.x * 8u), "s"(mod) : "memory");
    qp_pre_ops<0>(q, c, make_iseq<10>{});
    CODE_TOUCH(pc, "at", "op", (j * 4 + wave) & 7, threadIdx.x & 63, ldsb + AT_MISC + 4096);
    q.side = b < 4 * NBLK;
    const int blk = q.side ? b >> 1 : 0, part = (b & 1) * 4 + wave;
    q.sl = ldsb + AT_MISC + 8192 + wave * 4096;
    q.sw = reinterpret_cast<const char*>(win + part * 64 * FF + blk * 16);
    q.sm = reinterpret_cast<const char*>(mod + 3 * DM + part * 64);
    qp_pre_ops<10>(q, c, make_iseq<QS.npre - 10>{});
    GAP();
    q.wo_ = q.sl + G * 256 + q4 * 32 + p4 * 8; q.mo_ = q.sl + 2048 + G * 32;
    q.sd = ws + WS_WF + (part * (4 * NBLK) + blk) * 1024;
    q.su = q.sv = 0.f;
    asm volatile("s_waitcnt vmcnt(%c2)" : "+v"(cav), "+v"(sav) : "i"(qp_younger(1, 0)) : "memory");
    {
      float* tp = reinterpret_cast<float*>(smem + AT_MISC) + (threadIdx.x >> 2) * 16 + (threadIdx.x & 3) * 2;
      *reinterpret_cast<f32x2*>(tp) = f32x2{(1.f + cav[0]) * QSCALE, (1.f + cav[1]) * QSCALE};
      *reinterpret_cast<f32x2*>(tp + 8) = f32x2{sav[0] * QSCALE, sav[1] * QSCALE};
    }
    asm volatile("s_waitcnt vmcnt(%c2) lgkmcnt(0)\n\ts_barrier" : QP_X(0) : "i"(qp_younger(101, 0)) : "memory");
    t_read<0, 0>(q); t_read<0, 1>(q); t_read<0, 2>(q); t_read<0, 3>(q);
    qp_pre<0>(q, make_iseq<8>{});
    t_read<1, 0>(q); t_read<1, 1>(q); t_read<1, 2>(q); t_read<1, 3>(q);
    qp_pre<1>(q, make_iseq<8>{});
    asm volatile("s_waitcnt lgkmcnt(15)" : QP_T(0)::"memory");
    qp_h0(q, make_iseq<8>{});
    asm volatile("s_nop 1" ::: "memory");
    qp_steps(q, c, make_iseq<8>{});
    qp_steps(q, c, iseq<8, 9, 10, 11, 12, 13, 14, 15>{});
    qp_steps(q, c, iseq<16, 17, 18, 19, 20, 21, 22, 23>{});
    qp_steps(q, c, iseq<24, 25, 26, 27, 28, 29, 30, 31>{});
    asm volatile("s_waitcnt vmcnt(%c0)\n\ts_barrier" ::"i"(qp_younger(307, 32)) : "memory");
    k_reads(c.rb, make_iseq<16>{});
    c.kt = 2 - c.end0; c.vt = 1 - c.end0;
    {
      auto s1 = __builtin_amdgcn_permlane16_swap(__float_as_uint(q.su), __float_as_uint(q.sv), false, false);
      const float m = __uint_as_float(s1[0]) + __uint_as_float(s1[1]);
      auto s2 = __builtin_amdgcn_permlane32_swap(__float_as_uint(m), __float_as_uint(m), false, false);
      if (q.side && lane < 32) st_wt(reinterpret_cast<float*>(ws + WS_UV) + blk * 256 + part + lane * 8, __uint_as_float(s2[0]) + __uint_as_float(s2[1]));
    }
    q_saves(smem + (kg ? AT_MISC : WB0) + rg * 16384 + c.voff, make_iseq<16>{});
    barrier();
    q_fetches(ldsb + WB0 + rg * 16384 + c.voff, make_iseq<16>{});
    asm volatile("s_waitcnt lgkmcnt(0)\n\tv_xor_b32_e32 %0, 0x10000, %0" : "+v"(c.rb)::"memory");
    c.mv ^= 65536u;
  }
  Fin f;
  f.rx = kg ? 65536u : 0u;
  f.ro = (kg ? -16384u : 32768u) + rg * 16384u;
  f.mb = ldsb + AT_MISC + (wave ^ 2) * 1024;
  c.qa = ldsb + AT_MISC + rg * 16384;
  c.sig = reinterpret_cast<unsigned*>(ws + WS_CNT) + CNT_CX + (xcd * 16 + qb0) * 64;
  c.wi = ws + WS_WO + (xcd * 4 + (j & 3)) * 131072 + wave * 8192;
  c.wp = reinterpret_cast<const char*>(wsp) + (((j >> 2) & 1) * 1024 + (xcd * 4 + (j >> 3)) * 32 + (j & 3) * 8) * 4096 + wave * 8192;
  c.pe = 0;
  typedef u16* P16;
  typedef const __attribute__((address_space(4))) P16* ArgP;
  ArgP ka = (ArgP)__builtin_amdgcn_kernarg_segment_ptr();
  for (int it = 0; it < 2; ++it) {
    const int qb = it ? 15 - qb0 : qb0, T = qb + 1;
    c.dd = kg - 2 * qb - rg;
    c.ddq = it ? -1000 : kg - rg;
    c.dds = it && wave == 0 ? c.dd + 2 : -1000;
    c.pend = false;
    if (T & 1) {
      if (it) phaseA<1, 2>(c, f); else phaseA<1, 1>(c, f);
      phaseB<1, true>(c);
    } else {
      if (it) phaseA<0, 2>(c, f); else phaseA<0, 1>(c, f);
      phaseB<0, true>(c);
      phaseA<1, 0>(c, f);
      phaseB<1, false>(c);
    }
    for (int t = (T - 1) >> 1; t > 0; --t) {
      phaseA<0, 0>(c, f);
      phaseB<0, false>(c);
      phaseA<1, 0>(c, f);
      phaseB<1, false>(c);
    }
    f.cb[0] = ctx + (((xcd * 16 + qb) * 4 + rg * 2) * 32 + (j & 3) * 8 + kg * 4) * 1024;
    f.cb[1] = f.cb[0] + 32768;
    if (it) break;
    at_finish<1, false>(c, (c.rb ^ f.rx ^ 65536u) + (kg + rg) * 16384u, (f.mb ^ 2048u) + lane_id() * 16, nullptr, make_iseq<32>{});
    pf_line(c.wp + lane_id() * 128, smem + AT_MISC + 4096);
    asm volatile("" : "+s"(ka));
    if (j == 0 && wave == 0) pf_line(ka[5] + (lane_id() & 31) * 64, smem + AT_MISC + 4096);
    const int bc = b < 2 * NBLK ? b >> 1 : (b >> 1) - NBLK;
    if (b < 4 * NBLK && threadIdx.x == 0) arrive(reinterpret_cast<unsigned*>(ws + WS_CNT) + (64 + (bc < 25 ? bc / 5 : 5 + (bc - 25) / 6)) * 64, 1u);
    GAP();
  }
  if (__builtin_amdgcn_readfirstlane(c.pe) != 4u) wait_count(c.sig, ~0u, 4u);
  asm volatile("global_load_dword %0, %1, %2 sc1" : "=v"(c.pe) : "v"(0u), "s"(ws + WS_CNT + (64 + (j & 7)) * 256) : "memory");
  at_finish<1, true>(c, (c.rb ^ f.rx ^ 65536u) + (kg + rg) * 16384u, (f.mb ^ 2048u) + lane_id() * 16, ctx + ((xcd * 16 + qb0) * 4 + wave) * 32768, make_iseq<32>{});
  asm volatile(".Lct_op:");
  int wv = wave;
  asm volatile("" : "+s"(ka), "+s"(wv));
  const TP p = {ka[2], ka[4], ka[5], ka[6], ws};
  t4_main(f, c, smem, p, wv, c.pe);
  asm volatile(".Lct_end:");
}

extern "C" void kernel_launch(void* const* d_in, const int* in_sizes, int n_in, void* d_out, int out_size,
                              void* d_ws, size_t ws_size, hipStream_t stream) {
  const u16* x = (const u16*)d_in[0];
  const u16* ckv = (const u16*)d_in[1];
  const u16* wq = (const u16*)d_in[2];
  const u16* wk = (const u16*)d_in[3];
  const u16* wv = (const u16*)d_in[4];
  const u16* wo = (const u16*)d_in[5];
  const u16* win = (const u16*)d_in[7];
  const u16* wsp = (const u16*)d_in[8];
  const u16* bsp = (const u16*)d_in[9];
  const u16* wout = (const u16*)d_in[10];
  const u16* c = (const u16*)d_in[11];
  const u16* wmod = (const u16*)d_in[12];
  char* ws = (char*)d_ws;

  k_kv<<<256, 256, 0, stream>>>(ckv, wk, wv, ws + WS_KI, ws + WS_VI, c, wmod, (float*)(ws + WS_MOD), x, ws + WS_H1, wq, wo, wout, (unsigned*)(ws + WS_CNT));
  k_attn<<<256, 256, 0, stream>>>(ws, wq, x, win, wsp, bsp, (u16*)d_out);
}
```
